```python
import torch

def _softmax(x, axis=-1):
    return torch.softmax(x, dim=axis)
def _gelu(x):
    return torch.nn.functional.gelu(x, approximate="tanh")
def _silu(x):
    return torch.nn.functional.silu(x)
def _sigmoid(x):
    return torch.sigmoid(x)
def _layernorm(x, axis=-1, eps=1e-5):
    m = x.mean(dim=axis, keepdim=True)
    v = x.var(dim=axis, keepdim=True, unbiased=False)
    return (x - m) / torch.sqrt(v + eps)
def _scatter_add(tgt, idx, src, axis=0):
    return tgt.clone().index_add_(axis, idx, src)
def _segment_sum(src, seg, n):
    out = torch.zeros((n,) + src.shape[1:], dtype=src.dtype)
    return out.index_add_(0, seg, src)
def _pad(x, pw):
    flat = tuple(v for pair in reversed(pw) for v in pair)
    return torch.nn.functional.pad(x, flat)

kernel_name = 'parallel_sum__compressed_kv_cla__swiglu_ffn__prompt'

def setup_inputs():

    _w = (lambda a: (lambda t: t.to(torch.bfloat16) if t.is_floating_point() else t)(torch.as_tensor(a)))
    def _A_setup():
        B, H, Sq, Sk, D, M = 1, 32, 2048, 2048, 128, 512
        Dc = 4 * D
        return {
            "x": _w((lambda *s: torch.randn(*s, dtype=torch.bfloat16) * 0.05)(B, Sq, M)),
            "c_kv": _w((lambda *s: torch.randn(*s, dtype=torch.bfloat16) * 1.6)(B, Sk, Dc)),
            "w_q": _w((lambda *s: torch.randn(*s, dtype=torch.bfloat16) * 0.8)(M, H, D) * (M ** -0.5)),
            "w_uk": _w((lambda *s: torch.randn(*s, dtype=torch.bfloat16) * 0.8)(Dc, H, D) * (Dc ** -0.5)),
            "w_uv": _w((lambda *s: torch.randn(*s, dtype=torch.bfloat16) * 1.6)(Dc, H, D) * (Dc ** -0.5)),
            "w_o": _w((lambda *s: torch.randn(*s, dtype=torch.bfloat16) * 1.6)(H, D, M) * ((H * D) ** -0.5)),
        }
    _A_in = _A_setup()

    def _B_setup():
        B, S, D, F = 1, 2048, 512, 1792
        return {
            "x": _w((lambda *s: torch.randn(*s, dtype=torch.bfloat16) * 0.05)(B, S, D)),
            "w_gate": _w((lambda *s: torch.randn(*s, dtype=torch.bfloat16) * 0.05)(D, F)),
            "w_up": _w((lambda *s: torch.randn(*s, dtype=torch.bfloat16) * 0.05)(D, F)),
            "w_down": _w((lambda *s: torch.randn(*s, dtype=torch.bfloat16) * 0.05)(F, D)),
        }
    _B_in = _B_setup()

    return {
        "A_x": _A_in["x"],
        "A_c_kv": _A_in["c_kv"],
        "A_w_q": _A_in["w_q"],
        "A_w_uk": _A_in["w_uk"],
        "A_w_uv": _A_in["w_uv"],
        "A_w_o": _A_in["w_o"],
        "B_x": _B_in["x"],
        "B_w_gate": _B_in["w_gate"],
        "B_w_up": _B_in["w_up"],
        "B_w_down": _B_in["w_down"],
    }

def reference(A_x, A_c_kv, A_w_q, A_w_uk, A_w_uv, A_w_o, B_x, B_w_gate, B_w_up, B_w_down):
    def _A_ref(x, c_kv, w_q, w_uk, w_uv, w_o):
        B, Sq, M = x.shape[0], x.shape[1], x.shape[2]
        Sk = c_kv.shape[1]
        H, D = w_q.shape[1], w_q.shape[2]
        q = torch.einsum("bqm,mhd->bhqd", x, w_q)
        k = torch.einsum("bkc,chd->bhkd", c_kv, w_uk)
        v = torch.einsum("bkc,chd->bhkd", c_kv, w_uv)
        scores = torch.einsum("bhqd,bhkd->bhqk", q, k) * (D ** -0.5)
        off = Sk - Sq
        qi = torch.arange(Sq)[:, None]
        kj = torch.arange(Sk)[None, :]
        cmask = kj <= (qi + off)
        scores = torch.where(cmask[None, None], scores, -1e9)
        p = _softmax(scores, axis=-1)
        ctx = torch.einsum("bhqk,bhkd->bhqd", p, v)
        return torch.einsum("bhqd,hdm->bqm", ctx, w_o)

    def _B_ref(x, w_gate, w_up, w_down):
        g = _silu(x @ w_gate)
        u = x @ w_up
        return (g * u) @ w_down

    x = A_x
    xn = _layernorm(x, axis=-1)
    return x + _A_ref(xn, A_c_kv, A_w_q, A_w_uk, A_w_uv, A_w_o) + _B_ref(xn, B_w_gate, B_w_up, B_w_down)
```

```cpp
#include <hip/hip_runtime.h>
#include <hip/hip_bf16.h>
#include <stdint.h>
typedef unsigned short u16;
__device__ __forceinline__ float bf2f(u16 x){ return __uint_as_float(((unsigned)x)<<16); }
__device__ __forceinline__ u16 f2bf(float f){ unsigned u=__float_as_uint(f); u += 0x7fff + ((u>>16)&1); return (u16)(u>>16); }
constexpr int S=2048, M=512, H=32, D=128, HD=4096, F=1792;

__global__ void ln_k(const u16* x, u16* xn){
  int row=blockIdx.x; int t=threadIdx.x;
  float v[8]; float s=0;
  for(int i=0;i<8;i++){ v[i]=bf2f(x[row*M+t*8+i]); s+=v[i]; }
  for(int o=32;o>0;o>>=1) s+=__shfl_xor(s,o);
  float m=s/M; float q=0;
  for(int i=0;i<8;i++){ float d=v[i]-m; q+=d*d; }
  for(int o=32;o>0;o>>=1) q+=__shfl_xor(q,o);
  float r=1.0f/sqrtf(q/M+1e-5f);
  for(int i=0;i<8;i++) xn[row*M+t*8+i]=f2bf((v[i]-m)*r);
}
__global__ void gemm_k(const u16* A,const u16* B,u16* C,int N,int K,int lda,int ldb,int ldc){
  __shared__ float As[16][17], Bs[16][17];
  int tx=threadIdx.x, ty=threadIdx.y; int row=blockIdx.y*16+ty, col=blockIdx.x*16+tx;
  float acc=0;
  for(int k0=0;k0<K;k0+=16){
    As[ty][tx]=bf2f(A[(size_t)row*lda+k0+tx]); Bs[ty][tx]=bf2f(B[(size_t)(k0+ty)*ldb+col]);
    __syncthreads();
    for(int k=0;k<16;k++) acc+=As[ty][k]*Bs[k][tx];
    __syncthreads();
  }
  C[(size_t)row*ldc+col]=f2bf(acc);
}
__global__ void attn_k(const u16* Q,const u16* K,const u16* V,u16* ctx){
  int row=blockIdx.x, h=blockIdx.y, t=threadIdx.x;
  __shared__ float q[D]; __shared__ float p[S]; __shared__ float red[128];
  q[t]=bf2f(Q[(size_t)row*HD+h*D+t]); __syncthreads();
  float mx=-1e30f;
  for(int j=t;j<=row;j+=128){ float s=0; const u16* kp=K+(size_t)j*HD+h*D; for(int d=0;d<D;d++) s+=q[d]*bf2f(kp[d]); s*=0.08838834764831845f; p[j]=s; mx=fmaxf(mx,s);}
  red[t]=mx; __syncthreads();
  for(int o=64;o>0;o>>=1){ if(t<o) red[t]=fmaxf(red[t],red[t+o]); __syncthreads(); }
  mx=red[0]; __syncthreads();
  float l=0; for(int j=t;j<=row;j+=128){ float e=__expf(p[j]-mx); p[j]=e; l+=e; }
  red[t]=l; __syncthreads();
  for(int o=64;o>0;o>>=1){ if(t<o) red[t]+=red[t+o]; __syncthreads(); }
  l=red[0];
  float acc=0; for(int j=0;j<=row;j++) acc+=p[j]*bf2f(V[(size_t)j*HD+h*D+t]);
  ctx[(size_t)row*HD+h*D+t]=f2bf(acc/l);
}
__global__ void act_k(const u16* g,const u16* u,u16* hh,int n){
  int i=blockIdx.x*256+threadIdx.x; if(i<n){ float a=bf2f(g[i]); float s=a/(1.f+__expf(-a)); hh[i]=f2bf(s*bf2f(u[i])); }
}
__global__ void final_k(const u16* x,const u16* ctx,const u16* wo,const u16* hh,const u16* wd,u16* out){
  __shared__ float As[16][17], Bs[16][17];
  int tx=threadIdx.x, ty=threadIdx.y; int row=blockIdx.y*16+ty, col=blockIdx.x*16+tx;
  float acc=0;
  for(int k0=0;k0<HD;k0+=16){
    As[ty][tx]=bf2f(ctx[(size_t)row*HD+k0+tx]); Bs[ty][tx]=bf2f(wo[(size_t)(k0+ty)*M+col]);
    __syncthreads();
    for(int k=0;k<16;k++) acc+=As[ty][k]*Bs[k][tx];
    __syncthreads();
  }
  for(int k0=0;k0<F;k0+=16){
    As[ty][tx]=bf2f(hh[(size_t)row*F+k0+tx]); Bs[ty][tx]=bf2f(wd[(size_t)(k0+ty)*M+col]);
    __syncthreads();
    for(int k=0;k<16;k++) acc+=As[ty][k]*Bs[k][tx];
    __syncthreads();
  }
  out[(size_t)row*M+col]=f2bf(acc+bf2f(x[(size_t)row*M+col]));
}
extern "C" void kernel_launch(void* const* d_in, const int* in_sizes, int n_in, void* d_out, int out_size, void* d_ws, size_t ws_size, hipStream_t stream) {
  const u16* x=(const u16*)d_in[0]; const u16* ckv=(const u16*)d_in[1]; const u16* wq=(const u16*)d_in[2]; const u16* wuk=(const u16*)d_in[3];
  const u16* wuv=(const u16*)d_in[4]; const u16* wo=(const u16*)d_in[5]; const u16* wg=(const u16*)d_in[7]; const u16* wu=(const u16*)d_in[8]; const u16* wd=(const u16*)d_in[9];
  u16* ws=(u16*)d_ws; u16* xn=ws; u16* Q=xn+S*M; u16* K=Q+(size_t)S*HD; u16* V=K+(size_t)S*HD; u16* ctx=V+(size_t)S*HD; u16* g=ctx+(size_t)S*HD; u16* u=g+(size_t)S*F; u16* hh=u+(size_t)S*F;
  ln_k<<<S,64,0,stream>>>(x,xn);
  dim3 b(16,16);
  gemm_k<<<dim3(HD/16,S/16),b,0,stream>>>(xn,wq,Q,HD,M,M,HD,HD);
  gemm_k<<<dim3(HD/16,S/16),b,0,stream>>>(ckv,wuk,K,HD,M,M,HD,HD);
  gemm_k<<<dim3(HD/16,S/16),b,0,stream>>>(ckv,wuv,V,HD,M,M,HD,HD);
  gemm_k<<<dim3(F/16,S/16),b,0,stream>>>(xn,wg,g,F,M,M,F,F);
  gemm_k<<<dim3(F/16,S/16),b,0,stream>>>(xn,wu,u,F,M,M,F,F);
  attn_k<<<dim3(S,H),128,0,stream>>>(Q,K,V,ctx);
  act_k<<<(S*F+255)/256,256,0,stream>>>(g,u,hh,S*F);
  final_k<<<dim3(M/16,S/16),b,0,stream>>>(x,ctx,wo,hh,wd,(u16*)d_out);
}
```

```cpp
#include <hip/hip_runtime.h>
#include <stdint.h>
typedef unsigned short u16;

#define WS_XN  1048576
#define WS_K   4194304
#define WS_V   20971520
#define WS_CTX 46137344

__device__ __forceinline__ float bf2f(u16 x){ return __uint_as_float(((unsigned)x)<<16); }
__device__ __forceinline__ u16 f2bf(float f){ unsigned u=__float_as_uint(f); u += 0x7fff + ((u>>16)&1); return (u16)(u>>16); }

__device__ __forceinline__ void slow_attention(char* ws, const u16* wq, int h, int p){
  extern __shared__ float lds_f[];
  const int tid=threadIdx.x, w=tid>>6, lane=tid&63;
  float* xr=lds_f+w*4096; float* qr=xr+512; float* sc=qr+128;
  asm volatile("s_waitcnt vmcnt(0)" ::: "memory");
  __builtin_amdgcn_fence(__ATOMIC_ACQUIRE,"agent");
  asm volatile("s_waitcnt vmcnt(0)" ::: "memory");
  __syncthreads();
  for(int it=0;it<64;++it){
    const int row=((it<32)?(15-p):p)*128+w*32+(it&31);
    {
      const uint4 raw=*(const uint4*)(ws+WS_XN+(size_t)(row>>4)*16384+(lane>>2)*1024+((lane&3)*16+(row&15))*16);
      xr[lane*8+0]=__uint_as_float(raw.x<<16); xr[lane*8+1]=__uint_as_float(raw.x&0xffff0000u);
      xr[lane*8+2]=__uint_as_float(raw.y<<16); xr[lane*8+3]=__uint_as_float(raw.y&0xffff0000u);
      xr[lane*8+4]=__uint_as_float(raw.z<<16); xr[lane*8+5]=__uint_as_float(raw.z&0xffff0000u);
      xr[lane*8+6]=__uint_as_float(raw.w<<16); xr[lane*8+7]=__uint_as_float(raw.w&0xffff0000u);
    }
    __syncthreads();
    float q0=0.f,q1=0.f;
    for(int k=0;k<512;++k){ const u16* wr=wq+(size_t)k*4096+h*128; const float xv=xr[k]; q0+=xv*bf2f(wr[lane]); q1+=xv*bf2f(wr[lane+64]); }
    qr[lane]=bf2f(f2bf(q0*0.12751743f)); qr[lane+64]=bf2f(f2bf(q1*0.12751743f));
    __syncthreads();
    float mx=-INFINITY;
    for(int j0=0;j0<=row;j0+=64){
      const int j=j0+lane; float s=0.f;
      const char* kp=ws+WS_K+(size_t)h*524288+(size_t)(j>>4)*4096+(j&15)*16;
      for(int f=0;f<16;++f){
        const uint4 raw=*(const uint4*)(kp+(f>>2)*1024+(f&3)*256);
        const float* qq=qr+32*(f>>2)+4*(f&3);
        s+=qq[0]*__uint_as_float(raw.x<<16)+qq[1]*__uint_as_float(raw.x&0xffff0000u)+qq[2]*__uint_as_float(raw.y<<16)+qq[3]*__uint_as_float(raw.y&0xffff0000u);
        s+=qq[16]*__uint_as_float(raw.z<<16)+qq[17]*__uint_as_float(raw.z&0xffff0000u)+qq[18]*__uint_as_float(raw.w<<16)+qq[19]*__uint_as_float(raw.w&0xffff0000u);
      }
      if(j>row) s=-INFINITY;
      sc[j]=s; mx=fmaxf(mx,s);
    }
    #pragma unroll
    for(int o=32;o>0;o>>=1) mx=fmaxf(mx,__shfl_xor(mx,o));
    float l=0.f;
    for(int j0=0;j0<=row;j0+=64){ const float e=exp2f(sc[j0+lane]-mx); sc[j0+lane]=e; l+=e; }
    #pragma unroll
    for(int o=32;o>0;o>>=1) l+=__shfl_xor(l,o);
    __syncthreads();
    const float il=1.0f/l; const int g=lane>>4;
    for(int db=0;db<8;++db){
      float o=0.f;
      for(int t=0;t*64<=row;++t){
        for(int ks=0;ks<2;++ks){
          const uint4 raw=*(const uint4*)(ws+WS_V+(size_t)h*524288+(size_t)t*16384+(db*2+ks)*1024+lane*16);
          const float* pp=sc+t*64+32*ks+4*g;
          o+=pp[0]*__uint_as_float(raw.x<<16)+pp[1]*__uint_as_float(raw.x&0xffff0000u)+pp[2]*__uint_as_float(raw.y<<16)+pp[3]*__uint_as_float(raw.y&0xffff0000u);
          o+=pp[16]*__uint_as_float(raw.z<<16)+pp[17]*__uint_as_float(raw.z&0xffff0000u)+pp[18]*__uint_as_float(raw.w<<16)+pp[19]*__uint_as_float(raw.w&0xffff0000u);
        }
      }
      o+=__shfl_xor(o,16); o+=__shfl_xor(o,32);
      if(g==0){
        const int d=db*16+lane;
        *(u16*)(ws+WS_CTX+(size_t)(row>>4)*131072+(h*4+(d>>5))*1024+(((d>>2)&3)*16+(row&15))*16+(4*((d>>4)&1)+(d&3))*2)=f2bf(o*il);
      }
    }
    __syncthreads();
  }
  __threadfence();
  asm volatile("s_waitcnt vmcnt(0)" ::: "memory");
  __syncthreads();
}

struct KArgsS { const u16* x; const u16* ckv; const u16* wq; const u16* wuk; const u16* wuv; const u16* wo; const u16* wg; const u16* wu; const u16* wd; u16* out; char* ws; };
typedef const __attribute__((address_space(4))) KArgsS* KArgs;

__global__ __launch_bounds__(256,1) void k_main(const u16* x,const u16* ckv,const u16* wq,const u16* wuk,const u16* wuv,const u16* wo,
                                                const u16* wg,const u16* wu,const u16* wd,u16* out,char* ws){
  unsigned bad, epoch;
  KArgs ka=(KArgs)__builtin_amdgcn_kernarg_segment_ptr();
  asm volatile(
    "s_mov_b64 s[48:49], %2\n"
    "s_and_b32 s49, s49, 0xffff\n"
    "s_mov_b32 s50, 0x200000\n"
    "s_mov_b32 s51, 0x20000\n"
    "s_mov_b64 s[36:37], %3\n"
    "s_and_b32 s37, s37, 0xffff\n"
    "s_mov_b32 s38, 0x400000\n"
    "s_mov_b32 s39, 0x20000\n"
    "s_mov_b64 s[44:45], %8\n"
    "s_and_b32 s45, s45, 0xffff\n"
    "s_mov_b32 s46, 0x6000000\n"
    "s_mov_b32 s47, 0x20000\n"
    "v_lshrrev_b32 v12, 6, v0\n"
    "v_and_b32 v13, 63, v0\n"
    "v_readfirstlane_b32 s52, v12\n"
    "s_and_b32 s56, %9, 7\n"
    "s_lshr_b32 s57, %9, 3\n"
    "s_lshr_b32 s58, s57, 3\n"
    "s_lshl_b32 s56, s56, 2\n"
    "s_add_u32 s53, s56, s58\n"
    "s_and_b32 s54, s57, 7\n"
    "v_lshlrev_b32 v1, 4, v13\n"
    "v_cmp_eq_u32 vcc, 0, v13\n"
    "v_mov_b32 v14, 0x7ffffff0\n"
    "v_mov_b32 v15, 0\n"
    "v_cndmask_b32 v7, v14, v15, vcc\n"
    "s_mov_b64 s[72:73], %10\n"
    "s_and_b32 s73, s73, 0xffff\n"
    "s_mov_b32 s74, 0x200000\n"
    "s_mov_b32 s75, 0x20000\n"
    "v_and_b32 v12, 7, v13\n"
    "v_bfe_u32 v14, v13, 3, 2\n"
    "v_lshrrev_b32 v15, 5, v13\n"
    "v_lshlrev_b32 v16, 10, v12\n"
    "v_lshlrev_b32 v17, 4, v14\n"
    "v_add_u32 v16, v16, v17\n"
    "v_lshlrev_b32 v17, 6, v15\n"
    "v_add_u32 v16, v16, v17\n"
    "s_lshl_b32 s56, %9, 13\n"
    "s_lshl_b32 s57, s52, 8\n"
    "s_add_u32 s56, s56, s57\n"
    "buffer_load_dwordx4 v[96:99], v16, s[72:75], s56 offen offset:0\n"
    "buffer_load_dwordx4 v[100:103], v16, s[72:75], s56 offen offset:128\n"
    "v_lshlrev_b32 v140, 10, v15\n"
    "v_lshlrev_b32 v17, 8, v14\n"
    "v_add_u32 v140, v140, v17\n"
    "v_lshlrev_b32 v17, 4, v12\n"
    "v_add_u32 v140, v140, v17\n"
    "s_mov_b32 s96, 0\n"
    "buffer_load_dword v24, v7, s[44:47], s96 offen sc1\n"
    "v_lshlrev_b32 v12, 10, v13\n"
    "s_lshl_b32 s56, s54, 18\n"
    "s_lshl_b32 s57, s52, 16\n"
    "s_add_u32 s64, s56, s57\n"
    "s_and_b32 s58, s53, 3\n"
    "s_lshl_b32 s58, s58, 7\n"
    "s_add_u32 s59, s64, s58\n"
    "v_mov_b32 v141, v12\n"
    "s_mov_b32 s81, s59\n"
    "s_add_u32 s78, s59, 512\n"
    "v_and_b32 v14, 15, v13\n"
    "v_lshlrev_b32 v14, 13, v14\n"
    "v_bfe_u32 v15, v13, 4, 1\n"
    "v_lshlrev_b32 v15, 7, v15\n"
    "v_add_u32 v14, v14, v15\n"
    "v_cmp_gt_u32 vcc, 32, v13\n"
    "v_mov_b32 v15, 0x7ffffff0\n"
    "s_nop 0\n"
    "v_cndmask_b32 v29, v15, v14, vcc\n"
    "s_lshl_b32 s56, s54, 2\n"
    "s_add_u32 s56, s56, s52\n"
    "s_mul_i32 s79, s56, 57344\n"
    "s_lshl_b32 s56, s56, 17\n"
    "s_lshl_b32 s57, s53, 8\n"
    "s_add_u32 s88, s56, s57\n"
    "s_mov_b64 s[40:41], %4\n"
    "s_and_b32 s41, s41, 0xffff\n"
    "s_mov_b32 s42, 0x400000\n"
    "s_mov_b32 s43, 0x20000\n"
    "buffer_load_dword v31, v141, s[48:51], s81 offen\n"
    "buffer_load_dword v31, v29, s[36:39], s88 offen\n"
    "v_lshrrev_b32 v12, 3, v13\n"
    "v_and_b32 v14, 7, v13\n"
    "v_bfe_u32 v15, v13, 4, 1\n"
    "v_lshlrev_b32 v15, 1, v15\n"
    "v_xor_b32 v14, v14, v15\n"
    "v_lshlrev_b32 v16, 13, v12\n"
    "v_lshlrev_b32 v17, 4, v14\n"
    "v_add_u32 v2, v16, v17\n"
    "v_xor_b32 v14, 4, v14\n"
    "v_lshlrev_b32 v17, 4, v14\n"
    "v_add_u32 v16, 0x10000, v16\n"
    "v_add_u32 v3, v16, v17\n"
    "v_lshlrev_b32 v12, 10, v12\n"
    "v_and_b32 v14, 7, v13\n"
    "v_lshrrev_b32 v15, 4, v13\n"
    "v_xor_b32 v16, v14, v15\n"
    "v_lshlrev_b32 v16, 4, v16\n"
    "v_add_u32 v9, v12, v16\n"
    "v_or_b32 v15, 4, v15\n"
    "v_xor_b32 v16, v14, v15\n"
    "v_lshlrev_b32 v16, 4, v16\n"
    "v_add_u32 v10, v12, v16\n"
    "s_mul_i32 s56, s52, 24576\n"
    "s_add_u32 s65, s56, 65536\n"
    "s_lshl_b32 s60, s52, 11\n"
    "s_lshl_b32 s90, s52, 10\n"
    "s_lshl_b32 s56, s52, 17\n"
    "s_lshl_b32 s57, s53, 8\n"
    "s_add_u32 s61, s56, s57\n"
    "s_add_u32 s57, s64, 0\n"
    "s_add_u32 m0, s65, 0\n"
    "s_nop 0\n"
    "buffer_load_dwordx4 v9, s[48:51], s57 offen offset:0 lds\n"
    "s_add_u32 s57, s64, 8192\n"
    "s_add_u32 m0, s65, 1024\n"
    "s_nop 0\n"
    "buffer_load_dwordx4 v10, s[48:51], s57 offen offset:0 lds\n"
    "s_add_u32 s57, s64, 16384\n"
    "s_add_u32 m0, s65, 2048\n"
    "s_nop 0\n"
    "buffer_load_dwordx4 v9, s[48:51], s57 offen offset:0 lds\n"
    "s_add_u32 s57, s64, 24576\n"
    "s_add_u32 m0, s65, 3072\n"
    "s_nop 0\n"
    "buffer_load_dwordx4 v10, s[48:51], s57 offen offset:0 lds\n"
    "s_add_u32 s57, s64, 32768\n"
    "s_add_u32 m0, s65, 4096\n"
    "s_nop 0\n"
    "buffer_load_dwordx4 v9, s[48:51], s57 offen offset:0 lds\n"
    "s_add_u32 s57, s64, 40960\n"
    "s_add_u32 m0, s65, 5120\n"
    "s_nop 0\n"
    "buffer_load_dwordx4 v10, s[48:51], s57 offen offset:0 lds\n"
    "s_add_u32 s57, s64, 49152\n"
    "s_add_u32 m0, s65, 6144\n"
    "s_nop 0\n"
    "buffer_load_dwordx4 v9, s[48:51], s57 offen offset:0 lds\n"
    "s_add_u32 s57, s64, 57344\n"
    "s_add_u32 m0, s65, 7168\n"
    "s_nop 0\n"
    "buffer_load_dwordx4 v10, s[48:51], s57 offen offset:0 lds\n"
    "s_add_u32 s56, s61, 0x0\n"
    "s_add_u32 m0, s60, 0\n"
    "s_nop 0\n"
    "buffer_load_dwordx4 v2, s[36:39], s56 offen lds\n"
    "s_add_u32 m0, s60, 1024\n"
    "s_nop 0\n"
    "buffer_load_dwordx4 v3, s[36:39], s56 offen lds\n"
    "s_getpc_b64 s[84:85]\n"
    ".Lkm_pc:\n"
    "s_and_b32 s84, s84, 0xffffff80\n"
    "s_and_b32 s85, s85, 0xffff\n"
    "s_mov_b32 s86, .Lkm_end-.Lkm_pc\n"
    "s_mov_b32 s87, 0x20000\n"
    "s_lshr_b32 s58, %9, 3\n"
    "s_lshl_b32 s58, s58, 2\n"
    "s_add_u32 s58, s58, s52\n"
    "s_lshl_b32 s58, s58, 13\n"
    "v_lshlrev_b32 v12, 7, v13\n"
    "buffer_load_dword v31, v12, s[84:87], s58 offen\n"
    "s_add_u32 s57, s64, 0\n"
    "s_add_u32 m0, s65, 8064\n"
    "s_nop 0\n"
    "buffer_load_dwordx4 v9, s[48:51], s57 offen offset:128 lds\n"
    "s_add_u32 s57, s64, 8192\n"
    "s_add_u32 m0, s65, 9088\n"
    "s_nop 0\n"
    "buffer_load_dwordx4 v10, s[48:51], s57 offen offset:128 lds\n"
    "s_add_u32 s57, s64, 16384\n"
    "s_add_u32 m0, s65, 10112\n"
    "s_nop 0\n"
    "buffer_load_dwordx4 v9, s[48:51], s57 offen offset:128 lds\n"
    "s_add_u32 s57, s64, 24576\n"
    "s_add_u32 m0, s65, 11136\n"
    "s_nop 0\n"
    "buffer_load_dwordx4 v10, s[48:51], s57 offen offset:128 lds\n"
    "s_add_u32 s57, s64, 32768\n"
    "s_add_u32 m0, s65, 12160\n"
    "s_nop 0\n"
    "buffer_load_dwordx4 v9, s[48:51], s57 offen offset:128 lds\n"
    "s_add_u32 s57, s64, 40960\n"
    "s_add_u32 m0, s65, 13184\n"
    "s_nop 0\n"
    "buffer_load_dwordx4 v10, s[48:51], s57 offen offset:128 lds\n"
    "s_add_u32 s57, s64, 49152\n"
    "s_add_u32 m0, s65, 14208\n"
    "s_nop 0\n"
    "buffer_load_dwordx4 v9, s[48:51], s57 offen offset:128 lds\n"
    "s_add_u32 s57, s64, 57344\n"
    "s_add_u32 m0, s65, 15232\n"
    "s_nop 0\n"
    "buffer_load_dwordx4 v10, s[48:51], s57 offen offset:128 lds\n"
    "s_add_u32 s56, s61, 0x80000\n"
    "s_add_u32 m0, s60, 16384\n"
    "s_nop 0\n"
    "buffer_load_dwordx4 v2, s[36:39], s56 offen lds\n"
    "s_add_u32 m0, s60, 17408\n"
    "s_nop 0\n"
    "buffer_load_dwordx4 v3, s[36:39], s56 offen lds\n"
    "v_lshrrev_b32 v12, 4, v13\n"
    "v_bfe_u32 v14, v13, 2, 2\n"
    "v_lshlrev_b32 v15, 3, v12\n"
    "v_add_u32 v15, v15, v14\n"
    "v_lshlrev_b32 v15, 7, v15\n"
    "v_and_b32 v16, 3, v13\n"
    "v_lshlrev_b32 v16, 3, v16\n"
    "v_add_u32 v15, v15, v16\n"
    "v_lshrrev_b32 v14, 1, v14\n"
    "v_and_b32 v12, 1, v12\n"
    "v_lshlrev_b32 v12, 1, v12\n"
    "v_or_b32 v14, v14, v12\n"
    "v_xor_b32 v16, 0, v14\n"
    "v_lshlrev_b32 v16, 5, v16\n"
    "v_add_u32 v4, v15, v16\n"
    "v_xor_b32 v16, 1, v14\n"
    "v_lshlrev_b32 v16, 5, v16\n"
    "v_add_u32 v5, v15, v16\n"
    "v_xor_b32 v16, 2, v14\n"
    "v_lshlrev_b32 v16, 5, v16\n"
    "v_add_u32 v6, v15, v16\n"
    "v_xor_b32 v16, 3, v14\n"
    "v_lshlrev_b32 v16, 5, v16\n"
    "v_add_u32 v8, v15, v16\n"
    "v_and_b32 v12, 15, v13\n"
    "v_lshrrev_b32 v14, 4, v13\n"
    "v_bfe_u32 v15, v13, 1, 3\n"
    "v_xor_b32 v14, v14, v15\n"
    "v_lshlrev_b32 v12, 7, v12\n"
    "v_add_u32 v12, s65, v12\n"
    "v_xor_b32 v15, 0, v14\n"
    "v_lshlrev_b32 v15, 4, v15\n"
    "v_add_u32 v11, v12, v15\n"
    "v_xor_b32 v15, 4, v14\n"
    "v_lshlrev_b32 v15, 4, v15\n"
    "v_add_u32 v28, v12, v15\n"
    "v_cmp_gt_u32 vcc, 16, v13\n"
    "v_mov_b32 v16, 0x7ffffff0\n"
    "v_lshlrev_b32 v12, 10, v13\n"
    "v_cndmask_b32 v14, v16, v12, vcc\n"
    "v_and_b32 v12, 7, v13\n"
    "v_lshlrev_b32 v12, 13, v12\n"
    "v_bfe_u32 v17, v13, 3, 1\n"
    "v_lshlrev_b32 v17, 7, v17\n"
    "v_add_u32 v12, v12, v17\n"
    "v_cndmask_b32 v15, v16, v12, vcc\n"
    "s_lshl_b32 s56, s53, 19\n"
    "s_lshl_b32 s57, s54, 16\n"
    "s_add_u32 s56, s56, s57\n"
    "s_lshl_b32 s57, s52, 14\n"
    "s_add_u32 s56, s56, s57\n"
    "s_add_u32 s62, s56, 0x400000\n"
    "s_add_u32 s63, s56, 0x1400000\n"
    "s_sub_u32 s56, 15, s54\n"
    "s_lshl_b32 s56, s56, 2\n"
    "s_add_u32 s56, s56, s52\n"
    "s_lshl_b32 s57, s54, 2\n"
    "s_add_u32 s57, s57, s52\n"
    "s_lshr_b32 s58, s53, 2\n"
    "s_mul_i32 s58, s58, 7\n"
    "s_and_b32 s59, s53, 3\n"
    "s_cmp_eq_u32 s59, 3\n"
    "s_cselect_b32 s55, 1, 0\n"
    "s_lshl_b32 s59, s59, 1\n"
    "s_add_u32 s58, s58, s59\n"
    "s_lshl_b32 s69, s56, 15\n"
    "s_add_u32 s69, s69, 0x100000\n"
    "s_mul_i32 s67, s56, 114688\n"
    "s_lshl_b32 s59, s58, 10\n"
    "s_add_u32 s67, s67, s59\n"
    "s_add_u32 s67, s67, 0x2400000\n"
    "s_lshl_b32 s70, s57, 15\n"
    "s_add_u32 s70, s70, 0x100000\n"
    "s_mul_i32 s68, s57, 114688\n"
    "s_lshl_b32 s59, s58, 10\n"
    "s_add_u32 s68, s68, s59\n"
    "s_add_u32 s68, s68, 0x2400000\n"
    "s_lshl_b32 s89, s58, 6\n"
    "s_and_b32 s59, s89, 0xffffff80\n"
    "s_add_u32 s79, s79, s59\n"
    "s_mul_i32 s59, s52, 57344\n"
    "s_add_u32 s66, s89, s59\n"
    "s_lshr_b32 s58, %9, 1\n"
    "s_lshl_b32 s58, s58, 14\n"
    "s_and_b32 s59, %9, 1\n"
    "s_lshl_b32 s59, s59, 7\n"
    "s_add_u32 s58, s58, s59\n"
    "s_lshl_b32 s59, s52, 12\n"
    "s_add_u32 s58, s58, s59\n"
    "s_add_u32 s91, s58, 0x100000\n"
    "s_waitcnt vmcnt(25)\n"
    "v_lshlrev_b32 v104, 16, v96\n"
    "v_and_b32 v105, 0xffff0000, v96\n"
    "v_lshlrev_b32 v106, 16, v97\n"
    "v_and_b32 v107, 0xffff0000, v97\n"
    "v_lshlrev_b32 v108, 16, v98\n"
    "v_and_b32 v109, 0xffff0000, v98\n"
    "v_lshlrev_b32 v110, 16, v99\n"
    "v_and_b32 v111, 0xffff0000, v99\n"
    "s_waitcnt vmcnt(24)\n"
    "v_lshlrev_b32 v112, 16, v100\n"
    "v_and_b32 v113, 0xffff0000, v100\n"
    "v_lshlrev_b32 v114, 16, v101\n"
    "v_and_b32 v115, 0xffff0000, v101\n"
    "v_lshlrev_b32 v116, 16, v102\n"
    "v_and_b32 v117, 0xffff0000, v102\n"
    "v_lshlrev_b32 v118, 16, v103\n"
    "v_and_b32 v119, 0xffff0000, v103\n"
    "v_add_f32 v120, v104, v108\n"
    "v_add_f32 v121, v105, v109\n"
    "v_add_f32 v122, v106, v110\n"
    "v_add_f32 v123, v107, v111\n"
    "v_add_f32 v120, v120, v112\n"
    "v_add_f32 v121, v121, v113\n"
    "v_add_f32 v122, v122, v114\n"
    "v_add_f32 v123, v123, v115\n"
    "v_add_f32 v120, v120, v116\n"
    "v_add_f32 v121, v121, v117\n"
    "v_add_f32 v122, v122, v118\n"
    "v_add_f32 v123, v123, v119\n"
    "v_add_f32 v120, v120, v121\n"
    "v_add_f32 v122, v122, v123\n"
    "v_add_f32 v120, v120, v122\n"
    "s_nop 1\n"
    "v_add_f32_dpp v120, v120, v120 row_ror:8 row_mask:0xf bank_mask:0xf\n"
    "v_mov_b32 v127, v120\n"
    "s_nop 1\n"
    "v_permlane16_swap_b32 v120, v127\n"
    "v_add_f32 v120, v120, v127\n"
    "v_mov_b32 v127, v120\n"
    "s_nop 1\n"
    "v_permlane32_swap_b32 v120, v127\n"
    "v_add_f32 v120, v120, v127\n"
    "v_mul_f32 v124, 0x3c000000, v120\n"
    "v_sub_f32 v104, v104, v124\n"
    "v_sub_f32 v105, v105, v124\n"
    "v_sub_f32 v106, v106, v124\n"
    "v_sub_f32 v107, v107, v124\n"
    "v_sub_f32 v108, v108, v124\n"
    "v_sub_f32 v109, v109, v124\n"
    "v_sub_f32 v110, v110, v124\n"
    "v_sub_f32 v111, v111, v124\n"
    "v_sub_f32 v112, v112, v124\n"
    "v_sub_f32 v113, v113, v124\n"
    "v_sub_f32 v114, v114, v124\n"
    "v_sub_f32 v115, v115, v124\n"
    "v_sub_f32 v116, v116, v124\n"
    "v_sub_f32 v117, v117, v124\n"
    "v_sub_f32 v118, v118, v124\n"
    "v_sub_f32 v119, v119, v124\n"
    "v_mul_f32 v120, v104, v104\n"
    "v_mul_f32 v121, v105, v105\n"
    "v_mul_f32 v122, v106, v106\n"
    "v_mul_f32 v123, v107, v107\n"
    "v_fmac_f32 v120, v108, v108\n"
    "v_fmac_f32 v121, v109, v109\n"
    "v_fmac_f32 v122, v110, v110\n"
    "v_fmac_f32 v123, v111, v111\n"
    "v_fmac_f32 v120, v112, v112\n"
    "v_fmac_f32 v121, v113, v113\n"
    "v_fmac_f32 v122, v114, v114\n"
    "v_fmac_f32 v123, v115, v115\n"
    "v_fmac_f32 v120, v116, v116\n"
    "v_fmac_f32 v121, v117, v117\n"
    "v_fmac_f32 v122, v118, v118\n"
    "v_fmac_f32 v123, v119, v119\n"
    "v_add_f32 v120, v120, v121\n"
    "v_add_f32 v122, v122, v123\n"
    "v_add_f32 v125, v120, v122\n"
    "s_nop 1\n"
    "v_add_f32_dpp v125, v125, v125 row_ror:8 row_mask:0xf bank_mask:0xf\n"
    "v_mov_b32 v127, v125\n"
    "s_nop 1\n"
    "v_permlane16_swap_b32 v125, v127\n"
    "v_add_f32 v125, v125, v127\n"
    "v_mov_b32 v127, v125\n"
    "s_nop 1\n"
    "v_permlane32_swap_b32 v125, v127\n"
    "v_add_f32 v125, v125, v127\n"
    "v_and_b32 v126, 7, v0\n"
    "v_lshlrev_b32 v126, 3, v126\n"
    "s_lshl_b32 s58, s52, 6\n"
    "s_add_u32 s58, s58, 57344\n"
    "v_add_u32 v126, s58, v126\n"
    "ds_write_b64 v126, v[124:125]\n"
    "s_add_u32 s57, s64, 0\n"
    "s_add_u32 m0, s65, 16128\n"
    "s_nop 0\n"
    "buffer_load_dwordx4 v9, s[48:51], s57 offen offset:256 lds\n"
    "s_add_u32 s57, s64, 8192\n"
    "s_add_u32 m0, s65, 17152\n"
    "s_nop 0\n"
    "buffer_load_dwordx4 v10, s[48:51], s57 offen offset:256 lds\n"
    "s_add_u32 s57, s64, 16384\n"
    "s_add_u32 m0, s65, 18176\n"
    "s_nop 0\n"
    "buffer_load_dwordx4 v9, s[48:51], s57 offen offset:256 lds\n"
    "s_add_u32 s57, s64, 24576\n"
    "s_add_u32 m0, s65, 19200\n"
    "s_nop 0\n"
    "buffer_load_dwordx4 v10, s[48:51], s57 offen offset:256 lds\n"
    "s_add_u32 s57, s64, 32768\n"
    "s_add_u32 m0, s65, 20224\n"
    "s_nop 0\n"
    "buffer_load_dwordx4 v9, s[48:51], s57 offen offset:256 lds\n"
    "s_add_u32 s57, s64, 40960\n"
    "s_add_u32 m0, s65, 21248\n"
    "s_nop 0\n"
    "buffer_load_dwordx4 v10, s[48:51], s57 offen offset:256 lds\n"
    "s_add_u32 s57, s64, 49152\n"
    "s_add_u32 m0, s65, 22272\n"
    "s_nop 0\n"
    "buffer_load_dwordx4 v9, s[48:51], s57 offen offset:256 lds\n"
    "s_add_u32 s57, s64, 57344\n"
    "s_add_u32 m0, s65, 23296\n"
    "s_nop 0\n"
    "buffer_load_dwordx4 v10, s[48:51], s57 offen offset:256 lds\n"
    "s_waitcnt vmcnt(31)\n"
    "v_readfirstlane_b32 s99, v24\n"
    "s_nop 0\n"
    "s_mul_i32 s97, s99, 0x9e3779b1\n"
    "s_add_u32 s97, s97, 0x7f4a7c15\n"
    "s_waitcnt vmcnt(21)\n"
    "ds_read_b128 a[0:3], v11 offset:0\n"
    "ds_read_b128 a[64:67], v11 offset:2048\n"
    "ds_read_b128 a[128:131], v11 offset:4096\n"
    "ds_read_b128 a[192:195], v11 offset:6144\n"
    "ds_read_b128 a[4:7], v28 offset:0\n"
    "ds_read_b128 a[68:71], v28 offset:2048\n"
    "ds_read_b128 a[132:135], v28 offset:4096\n"
    "ds_read_b128 a[196:199], v28 offset:6144\n"
    "s_waitcnt vmcnt(19)\n"
    "s_waitcnt lgkmcnt(0)\n"
    "s_barrier\n"
    "v_and_b32 v126, 7, v0\n"
    "v_lshlrev_b32 v126, 3, v126\n"
    "v_add_u32 v126, 57344, v126\n"
    "ds_read_b64 v[128:129], v126 offset:0\n"
    "ds_read_b64 v[130:131], v126 offset:64\n"
    "ds_read_b64 v[132:133], v126 offset:128\n"
    "ds_read_b64 v[134:135], v126 offset:192\n"
    "ds_read_b64_tr_b16 v[224:225], v4 offset:0\n"
    "ds_read_b64_tr_b16 v[226:227], v4 offset:512\n"
    "ds_read_b64_tr_b16 v[228:229], v5 offset:0\n"
    "ds_read_b64_tr_b16 v[230:231], v5 offset:512\n"
    "ds_read_b64_tr_b16 v[232:233], v6 offset:0\n"
    "ds_read_b64_tr_b16 v[234:235], v6 offset:512\n"
    "ds_read_b64_tr_b16 v[236:237], v8 offset:0\n"
    "ds_read_b64_tr_b16 v[238:239], v8 offset:512\n"
    "ds_read_b64_tr_b16 v[240:241], v4 offset:4096\n"
    "ds_read_b64_tr_b16 v[242:243], v4 offset:4608\n"
    "s_waitcnt lgkmcnt(8)\n"
    "v_mfma_f32_16x16x32_bf16 v[160:163], v[224:227], a[0:3], 0\n"
    "s_add_u32 s57, s64, 0\n"
    "ds_read_b64_tr_b16 v[244:245], v5 offset:4096\n"
    "ds_read_b64_tr_b16 v[246:247], v5 offset:4608\n"
    "v_mfma_f32_16x16x32_bf16 v[164:167], v[224:227], a[64:67], 0\n"
    "s_waitcnt lgkmcnt(8)\n"
    "s_add_u32 s56, s61, 0x100000\n"
    "s_add_u32 s76, s61, 0x80\n"
    "v_mfma_f32_16x16x32_bf16 v[168:171], v[224:227], a[128:131], 0\n"
    "s_sub_u32 m0, s65, 384\n"
    "s_nop 0\n"
    "buffer_load_dwordx4 v9, s[48:51], s57 offen offset:384 lds\n"
    "v_add_f32 v120, v128, v130\n"
    "v_mfma_f32_16x16x32_bf16 v[172:175], v[224:227], a[192:195], 0\n"
    "s_waitcnt vmcnt(11)\n"
    "ds_read_b128 a[8:11], v11 offset:8192\n"
    "v_add_f32 v121, v132, v134\n"
    "v_mfma_f32_16x16x32_bf16 v[176:179], v[228:231], a[0:3], 0\n"
    "s_add_u32 s57, s64, 8192\n"
    "ds_read_b64_tr_b16 v[248:249], v6 offset:4096\n"
    "ds_read_b64_tr_b16 v[250:251], v6 offset:4608\n"
    "v_mfma_f32_16x16x32_bf16 v[180:183], v[228:231], a[64:67], 0\n"
    "s_waitcnt lgkmcnt(9)\n"
    "v_add_f32 v120, v120, v121\n"
    "v_mul_f32 v120, 0x3e800000, v120\n"
    "v_mfma_f32_16x16x32_bf16 v[184:187], v[228:231], a[128:131], 0\n"
    "s_add_u32 m0, s65, 640\n"
    "s_nop 0\n"
    "buffer_load_dwordx4 v10, s[48:51], s57 offen offset:384 lds\n"
    "v_sub_f32 v128, v128, v120\n"
    "v_mfma_f32_16x16x32_bf16 v[188:191], v[228:231], a[192:195], 0\n"
    "ds_read_b128 a[72:75], v11 offset:10240\n"
    "v_sub_f32 v130, v130, v120\n"
    "v_mfma_f32_16x16x32_bf16 v[192:195], v[232:235], a[0:3], 0\n"
    "s_add_u32 s57, s64, 16384\n"
    "ds_read_b64_tr_b16 v[252:253], v8 offset:4096\n"
    "ds_read_b64_tr_b16 v[254:255], v8 offset:4608\n"
    "v_mfma_f32_16x16x32_bf16 v[196:199], v[232:235], a[64:67], 0\n"
    "s_waitcnt lgkmcnt(10)\n"
    "ds_read_b128 a[136:139], v11 offset:12288\n"
    "v_sub_f32 v132, v132, v120\n"
    "v_mfma_f32_16x16x32_bf16 v[200:203], v[232:235], a[128:131], 0\n"
    "s_add_u32 m0, s65, 1664\n"
    "s_nop 0\n"
    "buffer_load_dwordx4 v9, s[48:51], s57 offen offset:384 lds\n"
    "v_sub_f32 v134, v134, v120\n"
    "v_mfma_f32_16x16x32_bf16 v[204:207], v[232:235], a[192:195], 0\n"
    "v_add_f32 v121, v129, v131\n"
    "v_add_f32 v122, v133, v135\n"
    "s_waitcnt vmcnt(11)\n"
    "s_barrier\n"
    "v_mfma_f32_16x16x32_bf16 v[208:211], v[236:239], a[0:3], 0\n"
    "s_add_u32 s57, s64, 24576\n"
    "ds_read_b64_tr_b16 v[224:225], v4 offset:16384\n"
    "ds_read_b64_tr_b16 v[226:227], v4 offset:16896\n"
    "v_mfma_f32_16x16x32_bf16 v[212:215], v[236:239], a[64:67], 0\n"
    "s_waitcnt lgkmcnt(11)\n"
    "ds_read_b128 a[200:203], v11 offset:14336\n"
    "v_mul_f32 v123, v128, v128\n"
    "v_mfma_f32_16x16x32_bf16 v[216:219], v[236:239], a[128:131], 0\n"
    "s_add_u32 m0, s65, 2688\n"
    "s_nop 0\n"
    "buffer_load_dwordx4 v10, s[48:51], s57 offen offset:384 lds\n"
    "v_fmac_f32 v123, v130, v130\n"
    "v_mfma_f32_16x16x32_bf16 v[220:223], v[236:239], a[192:195], 0\n"
    "ds_read_b128 a[12:15], v28 offset:8192\n"
    "v_fmac_f32 v123, v132, v132\n"
    "v_mfma_f32_16x16x32_bf16 v[160:163], v[240:243], a[4:7], v[160:163]\n"
    "s_add_u32 s57, s64, 32768\n"
    "ds_read_b64_tr_b16 v[228:229], v5 offset:16384\n"
    "ds_read_b64_tr_b16 v[230:231], v5 offset:16896\n"
    "v_mfma_f32_16x16x32_bf16 v[164:167], v[240:243], a[68:71], v[164:167]\n"
    "s_waitcnt lgkmcnt(13)\n"
    "v_fmac_f32 v123, v134, v134\n"
    "v_add_f32 v121, v121, v122\n"
    "v_mfma_f32_16x16x32_bf16 v[168:171], v[240:243], a[132:135], v[168:171]\n"
    "s_add_u32 m0, s60, 32768\n"
    "s_nop 0\n"
    "buffer_load_dwordx4 v2, s[36:39], s56 offen lds\n"
    "v_fmac_f32 v121, 0x43000000, v123\n"
    "v_mfma_f32_16x16x32_bf16 v[172:175], v[240:243], a[196:199], v[172:175]\n"
    "s_add_u32 m0, s65, 3712\n"
    "s_nop 0\n"
    "buffer_load_dwordx4 v9, s[48:51], s57 offen offset:384 lds\n"
    "v_mov_b32 v122, 0x3727c5ac\n"
    "v_mfma_f32_16x16x32_bf16 v[176:179], v[244:247], a[4:7], v[176:179]\n"
    "s_add_u32 s57, s64, 40960\n"
    "ds_read_b64_tr_b16 v[232:233], v6 offset:16384\n"
    "ds_read_b64_tr_b16 v[234:235], v6 offset:16896\n"
    "s_add_u32 m0, s60, 33792\n"
    "s_nop 0\n"
    "buffer_load_dwordx4 v3, s[36:39], s56 offen lds\n"
    "v_mfma_f32_16x16x32_bf16 v[180:183], v[244:247], a[68:71], v[180:183]\n"
    "s_waitcnt lgkmcnt(12)\n"
    "ds_read_b128 a[76:79], v28 offset:10240\n"
    "v_fmac_f32 v122, 0x3b000000, v121\n"
    "v_mfma_f32_16x16x32_bf16 v[184:187], v[244:247], a[132:135], v[184:187]\n"
    "s_add_u32 s56, s61, 0x180000\n"
    "ds_read_b128 a[140:143], v28 offset:12288\n"
    "v_mfma_f32_16x16x32_bf16 v[188:191], v[244:247], a[196:199], v[188:191]\n"
    "s_add_u32 m0, s65, 4736\n"
    "s_nop 0\n"
    "buffer_load_dwordx4 v10, s[48:51], s57 offen offset:384 lds\n"
    "v_mfma_f32_16x16x32_bf16 v[192:195], v[248:251], a[4:7], v[192:195]\n"
    "s_add_u32 s57, s64, 49152\n"
    "ds_read_b64_tr_b16 v[236:237], v8 offset:16384\n"
    "s_waitcnt lgkmcnt(8)\n"
    "ds_read_b64_tr_b16 v[238:239], v8 offset:16896\n"
    "v_mfma_f32_16x16x32_bf16 v[196:199], v[248:251], a[68:71], v[196:199]\n"
    "ds_read_b128 a[204:207], v28 offset:14336\n"
    "v_mfma_f32_16x16x32_bf16 v[200:203], v[248:251], a[132:135], v[200:203]\n"
    "v_rsq_f32 v122, v122\n"
    "v_mfma_f32_16x16x32_bf16 v[204:207], v[248:251], a[196:199], v[204:207]\n"
    "s_add_u32 m0, s65, 5760\n"
    "s_nop 0\n"
    "buffer_load_dwordx4 v9, s[48:51], s57 offen offset:384 lds\n"
    "v_sub_f32 v124, v124, v120\n"
    "v_mfma_f32_16x16x32_bf16 v[208:211], v[252:255], a[4:7], v[208:211]\n"
    "s_add_u32 s57, s64, 57344\n"
    "ds_read_b64_tr_b16 v[240:241], v4 offset:20480\n"
    "ds_read_b64_tr_b16 v[242:243], v4 offset:20992\n"
    "v_mfma_f32_16x16x32_bf16 v[212:215], v[252:255], a[68:71], v[212:215]\n"
    "v_mul_f32 v124, v124, v122\n"
    "v_fma_f32 v104, v104, v122, v124\n"
    "v_mfma_f32_16x16x32_bf16 v[216:219], v[252:255], a[132:135], v[216:219]\n"
    "v_fma_f32 v105, v105, v122, v124\n"
    "v_fma_f32 v106, v106, v122, v124\n"
    "v_mfma_f32_16x16x32_bf16 v[220:223], v[252:255], a[196:199], v[220:223]\n"
    "s_add_u32 m0, s65, 6784\n"
    "s_nop 0\n"
    "buffer_load_dwordx4 v10, s[48:51], s57 offen offset:384 lds\n"
    "v_fma_f32 v107, v107, v122, v124\n"
    "v_mfma_f32_16x16x32_bf16 v[160:163], v[224:227], a[8:11], v[160:163]\n"
    "s_add_u32 s57, s64, 0\n"
    "ds_read_b64_tr_b16 v[244:245], v5 offset:20480\n"
    "ds_read_b64_tr_b16 v[246:247], v5 offset:20992\n"
    "v_mfma_f32_16x16x32_bf16 v[164:167], v[224:227], a[72:75], v[164:167]\n"
    "s_waitcnt lgkmcnt(11)\n"
    "v_fma_f32 v108, v108, v122, v124\n"
    "v_fma_f32 v109, v109, v122, v124\n"
    "v_mfma_f32_16x16x32_bf16 v[168:171], v[224:227], a[136:139], v[168:171]\n"
    "v_fma_f32 v110, v110, v122, v124\n"
    "v_fma_f32 v111, v111, v122, v124\n"
    "v_mfma_f32_16x16x32_bf16 v[172:175], v[224:227], a[200:203], v[172:175]\n"
    "s_add_u32 m0, s65, 7680\n"
    "s_nop 0\n"
    "buffer_load_dwordx4 v9, s[48:51], s57 offen offset:512 lds\n"
    "v_fma_f32 v112, v112, v122, v124\n"
    "v_mfma_f32_16x16x32_bf16 v[176:179], v[228:231], a[8:11], v[176:179]\n"
    "s_add_u32 s57, s64, 8192\n"
    "ds_read_b64_tr_b16 v[248:249], v6 offset:20480\n"
    "ds_read_b64_tr_b16 v[250:251], v6 offset:20992\n"
    "v_mfma_f32_16x16x32_bf16 v[180:183], v[228:231], a[72:75], v[180:183]\n"
    "s_waitcnt lgkmcnt(11)\n"
    "v_fma_f32 v113, v113, v122, v124\n"
    "v_fma_f32 v114, v114, v122, v124\n"
    "v_mfma_f32_16x16x32_bf16 v[184:187], v[228:231], a[136:139], v[184:187]\n"
    "s_add_u32 m0, s60, 8192\n"
    "s_nop 0\n"
    "buffer_load_dwordx4 v2, s[36:39], s76 offen lds\n"
    "v_fma_f32 v115, v115, v122, v124\n"
    "v_mfma_f32_16x16x32_bf16 v[188:191], v[228:231], a[200:203], v[188:191]\n"
    "s_add_u32 m0, s65, 8704\n"
    "s_nop 0\n"
    "buffer_load_dwordx4 v10, s[48:51], s57 offen offset:512 lds\n"
    "v_fma_f32 v116, v116, v122, v124\n"
    "v_mfma_f32_16x16x32_bf16 v[192:195], v[232:235], a[8:11], v[192:195]\n"
    "s_add_u32 s57, s64, 16384\n"
    "ds_read_b64_tr_b16 v[252:253], v8 offset:20480\n"
    "ds_read_b64_tr_b16 v[254:255], v8 offset:20992\n"
    "s_add_u32 m0, s60, 9216\n"
    "s_nop 0\n"
    "buffer_load_dwordx4 v3, s[36:39], s76 offen lds\n"
    "v_mfma_f32_16x16x32_bf16 v[196:199], v[232:235], a[72:75], v[196:199]\n"
    "s_waitcnt lgkmcnt(9)\n"
    "s_waitcnt vmcnt(14)\n"
    "s_add_u32 s76, s61, 0x80080\n"
    "v_mfma_f32_16x16x32_bf16 v[200:203], v[232:235], a[136:139], v[200:203]\n"
    "ds_read_b128 a[16:19], v11 offset:16384\n"
    "v_fma_f32 v117, v117, v122, v124\n"
    "v_mfma_f32_16x16x32_bf16 v[204:207], v[232:235], a[200:203], v[204:207]\n"
    "s_add_u32 m0, s65, 9728\n"
    "s_nop 0\n"
    "buffer_load_dwordx4 v9, s[48:51], s57 offen offset:512 lds\n"
    "v_fma_f32 v118, v118, v122, v124\n"
    "s_waitcnt vmcnt(8)\n"
    "s_waitcnt vmcnt(1)\n"
    "s_barrier\n"
    "v_mfma_f32_16x16x32_bf16 v[208:211], v[236:239], a[8:11], v[208:211]\n"
    "s_add_u32 s57, s64, 24576\n"
    "ds_read_b64_tr_b16 v[224:225], v4 offset:32768\n"
    "ds_read_b64_tr_b16 v[226:227], v4 offset:33280\n"
    "v_mfma_f32_16x16x32_bf16 v[212:215], v[236:239], a[72:75], v[212:215]\n"
    "s_waitcnt lgkmcnt(9)\n"
    "ds_read_b128 a[80:83], v11 offset:18432\n"
    "v_fma_f32 v119, v119, v122, v124\n"
    "v_mfma_f32_16x16x32_bf16 v[216:219], v[236:239], a[136:139], v[216:219]\n"
    "v_cvt_pk_bf16_f32 v96, v104, v105\n"
    "v_cvt_pk_bf16_f32 v97, v106, v107\n"
    "v_mfma_f32_16x16x32_bf16 v[220:223], v[236:239], a[200:203], v[220:223]\n"
    "s_add_u32 m0, s65, 10752\n"
    "s_nop 0\n"
    "buffer_load_dwordx4 v10, s[48:51], s57 offen offset:512 lds\n"
    "v_cvt_pk_bf16_f32 v98, v108, v109\n"
    "v_mfma_f32_16x16x32_bf16 v[160:163], v[240:243], a[12:15], v[160:163]\n"
    "s_add_u32 s57, s64, 32768\n"
    "ds_read_b64_tr_b16 v[228:229], v5 offset:32768\n"
    "ds_read_b64_tr_b16 v[230:231], v5 offset:33280\n"
    "s_add_u32 s81, s78, 0\n"
    "buffer_load_dword v31, v14, s[48:51], s81 offen\n"
    "v_mfma_f32_16x16x32_bf16 v[164:167], v[240:243], a[76:79], v[164:167]\n"
    "s_waitcnt lgkmcnt(10)\n"
    "ds_read_b128 a[144:147], v11 offset:20480\n"
    "v_cvt_pk_bf16_f32 v99, v110, v111\n"
    "v_mfma_f32_16x16x32_bf16 v[168:171], v[240:243], a[140:143], v[168:171]\n"
    "ds_read_b128 a[208:211], v11 offset:22528\n"
    "v_cvt_pk_bf16_f32 v100, v112, v113\n"
    "v_mfma_f32_16x16x32_bf16 v[172:175], v[240:243], a[204:207], v[172:175]\n"
    "s_add_u32 m0, s65, 11776\n"
    "s_nop 0\n"
    "buffer_load_dwordx4 v9, s[48:51], s57 offen offset:512 lds\n"
    "v_cvt_pk_bf16_f32 v101, v114, v115\n"
    "v_mfma_f32_16x16x32_bf16 v[176:179], v[244:247], a[12:15], v[176:179]\n"
    "s_add_u32 s57, s64, 40960\n"
    "ds_read_b64_tr_b16 v[232:233], v6 offset:32768\n"
    "ds_read_b64_tr_b16 v[234:235], v6 offset:33280\n"
    "s_add_u32 s81, s78, 16384\n"
    "buffer_load_dword v31, v14, s[48:51], s81 offen\n"
    "v_mfma_f32_16x16x32_bf16 v[180:183], v[244:247], a[76:79], v[180:183]\n"
    "s_waitcnt lgkmcnt(12)\n"
    "ds_read_b128 a[20:23], v28 offset:16384\n"
    "v_cvt_pk_bf16_f32 v102, v116, v117\n"
    "v_mfma_f32_16x16x32_bf16 v[184:187], v[244:247], a[140:143], v[184:187]\n"
    "ds_read_b128 a[84:87], v28 offset:18432\n"
    "v_cvt_pk_bf16_f32 v103, v118, v119\n"
    "v_mfma_f32_16x16x32_bf16 v[188:191], v[244:247], a[204:207], v[188:191]\n"
    "s_add_u32 m0, s65, 12800\n"
    "s_nop 0\n"
    "buffer_load_dwordx4 v10, s[48:51], s57 offen offset:512 lds\n"
    "v_mfma_f32_16x16x32_bf16 v[192:195], v[248:251], a[12:15], v[192:195]\n"
    "s_add_u32 s57, s64, 49152\n"
    "ds_read_b64_tr_b16 v[236:237], v8 offset:32768\n"
    "s_waitcnt lgkmcnt(8)\n"
    "ds_read_b64_tr_b16 v[238:239], v8 offset:33280\n"
    "s_add_u32 s81, s78, 32768\n"
    "buffer_load_dword v31, v14, s[48:51], s81 offen\n"
    "v_mfma_f32_16x16x32_bf16 v[196:199], v[248:251], a[76:79], v[196:199]\n"
    "ds_read_b128 a[148:151], v28 offset:20480\n"
    "v_mfma_f32_16x16x32_bf16 v[200:203], v[248:251], a[140:143], v[200:203]\n"
    "ds_read_b128 a[212:215], v28 offset:22528\n"
    "v_mfma_f32_16x16x32_bf16 v[204:207], v[248:251], a[204:207], v[204:207]\n"
    "s_add_u32 m0, s65, 13824\n"
    "s_nop 0\n"
    "buffer_load_dwordx4 v9, s[48:51], s57 offen offset:512 lds\n"
    "buffer_store_dwordx4 v[96:99], v140, s[44:47], s91 offen offset:0 sc1\n"
    "v_mfma_f32_16x16x32_bf16 v[208:211], v[252:255], a[12:15], v[208:211]\n"
    "s_add_u32 s57, s64, 57344\n"
    "ds_read_b64_tr_b16 v[240:241], v4 offset:8192\n"
    "ds_read_b64_tr_b16 v[242:243], v4 offset:8704\n"
    "s_add_u32 s81, s78, 49152\n"
    "buffer_load_dword v31, v14, s[48:51], s81 offen\n"
    "v_mfma_f32_16x16x32_bf16 v[212:215], v[252:255], a[76:79], v[212:215]\n"
    "v_mfma_f32_16x16x32_bf16 v[216:219], v[252:255], a[140:143], v[216:219]\n"
    "v_mfma_f32_16x16x32_bf16 v[220:223], v[252:255], a[204:207], v[220:223]\n"
    "s_add_u32 m0, s65, 14848\n"
    "s_nop 0\n"
    "buffer_load_dwordx4 v10, s[48:51], s57 offen offset:512 lds\n"
    "buffer_store_dwordx4 v[100:103], v140, s[44:47], s91 offen offset:2048 sc1\n"
    "v_mfma_f32_16x16x32_bf16 v[160:163], v[224:227], a[16:19], v[160:163]\n"
    "ds_read_b64_tr_b16 v[244:245], v5 offset:8192\n"
    "ds_read_b64_tr_b16 v[246:247], v5 offset:8704\n"
    "v_mfma_f32_16x16x32_bf16 v[164:167], v[224:227], a[80:83], v[164:167]\n"
    "s_waitcnt lgkmcnt(14)\n"
    "s_add_u32 s57, s64, 0\n"
    "s_waitcnt lgkmcnt(13)\n"
    "v_mfma_f32_16x16x32_bf16 v[168:171], v[224:227], a[144:147], v[168:171]\n"
    "s_waitcnt lgkmcnt(12)\n"
    "v_mfma_f32_16x16x32_bf16 v[172:175], v[224:227], a[208:211], v[172:175]\n"
    "s_add_u32 m0, s60, 49152\n"
    "s_nop 0\n"
    "buffer_load_dwordx4 v2, s[36:39], s56 offen lds\n"
    "v_mfma_f32_16x16x32_bf16 v[176:179], v[228:231], a[16:19], v[176:179]\n"
    "ds_read_b64_tr_b16 v[248:249], v6 offset:8192\n"
    "ds_read_b64_tr_b16 v[250:251], v6 offset:8704\n"
    "v_mfma_f32_16x16x32_bf16 v[180:183], v[228:231], a[80:83], v[180:183]\n"
    "s_waitcnt lgkmcnt(12)\n"
    "v_mfma_f32_16x16x32_bf16 v[184:187], v[228:231], a[144:147], v[184:187]\n"
    "ds_read_b128 a[24:27], v11 offset:0\n"
    "v_mfma_f32_16x16x32_bf16 v[188:191], v[228:231], a[208:211], v[188:191]\n"
    "s_add_u32 m0, s60, 24576\n"
    "s_nop 0\n"
    "buffer_load_dwordx4 v2, s[36:39], s76 offen lds\n"
    "v_mfma_f32_16x16x32_bf16 v[192:195], v[232:235], a[16:19], v[192:195]\n"
    "ds_read_b64_tr_b16 v[252:253], v8 offset:8192\n"
    "ds_read_b64_tr_b16 v[254:255], v8 offset:8704\n"
    "v_mfma_f32_16x16x32_bf16 v[196:199], v[232:235], a[80:83], v[196:199]\n"
    "s_waitcnt lgkmcnt(11)\n"
    "v_mfma_f32_16x16x32_bf16 v[200:203], v[232:235], a[144:147], v[200:203]\n"
    "v_mfma_f32_16x16x32_bf16 v[204:207], v[232:235], a[208:211], v[204:207]\n"
    "s_add_u32 m0, s60, 50176\n"
    "s_nop 0\n"
    "buffer_load_dwordx4 v3, s[36:39], s56 offen lds\n"
    "v_mfma_f32_16x16x32_bf16 v[208:211], v[236:239], a[16:19], v[208:211]\n"
    "ds_read_b64_tr_b16 v[224:225], v4 offset:36864\n"
    "ds_read_b64_tr_b16 v[226:227], v4 offset:37376\n"
    "v_mfma_f32_16x16x32_bf16 v[212:215], v[236:239], a[80:83], v[212:215]\n"
    "s_waitcnt lgkmcnt(9)\n"
    "ds_read_b128 a[88:91], v11 offset:2048\n"
    "v_mfma_f32_16x16x32_bf16 v[216:219], v[236:239], a[144:147], v[216:219]\n"
    "s_add_u32 s56, s61, 0x200000\n"
    "v_mfma_f32_16x16x32_bf16 v[220:223], v[236:239], a[208:211], v[220:223]\n"
    "s_add_u32 m0, s60, 25600\n"
    "s_nop 0\n"
    "buffer_load_dwordx4 v3, s[36:39], s76 offen lds\n"
    "v_mfma_f32_16x16x32_bf16 v[32:35], v[240:243], a[0:3], 0\n"
    "ds_read_b64_tr_b16 v[228:229], v5 offset:36864\n"
    "ds_read_b64_tr_b16 v[230:231], v5 offset:37376\n"
    "v_mfma_f32_16x16x32_bf16 v[36:39], v[240:243], a[64:67], 0\n"
    "s_waitcnt lgkmcnt(10)\n"
    "s_add_u32 s76, s61, 0x100080\n"
    "v_mfma_f32_16x16x32_bf16 v[40:43], v[240:243], a[128:131], 0\n"
    "v_mfma_f32_16x16x32_bf16 v[44:47], v[240:243], a[192:195], 0\n"
    "s_add_u32 m0, s65, 15744\n"
    "s_nop 0\n"
    "buffer_load_dwordx4 v9, s[48:51], s57 offen offset:640 lds\n"
    "v_mfma_f32_16x16x32_bf16 v[48:51], v[244:247], a[0:3], 0\n"
    "ds_read_b64_tr_b16 v[232:233], v6 offset:36864\n"
    "ds_read_b64_tr_b16 v[234:235], v6 offset:37376\n"
    "v_mfma_f32_16x16x32_bf16 v[52:55], v[244:247], a[64:67], 0\n"
    "s_waitcnt lgkmcnt(10)\n"
    "s_add_u32 s57, s64, 8192\n"
    "v_mfma_f32_16x16x32_bf16 v[56:59], v[244:247], a[128:131], 0\n"
    "ds_read_b128 a[152:155], v11 offset:4096\n"
    "v_mfma_f32_16x16x32_bf16 v[60:63], v[244:247], a[192:195], 0\n"
    "s_add_u32 m0, s65, 16768\n"
    "s_nop 0\n"
    "buffer_load_dwordx4 v10, s[48:51], s57 offen offset:640 lds\n"
    "v_mfma_f32_16x16x32_bf16 v[64:67], v[248:251], a[0:3], 0\n"
    "ds_read_b64_tr_b16 v[236:237], v8 offset:36864\n"
    "ds_read_b64_tr_b16 v[238:239], v8 offset:37376\n"
    "v_mfma_f32_16x16x32_bf16 v[68:71], v[248:251], a[64:67], 0\n"
    "s_waitcnt lgkmcnt(10)\n"
    "s_add_u32 s57, s64, 16384\n"
    "v_mfma_f32_16x16x32_bf16 v[72:75], v[248:251], a[128:131], 0\n"
    "ds_read_b128 a[216:219], v11 offset:6144\n"
    "v_mfma_f32_16x16x32_bf16 v[76:79], v[248:251], a[192:195], 0\n"
    "s_add_u32 m0, s65, 17792\n"
    "s_nop 0\n"
    "buffer_load_dwordx4 v9, s[48:51], s57 offen offset:640 lds\n"
    "v_mfma_f32_16x16x32_bf16 v[80:83], v[252:255], a[0:3], 0\n"
    "ds_read_b64_tr_b16 v[240:241], v4 offset:12288\n"
    "ds_read_b64_tr_b16 v[242:243], v4 offset:12800\n"
    "v_mfma_f32_16x16x32_bf16 v[84:87], v[252:255], a[64:67], 0\n"
    "s_waitcnt lgkmcnt(11)\n"
    "s_add_u32 s57, s64, 24576\n"
    "v_mfma_f32_16x16x32_bf16 v[88:91], v[252:255], a[128:131], 0\n"
    "v_mfma_f32_16x16x32_bf16 v[92:95], v[252:255], a[192:195], 0\n"
    "s_add_u32 m0, s65, 18816\n"
    "s_nop 0\n"
    "buffer_load_dwordx4 v10, s[48:51], s57 offen offset:640 lds\n"
    "v_mfma_f32_16x16x32_bf16 v[160:163], v[224:227], a[20:23], v[160:163]\n"
    "ds_read_b64_tr_b16 v[244:245], v5 offset:12288\n"
    "ds_read_b64_tr_b16 v[246:247], v5 offset:12800\n"
    "v_mfma_f32_16x16x32_bf16 v[164:167], v[224:227], a[84:87], v[164:167]\n"
    "s_waitcnt lgkmcnt(10)\n"
    "s_add_u32 s57, s64, 32768\n"
    "v_mfma_f32_16x16x32_bf16 v[168:171], v[224:227], a[148:151], v[168:171]\n"
    "ds_read_b128 a[28:31], v28 offset:0\n"
    "v_mfma_f32_16x16x32_bf16 v[172:175], v[224:227], a[212:215], v[172:175]\n"
    "s_add_u32 m0, s65, 19840\n"
    "s_nop 0\n"
    "buffer_load_dwordx4 v9, s[48:51], s57 offen offset:640 lds\n"
    "v_mfma_f32_16x16x32_bf16 v[176:179], v[228:231], a[20:23], v[176:179]\n"
    "ds_read_b64_tr_b16 v[248:249], v6 offset:12288\n"
    "ds_read_b64_tr_b16 v[250:251], v6 offset:12800\n"
    "v_mfma_f32_16x16x32_bf16 v[180:183], v[228:231], a[84:87], v[180:183]\n"
    "s_waitcnt lgkmcnt(11)\n"
    "v_mfma_f32_16x16x32_bf16 v[184:187], v[228:231], a[148:151], v[184:187]\n"
    "v_mfma_f32_16x16x32_bf16 v[188:191], v[228:231], a[212:215], v[188:191]\n"
    "s_add_u32 m0, s60, 0\n"
    "s_nop 0\n"
    "buffer_load_dwordx4 v2, s[36:39], s56 offen lds\n"
    "v_mfma_f32_16x16x32_bf16 v[192:195], v[232:235], a[20:23], v[192:195]\n"
    "ds_read_b64_tr_b16 v[252:253], v8 offset:12288\n"
    "ds_read_b64_tr_b16 v[254:255], v8 offset:12800\n"
    "v_mfma_f32_16x16x32_bf16 v[196:199], v[232:235], a[84:87], v[196:199]\n"
    "s_waitcnt lgkmcnt(10)\n"
    "s_add_u32 s57, s64, 40960\n"
    "v_mfma_f32_16x16x32_bf16 v[200:203], v[232:235], a[148:151], v[200:203]\n"
    "ds_read_b128 a[92:95], v28 offset:2048\n"
    "v_mfma_f32_16x16x32_bf16 v[204:207], v[232:235], a[212:215], v[204:207]\n"
    "s_add_u32 m0, s65, 20864\n"
    "s_nop 0\n"
    "buffer_load_dwordx4 v10, s[48:51], s57 offen offset:640 lds\n"
    "s_waitcnt vmcnt(8)\n"
    "s_waitcnt vmcnt(7)\n"
    "s_barrier\n"
    "v_mfma_f32_16x16x32_bf16 v[208:211], v[236:239], a[20:23], v[208:211]\n"
    "ds_read_b64_tr_b16 v[224:225], v4 offset:49152\n"
    "ds_read_b64_tr_b16 v[226:227], v4 offset:49664\n"
    "v_mfma_f32_16x16x32_bf16 v[212:215], v[236:239], a[84:87], v[212:215]\n"
    "s_waitcnt lgkmcnt(10)\n"
    "ds_read_b128 a[156:159], v28 offset:4096\n"
    "v_mfma_f32_16x16x32_bf16 v[216:219], v[236:239], a[148:151], v[216:219]\n"
    "v_mfma_f32_16x16x32_bf16 v[220:223], v[236:239], a[212:215], v[220:223]\n"
    "s_add_u32 m0, s60, 40960\n"
    "s_nop 0\n"
    "buffer_load_dwordx4 v2, s[36:39], s76 offen lds\n"
    "v_mfma_f32_16x16x32_bf16 v[32:35], v[240:243], a[4:7], v[32:35]\n"
    "ds_read_b64_tr_b16 v[228:229], v5 offset:49152\n"
    "ds_read_b64_tr_b16 v[230:231], v5 offset:49664\n"
    "v_mfma_f32_16x16x32_bf16 v[36:39], v[240:243], a[68:71], v[36:39]\n"
    "s_waitcnt lgkmcnt(11)\n"
    "s_add_u32 s57, s64, 49152\n"
    "v_mfma_f32_16x16x32_bf16 v[40:43], v[240:243], a[132:135], v[40:43]\n"
    "v_mfma_f32_16x16x32_bf16 v[44:47], v[240:243], a[196:199], v[44:47]\n"
    "s_add_u32 m0, s65, 21888\n"
    "s_nop 0\n"
    "buffer_load_dwordx4 v9, s[48:51], s57 offen offset:640 lds\n"
    "v_mfma_f32_16x16x32_bf16 v[48:51], v[244:247], a[4:7], v[48:51]\n"
    "ds_read_b64_tr_b16 v[232:233], v6 offset:49152\n"
    "ds_read_b64_tr_b16 v[234:235], v6 offset:49664\n"
    "v_mfma_f32_16x16x32_bf16 v[52:55], v[244:247], a[68:71], v[52:55]\n"
    "s_waitcnt lgkmcnt(10)\n"
    "ds_read_b128 a[220:223], v28 offset:6144\n"
    "v_mfma_f32_16x16x32_bf16 v[56:59], v[244:247], a[132:135], v[56:59]\n"
    "v_mfma_f32_16x16x32_bf16 v[60:63], v[244:247], a[196:199], v[60:63]\n"
    "s_add_u32 m0, s60, 1024\n"
    "s_nop 0\n"
    "buffer_load_dwordx4 v3, s[36:39], s56 offen lds\n"
    "v_mfma_f32_16x16x32_bf16 v[64:67], v[248:251], a[4:7], v[64:67]\n"
    "ds_read_b64_tr_b16 v[236:237], v8 offset:49152\n"
    "ds_read_b64_tr_b16 v[238:239], v8 offset:49664\n"
    "v_mfma_f32_16x16x32_bf16 v[68:71], v[248:251], a[68:71], v[68:71]\n"
    "s_waitcnt lgkmcnt(11)\n"
    "s_add_u32 s57, s64, 57344\n"
    "s_add_u32 s56, s61, 0x280000\n"
    "v_mfma_f32_16x16x32_bf16 v[72:75], v[248:251], a[132:135], v[72:75]\n"
    "v_mfma_f32_16x16x32_bf16 v[76:79], v[248:251], a[196:199], v[76:79]\n"
    "s_add_u32 m0, s65, 22912\n"
    "s_nop 0\n"
    "buffer_load_dwordx4 v10, s[48:51], s57 offen offset:640 lds\n"
    "v_mfma_f32_16x16x32_bf16 v[80:83], v[252:255], a[4:7], v[80:83]\n"
    "ds_read_b64_tr_b16 v[240:241], v4 offset:24576\n"
    "ds_read_b64_tr_b16 v[242:243], v4 offset:25088\n"
    "v_mfma_f32_16x16x32_bf16 v[84:87], v[252:255], a[68:71], v[84:87]\n"
    "s_waitcnt lgkmcnt(10)\n"
    "v_mfma_f32_16x16x32_bf16 v[88:91], v[252:255], a[132:135], v[88:91]\n"
    "v_mfma_f32_16x16x32_bf16 v[92:95], v[252:255], a[196:199], v[92:95]\n"
    "s_add_u32 m0, s60, 41984\n"
    "s_nop 0\n"
    "buffer_load_dwordx4 v3, s[36:39], s76 offen lds\n"
    "v_mfma_f32_16x16x32_bf16 v[160:163], v[224:227], a[24:27], v[160:163]\n"
    "ds_read_b64_tr_b16 v[244:245], v5 offset:24576\n"
    "ds_read_b64_tr_b16 v[246:247], v5 offset:25088\n"
    "v_mfma_f32_16x16x32_bf16 v[164:167], v[224:227], a[88:91], v[164:167]\n"
    "s_waitcnt lgkmcnt(9)\n"
    "s_add_u32 s57, s64, 0\n"
    "s_add_u32 s76, s61, 0x180080\n"
    "v_mfma_f32_16x16x32_bf16 v[168:171], v[224:227], a[152:155], v[168:171]\n"
    "v_and_b32 v12, 63, v0\n"
    "v_mfma_f32_16x16x32_bf16 v[172:175], v[224:227], a[216:219], v[172:175]\n"
    "s_sub_u32 m0, s65, 768\n"
    "s_nop 0\n"
    "buffer_load_dwordx4 v9, s[48:51], s57 offen offset:768 lds\n"
    "v_mfma_f32_16x16x32_bf16 v[176:179], v[228:231], a[24:27], v[176:179]\n"
    "ds_read_b64_tr_b16 v[248:249], v6 offset:24576\n"
    "ds_read_b64_tr_b16 v[250:251], v6 offset:25088\n"
    "v_mfma_f32_16x16x32_bf16 v[180:183], v[228:231], a[88:91], v[180:183]\n"
    "s_waitcnt lgkmcnt(9)\n"
    "v_mfma_f32_16x16x32_bf16 v[184:187], v[228:231], a[152:155], v[184:187]\n"
    "ds_read_b128 a[32:35], v11 offset:8192\n"
    "v_mfma_f32_16x16x32_bf16 v[188:191], v[228:231], a[216:219], v[188:191]\n"
    "s_add_u32 s81, s88, 0\n"
    "buffer_load_dword v31, v15, s[40:43], s81 offen\n"
    "v_mfma_f32_16x16x32_bf16 v[192:195], v[232:235], a[24:27], v[192:195]\n"
    "ds_read_b64_tr_b16 v[252:253], v8 offset:24576\n"
    "ds_read_b64_tr_b16 v[254:255], v8 offset:25088\n"
    "v_mfma_f32_16x16x32_bf16 v[196:199], v[232:235], a[88:91], v[196:199]\n"
    "s_waitcnt lgkmcnt(9)\n"
    "s_add_u32 s57, s64, 8192\n"
    "v_mfma_f32_16x16x32_bf16 v[200:203], v[232:235], a[152:155], v[200:203]\n"
    "v_and_b32 v13, 7, v12\n"
    "v_mfma_f32_16x16x32_bf16 v[204:207], v[232:235], a[216:219], v[204:207]\n"
    "s_add_u32 m0, s65, 256\n"
    "s_nop 0\n"
    "buffer_load_dwordx4 v10, s[48:51], s57 offen offset:768 lds\n"
    "v_mfma_f32_16x16x32_bf16 v[208:211], v[236:239], a[24:27], v[208:211]\n"
    "ds_read_b64_tr_b16 v[224:225], v4 offset:53248\n"
    "ds_read_b64_tr_b16 v[226:227], v4 offset:53760\n"
    "v_mfma_f32_16x16x32_bf16 v[212:215], v[236:239], a[88:91], v[212:215]\n"
    "s_waitcnt lgkmcnt(9)\n"
    "ds_read_b128 a[96:99], v11 offset:10240\n"
    "v_mfma_f32_16x16x32_bf16 v[216:219], v[236:239], a[152:155], v[216:219]\n"
    "v_mfma_f32_16x16x32_bf16 v[220:223], v[236:239], a[216:219], v[220:223]\n"
    "s_add_u32 s81, s88, 65536\n"
    "buffer_load_dword v31, v15, s[40:43], s81 offen\n"
    "v_mfma_f32_16x16x32_bf16 v[32:35], v[240:243], a[8:11], v[32:35]\n"
    "ds_read_b64_tr_b16 v[228:229], v5 offset:53248\n"
    "ds_read_b64_tr_b16 v[230:231], v5 offset:53760\n"
    "v_mfma_f32_16x16x32_bf16 v[36:39], v[240:243], a[72:75], v[36:39]\n"
    "s_waitcnt lgkmcnt(10)\n"
    "s_add_u32 s57, s64, 16384\n"
    "v_mfma_f32_16x16x32_bf16 v[40:43], v[240:243], a[136:139], v[40:43]\n"
    "v_mul_u32_u24 v13, 3584, v13\n"
    "v_mfma_f32_16x16x32_bf16 v[44:47], v[240:243], a[200:203], v[44:47]\n"
    "s_add_u32 m0, s65, 1280\n"
    "s_nop 0\n"
    "buffer_load_dwordx4 v9, s[48:51], s57 offen offset:768 lds\n"
    "v_mfma_f32_16x16x32_bf16 v[48:51], v[244:247], a[8:11], v[48:51]\n"
    "ds_read_b64_tr_b16 v[232:233], v6 offset:53248\n"
    "ds_read_b64_tr_b16 v[234:235], v6 offset:53760\n"
    "v_mfma_f32_16x16x32_bf16 v[52:55], v[244:247], a[72:75], v[52:55]\n"
    "s_waitcnt lgkmcnt(10)\n"
    "ds_read_b128 a[160:163], v11 offset:12288\n"
    "v_mfma_f32_16x16x32_bf16 v[56:59], v[244:247], a[136:139], v[56:59]\n"
    "v_mfma_f32_16x16x32_bf16 v[60:63], v[244:247], a[200:203], v[60:63]\n"
    "s_add_u32 m0, s60, 16384\n"
    "s_nop 0\n"
    "buffer_load_dwordx4 v2, s[36:39], s56 offen lds\n"
    "v_mfma_f32_16x16x32_bf16 v[64:67], v[248:251], a[8:11], v[64:67]\n"
    "ds_read_b64_tr_b16 v[236:237], v8 offset:53248\n"
    "ds_read_b64_tr_b16 v[238:239], v8 offset:53760\n"
    "v_mfma_f32_16x16x32_bf16 v[68:71], v[248:251], a[72:75], v[68:71]\n"
    "s_waitcnt lgkmcnt(10)\n"
    "s_add_u32 s57, s64, 24576\n"
    "v_mfma_f32_16x16x32_bf16 v[72:75], v[248:251], a[136:139], v[72:75]\n"
    "ds_read_b128 a[224:227], v11 offset:14336\n"
    "v_bfe_u32 v14, v12, 3, 1\n"
    "v_mfma_f32_16x16x32_bf16 v[76:79], v[248:251], a[200:203], v[76:79]\n"
    "s_add_u32 m0, s65, 2304\n"
    "s_nop 0\n"
    "buffer_load_dwordx4 v10, s[48:51], s57 offen offset:768 lds\n"
    "v_mfma_f32_16x16x32_bf16 v[80:83], v[252:255], a[8:11], v[80:83]\n"
    "ds_read_b64_tr_b16 v[240:241], v4 offset:28672\n"
    "ds_read_b64_tr_b16 v[242:243], v4 offset:29184\n"
    "v_mfma_f32_16x16x32_bf16 v[84:87], v[252:255], a[72:75], v[84:87]\n"
    "s_waitcnt lgkmcnt(11)\n"
    "v_mfma_f32_16x16x32_bf16 v[88:91], v[252:255], a[136:139], v[88:91]\n"
    "v_mfma_f32_16x16x32_bf16 v[92:95], v[252:255], a[200:203], v[92:95]\n"
    "s_add_u32 m0, s60, 57344\n"
    "s_nop 0\n"
    "buffer_load_dwordx4 v2, s[36:39], s76 offen lds\n"
    "v_mfma_f32_16x16x32_bf16 v[160:163], v[224:227], a[28:31], v[160:163]\n"
    "ds_read_b64_tr_b16 v[244:245], v5 offset:28672\n"
    "ds_read_b64_tr_b16 v[246:247], v5 offset:29184\n"
    "v_mfma_f32_16x16x32_bf16 v[164:167], v[224:227], a[92:95], v[164:167]\n"
    "s_waitcnt lgkmcnt(10)\n"
    "s_add_u32 s57, s64, 32768\n"
    "v_mfma_f32_16x16x32_bf16 v[168:171], v[224:227], a[156:159], v[168:171]\n"
    "ds_read_b128 a[36:39], v28 offset:8192\n"
    "v_lshlrev_b32 v14, 7, v14\n"
    "v_mfma_f32_16x16x32_bf16 v[172:175], v[224:227], a[220:223], v[172:175]\n"
    "s_add_u32 m0, s65, 3328\n"
    "s_nop 0\n"
    "buffer_load_dwordx4 v9, s[48:51], s57 offen offset:768 lds\n"
    "v_mfma_f32_16x16x32_bf16 v[176:179], v[228:231], a[28:31], v[176:179]\n"
    "ds_read_b64_tr_b16 v[248:249], v6 offset:28672\n"
    "ds_read_b64_tr_b16 v[250:251], v6 offset:29184\n"
    "v_mfma_f32_16x16x32_bf16 v[180:183], v[228:231], a[92:95], v[180:183]\n"
    "s_waitcnt lgkmcnt(11)\n"
    "v_mfma_f32_16x16x32_bf16 v[184:187], v[228:231], a[156:159], v[184:187]\n"
    "v_mfma_f32_16x16x32_bf16 v[188:191], v[228:231], a[220:223], v[188:191]\n"
    "s_add_u32 m0, s60, 17408\n"
    "s_nop 0\n"
    "buffer_load_dwordx4 v3, s[36:39], s56 offen lds\n"
    "v_mfma_f32_16x16x32_bf16 v[192:195], v[232:235], a[28:31], v[192:195]\n"
    "ds_read_b64_tr_b16 v[252:253], v8 offset:28672\n"
    "ds_read_b64_tr_b16 v[254:255], v8 offset:29184\n"
    "v_mfma_f32_16x16x32_bf16 v[196:199], v[232:235], a[92:95], v[196:199]\n"
    "s_waitcnt lgkmcnt(10)\n"
    "s_add_u32 s57, s64, 40960\n"
    "s_add_u32 s56, s61, 0x300000\n"
    "v_mfma_f32_16x16x32_bf16 v[200:203], v[232:235], a[156:159], v[200:203]\n"
    "ds_read_b128 a[100:103], v28 offset:10240\n"
    "v_add_u32 v13, v13, v14\n"
    "v_mfma_f32_16x16x32_bf16 v[204:207], v[232:235], a[220:223], v[204:207]\n"
    "s_add_u32 m0, s65, 4352\n"
    "s_nop 0\n"
    "buffer_load_dwordx4 v10, s[48:51], s57 offen offset:768 lds\n"
    "s_waitcnt vmcnt(13)\n"
    "s_waitcnt vmcnt(11)\n"
    "s_barrier\n"
    "v_mfma_f32_16x16x32_bf16 v[208:211], v[236:239], a[28:31], v[208:211]\n"
    "ds_read_b64_tr_b16 v[224:225], v4 offset:0\n"
    "ds_read_b64_tr_b16 v[226:227], v4 offset:512\n"
    "v_mfma_f32_16x16x32_bf16 v[212:215], v[236:239], a[92:95], v[212:215]\n"
    "s_waitcnt lgkmcnt(10)\n"
    "ds_read_b128 a[164:167], v28 offset:12288\n"
    "v_mfma_f32_16x16x32_bf16 v[216:219], v[236:239], a[156:159], v[216:219]\n"
    "s_lshl_b32 s96, %9, 2\n"
    "s_add_u32 s96, s96, s52\n"
    "s_lshl_b32 s96, s96, 2\n"
    "s_add_u32 s96, s96, 0x1000\n"
    "v_mov_b32 v24, s97\n"
    "buffer_store_dword v24, v7, s[44:47], s96 offen sc1\n"
    "v_mfma_f32_16x16x32_bf16 v[220:223], v[236:239], a[220:223], v[220:223]\n"
    "s_add_u32 m0, s60, 58368\n"
    "s_nop 0\n"
    "buffer_load_dwordx4 v3, s[36:39], s76 offen lds\n"
    "v_mfma_f32_16x16x32_bf16 v[32:35], v[240:243], a[12:15], v[32:35]\n"
    "ds_read_b64_tr_b16 v[228:229], v5 offset:0\n"
    "ds_read_b64_tr_b16 v[230:231], v5 offset:512\n"
    "v_mfma_f32_16x16x32_bf16 v[36:39], v[240:243], a[76:79], v[36:39]\n"
    "s_waitcnt lgkmcnt(11)\n"
    "s_add_u32 s57, s64, 49152\n"
    "s_add_u32 s76, s61, 0x200080\n"
    "v_mfma_f32_16x16x32_bf16 v[40:43], v[240:243], a[140:143], v[40:43]\n"
    "v_mov_b32 v14, 0x7ffffff0\n"
    "v_mfma_f32_16x16x32_bf16 v[44:47], v[240:243], a[204:207], v[44:47]\n"
    "s_add_u32 m0, s65, 5376\n"
    "s_nop 0\n"
    "buffer_load_dwordx4 v9, s[48:51], s57 offen offset:768 lds\n"
    "v_mfma_f32_16x16x32_bf16 v[48:51], v[244:247], a[12:15], v[48:51]\n"
    "ds_read_b64_tr_b16 v[232:233], v6 offset:0\n"
    "ds_read_b64_tr_b16 v[234:235], v6 offset:512\n"
    "v_mfma_f32_16x16x32_bf16 v[52:55], v[244:247], a[76:79], v[52:55]\n"
    "s_waitcnt lgkmcnt(10)\n"
    "ds_read_b128 a[228:231], v28 offset:14336\n"
    "v_mfma_f32_16x16x32_bf16 v[56:59], v[244:247], a[140:143], v[56:59]\n"
    "v_mfma_f32_16x16x32_bf16 v[60:63], v[244:247], a[204:207], v[60:63]\n"
    "s_add_u32 m0, s60, 32768\n"
    "s_nop 0\n"
    "buffer_load_dwordx4 v2, s[36:39], s56 offen lds\n"
    "v_mfma_f32_16x16x32_bf16 v[64:67], v[248:251], a[12:15], v[64:67]\n"
    "ds_read_b64_tr_b16 v[236:237], v8 offset:0\n"
    "ds_read_b64_tr_b16 v[238:239], v8 offset:512\n"
    "v_mfma_f32_16x16x32_bf16 v[68:71], v[248:251], a[76:79], v[68:71]\n"
    "s_waitcnt lgkmcnt(11)\n"
    "s_add_u32 s57, s64, 57344\n"
    "v_mfma_f32_16x16x32_bf16 v[72:75], v[248:251], a[140:143], v[72:75]\n"
    "v_cmp_gt_u32 vcc, 16, v12\n"
    "s_nop 1\n"
    "v_cndmask_b32 v14, v14, v13, vcc\n"
    "v_mfma_f32_16x16x32_bf16 v[76:79], v[248:251], a[204:207], v[76:79]\n"
    "s_add_u32 m0, s65, 6400\n"
    "s_nop 0\n"
    "buffer_load_dwordx4 v10, s[48:51], s57 offen offset:768 lds\n"
    "v_mfma_f32_16x16x32_bf16 v[80:83], v[252:255], a[12:15], v[80:83]\n"
    "ds_read_b64_tr_b16 v[240:241], v4 offset:40960\n"
    "ds_read_b64_tr_b16 v[242:243], v4 offset:41472\n"
    "v_mfma_f32_16x16x32_bf16 v[84:87], v[252:255], a[76:79], v[84:87]\n"
    "s_waitcnt lgkmcnt(10)\n"
    "v_mfma_f32_16x16x32_bf16 v[88:91], v[252:255], a[140:143], v[88:91]\n"
    "v_mfma_f32_16x16x32_bf16 v[92:95], v[252:255], a[204:207], v[92:95]\n"
    "s_add_u32 m0, s60, 8192\n"
    "s_nop 0\n"
    "buffer_load_dwordx4 v2, s[36:39], s76 offen lds\n"
    "v_mfma_f32_16x16x32_bf16 v[160:163], v[224:227], a[32:35], v[160:163]\n"
    "ds_read_b64_tr_b16 v[244:245], v5 offset:40960\n"
    "ds_read_b64_tr_b16 v[246:247], v5 offset:41472\n"
    "v_mfma_f32_16x16x32_bf16 v[164:167], v[224:227], a[96:99], v[164:167]\n"
    "s_waitcnt lgkmcnt(9)\n"
    "s_add_u32 s57, s64, 0\n"
    "v_mfma_f32_16x16x32_bf16 v[168:171], v[224:227], a[160:163], v[168:171]\n"
    "v_mfma_f32_16x16x32_bf16 v[172:175], v[224:227], a[224:227], v[172:175]\n"
    "s_add_u32 m0, s65, 7296\n"
    "s_nop 0\n"
    "buffer_load_dwordx4 v9, s[48:51], s57 offen offset:896 lds\n"
    "v_mfma_f32_16x16x32_bf16 v[176:179], v[228:231], a[32:35], v[176:179]\n"
    "ds_read_b64_tr_b16 v[248:249], v6 offset:40960\n"
    "ds_read_b64_tr_b16 v[250:251], v6 offset:41472\n"
    "v_mfma_f32_16x16x32_bf16 v[180:183], v[228:231], a[96:99], v[180:183]\n"
    "s_waitcnt lgkmcnt(9)\n"
    "v_mfma_f32_16x16x32_bf16 v[184:187], v[228:231], a[160:163], v[184:187]\n"
    "ds_read_b128 a[40:43], v11 offset:16384\n"
    "v_mfma_f32_16x16x32_bf16 v[188:191], v[228:231], a[224:227], v[188:191]\n"
    "s_add_u32 m0, s60, 33792\n"
    "s_nop 0\n"
    "buffer_load_dwordx4 v3, s[36:39], s56 offen lds\n"
    "v_mfma_f32_16x16x32_bf16 v[192:195], v[232:235], a[32:35], v[192:195]\n"
    "ds_read_b64_tr_b16 v[252:253], v8 offset:40960\n"
    "ds_read_b64_tr_b16 v[254:255], v8 offset:41472\n"
    "v_mfma_f32_16x16x32_bf16 v[196:199], v[232:235], a[96:99], v[196:199]\n"
    "s_waitcnt lgkmcnt(9)\n"
    "s_add_u32 s57, s64, 8192\n"
    "s_add_u32 s56, s61, 0x380000\n"
    "v_mfma_f32_16x16x32_bf16 v[200:203], v[232:235], a[160:163], v[200:203]\n"
    "v_mfma_f32_16x16x32_bf16 v[204:207], v[232:235], a[224:227], v[204:207]\n"
    "s_add_u32 m0, s65, 8320\n"
    "s_nop 0\n"
    "buffer_load_dwordx4 v10, s[48:51], s57 offen offset:896 lds\n"
    "v_mfma_f32_16x16x32_bf16 v[208:211], v[236:239], a[32:35], v[208:211]\n"
    "ds_read_b64_tr_b16 v[224:225], v4 offset:4096\n"
    "ds_read_b64_tr_b16 v[226:227], v4 offset:4608\n"
    "v_mfma_f32_16x16x32_bf16 v[212:215], v[236:239], a[96:99], v[212:215]\n"
    "s_waitcnt lgkmcnt(9)\n"
    "ds_read_b128 a[104:107], v11 offset:18432\n"
    "v_mfma_f32_16x16x32_bf16 v[216:219], v[236:239], a[160:163], v[216:219]\n"
    "v_mfma_f32_16x16x32_bf16 v[220:223], v[236:239], a[224:227], v[220:223]\n"
    "s_add_u32 m0, s60, 9216\n"
    "s_nop 0\n"
    "buffer_load_dwordx4 v3, s[36:39], s76 offen lds\n"
    "v_mfma_f32_16x16x32_bf16 v[32:35], v[240:243], a[16:19], v[32:35]\n"
    "ds_read_b64_tr_b16 v[228:229], v5 offset:4096\n"
    "ds_read_b64_tr_b16 v[230:231], v5 offset:4608\n"
    "v_mfma_f32_16x16x32_bf16 v[36:39], v[240:243], a[80:83], v[36:39]\n"
    "s_waitcnt lgkmcnt(10)\n"
    "s_add_u32 s57, s64, 16384\n"
    "s_add_u32 s76, s61, 0x280080\n"
    "v_mfma_f32_16x16x32_bf16 v[40:43], v[240:243], a[144:147], v[40:43]\n"
    "v_mfma_f32_16x16x32_bf16 v[44:47], v[240:243], a[208:211], v[44:47]\n"
    "s_add_u32 m0, s65, 9344\n"
    "s_nop 0\n"
    "buffer_load_dwordx4 v9, s[48:51], s57 offen offset:896 lds\n"
    "v_mfma_f32_16x16x32_bf16 v[48:51], v[244:247], a[16:19], v[48:51]\n"
    "ds_read_b64_tr_b16 v[232:233], v6 offset:4096\n"
    "ds_read_b64_tr_b16 v[234:235], v6 offset:4608\n"
    "v_mfma_f32_16x16x32_bf16 v[52:55], v[244:247], a[80:83], v[52:55]\n"
    "s_waitcnt lgkmcnt(10)\n"
    "ds_read_b128 a[168:171], v11 offset:20480\n"
    "v_mfma_f32_16x16x32_bf16 v[56:59], v[244:247], a[144:147], v[56:59]\n"
    "v_mfma_f32_16x16x32_bf16 v[60:63], v[244:247], a[208:211], v[60:63]\n"
    "s_add_u32 m0, s60, 49152\n"
    "s_nop 0\n"
    "buffer_load_dwordx4 v2, s[36:39], s56 offen lds\n"
    "v_mfma_f32_16x16x32_bf16 v[64:67], v[248:251], a[16:19], v[64:67]\n"
    "ds_read_b64_tr_b16 v[236:237], v8 offset:4096\n"
    "ds_read_b64_tr_b16 v[238:239], v8 offset:4608\n"
    "v_mfma_f32_16x16x32_bf16 v[68:71], v[248:251], a[80:83], v[68:71]\n"
    "s_waitcnt lgkmcnt(10)\n"
    "s_add_u32 s57, s64, 24576\n"
    "v_mfma_f32_16x16x32_bf16 v[72:75], v[248:251], a[144:147], v[72:75]\n"
    "ds_read_b128 a[232:235], v11 offset:22528\n"
    "v_mfma_f32_16x16x32_bf16 v[76:79], v[248:251], a[208:211], v[76:79]\n"
    "s_add_u32 m0, s65, 10368\n"
    "s_nop 0\n"
    "buffer_load_dwordx4 v10, s[48:51], s57 offen offset:896 lds\n"
    "v_mfma_f32_16x16x32_bf16 v[80:83], v[252:255], a[16:19], v[80:83]\n"
    "ds_read_b64_tr_b16 v[240:241], v4 offset:45056\n"
    "ds_read_b64_tr_b16 v[242:243], v4 offset:45568\n"
    "v_mfma_f32_16x16x32_bf16 v[84:87], v[252:255], a[80:83], v[84:87]\n"
    "s_waitcnt lgkmcnt(11)\n"
    "v_mfma_f32_16x16x32_bf16 v[88:91], v[252:255], a[144:147], v[88:91]\n"
    "v_mfma_f32_16x16x32_bf16 v[92:95], v[252:255], a[208:211], v[92:95]\n"
    "s_add_u32 m0, s60, 24576\n"
    "s_nop 0\n"
    "buffer_load_dwordx4 v2, s[36:39], s76 offen lds\n"
    "v_mfma_f32_16x16x32_bf16 v[160:163], v[224:227], a[36:39], v[160:163]\n"
    "ds_read_b64_tr_b16 v[244:245], v5 offset:45056\n"
    "ds_read_b64_tr_b16 v[246:247], v5 offset:45568\n"
    "v_mfma_f32_16x16x32_bf16 v[164:167], v[224:227], a[100:103], v[164:167]\n"
    "s_waitcnt lgkmcnt(10)\n"
    "s_add_u32 s57, s64, 32768\n"
    "v_mfma_f32_16x16x32_bf16 v[168:171], v[224:227], a[164:167], v[168:171]\n"
    "ds_read_b128 a[44:47], v28 offset:16384\n"
    "v_mfma_f32_16x16x32_bf16 v[172:175], v[224:227], a[228:231], v[172:175]\n"
    "s_add_u32 m0, s65, 11392\n"
    "s_nop 0\n"
    "buffer_load_dwordx4 v9, s[48:51], s57 offen offset:896 lds\n"
    "v_mfma_f32_16x16x32_bf16 v[176:179], v[228:231], a[36:39], v[176:179]\n"
    "ds_read_b64_tr_b16 v[248:249], v6 offset:45056\n"
    "ds_read_b64_tr_b16 v[250:251], v6 offset:45568\n"
    "v_mfma_f32_16x16x32_bf16 v[180:183], v[228:231], a[100:103], v[180:183]\n"
    "s_waitcnt lgkmcnt(11)\n"
    "v_mfma_f32_16x16x32_bf16 v[184:187], v[228:231], a[164:167], v[184:187]\n"
    "v_mfma_f32_16x16x32_bf16 v[188:191], v[228:231], a[228:231], v[188:191]\n"
    "s_add_u32 m0, s60, 50176\n"
    "s_nop 0\n"
    "buffer_load_dwordx4 v3, s[36:39], s56 offen lds\n"
    "v_mfma_f32_16x16x32_bf16 v[192:195], v[232:235], a[36:39], v[192:195]\n"
    "ds_read_b64_tr_b16 v[252:253], v8 offset:45056\n"
    "ds_read_b64_tr_b16 v[254:255], v8 offset:45568\n"
    "v_mfma_f32_16x16x32_bf16 v[196:199], v[232:235], a[100:103], v[196:199]\n"
    "s_waitcnt lgkmcnt(10)\n"
    "s_add_u32 s57, s64, 40960\n"
    "v_mfma_f32_16x16x32_bf16 v[200:203], v[232:235], a[164:167], v[200:203]\n"
    "ds_read_b128 a[108:111], v28 offset:18432\n"
    "v_mfma_f32_16x16x32_bf16 v[204:207], v[232:235], a[228:231], v[204:207]\n"
    "s_add_u32 m0, s65, 12416\n"
    "s_nop 0\n"
    "buffer_load_dwordx4 v10, s[48:51], s57 offen offset:896 lds\n"
    "s_waitcnt vmcnt(18)\n"
    "s_waitcnt vmcnt(15)\n"
    "s_barrier\n"
    "v_mfma_f32_16x16x32_bf16 v[208:211], v[236:239], a[36:39], v[208:211]\n"
    "ds_read_b64_tr_b16 v[224:225], v4 offset:16384\n"
    "ds_read_b64_tr_b16 v[226:227], v4 offset:16896\n"
    "v_mfma_f32_16x16x32_bf16 v[212:215], v[236:239], a[100:103], v[212:215]\n"
    "s_waitcnt lgkmcnt(10)\n"
    "ds_read_b128 a[172:175], v28 offset:20480\n"
    "v_mfma_f32_16x16x32_bf16 v[216:219], v[236:239], a[164:167], v[216:219]\n"
    "s_add_u32 s56, s61, 0x0\n"
    "v_mfma_f32_16x16x32_bf16 v[220:223], v[236:239], a[228:231], v[220:223]\n"
    "s_add_u32 m0, s60, 25600\n"
    "s_nop 0\n"
    "buffer_load_dwordx4 v3, s[36:39], s76 offen lds\n"
    "v_mfma_f32_16x16x32_bf16 v[32:35], v[240:243], a[20:23], v[32:35]\n"
    "ds_read_b64_tr_b16 v[228:229], v5 offset:16384\n"
    "ds_read_b64_tr_b16 v[230:231], v5 offset:16896\n"
    "v_mfma_f32_16x16x32_bf16 v[36:39], v[240:243], a[84:87], v[36:39]\n"
    "s_waitcnt lgkmcnt(11)\n"
    "s_add_u32 s57, s64, 49152\n"
    "s_add_u32 s76, s61, 0x300080\n"
    "v_mfma_f32_16x16x32_bf16 v[40:43], v[240:243], a[148:151], v[40:43]\n"
    "v_mfma_f32_16x16x32_bf16 v[44:47], v[240:243], a[212:215], v[44:47]\n"
    "s_add_u32 m0, s65, 13440\n"
    "s_nop 0\n"
    "buffer_load_dwordx4 v9, s[48:51], s57 offen offset:896 lds\n"
    "v_mfma_f32_16x16x32_bf16 v[48:51], v[244:247], a[20:23], v[48:51]\n"
    "ds_read_b64_tr_b16 v[232:233], v6 offset:16384\n"
    "ds_read_b64_tr_b16 v[234:235], v6 offset:16896\n"
    "v_mfma_f32_16x16x32_bf16 v[52:55], v[244:247], a[84:87], v[52:55]\n"
    "s_waitcnt lgkmcnt(10)\n"
    "ds_read_b128 a[236:239], v28 offset:22528\n"
    "v_mfma_f32_16x16x32_bf16 v[56:59], v[244:247], a[148:151], v[56:59]\n"
    "v_mfma_f32_16x16x32_bf16 v[60:63], v[244:247], a[212:215], v[60:63]\n"
    "s_add_u32 m0, s60, 0\n"
    "s_nop 0\n"
    "buffer_load_dwordx4 v2, s[40:43], s56 offen lds\n"
    "v_mfma_f32_16x16x32_bf16 v[64:67], v[248:251], a[20:23], v[64:67]\n"
    "ds_read_b64_tr_b16 v[236:237], v8 offset:16384\n"
    "ds_read_b64_tr_b16 v[238:239], v8 offset:16896\n"
    "v_mfma_f32_16x16x32_bf16 v[68:71], v[248:251], a[84:87], v[68:71]\n"
    "s_waitcnt lgkmcnt(11)\n"
    "s_add_u32 s57, s64, 57344\n"
    "v_mfma_f32_16x16x32_bf16 v[72:75], v[248:251], a[148:151], v[72:75]\n"
    "v_mfma_f32_16x16x32_bf16 v[76:79], v[248:251], a[212:215], v[76:79]\n"
    "s_add_u32 m0, s65, 14464\n"
    "s_nop 0\n"
    "buffer_load_dwordx4 v10, s[48:51], s57 offen offset:896 lds\n"
    "v_mfma_f32_16x16x32_bf16 v[80:83], v[252:255], a[20:23], v[80:83]\n"
    "ds_read_b64_tr_b16 v[240:241], v4 offset:57344\n"
    "ds_read_b64_tr_b16 v[242:243], v4 offset:57856\n"
    "v_mfma_f32_16x16x32_bf16 v[84:87], v[252:255], a[84:87], v[84:87]\n"
    "s_waitcnt lgkmcnt(10)\n"
    "v_mfma_f32_16x16x32_bf16 v[88:91], v[252:255], a[148:151], v[88:91]\n"
    "v_mfma_f32_16x16x32_bf16 v[92:95], v[252:255], a[212:215], v[92:95]\n"
    "s_add_u32 m0, s60, 40960\n"
    "s_nop 0\n"
    "buffer_load_dwordx4 v2, s[36:39], s76 offen lds\n"
    "v_mfma_f32_16x16x32_bf16 v[160:163], v[224:227], a[40:43], v[160:163]\n"
    "ds_read_b64_tr_b16 v[244:245], v5 offset:57344\n"
    "ds_read_b64_tr_b16 v[246:247], v5 offset:57856\n"
    "v_mfma_f32_16x16x32_bf16 v[164:167], v[224:227], a[104:107], v[164:167]\n"
    "s_waitcnt lgkmcnt(9)\n"
    "v_mfma_f32_16x16x32_bf16 v[168:171], v[224:227], a[168:171], v[168:171]\n"
    "s_mov_b64 s[48:49], %5\n"
    "v_mfma_f32_16x16x32_bf16 v[172:175], v[224:227], a[232:235], v[172:175]\n"
    "s_add_u32 m0, s60, 1024\n"
    "s_nop 0\n"
    "buffer_load_dwordx4 v3, s[40:43], s56 offen lds\n"
    "v_mfma_f32_16x16x32_bf16 v[176:179], v[228:231], a[40:43], v[176:179]\n"
    "ds_read_b64_tr_b16 v[248:249], v6 offset:57344\n"
    "ds_read_b64_tr_b16 v[250:251], v6 offset:57856\n"
    "v_mfma_f32_16x16x32_bf16 v[180:183], v[228:231], a[104:107], v[180:183]\n"
    "s_waitcnt lgkmcnt(9)\n"
    "s_and_b32 s49, s49, 0xffff\n"
    "s_waitcnt vmcnt(18)\n"
    "v_mfma_f32_16x16x32_bf16 v[184:187], v[228:231], a[168:171], v[184:187]\n"
    "ds_read_b128 a[48:51], v11 offset:0\n"
    "v_mfma_f32_16x16x32_bf16 v[188:191], v[228:231], a[232:235], v[188:191]\n"
    "s_add_u32 m0, s60, 41984\n"
    "s_nop 0\n"
    "buffer_load_dwordx4 v3, s[36:39], s76 offen lds\n"
    "v_mfma_f32_16x16x32_bf16 v[192:195], v[232:235], a[40:43], v[192:195]\n"
    "ds_read_b64_tr_b16 v[252:253], v8 offset:57344\n"
    "ds_read_b64_tr_b16 v[254:255], v8 offset:57856\n"
    "v_mfma_f32_16x16x32_bf16 v[196:199], v[232:235], a[104:107], v[196:199]\n"
    "s_waitcnt lgkmcnt(9)\n"
    "s_mov_b32 s50, 0x1c0000\n"
    "s_mov_b32 s51, 0x20000\n"
    "v_mfma_f32_16x16x32_bf16 v[200:203], v[232:235], a[168:171], v[200:203]\n"
    "v_mfma_f32_16x16x32_bf16 v[204:207], v[232:235], a[232:235], v[204:207]\n"
    "v_mfma_f32_16x16x32_bf16 v[208:211], v[236:239], a[40:43], v[208:211]\n"
    "ds_read_b64_tr_b16 v[224:225], v4 offset:20480\n"
    "ds_read_b64_tr_b16 v[226:227], v4 offset:20992\n"
    "v_mfma_f32_16x16x32_bf16 v[212:215], v[236:239], a[104:107], v[212:215]\n"
    "s_waitcnt lgkmcnt(9)\n"
    "s_mov_b64 s[84:85], %6\n"
    "v_mfma_f32_16x16x32_bf16 v[216:219], v[236:239], a[168:171], v[216:219]\n"
    "ds_read_b128 a[112:115], v11 offset:2048\n"
    "v_mfma_f32_16x16x32_bf16 v[220:223], v[236:239], a[232:235], v[220:223]\n"
    "s_and_b32 s85, s85, 0xffff\n"
    "v_mfma_f32_16x16x32_bf16 v[32:35], v[240:243], a[24:27], v[32:35]\n"
    "ds_read_b64_tr_b16 v[228:229], v5 offset:20480\n"
    "ds_read_b64_tr_b16 v[230:231], v5 offset:20992\n"
    "v_mfma_f32_16x16x32_bf16 v[36:39], v[240:243], a[88:91], v[36:39]\n"
    "s_waitcnt lgkmcnt(10)\n"
    "s_mov_b32 s86, 0x1c0000\n"
    "v_mfma_f32_16x16x32_bf16 v[40:43], v[240:243], a[152:155], v[40:43]\n"
    "v_mfma_f32_16x16x32_bf16 v[44:47], v[240:243], a[216:219], v[44:47]\n"
    "ds_read_b128 a[176:179], v11 offset:4096\n"
    "v_mfma_f32_16x16x32_bf16 v[48:51], v[244:247], a[24:27], v[48:51]\n"
    "ds_read_b64_tr_b16 v[232:233], v6 offset:20480\n"
    "ds_read_b64_tr_b16 v[234:235], v6 offset:20992\n"
    "v_mfma_f32_16x16x32_bf16 v[52:55], v[244:247], a[88:91], v[52:55]\n"
    "s_waitcnt lgkmcnt(11)\n"
    "s_mov_b32 s87, 0x20000\n"
    "v_mfma_f32_16x16x32_bf16 v[56:59], v[244:247], a[152:155], v[56:59]\n"
    "v_mfma_f32_16x16x32_bf16 v[60:63], v[244:247], a[216:219], v[60:63]\n"
    "v_mfma_f32_16x16x32_bf16 v[64:67], v[248:251], a[24:27], v[64:67]\n"
    "ds_read_b64_tr_b16 v[236:237], v8 offset:20480\n"
    "ds_read_b64_tr_b16 v[238:239], v8 offset:20992\n"
    "v_mfma_f32_16x16x32_bf16 v[68:71], v[248:251], a[88:91], v[68:71]\n"
    "s_waitcnt lgkmcnt(10)\n"
    "v_mfma_f32_16x16x32_bf16 v[72:75], v[248:251], a[152:155], v[72:75]\n"
    "s_add_u32 s81, s79, 0\n"
    "buffer_load_dword v31, v14, s[48:51], s81 offen\n"
    "v_mfma_f32_16x16x32_bf16 v[76:79], v[248:251], a[216:219], v[76:79]\n"
    "ds_read_b128 a[240:243], v11 offset:6144\n"
    "v_mfma_f32_16x16x32_bf16 v[80:83], v[252:255], a[24:27], v[80:83]\n"
    "ds_read_b64_tr_b16 v[240:241], v4 offset:61440\n"
    "ds_read_b64_tr_b16 v[242:243], v4 offset:61952\n"
    "v_mfma_f32_16x16x32_bf16 v[84:87], v[252:255], a[88:91], v[84:87]\n"
    "s_waitcnt lgkmcnt(11)\n"
    "v_mfma_f32_16x16x32_bf16 v[88:91], v[252:255], a[152:155], v[88:91]\n"
    "v_mfma_f32_16x16x32_bf16 v[92:95], v[252:255], a[216:219], v[92:95]\n"
    "v_mfma_f32_16x16x32_bf16 v[160:163], v[224:227], a[44:47], v[160:163]\n"
    "ds_read_b64_tr_b16 v[244:245], v5 offset:61440\n"
    "ds_read_b64_tr_b16 v[246:247], v5 offset:61952\n"
    "v_mfma_f32_16x16x32_bf16 v[164:167], v[224:227], a[108:111], v[164:167]\n"
    "s_waitcnt lgkmcnt(10)\n"
    "ds_read_b128 a[52:55], v28 offset:0\n"
    "v_mfma_f32_16x16x32_bf16 v[168:171], v[224:227], a[172:175], v[168:171]\n"
    "v_mfma_f32_16x16x32_bf16 v[172:175], v[224:227], a[236:239], v[172:175]\n"
    "v_mfma_f32_16x16x32_bf16 v[176:179], v[228:231], a[44:47], v[176:179]\n"
    "ds_read_b64_tr_b16 v[248:249], v6 offset:61440\n"
    "ds_read_b64_tr_b16 v[250:251], v6 offset:61952\n"
    "v_mfma_f32_16x16x32_bf16 v[180:183], v[228:231], a[108:111], v[180:183]\n"
    "s_waitcnt lgkmcnt(10)\n"
    "v_mfma_f32_16x16x32_bf16 v[184:187], v[228:231], a[172:175], v[184:187]\n"
    "v_mfma_f32_16x16x32_bf16 v[188:191], v[228:231], a[236:239], v[188:191]\n"
    "ds_read_b128 a[116:119], v28 offset:2048\n"
    "v_mfma_f32_16x16x32_bf16 v[192:195], v[232:235], a[44:47], v[192:195]\n"
    "ds_read_b64_tr_b16 v[252:253], v8 offset:61440\n"
    "ds_read_b64_tr_b16 v[254:255], v8 offset:61952\n"
    "v_mfma_f32_16x16x32_bf16 v[196:199], v[232:235], a[108:111], v[196:199]\n"
    "s_waitcnt lgkmcnt(11)\n"
    "v_mfma_f32_16x16x32_bf16 v[200:203], v[232:235], a[172:175], v[200:203]\n"
    "v_mfma_f32_16x16x32_bf16 v[204:207], v[232:235], a[236:239], v[204:207]\n"
    "s_waitcnt vmcnt(17)\n"
    "s_waitcnt vmcnt(15)\n"
    "s_barrier\n"
    "v_mfma_f32_16x16x32_bf16 v[208:211], v[236:239], a[44:47], v[208:211]\n"
    "ds_read_b64_tr_b16 v[224:225], v4 offset:32768\n"
    "ds_read_b64_tr_b16 v[226:227], v4 offset:33280\n"
    "v_mfma_f32_16x16x32_bf16 v[212:215], v[236:239], a[108:111], v[212:215]\n"
    "s_waitcnt lgkmcnt(10)\n"
    "ds_read_b128 a[180:183], v28 offset:4096\n"
    "v_mfma_f32_16x16x32_bf16 v[216:219], v[236:239], a[172:175], v[216:219]\n"
    "s_add_u32 s56, s61, 0x80000\n"
    "s_add_u32 s76, s61, 0x380080\n"
    "v_mfma_f32_16x16x32_bf16 v[220:223], v[236:239], a[236:239], v[220:223]\n"
    "s_add_u32 m0, s60, 16384\n"
    "s_nop 0\n"
    "buffer_load_dwordx4 v2, s[40:43], s56 offen lds\n"
    "v_mfma_f32_16x16x32_bf16 v[32:35], v[240:243], a[28:31], v[32:35]\n"
    "ds_read_b64_tr_b16 v[228:229], v5 offset:32768\n"
    "ds_read_b64_tr_b16 v[230:231], v5 offset:33280\n"
    "v_mfma_f32_16x16x32_bf16 v[36:39], v[240:243], a[92:95], v[36:39]\n"
    "s_waitcnt lgkmcnt(11)\n"
    "v_mfma_f32_16x16x32_bf16 v[40:43], v[240:243], a[156:159], v[40:43]\n"
    "v_mfma_f32_16x16x32_bf16 v[44:47], v[240:243], a[220:223], v[44:47]\n"
    "s_add_u32 s81, s79, 0\n"
    "buffer_load_dword v31, v14, s[84:87], s81 offen\n"
    "v_mfma_f32_16x16x32_bf16 v[48:51], v[244:247], a[28:31], v[48:51]\n"
    "ds_read_b64_tr_b16 v[232:233], v6 offset:32768\n"
    "ds_read_b64_tr_b16 v[234:235], v6 offset:33280\n"
    "v_mfma_f32_16x16x32_bf16 v[52:55], v[244:247], a[92:95], v[52:55]\n"
    "s_waitcnt lgkmcnt(10)\n"
    "ds_read_b128 a[244:247], v28 offset:6144\n"
    "v_mfma_f32_16x16x32_bf16 v[56:59], v[244:247], a[156:159], v[56:59]\n"
    "v_mfma_f32_16x16x32_bf16 v[60:63], v[244:247], a[220:223], v[60:63]\n"
    "s_add_u32 m0, s60, 57344\n"
    "s_nop 0\n"
    "buffer_load_dwordx4 v2, s[36:39], s76 offen lds\n"
    "v_mfma_f32_16x16x32_bf16 v[64:67], v[248:251], a[28:31], v[64:67]\n"
    "ds_read_b64_tr_b16 v[236:237], v8 offset:32768\n"
    "ds_read_b64_tr_b16 v[238:239], v8 offset:33280\n"
    "v_mfma_f32_16x16x32_bf16 v[68:71], v[248:251], a[92:95], v[68:71]\n"
    "s_waitcnt lgkmcnt(10)\n"
    "v_mfma_f32_16x16x32_bf16 v[72:75], v[248:251], a[156:159], v[72:75]\n"
    "v_mfma_f32_16x16x32_bf16 v[76:79], v[248:251], a[220:223], v[76:79]\n"
    "s_add_u32 m0, s60, 17408\n"
    "s_nop 0\n"
    "buffer_load_dwordx4 v3, s[40:43], s56 offen lds\n"
    "v_mfma_f32_16x16x32_bf16 v[80:83], v[252:255], a[28:31], v[80:83]\n"
    "ds_read_b64_tr_b16 v[240:241], v4 offset:8192\n"
    "ds_read_b64_tr_b16 v[242:243], v4 offset:8704\n"
    "v_mfma_f32_16x16x32_bf16 v[84:87], v[252:255], a[92:95], v[84:87]\n"
    "s_waitcnt lgkmcnt(10)\n"
    "v_mfma_f32_16x16x32_bf16 v[88:91], v[252:255], a[156:159], v[88:91]\n"
    "v_mfma_f32_16x16x32_bf16 v[92:95], v[252:255], a[220:223], v[92:95]\n"
    "s_add_u32 m0, s60, 58368\n"
    "s_nop 0\n"
    "buffer_load_dwordx4 v3, s[36:39], s76 offen lds\n"
    "v_mfma_f32_16x16x32_bf16 v[160:163], v[224:227], a[48:51], v[160:163]\n"
    "ds_read_b64_tr_b16 v[244:245], v5 offset:8192\n"
    "ds_read_b64_tr_b16 v[246:247], v5 offset:8704\n"
    "v_mfma_f32_16x16x32_bf16 v[164:167], v[224:227], a[112:115], v[164:167]\n"
    "s_waitcnt lgkmcnt(9)\n"
    "v_mfma_f32_16x16x32_bf16 v[168:171], v[224:227], a[176:179], v[168:171]\n"
    "v_mfma_f32_16x16x32_bf16 v[172:175], v[224:227], a[240:243], v[172:175]\n"
    "v_mfma_f32_16x16x32_bf16 v[176:179], v[228:231], a[48:51], v[176:179]\n"
    "ds_read_b64_tr_b16 v[248:249], v6 offset:8192\n"
    "ds_read_b64_tr_b16 v[250:251], v6 offset:8704\n"
    "v_mfma_f32_16x16x32_bf16 v[180:183], v[228:231], a[112:115], v[180:183]\n"
    "s_waitcnt lgkmcnt(9)\n"
    "s_waitcnt vmcnt(9)\n"
    "v_mfma_f32_16x16x32_bf16 v[184:187], v[228:231], a[176:179], v[184:187]\n"
    "s_load_dwordx2 s[72:73], %11, 0x28\n"
    "s_load_dwordx2 s[74:75], %11, 0x40\n"
    "v_and_b32 v136, 63, v0\n"
    "v_lshlrev_b32 v137, 7, v136\n"
    "v_mov_b32 v138, 0x7ffffff0\n"
    "s_lshl_b32 s81, %9, 2\n"
    "s_add_u32 s81, s81, s52\n"
    "s_waitcnt lgkmcnt(0)\n"
    "s_mov_b64 s[82:83], s[74:75]\n"
    "s_and_b32 s73, s73, 0xffff\n"
    "s_mov_b32 s74, 0x400000\n"
    "s_mov_b32 s75, 0x20000\n"
    "s_mul_i32 s80, s81, 4096\n"
    "v_cmp_gt_u32 vcc, 32, v136\n"
    "v_add_u32 v139, s80, v137\n"
    "s_nop 0\n"
    "v_cndmask_b32 v139, v138, v139, vcc\n"
    "buffer_load_dword v31, v139, s[72:75], 0 offen\n"
    "s_mov_b64 s[72:73], s[82:83]\n"
    "s_and_b32 s73, s73, 0xffff\n"
    "s_mov_b32 s74, 0x1c0000\n"
    "s_mov_b32 s75, 0x20000\n"
    "s_mul_i32 s80, s81, 1792\n"
    "v_cmp_gt_u32 vcc, 14, v136\n"
    "v_add_u32 v139, s80, v137\n"
    "s_nop 0\n"
    "v_cndmask_b32 v139, v138, v139, vcc\n"
    "buffer_load_dword v31, v139, s[72:75], 0 offen\n"
    "v_mfma_f32_16x16x32_bf16 v[188:191], v[228:231], a[240:243], v[188:191]\n"
    "ds_read_b128 a[56:59], v11 offset:8192\n"
    "v_mfma_f32_16x16x32_bf16 v[192:195], v[232:235], a[48:51], v[192:195]\n"
    "ds_read_b64_tr_b16 v[252:253], v8 offset:8192\n"
    "ds_read_b64_tr_b16 v[254:255], v8 offset:8704\n"
    "v_mfma_f32_16x16x32_bf16 v[196:199], v[232:235], a[112:115], v[196:199]\n"
    "v_mfma_f32_16x16x32_bf16 v[200:203], v[232:235], a[176:179], v[200:203]\n"
    "ds_read_b128 a[120:123], v11 offset:10240\n"
    "v_mfma_f32_16x16x32_bf16 v[204:207], v[232:235], a[240:243], v[204:207]\n"
    "v_mfma_f32_16x16x32_bf16 v[208:211], v[236:239], a[48:51], v[208:211]\n"
    "ds_read_b64_tr_b16 v[224:225], v4 offset:36864\n"
    "ds_read_b64_tr_b16 v[226:227], v4 offset:37376\n"
    "v_mfma_f32_16x16x32_bf16 v[212:215], v[236:239], a[112:115], v[212:215]\n"
    "v_mfma_f32_16x16x32_bf16 v[216:219], v[236:239], a[176:179], v[216:219]\n"
    "s_add_u32 s81, s79, 28672\n"
    "buffer_load_dword v31, v14, s[48:51], s81 offen\n"
    "v_mfma_f32_16x16x32_bf16 v[220:223], v[236:239], a[240:243], v[220:223]\n"
    "ds_read_b128 a[184:187], v11 offset:12288\n"
    "v_mfma_f32_16x16x32_bf16 v[32:35], v[240:243], a[32:35], v[32:35]\n"
    "ds_read_b64_tr_b16 v[228:229], v5 offset:36864\n"
    "ds_read_b64_tr_b16 v[230:231], v5 offset:37376\n"
    "v_mfma_f32_16x16x32_bf16 v[36:39], v[240:243], a[96:99], v[36:39]\n"
    "v_mfma_f32_16x16x32_bf16 v[40:43], v[240:243], a[160:163], v[40:43]\n"
    "v_mfma_f32_16x16x32_bf16 v[44:47], v[240:243], a[224:227], v[44:47]\n"
    "v_mfma_f32_16x16x32_bf16 v[48:51], v[244:247], a[32:35], v[48:51]\n"
    "ds_read_b64_tr_b16 v[232:233], v6 offset:36864\n"
    "ds_read_b64_tr_b16 v[234:235], v6 offset:37376\n"
    "v_mfma_f32_16x16x32_bf16 v[52:55], v[244:247], a[96:99], v[52:55]\n"
    "ds_read_b128 a[248:251], v11 offset:14336\n"
    "v_mfma_f32_16x16x32_bf16 v[56:59], v[244:247], a[160:163], v[56:59]\n"
    "v_mfma_f32_16x16x32_bf16 v[60:63], v[244:247], a[224:227], v[60:63]\n"
    "v_mfma_f32_16x16x32_bf16 v[64:67], v[248:251], a[32:35], v[64:67]\n"
    "ds_read_b64_tr_b16 v[236:237], v8 offset:36864\n"
    "ds_read_b64_tr_b16 v[238:239], v8 offset:37376\n"
    "v_mfma_f32_16x16x32_bf16 v[68:71], v[248:251], a[96:99], v[68:71]\n"
    "s_waitcnt lgkmcnt(11)\n"
    "ds_read_b128 a[60:63], v28 offset:8192\n"
    "v_mfma_f32_16x16x32_bf16 v[72:75], v[248:251], a[160:163], v[72:75]\n"
    "v_mfma_f32_16x16x32_bf16 v[76:79], v[248:251], a[224:227], v[76:79]\n"
    "v_mfma_f32_16x16x32_bf16 v[80:83], v[252:255], a[32:35], v[80:83]\n"
    "ds_read_b64_tr_b16 v[240:241], v4 offset:12288\n"
    "ds_read_b64_tr_b16 v[242:243], v4 offset:12800\n"
    "v_mfma_f32_16x16x32_bf16 v[84:87], v[252:255], a[96:99], v[84:87]\n"
    "s_waitcnt lgkmcnt(11)\n"
    "ds_read_b128 a[124:127], v28 offset:10240\n"
    "v_mfma_f32_16x16x32_bf16 v[88:91], v[252:255], a[160:163], v[88:91]\n"
    "v_mfma_f32_16x16x32_bf16 v[92:95], v[252:255], a[224:227], v[92:95]\n"
    "v_mfma_f32_16x16x32_bf16 v[160:163], v[224:227], a[52:55], v[160:163]\n"
    "ds_read_b64_tr_b16 v[244:245], v5 offset:12288\n"
    "ds_read_b64_tr_b16 v[246:247], v5 offset:12800\n"
    "v_mfma_f32_16x16x32_bf16 v[164:167], v[224:227], a[116:119], v[164:167]\n"
    "s_waitcnt lgkmcnt(11)\n"
    "v_mfma_f32_16x16x32_bf16 v[168:171], v[224:227], a[180:183], v[168:171]\n"
    "ds_read_b128 a[188:191], v28 offset:12288\n"
    "v_mfma_f32_16x16x32_bf16 v[172:175], v[224:227], a[244:247], v[172:175]\n"
    "v_mfma_f32_16x16x32_bf16 v[176:179], v[228:231], a[52:55], v[176:179]\n"
    "ds_read_b64_tr_b16 v[248:249], v6 offset:12288\n"
    "ds_read_b64_tr_b16 v[250:251], v6 offset:12800\n"
    "v_mfma_f32_16x16x32_bf16 v[180:183], v[228:231], a[116:119], v[180:183]\n"
    "s_waitcnt lgkmcnt(12)\n"
    "v_mfma_f32_16x16x32_bf16 v[184:187], v[228:231], a[180:183], v[184:187]\n"
    "s_add_u32 s81, s79, 28672\n"
    "buffer_load_dword v31, v14, s[84:87], s81 offen\n"
    "v_mfma_f32_16x16x32_bf16 v[188:191], v[228:231], a[244:247], v[188:191]\n"
    "ds_read_b128 a[252:255], v28 offset:14336\n"
    "v_mfma_f32_16x16x32_bf16 v[192:195], v[232:235], a[52:55], v[192:195]\n"
    "ds_read_b64_tr_b16 v[252:253], v8 offset:12288\n"
    "ds_read_b64_tr_b16 v[254:255], v8 offset:12800\n"
    "v_mfma_f32_16x16x32_bf16 v[196:199], v[232:235], a[116:119], v[196:199]\n"
    "s_waitcnt lgkmcnt(12)\n"
    "v_mfma_f32_16x16x32_bf16 v[200:203], v[232:235], a[180:183], v[200:203]\n"
    "v_mfma_f32_16x16x32_bf16 v[204:207], v[232:235], a[244:247], v[204:207]\n"
    "s_barrier\n"
    "v_mfma_f32_16x16x32_bf16 v[208:211], v[236:239], a[52:55], v[208:211]\n"
    "ds_read_b64_tr_b16 v[224:225], v4 offset:49152\n"
    "ds_read_b64_tr_b16 v[226:227], v4 offset:49664\n"
    "v_mfma_f32_16x16x32_bf16 v[212:215], v[236:239], a[116:119], v[212:215]\n"
    "s_waitcnt lgkmcnt(11)\n"
    "v_and_b32 v100, 63, v0\n"
    "v_mfma_f32_16x16x32_bf16 v[216:219], v[236:239], a[180:183], v[216:219]\n"
    "s_add_u32 s56, s61, 0x100000\n"
    "v_lshrrev_b32 v101, 2, v100\n"
    "v_mfma_f32_16x16x32_bf16 v[220:223], v[236:239], a[244:247], v[220:223]\n"
    "s_add_u32 m0, s60, 32768\n"
    "s_nop 0\n"
    "buffer_load_dwordx4 v2, s[40:43], s56 offen lds\n"
    "v_mfma_f32_16x16x32_bf16 v[32:35], v[240:243], a[36:39], v[32:35]\n"
    "ds_read_b64_tr_b16 v[228:229], v5 offset:49152\n"
    "ds_read_b64_tr_b16 v[230:231], v5 offset:49664\n"
    "v_mfma_f32_16x16x32_bf16 v[36:39], v[240:243], a[100:103], v[36:39]\n"
    "s_waitcnt lgkmcnt(10)\n"
    "v_mul_u32_u24 v101, 3584, v101\n"
    "v_mfma_f32_16x16x32_bf16 v[40:43], v[240:243], a[164:167], v[40:43]\n"
    "s_add_u32 s76, s61, 0x80\n"
    "v_and_b32 v102, 3, v100\n"
    "v_mfma_f32_16x16x32_bf16 v[44:47], v[240:243], a[228:231], v[44:47]\n"
    "s_add_u32 m0, s60, 8192\n"
    "s_nop 0\n"
    "buffer_load_dwordx4 v2, s[40:43], s76 offen lds\n"
    "v_mfma_f32_16x16x32_bf16 v[48:51], v[244:247], a[36:39], v[48:51]\n"
    "ds_read_b64_tr_b16 v[232:233], v6 offset:49152\n"
    "ds_read_b64_tr_b16 v[234:235], v6 offset:49664\n"
    "v_mfma_f32_16x16x32_bf16 v[52:55], v[244:247], a[100:103], v[52:55]\n"
    "s_waitcnt lgkmcnt(9)\n"
    "v_lshrrev_b32 v9, 5, v100\n"
    "v_mfma_f32_16x16x32_bf16 v[56:59], v[244:247], a[164:167], v[56:59]\n"
    "v_lshlrev_b32 v9, 1, v9\n"
    "v_mfma_f32_16x16x32_bf16 v[60:63], v[244:247], a[228:231], v[60:63]\n"
    "s_add_u32 m0, s60, 33792\n"
    "s_nop 0\n"
    "buffer_load_dwordx4 v3, s[40:43], s56 offen lds\n"
    "v_xor_b32 v102, v102, v9\n"
    "v_mfma_f32_16x16x32_bf16 v[64:67], v[248:251], a[36:39], v[64:67]\n"
    "ds_read_b64_tr_b16 v[236:237], v8 offset:49152\n"
    "ds_read_b64_tr_b16 v[238:239], v8 offset:49664\n"
    "v_mfma_f32_16x16x32_bf16 v[68:71], v[248:251], a[100:103], v[68:71]\n"
    "s_waitcnt lgkmcnt(8)\n"
    "v_lshlrev_b32 v102, 4, v102\n"
    "v_mfma_f32_16x16x32_bf16 v[72:75], v[248:251], a[164:167], v[72:75]\n"
    "v_mfma_f32_16x16x32_bf16 v[76:79], v[248:251], a[228:231], v[76:79]\n"
    "s_add_u32 m0, s60, 9216\n"
    "s_nop 0\n"
    "buffer_load_dwordx4 v3, s[40:43], s76 offen lds\n"
    "v_add_u32 v9, v101, v102\n"
    "v_mfma_f32_16x16x32_bf16 v[80:83], v[252:255], a[36:39], v[80:83]\n"
    "ds_read_b64_tr_b16 v[240:241], v4 offset:24576\n"
    "ds_read_b64_tr_b16 v[242:243], v4 offset:25088\n"
    "v_mfma_f32_16x16x32_bf16 v[84:87], v[252:255], a[100:103], v[84:87]\n"
    "s_waitcnt lgkmcnt(8)\n"
    "v_lshrrev_b32 v101, 4, v100\n"
    "v_mfma_f32_16x16x32_bf16 v[88:91], v[252:255], a[164:167], v[88:91]\n"
    "v_mfma_f32_16x16x32_bf16 v[92:95], v[252:255], a[228:231], v[92:95]\n"
    "v_lshlrev_b32 v102, 3, v101\n"
    "v_mfma_f32_16x16x32_bf16 v[160:163], v[224:227], a[56:59], v[160:163]\n"
    "ds_read_b64_tr_b16 v[244:245], v5 offset:24576\n"
    "ds_read_b64_tr_b16 v[246:247], v5 offset:25088\n"
    "v_mfma_f32_16x16x32_bf16 v[164:167], v[224:227], a[120:123], v[164:167]\n"
    "s_waitcnt lgkmcnt(8)\n"
    "v_bfe_u32 v10, v100, 2, 2\n"
    "v_mfma_f32_16x16x32_bf16 v[168:171], v[224:227], a[184:187], v[168:171]\n"
    "v_add_u32 v102, v102, v10\n"
    "v_mfma_f32_16x16x32_bf16 v[172:175], v[224:227], a[248:251], v[172:175]\n"
    "v_mfma_f32_16x16x32_bf16 v[176:179], v[228:231], a[56:59], v[176:179]\n"
    "ds_read_b64_tr_b16 v[248:249], v6 offset:24576\n"
    "ds_read_b64_tr_b16 v[250:251], v6 offset:25088\n"
    "v_mfma_f32_16x16x32_bf16 v[180:183], v[228:231], a[120:123], v[180:183]\n"
    "s_waitcnt lgkmcnt(8)\n"
    "v_lshlrev_b32 v102, 6, v102\n"
    "v_mfma_f32_16x16x32_bf16 v[184:187], v[228:231], a[184:187], v[184:187]\n"
    "v_and_b32 v100, 3, v100\n"
    "v_mfma_f32_16x16x32_bf16 v[188:191], v[228:231], a[248:251], v[188:191]\n"
    "v_mfma_f32_16x16x32_bf16 v[192:195], v[232:235], a[56:59], v[192:195]\n"
    "ds_read_b64_tr_b16 v[252:253], v8 offset:24576\n"
    "ds_read_b64_tr_b16 v[254:255], v8 offset:25088\n"
    "v_mfma_f32_16x16x32_bf16 v[196:199], v[232:235], a[120:123], v[196:199]\n"
    "s_waitcnt lgkmcnt(8)\n"
    "v_lshlrev_b32 v100, 3, v100\n"
    "v_mfma_f32_16x16x32_bf16 v[200:203], v[232:235], a[184:187], v[200:203]\n"
    "v_add_u32 v102, v102, v100\n"
    "v_mfma_f32_16x16x32_bf16 v[204:207], v[232:235], a[248:251], v[204:207]\n"
    "v_and_b32 v101, 1, v101\n"
    "v_mfma_f32_16x16x32_bf16 v[208:211], v[236:239], a[56:59], v[208:211]\n"
    "ds_read_b64_tr_b16 v[224:225], v4 offset:53248\n"
    "ds_read_b64_tr_b16 v[226:227], v4 offset:53760\n"
    "v_mfma_f32_16x16x32_bf16 v[212:215], v[236:239], a[120:123], v[212:215]\n"
    "s_waitcnt lgkmcnt(8)\n"
    "v_lshlrev_b32 v101, 5, v101\n"
    "v_mfma_f32_16x16x32_bf16 v[216:219], v[236:239], a[184:187], v[216:219]\n"
    "v_mfma_f32_16x16x32_bf16 v[220:223], v[236:239], a[248:251], v[220:223]\n"
    "v_add_u32 v10, v102, v101\n"
    "v_mfma_f32_16x16x32_bf16 v[32:35], v[240:243], a[40:43], v[32:35]\n"
    "ds_read_b64_tr_b16 v[228:229], v5 offset:53248\n"
    "ds_read_b64_tr_b16 v[230:231], v5 offset:53760\n"
    "v_mfma_f32_16x16x32_bf16 v[36:39], v[240:243], a[104:107], v[36:39]\n"
    "s_waitcnt lgkmcnt(8)\n"
    "v_xor_b32 v101, 32, v101\n"
    "v_mfma_f32_16x16x32_bf16 v[40:43], v[240:243], a[168:171], v[40:43]\n"
    "v_mfma_f32_16x16x32_bf16 v[44:47], v[240:243], a[232:235], v[44:47]\n"
    "v_add_u32 v11, v102, v101\n"
    "v_mfma_f32_16x16x32_bf16 v[48:51], v[244:247], a[40:43], v[48:51]\n"
    "ds_read_b64_tr_b16 v[232:233], v6 offset:53248\n"
    "ds_read_b64_tr_b16 v[234:235], v6 offset:53760\n"
    "v_mfma_f32_16x16x32_bf16 v[52:55], v[244:247], a[104:107], v[52:55]\n"
    "s_waitcnt lgkmcnt(8)\n"
    "v_mfma_f32_16x16x32_bf16 v[56:59], v[244:247], a[168:171], v[56:59]\n"
    "v_mfma_f32_16x16x32_bf16 v[60:63], v[244:247], a[232:235], v[60:63]\n"
    "v_mfma_f32_16x16x32_bf16 v[64:67], v[248:251], a[40:43], v[64:67]\n"
    "ds_read_b64_tr_b16 v[236:237], v8 offset:53248\n"
    "ds_read_b64_tr_b16 v[238:239], v8 offset:53760\n"
    "v_mfma_f32_16x16x32_bf16 v[68:71], v[248:251], a[104:107], v[68:71]\n"
    "s_waitcnt lgkmcnt(8)\n"
    "s_add_u32 s77, s66, 0x0\n"
    "v_mfma_f32_16x16x32_bf16 v[72:75], v[248:251], a[168:171], v[72:75]\n"
    "s_add_u32 m0, s90, 65536\n"
    "s_nop 0\n"
    "buffer_load_dwordx4 v9, s[48:51], s77 offen lds\n"
    "v_mfma_f32_16x16x32_bf16 v[76:79], v[248:251], a[232:235], v[76:79]\n"
    "v_mfma_f32_16x16x32_bf16 v[80:83], v[252:255], a[40:43], v[80:83]\n"
    "ds_read_b64_tr_b16 v[240:241], v4 offset:28672\n"
    "ds_read_b64_tr_b16 v[242:243], v4 offset:29184\n"
    "v_mfma_f32_16x16x32_bf16 v[84:87], v[252:255], a[104:107], v[84:87]\n"
    "s_waitcnt lgkmcnt(8)\n"
    "v_mfma_f32_16x16x32_bf16 v[88:91], v[252:255], a[168:171], v[88:91]\n"
    "v_mfma_f32_16x16x32_bf16 v[92:95], v[252:255], a[232:235], v[92:95]\n"
    "v_mfma_f32_16x16x32_bf16 v[160:163], v[224:227], a[60:63], v[160:163]\n"
    "ds_read_b64_tr_b16 v[244:245], v5 offset:28672\n"
    "ds_read_b64_tr_b16 v[246:247], v5 offset:29184\n"
    "v_mfma_f32_16x16x32_bf16 v[164:167], v[224:227], a[124:127], v[164:167]\n"
    "s_waitcnt lgkmcnt(8)\n"
    "v_mfma_f32_16x16x32_bf16 v[168:171], v[224:227], a[188:191], v[168:171]\n"
    "s_add_u32 m0, s90, 69632\n"
    "s_nop 0\n"
    "buffer_load_dwordx4 v9, s[84:87], s77 offen lds\n"
    "v_mfma_f32_16x16x32_bf16 v[172:175], v[224:227], a[252:255], v[172:175]\n"
    "v_mfma_f32_16x16x32_bf16 v[176:179], v[228:231], a[60:63], v[176:179]\n"
    "ds_read_b64_tr_b16 v[248:249], v6 offset:28672\n"
    "ds_read_b64_tr_b16 v[250:251], v6 offset:29184\n"
    "v_mfma_f32_16x16x32_bf16 v[180:183], v[228:231], a[124:127], v[180:183]\n"
    "s_waitcnt lgkmcnt(8)\n"
    "v_mfma_f32_16x16x32_bf16 v[184:187], v[228:231], a[188:191], v[184:187]\n"
    "v_mfma_f32_16x16x32_bf16 v[188:191], v[228:231], a[252:255], v[188:191]\n"
    "v_mfma_f32_16x16x32_bf16 v[192:195], v[232:235], a[60:63], v[192:195]\n"
    "ds_read_b64_tr_b16 v[252:253], v8 offset:28672\n"
    "ds_read_b64_tr_b16 v[254:255], v8 offset:29184\n"
    "v_mfma_f32_16x16x32_bf16 v[196:199], v[232:235], a[124:127], v[196:199]\n"
    "s_waitcnt lgkmcnt(8)\n"
    "v_mfma_f32_16x16x32_bf16 v[200:203], v[232:235], a[188:191], v[200:203]\n"
    "v_mfma_f32_16x16x32_bf16 v[204:207], v[232:235], a[252:255], v[204:207]\n"
    "s_waitcnt vmcnt(17)\n"
    "s_waitcnt vmcnt(16)\n"
    "s_barrier\n"
    "v_mfma_f32_16x16x32_bf16 v[208:211], v[236:239], a[60:63], v[208:211]\n"
    "ds_read_b64_tr_b16 v[224:225], v4 offset:0\n"
    "ds_read_b64_tr_b16 v[226:227], v4 offset:512\n"
    "v_mfma_f32_16x16x32_bf16 v[212:215], v[236:239], a[124:127], v[212:215]\n"
    "s_waitcnt lgkmcnt(8)\n"
    "s_add_u32 s77, s66, 0x38000\n"
    "s_add_u32 s56, s61, 0x180000\n"
    "v_mfma_f32_16x16x32_bf16 v[216:219], v[236:239], a[188:191], v[216:219]\n"
    "s_add_u32 m0, s90, 73728\n"
    "s_nop 0\n"
    "buffer_load_dwordx4 v9, s[48:51], s77 offen lds\n"
    "v_mfma_f32_16x16x32_bf16 v[220:223], v[236:239], a[252:255], v[220:223]\n"
    "s_add_u32 s76, s61, 0x80080\n"
    "v_mfma_f32_16x16x32_bf16 v[32:35], v[240:243], a[44:47], v[32:35]\n"
    "ds_read_b64_tr_b16 v[228:229], v5 offset:0\n"
    "ds_read_b64_tr_b16 v[230:231], v5 offset:512\n"
    "v_mfma_f32_16x16x32_bf16 v[36:39], v[240:243], a[108:111], v[36:39]\n"
    "s_waitcnt lgkmcnt(8)\n"
    "v_mfma_f32_16x16x32_bf16 v[40:43], v[240:243], a[172:175], v[40:43]\n"
    "s_add_u32 m0, s60, 49152\n"
    "s_nop 0\n"
    "buffer_load_dwordx4 v2, s[40:43], s56 offen lds\n"
    "v_cvt_pk_bf16_f32 v160, v160, v161\n"
    "v_mfma_f32_16x16x32_bf16 v[44:47], v[240:243], a[236:239], v[44:47]\n"
    "v_cvt_pk_bf16_f32 v161, v162, v163\n"
    "v_cvt_pk_bf16_f32 v162, v176, v177\n"
    "v_mfma_f32_16x16x32_bf16 v[48:51], v[244:247], a[44:47], v[48:51]\n"
    "ds_read_b64_tr_b16 v[232:233], v6 offset:0\n"
    "ds_read_b64_tr_b16 v[234:235], v6 offset:512\n"
    "v_mfma_f32_16x16x32_bf16 v[52:55], v[244:247], a[108:111], v[52:55]\n"
    "s_waitcnt lgkmcnt(8)\n"
    "v_cvt_pk_bf16_f32 v163, v178, v179\n"
    "v_cvt_pk_bf16_f32 v164, v164, v165\n"
    "v_mfma_f32_16x16x32_bf16 v[56:59], v[244:247], a[172:175], v[56:59]\n"
    "s_add_u32 m0, s60, 24576\n"
    "s_nop 0\n"
    "buffer_load_dwordx4 v2, s[40:43], s76 offen lds\n"
    "v_cvt_pk_bf16_f32 v165, v166, v167\n"
    "v_mfma_f32_16x16x32_bf16 v[60:63], v[244:247], a[236:239], v[60:63]\n"
    "v_cvt_pk_bf16_f32 v166, v180, v181\n"
    "v_cvt_pk_bf16_f32 v167, v182, v183\n"
    "v_mfma_f32_16x16x32_bf16 v[64:67], v[248:251], a[44:47], v[64:67]\n"
    "ds_read_b64_tr_b16 v[236:237], v8 offset:0\n"
    "ds_read_b64_tr_b16 v[238:239], v8 offset:512\n"
    "v_mfma_f32_16x16x32_bf16 v[68:71], v[248:251], a[108:111], v[68:71]\n"
    "s_waitcnt lgkmcnt(8)\n"
    "v_cvt_pk_bf16_f32 v168, v168, v169\n"
    "v_cvt_pk_bf16_f32 v169, v170, v171\n"
    "v_mfma_f32_16x16x32_bf16 v[72:75], v[248:251], a[172:175], v[72:75]\n"
    "s_add_u32 m0, s90, 77824\n"
    "s_nop 0\n"
    "buffer_load_dwordx4 v9, s[84:87], s77 offen lds\n"
    "v_cvt_pk_bf16_f32 v170, v184, v185\n"
    "v_mfma_f32_16x16x32_bf16 v[76:79], v[248:251], a[236:239], v[76:79]\n"
    "v_cvt_pk_bf16_f32 v171, v186, v187\n"
    "v_cvt_pk_bf16_f32 v172, v172, v173\n"
    "v_mfma_f32_16x16x32_bf16 v[80:83], v[252:255], a[44:47], v[80:83]\n"
    "ds_read_b64_tr_b16 v[240:241], v4 offset:40960\n"
    "ds_read_b64_tr_b16 v[242:243], v4 offset:41472\n"
    "v_mfma_f32_16x16x32_bf16 v[84:87], v[252:255], a[108:111], v[84:87]\n"
    "s_waitcnt lgkmcnt(8)\n"
    "v_cvt_pk_bf16_f32 v173, v174, v175\n"
    "v_cvt_pk_bf16_f32 v174, v188, v189\n"
    "v_mfma_f32_16x16x32_bf16 v[88:91], v[252:255], a[172:175], v[88:91]\n"
    "s_add_u32 m0, s60, 50176\n"
    "s_nop 0\n"
    "buffer_load_dwordx4 v3, s[40:43], s56 offen lds\n"
    "v_cvt_pk_bf16_f32 v175, v190, v191\n"
    "v_mfma_f32_16x16x32_bf16 v[92:95], v[252:255], a[236:239], v[92:95]\n"
    "v_cvt_pk_bf16_f32 v176, v192, v193\n"
    "v_cvt_pk_bf16_f32 v177, v194, v195\n"
    "v_mfma_f32_16x16x32_bf16 v[96:99], a[0:3], v[224:227], 0\n"
    "ds_read_b64_tr_b16 v[244:245], v5 offset:40960\n"
    "ds_read_b64_tr_b16 v[246:247], v5 offset:41472\n"
    "v_mfma_f32_16x16x32_bf16 v[100:103], a[64:67], v[224:227], 0\n"
    "s_waitcnt lgkmcnt(8)\n"
    "v_cvt_pk_bf16_f32 v178, v208, v209\n"
    "v_cvt_pk_bf16_f32 v179, v210, v211\n"
    "v_mfma_f32_16x16x32_bf16 v[104:107], a[128:131], v[224:227], 0\n"
    "s_add_u32 m0, s60, 25600\n"
    "s_nop 0\n"
    "buffer_load_dwordx4 v3, s[40:43], s76 offen lds\n"
    "v_cvt_pk_bf16_f32 v180, v196, v197\n"
    "v_mfma_f32_16x16x32_bf16 v[108:111], a[192:195], v[224:227], 0\n"
    "s_add_u32 s77, s66, 0x70000\n"
    "v_cvt_pk_bf16_f32 v181, v198, v199\n"
    "v_cvt_pk_bf16_f32 v182, v212, v213\n"
    "v_mfma_f32_16x16x32_bf16 v[112:115], a[0:3], v[228:231], 0\n"
    "ds_read_b64_tr_b16 v[248:249], v6 offset:40960\n"
    "ds_read_b64_tr_b16 v[250:251], v6 offset:41472\n"
    "v_mfma_f32_16x16x32_bf16 v[116:119], a[64:67], v[228:231], 0\n"
    "s_waitcnt lgkmcnt(8)\n"
    "v_cvt_pk_bf16_f32 v183, v214, v215\n"
    "v_cvt_pk_bf16_f32 v184, v200, v201\n"
    "v_mfma_f32_16x16x32_bf16 v[120:123], a[128:131], v[228:231], 0\n"
    "s_add_u32 m0, s90, 81920\n"
    "s_nop 0\n"
    "buffer_load_dwordx4 v9, s[48:51], s77 offen lds\n"
    "v_cvt_pk_bf16_f32 v185, v202, v203\n"
    "v_mfma_f32_16x16x32_bf16 v[124:127], a[192:195], v[228:231], 0\n"
    "v_cvt_pk_bf16_f32 v186, v216, v217\n"
    "v_cvt_pk_bf16_f32 v187, v218, v219\n"
    "v_mfma_f32_16x16x32_bf16 v[128:131], a[0:3], v[232:235], 0\n"
    "ds_read_b64_tr_b16 v[252:253], v8 offset:40960\n"
    "ds_read_b64_tr_b16 v[254:255], v8 offset:41472\n"
    "v_mfma_f32_16x16x32_bf16 v[132:135], a[64:67], v[232:235], 0\n"
    "s_waitcnt lgkmcnt(8)\n"
    "v_cvt_pk_bf16_f32 v188, v204, v205\n"
    "v_cvt_pk_bf16_f32 v189, v206, v207\n"
    "v_mfma_f32_16x16x32_bf16 v[136:139], a[128:131], v[232:235], 0\n"
    "v_cvt_pk_bf16_f32 v190, v220, v221\n"
    "v_cvt_pk_bf16_f32 v191, v222, v223\n"
    "v_mfma_f32_16x16x32_bf16 v[140:143], a[192:195], v[232:235], 0\n"
    "v_mfma_f32_16x16x32_bf16 v[144:147], a[0:3], v[236:239], 0\n"
    "ds_read_b64_tr_b16 v[224:225], v4 offset:4096\n"
    "ds_read_b64_tr_b16 v[226:227], v4 offset:4608\n"
    "v_mfma_f32_16x16x32_bf16 v[148:151], a[64:67], v[236:239], 0\n"
    "s_waitcnt lgkmcnt(8)\n"
    "v_mfma_f32_16x16x32_bf16 v[152:155], a[128:131], v[236:239], 0\n"
    "s_add_u32 m0, s90, 86016\n"
    "s_nop 0\n"
    "buffer_load_dwordx4 v9, s[84:87], s77 offen lds\n"
    "v_mfma_f32_16x16x32_bf16 v[156:159], a[192:195], v[236:239], 0\n"
    "v_mfma_f32_16x16x32_bf16 v[32:35], v[240:243], a[48:51], v[32:35]\n"
    "ds_read_b64_tr_b16 v[228:229], v5 offset:4096\n"
    "ds_read_b64_tr_b16 v[230:231], v5 offset:4608\n"
    "v_mfma_f32_16x16x32_bf16 v[36:39], v[240:243], a[112:115], v[36:39]\n"
    "s_waitcnt lgkmcnt(8)\n"
    "v_mfma_f32_16x16x32_bf16 v[40:43], v[240:243], a[176:179], v[40:43]\n"
    "v_mfma_f32_16x16x32_bf16 v[44:47], v[240:243], a[240:243], v[44:47]\n"
    "v_mfma_f32_16x16x32_bf16 v[48:51], v[244:247], a[48:51], v[48:51]\n"
    "ds_read_b64_tr_b16 v[232:233], v6 offset:4096\n"
    "ds_read_b64_tr_b16 v[234:235], v6 offset:4608\n"
    "v_mfma_f32_16x16x32_bf16 v[52:55], v[244:247], a[112:115], v[52:55]\n"
    "s_waitcnt lgkmcnt(8)\n"
    "v_mfma_f32_16x16x32_bf16 v[56:59], v[244:247], a[176:179], v[56:59]\n"
    "v_mfma_f32_16x16x32_bf16 v[60:63], v[244:247], a[240:243], v[60:63]\n"
    "s_mov_b32 s80, 0x3e0293ee\n"
    "s_add_u32 s77, s66, 0xa8000\n"
    "v_mfma_f32_16x16x32_bf16 v[64:67], v[248:251], a[48:51], v[64:67]\n"
    "ds_read_b64_tr_b16 v[236:237], v8 offset:4096\n"
    "ds_read_b64_tr_b16 v[238:239], v8 offset:4608\n"
    "v_mfma_f32_16x16x32_bf16 v[68:71], v[248:251], a[112:115], v[68:71]\n"
    "s_waitcnt lgkmcnt(8)\n"
    "v_mfma_f32_16x16x32_bf16 v[72:75], v[248:251], a[176:179], v[72:75]\n"
    "s_add_u32 m0, s90, 90112\n"
    "s_nop 0\n"
    "buffer_load_dwordx4 v9, s[48:51], s77 offen lds\n"
    "v_mfma_f32_16x16x32_bf16 v[76:79], v[248:251], a[240:243], v[76:79]\n"
    "v_mfma_f32_16x16x32_bf16 v[80:83], v[252:255], a[48:51], v[80:83]\n"
    "ds_read_b64_tr_b16 v[240:241], v4 offset:45056\n"
    "ds_read_b64_tr_b16 v[242:243], v4 offset:45568\n"
    "v_mfma_f32_16x16x32_bf16 v[84:87], v[252:255], a[112:115], v[84:87]\n"
    "s_waitcnt lgkmcnt(8)\n"
    "s_mov_b64 s[36:37], %7\n"
    "v_mfma_f32_16x16x32_bf16 v[88:91], v[252:255], a[176:179], v[88:91]\n"
    "s_add_u32 s58, s62, 0x0\n"
    "buffer_store_dwordx4 v[160:163], v1, s[44:47], s58 offen sc1\n"
    "v_mfma_f32_16x16x32_bf16 v[92:95], v[252:255], a[240:243], v[92:95]\n"
    "v_mfma_f32_16x16x32_bf16 v[96:99], a[4:7], v[224:227], v[96:99]\n"
    "ds_read_b64_tr_b16 v[244:245], v5 offset:45056\n"
    "ds_read_b64_tr_b16 v[246:247], v5 offset:45568\n"
    "v_mfma_f32_16x16x32_bf16 v[100:103], a[68:71], v[224:227], v[100:103]\n"
    "s_waitcnt lgkmcnt(8)\n"
    "v_mfma_f32_16x16x32_bf16 v[104:107], a[132:135], v[224:227], v[104:107]\n"
    "s_add_u32 m0, s90, 94208\n"
    "s_nop 0\n"
    "buffer_load_dwordx4 v9, s[84:87], s77 offen lds\n"
    "v_mfma_f32_16x16x32_bf16 v[108:111], a[196:199], v[224:227], v[108:111]\n"
    "s_and_b32 s37, s37, 0xffff\n"
    "v_mfma_f32_16x16x32_bf16 v[112:115], a[4:7], v[228:231], v[112:115]\n"
    "ds_read_b64_tr_b16 v[248:249], v6 offset:45056\n"
    "ds_read_b64_tr_b16 v[250:251], v6 offset:45568\n"
    "v_mfma_f32_16x16x32_bf16 v[116:119], a[68:71], v[228:231], v[116:119]\n"
    "s_waitcnt lgkmcnt(8)\n"
    "v_mfma_f32_16x16x32_bf16 v[120:123], a[132:135], v[228:231], v[120:123]\n"
    "s_add_u32 s58, s62, 0x1000\n"
    "buffer_store_dwordx4 v[164:167], v1, s[44:47], s58 offen sc1\n"
    "v_mfma_f32_16x16x32_bf16 v[124:127], a[196:199], v[228:231], v[124:127]\n"
    "v_mfma_f32_16x16x32_bf16 v[128:131], a[4:7], v[232:235], v[128:131]\n"
    "ds_read_b64_tr_b16 v[252:253], v8 offset:45056\n"
    "ds_read_b64_tr_b16 v[254:255], v8 offset:45568\n"
    "v_mfma_f32_16x16x32_bf16 v[132:135], a[68:71], v[232:235], v[132:135]\n"
    "s_waitcnt lgkmcnt(8)\n"
    "s_mov_b32 s38, 0x400000\n"
    "v_mfma_f32_16x16x32_bf16 v[136:139], a[132:135], v[232:235], v[136:139]\n"
    "v_mfma_f32_16x16x32_bf16 v[140:143], a[196:199], v[232:235], v[140:143]\n"
    "s_waitcnt vmcnt(23)\n"
    "s_waitcnt vmcnt(22)\n"
    "s_barrier\n"
    "v_mfma_f32_16x16x32_bf16 v[144:147], a[4:7], v[236:239], v[144:147]\n"
    "ds_read_b64_tr_b16 v[224:225], v4 offset:16384\n"
    "ds_read_b64_tr_b16 v[226:227], v4 offset:16896\n"
    "v_mfma_f32_16x16x32_bf16 v[148:151], a[68:71], v[236:239], v[148:151]\n"
    "s_waitcnt lgkmcnt(8)\n"
    "s_add_u32 s77, s66, 0xe0000\n"
    "s_add_u32 s56, s61, 0x200000\n"
    "v_mfma_f32_16x16x32_bf16 v[152:155], a[132:135], v[236:239], v[152:155]\n"
    "s_add_u32 m0, s90, 98304\n"
    "s_nop 0\n"
    "buffer_load_dwordx4 v9, s[48:51], s77 offen lds\n"
    "v_mfma_f32_16x16x32_bf16 v[156:159], a[196:199], v[236:239], v[156:159]\n"
    "s_mov_b32 s39, 0x20000\n"
    "s_add_u32 s76, s61, 0x100080\n"
    "v_mfma_f32_16x16x32_bf16 v[32:35], v[240:243], a[52:55], v[32:35]\n"
    "ds_read_b64_tr_b16 v[228:229], v5 offset:16384\n"
    "ds_read_b64_tr_b16 v[230:231], v5 offset:16896\n"
    "v_mfma_f32_16x16x32_bf16 v[36:39], v[240:243], a[116:119], v[36:39]\n"
    "s_waitcnt lgkmcnt(8)\n"
    "v_mfma_f32_16x16x32_bf16 v[40:43], v[240:243], a[180:183], v[40:43]\n"
    "s_add_u32 s58, s62, 0x2000\n"
    "buffer_store_dwordx4 v[168:171], v1, s[44:47], s58 offen sc1\n"
    "v_mfma_f32_16x16x32_bf16 v[44:47], v[240:243], a[244:247], v[44:47]\n"
    "v_mfma_f32_16x16x32_bf16 v[48:51], v[244:247], a[52:55], v[48:51]\n"
    "ds_read_b64_tr_b16 v[232:233], v6 offset:16384\n"
    "ds_read_b64_tr_b16 v[234:235], v6 offset:16896\n"
    "v_mfma_f32_16x16x32_bf16 v[52:55], v[244:247], a[116:119], v[52:55]\n"
    "s_waitcnt lgkmcnt(8)\n"
    "v_mfma_f32_16x16x32_bf16 v[56:59], v[244:247], a[180:183], v[56:59]\n"
    "s_add_u32 m0, s60, 0\n"
    "s_nop 0\n"
    "buffer_load_dwordx4 v2, s[40:43], s56 offen lds\n"
    "v_mfma_f32_16x16x32_bf16 v[60:63], v[244:247], a[244:247], v[60:63]\n"
    "v_mfma_f32_16x16x32_bf16 v[64:67], v[248:251], a[52:55], v[64:67]\n"
    "ds_read_b64_tr_b16 v[236:237], v8 offset:16384\n"
    "ds_read_b64_tr_b16 v[238:239], v8 offset:16896\n"
    "v_mfma_f32_16x16x32_bf16 v[68:71], v[248:251], a[116:119], v[68:71]\n"
    "s_waitcnt lgkmcnt(8)\n"
    "v_mfma_f32_16x16x32_bf16 v[72:75], v[248:251], a[180:183], v[72:75]\n"
    "s_add_u32 m0, s90, 102400\n"
    "s_nop 0\n"
    "buffer_load_dwordx4 v9, s[84:87], s77 offen lds\n"
    "v_mfma_f32_16x16x32_bf16 v[76:79], v[248:251], a[244:247], v[76:79]\n"
    "v_mfma_f32_16x16x32_bf16 v[80:83], v[252:255], a[52:55], v[80:83]\n"
    "ds_read_b64_tr_b16 v[240:241], v4 offset:57344\n"
    "ds_read_b64_tr_b16 v[242:243], v4 offset:57856\n"
    "v_mfma_f32_16x16x32_bf16 v[84:87], v[252:255], a[116:119], v[84:87]\n"
    "s_waitcnt lgkmcnt(8)\n"
    "v_mfma_f32_16x16x32_bf16 v[88:91], v[252:255], a[180:183], v[88:91]\n"
    "s_add_u32 m0, s60, 40960\n"
    "s_nop 0\n"
    "buffer_load_dwordx4 v2, s[40:43], s76 offen lds\n"
    "v_mfma_f32_16x16x32_bf16 v[92:95], v[252:255], a[244:247], v[92:95]\n"
    "v_mfma_f32_16x16x32_bf16 v[96:99], a[8:11], v[224:227], v[96:99]\n"
    "ds_read_b64_tr_b16 v[244:245], v5 offset:57344\n"
    "ds_read_b64_tr_b16 v[246:247], v5 offset:57856\n"
    "v_mfma_f32_16x16x32_bf16 v[100:103], a[72:75], v[224:227], v[100:103]\n"
    "s_waitcnt lgkmcnt(8)\n"
    "s_add_u32 s77, s66, 0x118000\n"
    "v_mfma_f32_16x16x32_bf16 v[104:107], a[136:139], v[224:227], v[104:107]\n"
    "s_add_u32 m0, s90, 106496\n"
    "s_nop 0\n"
    "buffer_load_dwordx4 v9, s[48:51], s77 offen lds\n"
    "v_mfma_f32_16x16x32_bf16 v[108:111], a[200:203], v[224:227], v[108:111]\n"
    "v_mfma_f32_16x16x32_bf16 v[112:115], a[8:11], v[228:231], v[112:115]\n"
    "ds_read_b64_tr_b16 v[248:249], v6 offset:57344\n"
    "ds_read_b64_tr_b16 v[250:251], v6 offset:57856\n"
    "v_mfma_f32_16x16x32_bf16 v[116:119], a[72:75], v[228:231], v[116:119]\n"
    "s_waitcnt lgkmcnt(8)\n"
    "v_mfma_f32_16x16x32_bf16 v[120:123], a[136:139], v[228:231], v[120:123]\n"
    "s_add_u32 s58, s62, 0x3000\n"
    "buffer_store_dwordx4 v[172:175], v1, s[44:47], s58 offen sc1\n"
    "v_mfma_f32_16x16x32_bf16 v[124:127], a[200:203], v[228:231], v[124:127]\n"
    "v_mfma_f32_16x16x32_bf16 v[128:131], a[8:11], v[232:235], v[128:131]\n"
    "ds_read_b64_tr_b16 v[252:253], v8 offset:57344\n"
    "ds_read_b64_tr_b16 v[254:255], v8 offset:57856\n"
    "v_mfma_f32_16x16x32_bf16 v[132:135], a[72:75], v[232:235], v[132:135]\n"
    "s_waitcnt lgkmcnt(8)\n"
    "v_mfma_f32_16x16x32_bf16 v[136:139], a[136:139], v[232:235], v[136:139]\n"
    "s_add_u32 m0, s60, 1024\n"
    "s_nop 0\n"
    "buffer_load_dwordx4 v3, s[40:43], s56 offen lds\n"
    "v_mfma_f32_16x16x32_bf16 v[140:143], a[200:203], v[232:235], v[140:143]\n"
    "v_mfma_f32_16x16x32_bf16 v[144:147], a[8:11], v[236:239], v[144:147]\n"
    "ds_read_b64_tr_b16 v[224:225], v4 offset:20480\n"
    "ds_read_b64_tr_b16 v[226:227], v4 offset:20992\n"
    "v_mfma_f32_16x16x32_bf16 v[148:151], a[72:75], v[236:239], v[148:151]\n"
    "s_waitcnt lgkmcnt(8)\n"
    "v_mfma_f32_16x16x32_bf16 v[152:155], a[136:139], v[236:239], v[152:155]\n"
    "s_add_u32 m0, s90, 110592\n"
    "s_nop 0\n"
    "buffer_load_dwordx4 v9, s[84:87], s77 offen lds\n"
    "v_mfma_f32_16x16x32_bf16 v[156:159], a[200:203], v[236:239], v[156:159]\n"
    "v_mfma_f32_16x16x32_bf16 v[32:35], v[240:243], a[56:59], v[32:35]\n"
    "ds_read_b64_tr_b16 v[228:229], v5 offset:20480\n"
    "ds_read_b64_tr_b16 v[230:231], v5 offset:20992\n"
    "v_mfma_f32_16x16x32_bf16 v[36:39], v[240:243], a[120:123], v[36:39]\n"
    "s_waitcnt lgkmcnt(8)\n"
    "v_mfma_f32_16x16x32_bf16 v[40:43], v[240:243], a[184:187], v[40:43]\n"
    "s_add_u32 s58, s62, 0x400\n"
    "buffer_store_dwordx4 v[176:179], v1, s[44:47], s58 offen sc1\n"
    "v_mfma_f32_16x16x32_bf16 v[44:47], v[240:243], a[248:251], v[44:47]\n"
    "v_mfma_f32_16x16x32_bf16 v[48:51], v[244:247], a[56:59], v[48:51]\n"
    "ds_read_b64_tr_b16 v[232:233], v6 offset:20480\n"
    "ds_read_b64_tr_b16 v[234:235], v6 offset:20992\n"
    "v_mfma_f32_16x16x32_bf16 v[52:55], v[244:247], a[120:123], v[52:55]\n"
    "s_waitcnt lgkmcnt(8)\n"
    "v_mfma_f32_16x16x32_bf16 v[56:59], v[244:247], a[184:187], v[56:59]\n"
    "s_add_u32 m0, s60, 41984\n"
    "s_nop 0\n"
    "buffer_load_dwordx4 v3, s[40:43], s76 offen lds\n"
    "v_mfma_f32_16x16x32_bf16 v[60:63], v[244:247], a[248:251], v[60:63]\n"
    "s_add_u32 s77, s66, 0x150000\n"
    "v_mfma_f32_16x16x32_bf16 v[64:67], v[248:251], a[56:59], v[64:67]\n"
    "ds_read_b64_tr_b16 v[236:237], v8 offset:20480\n"
    "ds_read_b64_tr_b16 v[238:239], v8 offset:20992\n"
    "v_mfma_f32_16x16x32_bf16 v[68:71], v[248:251], a[120:123], v[68:71]\n"
    "s_waitcnt lgkmcnt(8)\n"
    "v_mfma_f32_16x16x32_bf16 v[72:75], v[248:251], a[184:187], v[72:75]\n"
    "s_add_u32 m0, s90, 114688\n"
    "s_nop 0\n"
    "buffer_load_dwordx4 v9, s[48:51], s77 offen lds\n"
    "v_mfma_f32_16x16x32_bf16 v[76:79], v[248:251], a[248:251], v[76:79]\n"
    "v_mfma_f32_16x16x32_bf16 v[80:83], v[252:255], a[56:59], v[80:83]\n"
    "ds_read_b64_tr_b16 v[240:241], v4 offset:61440\n"
    "ds_read_b64_tr_b16 v[242:243], v4 offset:61952\n"
    "v_mfma_f32_16x16x32_bf16 v[84:87], v[252:255], a[120:123], v[84:87]\n"
    "s_waitcnt lgkmcnt(8)\n"
    "v_mfma_f32_16x16x32_bf16 v[88:91], v[252:255], a[184:187], v[88:91]\n"
    "s_add_u32 s58, s62, 0x1400\n"
    "buffer_store_dwordx4 v[180:183], v1, s[44:47], s58 offen sc1\n"
    "v_mfma_f32_16x16x32_bf16 v[92:95], v[252:255], a[248:251], v[92:95]\n"
    "v_mfma_f32_16x16x32_bf16 v[96:99], a[12:15], v[224:227], v[96:99]\n"
    "ds_read_b64_tr_b16 v[244:245], v5 offset:61440\n"
    "ds_read_b64_tr_b16 v[246:247], v5 offset:61952\n"
    "v_mfma_f32_16x16x32_bf16 v[100:103], a[76:79], v[224:227], v[100:103]\n"
    "s_waitcnt lgkmcnt(8)\n"
    "v_mfma_f32_16x16x32_bf16 v[104:107], a[140:143], v[224:227], v[104:107]\n"
    "s_add_u32 m0, s90, 118784\n"
    "s_nop 0\n"
    "buffer_load_dwordx4 v9, s[84:87], s77 offen lds\n"
    "v_mfma_f32_16x16x32_bf16 v[108:111], a[204:207], v[224:227], v[108:111]\n"
    "v_and_b32 v13, 63, v0\n"
    "v_and_b32 v25, 15, v13\n"
    "v_lshlrev_b32 v25, 2, v25\n"
    "s_sub_u32 s59, s69, 0x100000\n"
    "s_lshr_b32 s59, s59, 9\n"
    "s_sub_u32 s95, s70, 0x100000\n"
    "s_lshr_b32 s95, s95, 9\n"
    "v_mov_b32 v12, s59\n"
    "v_mov_b32 v26, s95\n"
    "v_and_b32 v13, 16, v13\n"
    "v_cmp_eq_u32 vcc, 0, v13\n"
    "s_nop 1\n"
    "v_cndmask_b32 v12, v26, v12, vcc\n"
    "v_add_u32 v29, v12, v25\n"
    "s_mov_b32 s96, 0x1000\n"
    "buffer_load_dword v24, v29, s[44:47], s96 offen sc1\n"
    "v_mfma_f32_16x16x32_bf16 v[112:115], a[12:15], v[228:231], v[112:115]\n"
    "ds_read_b64_tr_b16 v[248:249], v6 offset:61440\n"
    "ds_read_b64_tr_b16 v[250:251], v6 offset:61952\n"
    "v_mfma_f32_16x16x32_bf16 v[116:119], a[76:79], v[228:231], v[116:119]\n"
    "s_waitcnt lgkmcnt(8)\n"
    "v_mfma_f32_16x16x32_bf16 v[120:123], a[140:143], v[228:231], v[120:123]\n"
    "s_add_u32 s58, s62, 0x2400\n"
    "buffer_store_dwordx4 v[184:187], v1, s[44:47], s58 offen sc1\n"
    "v_mfma_f32_16x16x32_bf16 v[124:127], a[204:207], v[228:231], v[124:127]\n"
    "v_mfma_f32_16x16x32_bf16 v[128:131], a[12:15], v[232:235], v[128:131]\n"
    "ds_read_b64_tr_b16 v[252:253], v8 offset:61440\n"
    "ds_read_b64_tr_b16 v[254:255], v8 offset:61952\n"
    "v_mfma_f32_16x16x32_bf16 v[132:135], a[76:79], v[232:235], v[132:135]\n"
    "s_waitcnt lgkmcnt(8)\n"
    "v_mfma_f32_16x16x32_bf16 v[136:139], a[140:143], v[232:235], v[136:139]\n"
    "v_mfma_f32_16x16x32_bf16 v[140:143], a[204:207], v[232:235], v[140:143]\n"
    "s_add_u32 s77, s66, 0x188000\n"
    "s_waitcnt vmcnt(31)\n"
    "s_waitcnt vmcnt(30)\n"
    "s_barrier\n"
    "v_mfma_f32_16x16x32_bf16 v[144:147], a[12:15], v[236:239], v[144:147]\n"
    "ds_read_b64_tr_b16 v[224:225], v4 offset:32768\n"
    "ds_read_b64_tr_b16 v[226:227], v4 offset:33280\n"
    "v_mfma_f32_16x16x32_bf16 v[148:151], a[76:79], v[236:239], v[148:151]\n"
    "s_waitcnt lgkmcnt(8)\n"
    "s_add_u32 s56, s61, 0x280000\n"
    "s_add_u32 s76, s61, 0x180080\n"
    "v_mfma_f32_16x16x32_bf16 v[152:155], a[140:143], v[236:239], v[152:155]\n"
    "s_add_u32 m0, s90, 122880\n"
    "s_nop 0\n"
    "buffer_load_dwordx4 v9, s[48:51], s77 offen lds\n"
    "v_mfma_f32_16x16x32_bf16 v[156:159], a[204:207], v[236:239], v[156:159]\n"
    "v_mfma_f32_16x16x32_bf16 v[32:35], v[240:243], a[60:63], v[32:35]\n"
    "ds_read_b64_tr_b16 v[228:229], v5 offset:32768\n"
    "ds_read_b64_tr_b16 v[230:231], v5 offset:33280\n"
    "v_mfma_f32_16x16x32_bf16 v[36:39], v[240:243], a[124:127], v[36:39]\n"
    "s_waitcnt lgkmcnt(8)\n"
    "v_mfma_f32_16x16x32_bf16 v[40:43], v[240:243], a[188:191], v[40:43]\n"
    "s_add_u32 m0, s60, 16384\n"
    "s_nop 0\n"
    "buffer_load_dwordx4 v2, s[40:43], s56 offen lds\n"
    "v_mfma_f32_16x16x32_bf16 v[44:47], v[240:243], a[252:255], v[44:47]\n"
    "v_mfma_f32_16x16x32_bf16 v[48:51], v[244:247], a[60:63], v[48:51]\n"
    "ds_read_b64_tr_b16 v[232:233], v6 offset:32768\n"
    "ds_read_b64_tr_b16 v[234:235], v6 offset:33280\n"
    "v_mfma_f32_16x16x32_bf16 v[52:55], v[244:247], a[124:127], v[52:55]\n"
    "s_waitcnt lgkmcnt(8)\n"
    "v_cvt_pk_bf16_f32 v192, v32, v33\n"
    "v_cvt_pk_bf16_f32 v193, v34, v35\n"
    "v_mfma_f32_16x16x32_bf16 v[56:59], v[244:247], a[188:191], v[56:59]\n"
    "s_add_u32 m0, s90, 126976\n"
    "s_nop 0\n"
    "buffer_load_dwordx4 v9, s[84:87], s77 offen lds\n"
    "v_cvt_pk_bf16_f32 v196, v36, v37\n"
    "v_mfma_f32_16x16x32_bf16 v[60:63], v[244:247], a[252:255], v[60:63]\n"
    "v_cvt_pk_bf16_f32 v197, v38, v39\n"
    "v_cvt_pk_bf16_f32 v200, v40, v41\n"
    "v_mfma_f32_16x16x32_bf16 v[64:67], v[248:251], a[60:63], v[64:67]\n"
    "ds_read_b64_tr_b16 v[236:237], v8 offset:32768\n"
    "ds_read_b64_tr_b16 v[238:239], v8 offset:33280\n"
    "v_mfma_f32_16x16x32_bf16 v[68:71], v[248:251], a[124:127], v[68:71]\n"
    "s_waitcnt lgkmcnt(8)\n"
    "v_cvt_pk_bf16_f32 v201, v42, v43\n"
    "v_cvt_pk_bf16_f32 v204, v44, v45\n"
    "v_mfma_f32_16x16x32_bf16 v[72:75], v[248:251], a[188:191], v[72:75]\n"
    "s_add_u32 s58, s62, 0x3400\n"
    "buffer_store_dwordx4 v[188:191], v1, s[44:47], s58 offen sc1\n"
    "v_cvt_pk_bf16_f32 v205, v46, v47\n"
    "v_mfma_f32_16x16x32_bf16 v[76:79], v[248:251], a[252:255], v[76:79]\n"
    "v_cvt_pk_bf16_f32 v194, v48, v49\n"
    "v_cvt_pk_bf16_f32 v195, v50, v51\n"
    "v_mfma_f32_16x16x32_bf16 v[80:83], v[252:255], a[60:63], v[80:83]\n"
    "ds_read_b64_tr_b16 v[240:241], v4 offset:8192\n"
    "ds_read_b64_tr_b16 v[242:243], v4 offset:8704\n"
    "v_mfma_f32_16x16x32_bf16 v[84:87], v[252:255], a[124:127], v[84:87]\n"
    "s_waitcnt lgkmcnt(8)\n"
    "v_cvt_pk_bf16_f32 v198, v52, v53\n"
    "v_cvt_pk_bf16_f32 v199, v54, v55\n"
    "v_mfma_f32_16x16x32_bf16 v[88:91], v[252:255], a[188:191], v[88:91]\n"
    "s_add_u32 m0, s60, 57344\n"
    "s_nop 0\n"
    "buffer_load_dwordx4 v2, s[40:43], s76 offen lds\n"
    "v_cvt_pk_bf16_f32 v202, v56, v57\n"
    "v_mfma_f32_16x16x32_bf16 v[92:95], v[252:255], a[252:255], v[92:95]\n"
    "v_cvt_pk_bf16_f32 v203, v58, v59\n"
    "v_cvt_pk_bf16_f32 v206, v60, v61\n"
    "v_mfma_f32_16x16x32_bf16 v[96:99], a[16:19], v[224:227], v[96:99]\n"
    "ds_read_b64_tr_b16 v[244:245], v5 offset:8192\n"
    "ds_read_b64_tr_b16 v[246:247], v5 offset:8704\n"
    "v_mfma_f32_16x16x32_bf16 v[100:103], a[80:83], v[224:227], v[100:103]\n"
    "s_waitcnt lgkmcnt(8)\n"
    "v_cvt_pk_bf16_f32 v207, v62, v63\n"
    "v_cvt_pk_bf16_f32 v208, v64, v65\n"
    "v_mfma_f32_16x16x32_bf16 v[104:107], a[144:147], v[224:227], v[104:107]\n"
    "s_add_u32 m0, s60, 17408\n"
    "s_nop 0\n"
    "buffer_load_dwordx4 v3, s[40:43], s56 offen lds\n"
    "v_cvt_pk_bf16_f32 v209, v66, v67\n"
    "v_mfma_f32_16x16x32_bf16 v[108:111], a[208:211], v[224:227], v[108:111]\n"
    "s_waitcnt vmcnt(7)\n"
    "v_cmp_eq_u32 vcc, s97, v24\n"
    "s_nop 0\n"
    "s_cmp_eq_u64 vcc, -1\n"
    "s_cbranch_scc1 L_pok_1_%=\n"
    "s_mov_b32 s96, 0x1000\n"
    "s_mov_b32 s94, 2000000\n"
    "L_psp_2_%=:\n"
    "s_sleep 2\n"
    "buffer_load_dword v24, v29, s[44:47], s96 offen sc1\n"
    "s_waitcnt vmcnt(0)\n"
    "v_cmp_eq_u32 vcc, s97, v24\n"
    "s_nop 4\n"
    "s_cmp_eq_u64 vcc, -1\n"
    "s_cbranch_scc1 L_pok_1_%=\n"
    "s_sub_u32 s94, s94, 1\n"
    "s_cmp_lg_u32 s94, 0\n"
    "s_cbranch_scc1 L_psp_2_%=\n"
    "L_pok_1_%=:\n"
    "v_mfma_f32_16x16x32_bf16 v[112:115], a[16:19], v[228:231], v[112:115]\n"
    "ds_read_b64_tr_b16 v[248:249], v6 offset:8192\n"
    "ds_read_b64_tr_b16 v[250:251], v6 offset:8704\n"
    "v_mfma_f32_16x16x32_bf16 v[116:119], a[80:83], v[228:231], v[116:119]\n"
    "s_waitcnt lgkmcnt(8)\n"
    "v_cvt_pk_bf16_f32 v212, v68, v69\n"
    "v_cvt_pk_bf16_f32 v213, v70, v71\n"
    "v_mfma_f32_16x16x32_bf16 v[120:123], a[144:147], v[228:231], v[120:123]\n"
    "s_add_u32 m0, s60, 58368\n"
    "s_nop 0\n"
    "buffer_load_dwordx4 v3, s[40:43], s76 offen lds\n"
    "v_cvt_pk_bf16_f32 v216, v72, v73\n"
    "v_mfma_f32_16x16x32_bf16 v[124:127], a[208:211], v[228:231], v[124:127]\n"
    "v_cvt_pk_bf16_f32 v217, v74, v75\n"
    "v_cvt_pk_bf16_f32 v220, v76, v77\n"
    "v_mfma_f32_16x16x32_bf16 v[128:131], a[16:19], v[232:235], v[128:131]\n"
    "ds_read_b64_tr_b16 v[252:253], v8 offset:8192\n"
    "ds_read_b64_tr_b16 v[254:255], v8 offset:8704\n"
    "v_mfma_f32_16x16x32_bf16 v[132:135], a[80:83], v[232:235], v[132:135]\n"
    "s_waitcnt lgkmcnt(8)\n"
    "v_cvt_pk_bf16_f32 v221, v78, v79\n"
    "v_cvt_pk_bf16_f32 v210, v80, v81\n"
    "v_mfma_f32_16x16x32_bf16 v[136:139], a[144:147], v[232:235], v[136:139]\n"
    "v_cvt_pk_bf16_f32 v211, v82, v83\n"
    "v_cvt_pk_bf16_f32 v214, v84, v85\n"
    "v_mfma_f32_16x16x32_bf16 v[140:143], a[208:211], v[232:235], v[140:143]\n"
    "v_cvt_pk_bf16_f32 v215, v86, v87\n"
    "v_mfma_f32_16x16x32_bf16 v[144:147], a[16:19], v[236:239], v[144:147]\n"
    "ds_read_b64_tr_b16 v[224:225], v4 offset:36864\n"
    "ds_read_b64_tr_b16 v[226:227], v4 offset:37376\n"
    "v_mfma_f32_16x16x32_bf16 v[148:151], a[80:83], v[236:239], v[148:151]\n"
    "s_waitcnt lgkmcnt(8)\n"
    "v_cvt_pk_bf16_f32 v218, v88, v89\n"
    "v_cvt_pk_bf16_f32 v219, v90, v91\n"
    "v_mfma_f32_16x16x32_bf16 v[152:155], a[144:147], v[236:239], v[152:155]\n"
    "v_cvt_pk_bf16_f32 v222, v92, v93\n"
    "v_cvt_pk_bf16_f32 v223, v94, v95\n"
    "v_mfma_f32_16x16x32_bf16 v[156:159], a[208:211], v[236:239], v[156:159]\n"
    "v_mfma_f32_16x16x32_bf16 v[32:35], a[0:3], v[240:243], 0\n"
    "ds_read_b64_tr_b16 v[228:229], v5 offset:36864\n"
    "ds_read_b64_tr_b16 v[230:231], v5 offset:37376\n"
    "v_mfma_f32_16x16x32_bf16 v[36:39], a[64:67], v[240:243], 0\n"
    "s_waitcnt lgkmcnt(8)\n"
    "v_mfma_f32_16x16x32_bf16 v[40:43], a[128:131], v[240:243], 0\n"
    "v_mfma_f32_16x16x32_bf16 v[44:47], a[192:195], v[240:243], 0\n"
    "v_mfma_f32_16x16x32_bf16 v[48:51], a[0:3], v[244:247], 0\n"
    "ds_read_b64_tr_b16 v[232:233], v6 offset:36864\n"
    "ds_read_b64_tr_b16 v[234:235], v6 offset:37376\n"
    "v_mfma_f32_16x16x32_bf16 v[52:55], a[64:67], v[244:247], 0\n"
    "s_waitcnt lgkmcnt(8)\n"
    "v_mfma_f32_16x16x32_bf16 v[56:59], a[128:131], v[244:247], 0\n"
    "v_mfma_f32_16x16x32_bf16 v[60:63], a[192:195], v[244:247], 0\n"
    "v_mfma_f32_16x16x32_bf16 v[64:67], a[0:3], v[248:251], 0\n"
    "ds_read_b64_tr_b16 v[236:237], v8 offset:36864\n"
    "ds_read_b64_tr_b16 v[238:239], v8 offset:37376\n"
    "v_mfma_f32_16x16x32_bf16 v[68:71], a[64:67], v[248:251], 0\n"
    "s_waitcnt lgkmcnt(8)\n"
    "v_mfma_f32_16x16x32_bf16 v[72:75], a[128:131], v[248:251], 0\n"
    "v_mfma_f32_16x16x32_bf16 v[76:79], a[192:195], v[248:251], 0\n"
    "v_mfma_f32_16x16x32_bf16 v[80:83], a[0:3], v[252:255], 0\n"
    "ds_read_b64_tr_b16 v[240:241], v4 offset:12288\n"
    "ds_read_b64_tr_b16 v[242:243], v4 offset:12800\n"
    "v_mfma_f32_16x16x32_bf16 v[84:87], a[64:67], v[252:255], 0\n"
    "s_waitcnt lgkmcnt(8)\n"
    "v_mfma_f32_16x16x32_bf16 v[88:91], a[128:131], v[252:255], 0\n"
    "v_mfma_f32_16x16x32_bf16 v[92:95], a[192:195], v[252:255], 0\n"
    "v_mfma_f32_16x16x32_bf16 v[96:99], a[20:23], v[224:227], v[96:99]\n"
    "ds_read_b64_tr_b16 v[244:245], v5 offset:12288\n"
    "ds_read_b64_tr_b16 v[246:247], v5 offset:12800\n"
    "v_mfma_f32_16x16x32_bf16 v[100:103], a[84:87], v[224:227], v[100:103]\n"
    "s_waitcnt lgkmcnt(8)\n"
    "v_mfma_f32_16x16x32_bf16 v[104:107], a[148:151], v[224:227], v[104:107]\n"
    "s_add_u32 s59, s69, 0\n"
    "buffer_load_dwordx4 a[0:3], v1, s[44:47], s59 offen offset:0 sc1\n"
    "v_mfma_f32_16x16x32_bf16 v[108:111], a[212:215], v[224:227], v[108:111]\n"
    "v_mfma_f32_16x16x32_bf16 v[112:115], a[20:23], v[228:231], v[112:115]\n"
    "ds_read_b64_tr_b16 v[248:249], v6 offset:12288\n"
    "ds_read_b64_tr_b16 v[250:251], v6 offset:12800\n"
    "v_mfma_f32_16x16x32_bf16 v[116:119], a[84:87], v[228:231], v[116:119]\n"
    "s_waitcnt lgkmcnt(8)\n"
    "v_mfma_f32_16x16x32_bf16 v[120:123], a[148:151], v[228:231], v[120:123]\n"
    "s_add_u32 s58, s62, 0x800\n"
    "buffer_store_dwordx4 v[192:195], v1, s[44:47], s58 offen sc1\n"
    "v_mfma_f32_16x16x32_bf16 v[124:127], a[212:215], v[228:231], v[124:127]\n"
    "v_mfma_f32_16x16x32_bf16 v[128:131], a[20:23], v[232:235], v[128:131]\n"
    "ds_read_b64_tr_b16 v[252:253], v8 offset:12288\n"
    "ds_read_b64_tr_b16 v[254:255], v8 offset:12800\n"
    "v_mfma_f32_16x16x32_bf16 v[132:135], a[84:87], v[232:235], v[132:135]\n"
    "s_waitcnt lgkmcnt(8)\n"
    "v_mfma_f32_16x16x32_bf16 v[136:139], a[148:151], v[232:235], v[136:139]\n"
    "v_mfma_f32_16x16x32_bf16 v[140:143], a[212:215], v[232:235], v[140:143]\n"
    "s_add_u32 s59, s69, 16384\n"
    "buffer_load_dwordx4 a[64:67], v1, s[44:47], s59 offen offset:0 sc1\n"
    "s_barrier\n"
    "v_mfma_f32_16x16x32_bf16 v[144:147], a[20:23], v[236:239], v[144:147]\n"
    "ds_read_b64_tr_b16 v[224:225], v4 offset:49152\n"
    "ds_read_b64_tr_b16 v[226:227], v4 offset:49664\n"
    "v_mfma_f32_16x16x32_bf16 v[148:151], a[84:87], v[236:239], v[148:151]\n"
    "s_waitcnt lgkmcnt(8)\n"
    "s_add_u32 s56, s61, 0x300000\n"
    "s_add_u32 s76, s61, 0x200080\n"
    "v_mfma_f32_16x16x32_bf16 v[152:155], a[148:151], v[236:239], v[152:155]\n"
    "v_mfma_f32_16x16x32_bf16 v[156:159], a[212:215], v[236:239], v[156:159]\n"
    "s_add_u32 m0, s60, 32768\n"
    "s_nop 0\n"
    "buffer_load_dwordx4 v2, s[40:43], s56 offen lds\n"
    "v_mfma_f32_16x16x32_bf16 v[32:35], a[4:7], v[240:243], v[32:35]\n"
    "ds_read_b64_tr_b16 v[228:229], v5 offset:49152\n"
    "ds_read_b64_tr_b16 v[230:231], v5 offset:49664\n"
    "v_mfma_f32_16x16x32_bf16 v[36:39], a[68:71], v[240:243], v[36:39]\n"
    "s_waitcnt lgkmcnt(8)\n"
    "v_mfma_f32_16x16x32_bf16 v[40:43], a[132:135], v[240:243], v[40:43]\n"
    "v_mfma_f32_16x16x32_bf16 v[44:47], a[196:199], v[240:243], v[44:47]\n"
    "s_add_u32 m0, s60, 8192\n"
    "s_nop 0\n"
    "buffer_load_dwordx4 v2, s[40:43], s76 offen lds\n"
    "v_mfma_f32_16x16x32_bf16 v[48:51], a[4:7], v[244:247], v[48:51]\n"
    "ds_read_b64_tr_b16 v[232:233], v6 offset:49152\n"
    "ds_read_b64_tr_b16 v[234:235], v6 offset:49664\n"
    "v_mfma_f32_16x16x32_bf16 v[52:55], a[68:71], v[244:247], v[52:55]\n"
    "s_waitcnt lgkmcnt(8)\n"
    "v_mfma_f32_16x16x32_bf16 v[56:59], a[132:135], v[244:247], v[56:59]\n"
    "v_mfma_f32_16x16x32_bf16 v[60:63], a[196:199], v[244:247], v[60:63]\n"
    "s_add_u32 s59, s70, 0\n"
    "buffer_load_dwordx4 a[128:131], v1, s[44:47], s59 offen offset:0 sc1\n"
    "v_mfma_f32_16x16x32_bf16 v[64:67], a[4:7], v[248:251], v[64:67]\n"
    "ds_read_b64_tr_b16 v[236:237], v8 offset:49152\n"
    "ds_read_b64_tr_b16 v[238:239], v8 offset:49664\n"
    "v_mfma_f32_16x16x32_bf16 v[68:71], a[68:71], v[248:251], v[68:71]\n"
    "s_waitcnt lgkmcnt(8)\n"
    "v_mfma_f32_16x16x32_bf16 v[72:75], a[132:135], v[248:251], v[72:75]\n"
    "v_mfma_f32_16x16x32_bf16 v[76:79], a[196:199], v[248:251], v[76:79]\n"
    "s_add_u32 m0, s60, 33792\n"
    "s_nop 0\n"
    "buffer_load_dwordx4 v3, s[40:43], s56 offen lds\n"
    "v_mfma_f32_16x16x32_bf16 v[80:83], a[4:7], v[252:255], v[80:83]\n"
    "ds_read_b64_tr_b16 v[240:241], v4 offset:24576\n"
    "ds_read_b64_tr_b16 v[242:243], v4 offset:25088\n"
    "v_mfma_f32_16x16x32_bf16 v[84:87], a[68:71], v[252:255], v[84:87]\n"
    "s_waitcnt lgkmcnt(8)\n"
    "v_mfma_f32_16x16x32_bf16 v[88:91], a[132:135], v[252:255], v[88:91]\n"
    "v_mfma_f32_16x16x32_bf16 v[92:95], a[196:199], v[252:255], v[92:95]\n"
    "s_add_u32 m0, s60, 9216\n"
    "s_nop 0\n"
    "buffer_load_dwordx4 v3, s[40:43], s76 offen lds\n"
    "v_mfma_f32_16x16x32_bf16 v[96:99], a[24:27], v[224:227], v[96:99]\n"
    "ds_read_b64_tr_b16 v[244:245], v5 offset:24576\n"
    "ds_read_b64_tr_b16 v[246:247], v5 offset:25088\n"
    "v_mfma_f32_16x16x32_bf16 v[100:103], a[88:91], v[224:227], v[100:103]\n"
    "s_waitcnt lgkmcnt(8)\n"
    "v_mfma_f32_16x16x32_bf16 v[104:107], a[152:155], v[224:227], v[104:107]\n"
    "v_mfma_f32_16x16x32_bf16 v[108:111], a[216:219], v[224:227], v[108:111]\n"
    "s_add_u32 s59, s70, 16384\n"
    "buffer_load_dwordx4 a[192:195], v1, s[44:47], s59 offen offset:0 sc1\n"
    "v_mfma_f32_16x16x32_bf16 v[112:115], a[24:27], v[228:231], v[112:115]\n"
    "ds_read_b64_tr_b16 v[248:249], v6 offset:24576\n"
    "ds_read_b64_tr_b16 v[250:251], v6 offset:25088\n"
    "v_mfma_f32_16x16x32_bf16 v[116:119], a[88:91], v[228:231], v[116:119]\n"
    "s_waitcnt lgkmcnt(8)\n"
    "v_mfma_f32_16x16x32_bf16 v[120:123], a[152:155], v[228:231], v[120:123]\n"
    "v_mfma_f32_16x16x32_bf16 v[124:127], a[216:219], v[228:231], v[124:127]\n"
    "s_add_u32 s59, s69, 0\n"
    "buffer_load_dwordx4 a[4:7], v1, s[44:47], s59 offen offset:1024 sc1\n"
    "v_mfma_f32_16x16x32_bf16 v[128:131], a[24:27], v[232:235], v[128:131]\n"
    "ds_read_b64_tr_b16 v[252:253], v8 offset:24576\n"
    "ds_read_b64_tr_b16 v[254:255], v8 offset:25088\n"
    "v_mfma_f32_16x16x32_bf16 v[132:135], a[88:91], v[232:235], v[132:135]\n"
    "s_waitcnt lgkmcnt(8)\n"
    "v_mfma_f32_16x16x32_bf16 v[136:139], a[152:155], v[232:235], v[136:139]\n"
    "v_mfma_f32_16x16x32_bf16 v[140:143], a[216:219], v[232:235], v[140:143]\n"
    "s_add_u32 s59, s69, 16384\n"
    "buffer_load_dwordx4 a[68:71], v1, s[44:47], s59 offen offset:1024 sc1\n"
    "v_mfma_f32_16x16x32_bf16 v[144:147], a[24:27], v[236:239], v[144:147]\n"
    "ds_read_b64_tr_b16 v[224:225], v4 offset:53248\n"
    "ds_read_b64_tr_b16 v[226:227], v4 offset:53760\n"
    "v_mfma_f32_16x16x32_bf16 v[148:151], a[88:91], v[236:239], v[148:151]\n"
    "s_waitcnt lgkmcnt(8)\n"
    "v_mfma_f32_16x16x32_bf16 v[152:155], a[152:155], v[236:239], v[152:155]\n"
    "v_mfma_f32_16x16x32_bf16 v[156:159], a[216:219], v[236:239], v[156:159]\n"
    "s_add_u32 s58, s62, 0x1800\n"
    "buffer_store_dwordx4 v[196:199], v1, s[44:47], s58 offen sc1\n"
    "v_mfma_f32_16x16x32_bf16 v[32:35], a[8:11], v[240:243], v[32:35]\n"
    "ds_read_b64_tr_b16 v[228:229], v5 offset:53248\n"
    "ds_read_b64_tr_b16 v[230:231], v5 offset:53760\n"
    "v_mfma_f32_16x16x32_bf16 v[36:39], a[72:75], v[240:243], v[36:39]\n"
    "s_waitcnt lgkmcnt(8)\n"
    "v_mfma_f32_16x16x32_bf16 v[40:43], a[136:139], v[240:243], v[40:43]\n"
    "v_mfma_f32_16x16x32_bf16 v[44:47], a[200:203], v[240:243], v[44:47]\n"
    "v_mfma_f32_16x16x32_bf16 v[48:51], a[8:11], v[244:247], v[48:51]\n"
    "ds_read_b64_tr_b16 v[232:233], v6 offset:53248\n"
    "ds_read_b64_tr_b16 v[234:235], v6 offset:53760\n"
    "v_mfma_f32_16x16x32_bf16 v[52:55], a[72:75], v[244:247], v[52:55]\n"
    "s_waitcnt lgkmcnt(8)\n"
    "v_mfma_f32_16x16x32_bf16 v[56:59], a[136:139], v[244:247], v[56:59]\n"
    "s_add_u32 s59, s70, 0\n"
    "buffer_load_dwordx4 a[132:135], v1, s[44:47], s59 offen offset:1024 sc1\n"
    "v_mfma_f32_16x16x32_bf16 v[60:63], a[200:203], v[244:247], v[60:63]\n"
    "v_mfma_f32_16x16x32_bf16 v[64:67], a[8:11], v[248:251], v[64:67]\n"
    "ds_read_b64_tr_b16 v[236:237], v8 offset:53248\n"
    "ds_read_b64_tr_b16 v[238:239], v8 offset:53760\n"
    "v_mfma_f32_16x16x32_bf16 v[68:71], a[72:75], v[248:251], v[68:71]\n"
    "s_waitcnt lgkmcnt(8)\n"
    "v_mfma_f32_16x16x32_bf16 v[72:75], a[136:139], v[248:251], v[72:75]\n"
    "v_mfma_f32_16x16x32_bf16 v[76:79], a[200:203], v[248:251], v[76:79]\n"
    "v_mfma_f32_16x16x32_bf16 v[80:83], a[8:11], v[252:255], v[80:83]\n"
    "ds_read_b64_tr_b16 v[240:241], v4 offset:28672\n"
    "ds_read_b64_tr_b16 v[242:243], v4 offset:29184\n"
    "v_mfma_f32_16x16x32_bf16 v[84:87], a[72:75], v[252:255], v[84:87]\n"
    "s_waitcnt lgkmcnt(8)\n"
    "v_mfma_f32_16x16x32_bf16 v[88:91], a[136:139], v[252:255], v[88:91]\n"
    "v_mfma_f32_16x16x32_bf16 v[92:95], a[200:203], v[252:255], v[92:95]\n"
    "v_mfma_f32_16x16x32_bf16 v[96:99], a[28:31], v[224:227], v[96:99]\n"
    "ds_read_b64_tr_b16 v[244:245], v5 offset:28672\n"
    "ds_read_b64_tr_b16 v[246:247], v5 offset:29184\n"
    "v_mfma_f32_16x16x32_bf16 v[100:103], a[92:95], v[224:227], v[100:103]\n"
    "s_waitcnt lgkmcnt(8)\n"
    "v_mfma_f32_16x16x32_bf16 v[104:107], a[156:159], v[224:227], v[104:107]\n"
    "s_add_u32 s59, s70, 16384\n"
    "buffer_load_dwordx4 a[196:199], v1, s[44:47], s59 offen offset:1024 sc1\n"
    "v_mfma_f32_16x16x32_bf16 v[108:111], a[220:223], v[224:227], v[108:111]\n"
    "v_mfma_f32_16x16x32_bf16 v[112:115], a[28:31], v[228:231], v[112:115]\n"
    "ds_read_b64_tr_b16 v[248:249], v6 offset:28672\n"
    "ds_read_b64_tr_b16 v[250:251], v6 offset:29184\n"
    "v_mfma_f32_16x16x32_bf16 v[116:119], a[92:95], v[228:231], v[116:119]\n"
    "s_waitcnt lgkmcnt(8)\n"
    "v_mfma_f32_16x16x32_bf16 v[120:123], a[156:159], v[228:231], v[120:123]\n"
    "s_add_u32 s59, s69, 0\n"
    "buffer_load_dwordx4 a[8:11], v1, s[44:47], s59 offen offset:2048 sc1\n"
    "v_mfma_f32_16x16x32_bf16 v[124:127], a[220:223], v[228:231], v[124:127]\n"
    "v_mfma_f32_16x16x32_bf16 v[128:131], a[28:31], v[232:235], v[128:131]\n"
    "ds_read_b64_tr_b16 v[252:253], v8 offset:28672\n"
    "ds_read_b64_tr_b16 v[254:255], v8 offset:29184\n"
    "v_mfma_f32_16x16x32_bf16 v[132:135], a[92:95], v[232:235], v[132:135]\n"
    "s_waitcnt lgkmcnt(8)\n"
    "v_mfma_f32_16x16x32_bf16 v[136:139], a[156:159], v[232:235], v[136:139]\n"
    "v_mfma_f32_16x16x32_bf16 v[140:143], a[220:223], v[232:235], v[140:143]\n"
    "s_add_u32 s59, s69, 16384\n"
    "buffer_load_dwordx4 a[72:75], v1, s[44:47], s59 offen offset:2048 sc1\n"
    "s_barrier\n"
    "v_mfma_f32_16x16x32_bf16 v[144:147], a[28:31], v[236:239], v[144:147]\n"
    "ds_read_b64_tr_b16 v[224:225], v4 offset:0\n"
    "ds_read_b64_tr_b16 v[226:227], v4 offset:512\n"
    "v_mfma_f32_16x16x32_bf16 v[148:151], a[92:95], v[236:239], v[148:151]\n"
    "s_waitcnt lgkmcnt(8)\n"
    "s_add_u32 s56, s61, 0x380000\n"
    "s_add_u32 s76, s61, 0x280080\n"
    "v_mfma_f32_16x16x32_bf16 v[152:155], a[156:159], v[236:239], v[152:155]\n"
    "v_mfma_f32_16x16x32_bf16 v[156:159], a[220:223], v[236:239], v[156:159]\n"
    "s_add_u32 m0, s60, 49152\n"
    "s_nop 0\n"
    "buffer_load_dwordx4 v2, s[40:43], s56 offen lds\n"
    "v_mfma_f32_16x16x32_bf16 v[32:35], a[12:15], v[240:243], v[32:35]\n"
    "ds_read_b64_tr_b16 v[228:229], v5 offset:0\n"
    "ds_read_b64_tr_b16 v[230:231], v5 offset:512\n"
    "v_mfma_f32_16x16x32_bf16 v[36:39], a[76:79], v[240:243], v[36:39]\n"
    "s_waitcnt lgkmcnt(8)\n"
    "v_mfma_f32_16x16x32_bf16 v[40:43], a[140:143], v[240:243], v[40:43]\n"
    "v_mfma_f32_16x16x32_bf16 v[44:47], a[204:207], v[240:243], v[44:47]\n"
    "s_add_u32 m0, s60, 24576\n"
    "s_nop 0\n"
    "buffer_load_dwordx4 v2, s[40:43], s76 offen lds\n"
    "v_mfma_f32_16x16x32_bf16 v[48:51], a[12:15], v[244:247], v[48:51]\n"
    "ds_read_b64_tr_b16 v[232:233], v6 offset:0\n"
    "ds_read_b64_tr_b16 v[234:235], v6 offset:512\n"
    "v_mfma_f32_16x16x32_bf16 v[52:55], a[76:79], v[244:247], v[52:55]\n"
    "s_waitcnt lgkmcnt(8)\n"
    "v_mfma_f32_16x16x32_bf16 v[56:59], a[140:143], v[244:247], v[56:59]\n"
    "v_mfma_f32_16x16x32_bf16 v[60:63], a[204:207], v[244:247], v[60:63]\n"
    "s_add_u32 s59, s70, 0\n"
    "buffer_load_dwordx4 a[136:139], v1, s[44:47], s59 offen offset:2048 sc1\n"
    "v_mfma_f32_16x16x32_bf16 v[64:67], a[12:15], v[248:251], v[64:67]\n"
    "ds_read_b64_tr_b16 v[236:237], v8 offset:0\n"
    "ds_read_b64_tr_b16 v[238:239], v8 offset:512\n"
    "v_mfma_f32_16x16x32_bf16 v[68:71], a[76:79], v[248:251], v[68:71]\n"
    "s_waitcnt lgkmcnt(8)\n"
    "v_mfma_f32_16x16x32_bf16 v[72:75], a[140:143], v[248:251], v[72:75]\n"
    "v_mfma_f32_16x16x32_bf16 v[76:79], a[204:207], v[248:251], v[76:79]\n"
    "s_add_u32 m0, s60, 50176\n"
    "s_nop 0\n"
    "buffer_load_dwordx4 v3, s[40:43], s56 offen lds\n"
    "v_mfma_f32_16x16x32_bf16 v[80:83], a[12:15], v[252:255], v[80:83]\n"
    "ds_read_b64_tr_b16 v[240:241], v4 offset:40960\n"
    "ds_read_b64_tr_b16 v[242:243], v4 offset:41472\n"
    "v_mfma_f32_16x16x32_bf16 v[84:87], a[76:79], v[252:255], v[84:87]\n"
    "s_waitcnt lgkmcnt(8)\n"
    "v_mfma_f32_16x16x32_bf16 v[88:91], a[140:143], v[252:255], v[88:91]\n"
    "v_mfma_f32_16x16x32_bf16 v[92:95], a[204:207], v[252:255], v[92:95]\n"
    "s_add_u32 m0, s60, 25600\n"
    "s_nop 0\n"
    "buffer_load_dwordx4 v3, s[40:43], s76 offen lds\n"
    "v_mfma_f32_16x16x32_bf16 v[96:99], a[32:35], v[224:227], v[96:99]\n"
    "ds_read_b64_tr_b16 v[244:245], v5 offset:40960\n"
    "ds_read_b64_tr_b16 v[246:247], v5 offset:41472\n"
    "v_mfma_f32_16x16x32_bf16 v[100:103], a[96:99], v[224:227], v[100:103]\n"
    "s_waitcnt lgkmcnt(8)\n"
    "v_mfma_f32_16x16x32_bf16 v[104:107], a[160:163], v[224:227], v[104:107]\n"
    "v_mfma_f32_16x16x32_bf16 v[108:111], a[224:227], v[224:227], v[108:111]\n"
    "s_add_u32 s59, s70, 16384\n"
    "buffer_load_dwordx4 a[200:203], v1, s[44:47], s59 offen offset:2048 sc1\n"
    "v_mfma_f32_16x16x32_bf16 v[112:115], a[32:35], v[228:231], v[112:115]\n"
    "ds_read_b64_tr_b16 v[248:249], v6 offset:40960\n"
    "ds_read_b64_tr_b16 v[250:251], v6 offset:41472\n"
    "v_mfma_f32_16x16x32_bf16 v[116:119], a[96:99], v[228:231], v[116:119]\n"
    "s_waitcnt lgkmcnt(8)\n"
    "v_mfma_f32_16x16x32_bf16 v[120:123], a[160:163], v[228:231], v[120:123]\n"
    "v_mfma_f32_16x16x32_bf16 v[124:127], a[224:227], v[228:231], v[124:127]\n"
    "s_add_u32 s59, s69, 0\n"
    "buffer_load_dwordx4 a[12:15], v1, s[44:47], s59 offen offset:3072 sc1\n"
    "v_mfma_f32_16x16x32_bf16 v[128:131], a[32:35], v[232:235], v[128:131]\n"
    "ds_read_b64_tr_b16 v[252:253], v8 offset:40960\n"
    "ds_read_b64_tr_b16 v[254:255], v8 offset:41472\n"
    "v_mfma_f32_16x16x32_bf16 v[132:135], a[96:99], v[232:235], v[132:135]\n"
    "s_waitcnt lgkmcnt(8)\n"
    "v_mfma_f32_16x16x32_bf16 v[136:139], a[160:163], v[232:235], v[136:139]\n"
    "v_mfma_f32_16x16x32_bf16 v[140:143], a[224:227], v[232:235], v[140:143]\n"
    "s_add_u32 s59, s69, 16384\n"
    "buffer_load_dwordx4 a[76:79], v1, s[44:47], s59 offen offset:3072 sc1\n"
    "v_mfma_f32_16x16x32_bf16 v[144:147], a[32:35], v[236:239], v[144:147]\n"
    "ds_read_b64_tr_b16 v[224:225], v4 offset:4096\n"
    "ds_read_b64_tr_b16 v[226:227], v4 offset:4608\n"
    "v_mfma_f32_16x16x32_bf16 v[148:151], a[96:99], v[236:239], v[148:151]\n"
    "s_waitcnt lgkmcnt(8)\n"
    "v_mfma_f32_16x16x32_bf16 v[152:155], a[160:163], v[236:239], v[152:155]\n"
    "v_mfma_f32_16x16x32_bf16 v[156:159], a[224:227], v[236:239], v[156:159]\n"
    "s_add_u32 s58, s62, 0x2800\n"
    "buffer_store_dwordx4 v[200:203], v1, s[44:47], s58 offen sc1\n"
    "v_mfma_f32_16x16x32_bf16 v[32:35], a[16:19], v[240:243], v[32:35]\n"
    "ds_read_b64_tr_b16 v[228:229], v5 offset:4096\n"
    "ds_read_b64_tr_b16 v[230:231], v5 offset:4608\n"
    "v_mfma_f32_16x16x32_bf16 v[36:39], a[80:83], v[240:243], v[36:39]\n"
    "s_waitcnt lgkmcnt(8)\n"
    "v_mfma_f32_16x16x32_bf16 v[40:43], a[144:147], v[240:243], v[40:43]\n"
    "v_mfma_f32_16x16x32_bf16 v[44:47], a[208:211], v[240:243], v[44:47]\n"
    "v_mfma_f32_16x16x32_bf16 v[48:51], a[16:19], v[244:247], v[48:51]\n"
    "ds_read_b64_tr_b16 v[232:233], v6 offset:4096\n"
    "ds_read_b64_tr_b16 v[234:235], v6 offset:4608\n"
    "v_mfma_f32_16x16x32_bf16 v[52:55], a[80:83], v[244:247], v[52:55]\n"
    "s_waitcnt lgkmcnt(8)\n"
    "v_mfma_f32_16x16x32_bf16 v[56:59], a[144:147], v[244:247], v[56:59]\n"
    "s_add_u32 s59, s70, 0\n"
    "buffer_load_dwordx4 a[140:143], v1, s[44:47], s59 offen offset:3072 sc1\n"
    "v_mfma_f32_16x16x32_bf16 v[60:63], a[208:211], v[244:247], v[60:63]\n"
    "v_mfma_f32_16x16x32_bf16 v[64:67], a[16:19], v[248:251], v[64:67]\n"
    "ds_read_b64_tr_b16 v[236:237], v8 offset:4096\n"
    "ds_read_b64_tr_b16 v[238:239], v8 offset:4608\n"
    "v_mfma_f32_16x16x32_bf16 v[68:71], a[80:83], v[248:251], v[68:71]\n"
    "s_waitcnt lgkmcnt(8)\n"
    "v_mfma_f32_16x16x32_bf16 v[72:75], a[144:147], v[248:251], v[72:75]\n"
    "v_mfma_f32_16x16x32_bf16 v[76:79], a[208:211], v[248:251], v[76:79]\n"
    "v_mfma_f32_16x16x32_bf16 v[80:83], a[16:19], v[252:255], v[80:83]\n"
    "ds_read_b64_tr_b16 v[240:241], v4 offset:45056\n"
    "ds_read_b64_tr_b16 v[242:243], v4 offset:45568\n"
    "v_mfma_f32_16x16x32_bf16 v[84:87], a[80:83], v[252:255], v[84:87]\n"
    "s_waitcnt lgkmcnt(8)\n"
    "v_mfma_f32_16x16x32_bf16 v[88:91], a[144:147], v[252:255], v[88:91]\n"
    "v_mfma_f32_16x16x32_bf16 v[92:95], a[208:211], v[252:255], v[92:95]\n"
    "v_mfma_f32_16x16x32_bf16 v[96:99], a[36:39], v[224:227], v[96:99]\n"
    "ds_read_b64_tr_b16 v[244:245], v5 offset:45056\n"
    "ds_read_b64_tr_b16 v[246:247], v5 offset:45568\n"
    "v_mfma_f32_16x16x32_bf16 v[100:103], a[100:103], v[224:227], v[100:103]\n"
    "s_waitcnt lgkmcnt(8)\n"
    "v_mfma_f32_16x16x32_bf16 v[104:107], a[164:167], v[224:227], v[104:107]\n"
    "s_add_u32 s59, s70, 16384\n"
    "buffer_load_dwordx4 a[204:207], v1, s[44:47], s59 offen offset:3072 sc1\n"
    "v_mfma_f32_16x16x32_bf16 v[108:111], a[228:231], v[224:227], v[108:111]\n"
    "v_mfma_f32_16x16x32_bf16 v[112:115], a[36:39], v[228:231], v[112:115]\n"
    "ds_read_b64_tr_b16 v[248:249], v6 offset:45056\n"
    "ds_read_b64_tr_b16 v[250:251], v6 offset:45568\n"
    "v_mfma_f32_16x16x32_bf16 v[116:119], a[100:103], v[228:231], v[116:119]\n"
    "s_waitcnt lgkmcnt(8)\n"
    "v_mfma_f32_16x16x32_bf16 v[120:123], a[164:167], v[228:231], v[120:123]\n"
    "s_add_u32 s59, s69, 4096\n"
    "buffer_load_dwordx4 a[16:19], v1, s[44:47], s59 offen offset:0 sc1\n"
    "v_mfma_f32_16x16x32_bf16 v[124:127], a[228:231], v[228:231], v[124:127]\n"
    "v_mfma_f32_16x16x32_bf16 v[128:131], a[36:39], v[232:235], v[128:131]\n"
    "ds_read_b64_tr_b16 v[252:253], v8 offset:45056\n"
    "ds_read_b64_tr_b16 v[254:255], v8 offset:45568\n"
    "v_mfma_f32_16x16x32_bf16 v[132:135], a[100:103], v[232:235], v[132:135]\n"
    "s_waitcnt lgkmcnt(8)\n"
    "v_mfma_f32_16x16x32_bf16 v[136:139], a[164:167], v[232:235], v[136:139]\n"
    "s_add_u32 s58, s62, 0x3800\n"
    "buffer_store_dwordx4 v[204:207], v1, s[44:47], s58 offen sc1\n"
    "v_mfma_f32_16x16x32_bf16 v[140:143], a[228:231], v[232:235], v[140:143]\n"
    "s_waitcnt vmcnt(30)\n"
    "s_waitcnt vmcnt(29)\n"
    "s_barrier\n"
    "v_mfma_f32_16x16x32_bf16 v[144:147], a[36:39], v[236:239], v[144:147]\n"
    "ds_read_b64_tr_b16 v[224:225], v4 offset:16384\n"
    "ds_read_b64_tr_b16 v[226:227], v4 offset:16896\n"
    "v_mfma_f32_16x16x32_bf16 v[148:151], a[100:103], v[236:239], v[148:151]\n"
    "s_waitcnt lgkmcnt(8)\n"
    "s_add_u32 s76, s61, 0x300080\n"
    "v_mfma_f32_16x16x32_bf16 v[152:155], a[164:167], v[236:239], v[152:155]\n"
    "s_add_u32 s59, s69, 20480\n"
    "buffer_load_dwordx4 a[80:83], v1, s[44:47], s59 offen offset:0 sc1\n"
    "v_mfma_f32_16x16x32_bf16 v[156:159], a[228:231], v[236:239], v[156:159]\n"
    "v_mfma_f32_16x16x32_bf16 v[32:35], a[20:23], v[240:243], v[32:35]\n"
    "ds_read_b64_tr_b16 v[228:229], v5 offset:16384\n"
    "ds_read_b64_tr_b16 v[230:231], v5 offset:16896\n"
    "v_mfma_f32_16x16x32_bf16 v[36:39], a[84:87], v[240:243], v[36:39]\n"
    "s_waitcnt lgkmcnt(8)\n"
    "v_mfma_f32_16x16x32_bf16 v[40:43], a[148:151], v[240:243], v[40:43]\n"
    "s_add_u32 m0, s60, 40960\n"
    "s_nop 0\n"
    "buffer_load_dwordx4 v2, s[40:43], s76 offen lds\n"
    "v_mfma_f32_16x16x32_bf16 v[44:47], a[212:215], v[240:243], v[44:47]\n"
    "v_mfma_f32_16x16x32_bf16 v[48:51], a[20:23], v[244:247], v[48:51]\n"
    "ds_read_b64_tr_b16 v[232:233], v6 offset:16384\n"
    "ds_read_b64_tr_b16 v[234:235], v6 offset:16896\n"
    "v_mfma_f32_16x16x32_bf16 v[52:55], a[84:87], v[244:247], v[52:55]\n"
    "s_waitcnt lgkmcnt(8)\n"
    "v_mfma_f32_16x16x32_bf16 v[56:59], a[148:151], v[244:247], v[56:59]\n"
    "s_add_u32 s59, s70, 4096\n"
    "buffer_load_dwordx4 a[144:147], v1, s[44:47], s59 offen offset:0 sc1\n"
    "v_mfma_f32_16x16x32_bf16 v[60:63], a[212:215], v[244:247], v[60:63]\n"
    "v_mfma_f32_16x16x32_bf16 v[64:67], a[20:23], v[248:251], v[64:67]\n"
    "ds_read_b64_tr_b16 v[236:237], v8 offset:16384\n"
    "ds_read_b64_tr_b16 v[238:239], v8 offset:16896\n"
    "v_mfma_f32_16x16x32_bf16 v[68:71], a[84:87], v[248:251], v[68:71]\n"
    "s_waitcnt lgkmcnt(8)\n"
    "v_mfma_f32_16x16x32_bf16 v[72:75], a[148:151], v[248:251], v[72:75]\n"
    "s_add_u32 m0, s60, 41984\n"
    "s_nop 0\n"
    "buffer_load_dwordx4 v3, s[40:43], s76 offen lds\n"
    "v_mfma_f32_16x16x32_bf16 v[76:79], a[212:215], v[248:251], v[76:79]\n"
    "v_mfma_f32_16x16x32_bf16 v[80:83], a[20:23], v[252:255], v[80:83]\n"
    "ds_read_b64_tr_b16 v[240:241], v4 offset:57344\n"
    "ds_read_b64_tr_b16 v[242:243], v4 offset:57856\n"
    "v_mfma_f32_16x16x32_bf16 v[84:87], a[84:87], v[252:255], v[84:87]\n"
    "s_waitcnt lgkmcnt(8)\n"
    "v_mfma_f32_16x16x32_bf16 v[88:91], a[148:151], v[252:255], v[88:91]\n"
    "v_mfma_f32_16x16x32_bf16 v[92:95], a[212:215], v[252:255], v[92:95]\n"
    "v_mfma_f32_16x16x32_bf16 v[96:99], a[40:43], v[224:227], v[96:99]\n"
    "ds_read_b64_tr_b16 v[244:245], v5 offset:57344\n"
    "ds_read_b64_tr_b16 v[246:247], v5 offset:57856\n"
    "v_mfma_f32_16x16x32_bf16 v[100:103], a[104:107], v[224:227], v[100:103]\n"
    "s_waitcnt lgkmcnt(8)\n"
    "v_mfma_f32_16x16x32_bf16 v[104:107], a[168:171], v[224:227], v[104:107]\n"
    "s_add_u32 s59, s70, 20480\n"
    "buffer_load_dwordx4 a[208:211], v1, s[44:47], s59 offen offset:0 sc1\n"
    "v_mfma_f32_16x16x32_bf16 v[108:111], a[232:235], v[224:227], v[108:111]\n"
    "v_mfma_f32_16x16x32_bf16 v[112:115], a[40:43], v[228:231], v[112:115]\n"
    "ds_read_b64_tr_b16 v[248:249], v6 offset:57344\n"
    "ds_read_b64_tr_b16 v[250:251], v6 offset:57856\n"
    "v_mfma_f32_16x16x32_bf16 v[116:119], a[104:107], v[228:231], v[116:119]\n"
    "s_waitcnt lgkmcnt(8)\n"
    "v_mfma_f32_16x16x32_bf16 v[120:123], a[168:171], v[228:231], v[120:123]\n"
    "s_add_u32 s59, s69, 4096\n"
    "buffer_load_dwordx4 a[20:23], v1, s[44:47], s59 offen offset:1024 sc1\n"
    "v_mfma_f32_16x16x32_bf16 v[124:127], a[232:235], v[228:231], v[124:127]\n"
    "v_mfma_f32_16x16x32_bf16 v[128:131], a[40:43], v[232:235], v[128:131]\n"
    "ds_read_b64_tr_b16 v[252:253], v8 offset:57344\n"
    "ds_read_b64_tr_b16 v[254:255], v8 offset:57856\n"
    "v_mfma_f32_16x16x32_bf16 v[132:135], a[104:107], v[232:235], v[132:135]\n"
    "s_waitcnt lgkmcnt(8)\n"
    "v_mfma_f32_16x16x32_bf16 v[136:139], a[168:171], v[232:235], v[136:139]\n"
    "s_add_u32 s81, s88, 0\n"
    "buffer_load_dword v31, v15, s[36:39], s81 offen\n"
    "v_mfma_f32_16x16x32_bf16 v[140:143], a[232:235], v[232:235], v[140:143]\n"
    "v_mfma_f32_16x16x32_bf16 v[144:147], a[40:43], v[236:239], v[144:147]\n"
    "ds_read_b64_tr_b16 v[224:225], v4 offset:20480\n"
    "ds_read_b64_tr_b16 v[226:227], v4 offset:20992\n"
    "v_mfma_f32_16x16x32_bf16 v[148:151], a[104:107], v[236:239], v[148:151]\n"
    "s_waitcnt lgkmcnt(8)\n"
    "v_mfma_f32_16x16x32_bf16 v[152:155], a[168:171], v[236:239], v[152:155]\n"
    "s_add_u32 s59, s69, 20480\n"
    "buffer_load_dwordx4 a[84:87], v1, s[44:47], s59 offen offset:1024 sc1\n"
    "v_mfma_f32_16x16x32_bf16 v[156:159], a[232:235], v[236:239], v[156:159]\n"
    "v_mfma_f32_16x16x32_bf16 v[32:35], a[24:27], v[240:243], v[32:35]\n"
    "ds_read_b64_tr_b16 v[228:229], v5 offset:20480\n"
    "ds_read_b64_tr_b16 v[230:231], v5 offset:20992\n"
    "v_mfma_f32_16x16x32_bf16 v[36:39], a[88:91], v[240:243], v[36:39]\n"
    "s_waitcnt lgkmcnt(8)\n"
    "v_mfma_f32_16x16x32_bf16 v[40:43], a[152:155], v[240:243], v[40:43]\n"
    "s_add_u32 s58, s62, 0xc00\n"
    "buffer_store_dwordx4 v[208:211], v1, s[44:47], s58 offen sc1\n"
    "v_mfma_f32_16x16x32_bf16 v[44:47], a[216:219], v[240:243], v[44:47]\n"
    "v_mfma_f32_16x16x32_bf16 v[48:51], a[24:27], v[244:247], v[48:51]\n"
    "ds_read_b64_tr_b16 v[232:233], v6 offset:20480\n"
    "ds_read_b64_tr_b16 v[234:235], v6 offset:20992\n"
    "v_mfma_f32_16x16x32_bf16 v[52:55], a[88:91], v[244:247], v[52:55]\n"
    "s_waitcnt lgkmcnt(8)\n"
    "v_mfma_f32_16x16x32_bf16 v[56:59], a[152:155], v[244:247], v[56:59]\n"
    "s_add_u32 s59, s70, 4096\n"
    "buffer_load_dwordx4 a[148:151], v1, s[44:47], s59 offen offset:1024 sc1\n"
    "v_mfma_f32_16x16x32_bf16 v[60:63], a[216:219], v[244:247], v[60:63]\n"
    "v_mfma_f32_16x16x32_bf16 v[64:67], a[24:27], v[248:251], v[64:67]\n"
    "ds_read_b64_tr_b16 v[236:237], v8 offset:20480\n"
    "ds_read_b64_tr_b16 v[238:239], v8 offset:20992\n"
    "v_mfma_f32_16x16x32_bf16 v[68:71], a[88:91], v[248:251], v[68:71]\n"
    "s_waitcnt lgkmcnt(8)\n"
    "v_mfma_f32_16x16x32_bf16 v[72:75], a[152:155], v[248:251], v[72:75]\n"
    "v_mfma_f32_16x16x32_bf16 v[76:79], a[216:219], v[248:251], v[76:79]\n"
    "v_mfma_f32_16x16x32_bf16 v[80:83], a[24:27], v[252:255], v[80:83]\n"
    "ds_read_b64_tr_b16 v[240:241], v4 offset:61440\n"
    "ds_read_b64_tr_b16 v[242:243], v4 offset:61952\n"
    "v_mfma_f32_16x16x32_bf16 v[84:87], a[88:91], v[252:255], v[84:87]\n"
    "s_waitcnt lgkmcnt(8)\n"
    "v_mfma_f32_16x16x32_bf16 v[88:91], a[152:155], v[252:255], v[88:91]\n"
    "v_mfma_f32_16x16x32_bf16 v[92:95], a[216:219], v[252:255], v[92:95]\n"
    "v_mfma_f32_16x16x32_bf16 v[96:99], a[44:47], v[224:227], v[96:99]\n"
    "ds_read_b64_tr_b16 v[244:245], v5 offset:61440\n"
    "ds_read_b64_tr_b16 v[246:247], v5 offset:61952\n"
    "v_mfma_f32_16x16x32_bf16 v[100:103], a[108:111], v[224:227], v[100:103]\n"
    "s_waitcnt lgkmcnt(8)\n"
    "v_mfma_f32_16x16x32_bf16 v[104:107], a[172:175], v[224:227], v[104:107]\n"
    "s_add_u32 s59, s70, 20480\n"
    "buffer_load_dwordx4 a[212:215], v1, s[44:47], s59 offen offset:1024 sc1\n"
    "v_mfma_f32_16x16x32_bf16 v[108:111], a[236:239], v[224:227], v[108:111]\n"
    "v_mfma_f32_16x16x32_bf16 v[112:115], a[44:47], v[228:231], v[112:115]\n"
    "ds_read_b64_tr_b16 v[248:249], v6 offset:61440\n"
    "ds_read_b64_tr_b16 v[250:251], v6 offset:61952\n"
    "v_mfma_f32_16x16x32_bf16 v[116:119], a[108:111], v[228:231], v[116:119]\n"
    "s_waitcnt lgkmcnt(8)\n"
    "v_mfma_f32_16x16x32_bf16 v[120:123], a[172:175], v[228:231], v[120:123]\n"
    "s_add_u32 s59, s69, 4096\n"
    "buffer_load_dwordx4 a[24:27], v1, s[44:47], s59 offen offset:2048 sc1\n"
    "v_mfma_f32_16x16x32_bf16 v[124:127], a[236:239], v[228:231], v[124:127]\n"
    "v_mfma_f32_16x16x32_bf16 v[128:131], a[44:47], v[232:235], v[128:131]\n"
    "ds_read_b64_tr_b16 v[252:253], v8 offset:61440\n"
    "ds_read_b64_tr_b16 v[254:255], v8 offset:61952\n"
    "v_mfma_f32_16x16x32_bf16 v[132:135], a[108:111], v[232:235], v[132:135]\n"
    "s_waitcnt lgkmcnt(8)\n"
    "v_mfma_f32_16x16x32_bf16 v[136:139], a[172:175], v[232:235], v[136:139]\n"
    "v_mfma_f32_16x16x32_bf16 v[140:143], a[236:239], v[232:235], v[140:143]\n"
    "s_add_u32 s59, s69, 20480\n"
    "buffer_load_dwordx4 a[88:91], v1, s[44:47], s59 offen offset:2048 sc1\n"
    "s_waitcnt vmcnt(35)\n"
    "s_waitcnt vmcnt(34)\n"
    "s_barrier\n"
    "v_mfma_f32_16x16x32_bf16 v[144:147], a[44:47], v[236:239], v[144:147]\n"
    "ds_read_b64_tr_b16 v[224:225], v4 offset:32768\n"
    "ds_read_b64_tr_b16 v[226:227], v4 offset:33280\n"
    "v_mfma_f32_16x16x32_bf16 v[148:151], a[108:111], v[236:239], v[148:151]\n"
    "s_waitcnt lgkmcnt(8)\n"
    "s_add_u32 s76, s61, 0x380080\n"
    "v_mfma_f32_16x16x32_bf16 v[152:155], a[172:175], v[236:239], v[152:155]\n"
    "v_mfma_f32_16x16x32_bf16 v[156:159], a[236:239], v[236:239], v[156:159]\n"
    "s_add_u32 m0, s60, 57344\n"
    "s_nop 0\n"
    "buffer_load_dwordx4 v2, s[40:43], s76 offen lds\n"
    "v_mfma_f32_16x16x32_bf16 v[32:35], a[28:31], v[240:243], v[32:35]\n"
    "ds_read_b64_tr_b16 v[228:229], v5 offset:32768\n"
    "ds_read_b64_tr_b16 v[230:231], v5 offset:33280\n"
    "v_mfma_f32_16x16x32_bf16 v[36:39], a[92:95], v[240:243], v[36:39]\n"
    "s_waitcnt lgkmcnt(8)\n"
    "v_mfma_f32_16x16x32_bf16 v[40:43], a[156:159], v[240:243], v[40:43]\n"
    "v_mfma_f32_16x16x32_bf16 v[44:47], a[220:223], v[240:243], v[44:47]\n"
    "s_add_u32 m0, s60, 58368\n"
    "s_nop 0\n"
    "buffer_load_dwordx4 v3, s[40:43], s76 offen lds\n"
    "v_mfma_f32_16x16x32_bf16 v[48:51], a[28:31], v[244:247], v[48:51]\n"
    "ds_read_b64_tr_b16 v[232:233], v6 offset:32768\n"
    "ds_read_b64_tr_b16 v[234:235], v6 offset:33280\n"
    "v_mfma_f32_16x16x32_bf16 v[52:55], a[92:95], v[244:247], v[52:55]\n"
    "s_waitcnt lgkmcnt(8)\n"
    "v_mfma_f32_16x16x32_bf16 v[56:59], a[156:159], v[244:247], v[56:59]\n"
    "v_mfma_f32_16x16x32_bf16 v[60:63], a[220:223], v[244:247], v[60:63]\n"
    "s_add_u32 s59, s70, 4096\n"
    "buffer_load_dwordx4 a[152:155], v1, s[44:47], s59 offen offset:2048 sc1\n"
    "v_mfma_f32_16x16x32_bf16 v[64:67], a[28:31], v[248:251], v[64:67]\n"
    "ds_read_b64_tr_b16 v[236:237], v8 offset:32768\n"
    "ds_read_b64_tr_b16 v[238:239], v8 offset:33280\n"
    "v_mfma_f32_16x16x32_bf16 v[68:71], a[92:95], v[248:251], v[68:71]\n"
    "s_waitcnt lgkmcnt(8)\n"
    "v_mfma_f32_16x16x32_bf16 v[72:75], a[156:159], v[248:251], v[72:75]\n"
    "v_mfma_f32_16x16x32_bf16 v[76:79], a[220:223], v[248:251], v[76:79]\n"
    "s_add_u32 s58, s62, 0x1c00\n"
    "buffer_store_dwordx4 v[212:215], v1, s[44:47], s58 offen sc1\n"
    "v_mfma_f32_16x16x32_bf16 v[80:83], a[28:31], v[252:255], v[80:83]\n"
    "ds_read_b64_tr_b16 v[240:241], v4 offset:8192\n"
    "ds_read_b64_tr_b16 v[242:243], v4 offset:8704\n"
    "v_mfma_f32_16x16x32_bf16 v[84:87], a[92:95], v[252:255], v[84:87]\n"
    "s_waitcnt lgkmcnt(8)\n"
    "v_mfma_f32_16x16x32_bf16 v[88:91], a[156:159], v[252:255], v[88:91]\n"
    "v_mfma_f32_16x16x32_bf16 v[92:95], a[220:223], v[252:255], v[92:95]\n"
    "v_mfma_f32_16x16x32_bf16 v[96:99], a[48:51], v[224:227], v[96:99]\n"
    "ds_read_b64_tr_b16 v[244:245], v5 offset:8192\n"
    "ds_read_b64_tr_b16 v[246:247], v5 offset:8704\n"
    "v_mfma_f32_16x16x32_bf16 v[100:103], a[112:115], v[224:227], v[100:103]\n"
    "s_waitcnt lgkmcnt(8)\n"
    "v_mfma_f32_16x16x32_bf16 v[104:107], a[176:179], v[224:227], v[104:107]\n"
    "s_add_u32 s59, s70, 20480\n"
    "buffer_load_dwordx4 a[216:219], v1, s[44:47], s59 offen offset:2048 sc1\n"
    "v_mfma_f32_16x16x32_bf16 v[108:111], a[240:243], v[224:227], v[108:111]\n"
    "v_mfma_f32_16x16x32_bf16 v[112:115], a[48:51], v[228:231], v[112:115]\n"
    "ds_read_b64_tr_b16 v[248:249], v6 offset:8192\n"
    "ds_read_b64_tr_b16 v[250:251], v6 offset:8704\n"
    "v_mfma_f32_16x16x32_bf16 v[116:119], a[112:115], v[228:231], v[116:119]\n"
    "s_waitcnt lgkmcnt(8)\n"
    "v_mfma_f32_16x16x32_bf16 v[120:123], a[176:179], v[228:231], v[120:123]\n"
    "s_add_u32 s59, s69, 4096\n"
    "buffer_load_dwordx4 a[28:31], v1, s[44:47], s59 offen offset:3072 sc1\n"
    "v_mfma_f32_16x16x32_bf16 v[124:127], a[240:243], v[228:231], v[124:127]\n"
    "v_mfma_f32_16x16x32_bf16 v[128:131], a[48:51], v[232:235], v[128:131]\n"
    "ds_read_b64_tr_b16 v[252:253], v8 offset:8192\n"
    "ds_read_b64_tr_b16 v[254:255], v8 offset:8704\n"
    "v_mfma_f32_16x16x32_bf16 v[132:135], a[112:115], v[232:235], v[132:135]\n"
    "s_waitcnt lgkmcnt(8)\n"
    "v_mfma_f32_16x16x32_bf16 v[136:139], a[176:179], v[232:235], v[136:139]\n"
    "v_mfma_f32_16x16x32_bf16 v[140:143], a[240:243], v[232:235], v[140:143]\n"
    "s_add_u32 s59, s69, 20480\n"
    "buffer_load_dwordx4 a[92:95], v1, s[44:47], s59 offen offset:3072 sc1\n"
    "v_mfma_f32_16x16x32_bf16 v[144:147], a[48:51], v[236:239], v[144:147]\n"
    "ds_read_b64_tr_b16 v[224:225], v4 offset:36864\n"
    "ds_read_b64_tr_b16 v[226:227], v4 offset:37376\n"
    "v_mfma_f32_16x16x32_bf16 v[148:151], a[112:115], v[236:239], v[148:151]\n"
    "s_waitcnt lgkmcnt(8)\n"
    "v_mfma_f32_16x16x32_bf16 v[152:155], a[176:179], v[236:239], v[152:155]\n"
    "v_mfma_f32_16x16x32_bf16 v[156:159], a[240:243], v[236:239], v[156:159]\n"
    "v_mfma_f32_16x16x32_bf16 v[32:35], a[32:35], v[240:243], v[32:35]\n"
    "ds_read_b64_tr_b16 v[228:229], v5 offset:36864\n"
    "ds_read_b64_tr_b16 v[230:231], v5 offset:37376\n"
    "v_mfma_f32_16x16x32_bf16 v[36:39], a[96:99], v[240:243], v[36:39]\n"
    "s_waitcnt lgkmcnt(8)\n"
    "v_mfma_f32_16x16x32_bf16 v[40:43], a[160:163], v[240:243], v[40:43]\n"
    "v_mfma_f32_16x16x32_bf16 v[44:47], a[224:227], v[240:243], v[44:47]\n"
    "v_mfma_f32_16x16x32_bf16 v[48:51], a[32:35], v[244:247], v[48:51]\n"
    "ds_read_b64_tr_b16 v[232:233], v6 offset:36864\n"
    "ds_read_b64_tr_b16 v[234:235], v6 offset:37376\n"
    "v_mfma_f32_16x16x32_bf16 v[52:55], a[96:99], v[244:247], v[52:55]\n"
    "s_waitcnt lgkmcnt(8)\n"
    "v_mfma_f32_16x16x32_bf16 v[56:59], a[160:163], v[244:247], v[56:59]\n"
    "s_add_u32 s59, s70, 4096\n"
    "buffer_load_dwordx4 a[156:159], v1, s[44:47], s59 offen offset:3072 sc1\n"
    "v_mfma_f32_16x16x32_bf16 v[60:63], a[224:227], v[244:247], v[60:63]\n"
    "v_mfma_f32_16x16x32_bf16 v[64:67], a[32:35], v[248:251], v[64:67]\n"
    "ds_read_b64_tr_b16 v[236:237], v8 offset:36864\n"
    "ds_read_b64_tr_b16 v[238:239], v8 offset:37376\n"
    "v_mfma_f32_16x16x32_bf16 v[68:71], a[96:99], v[248:251], v[68:71]\n"
    "s_waitcnt lgkmcnt(8)\n"
    "v_mfma_f32_16x16x32_bf16 v[72:75], a[160:163], v[248:251], v[72:75]\n"
    "v_mfma_f32_16x16x32_bf16 v[76:79], a[224:227], v[248:251], v[76:79]\n"
    "v_mfma_f32_16x16x32_bf16 v[80:83], a[32:35], v[252:255], v[80:83]\n"
    "ds_read_b64_tr_b16 v[240:241], v4 offset:12288\n"
    "ds_read_b64_tr_b16 v[242:243], v4 offset:12800\n"
    "v_mfma_f32_16x16x32_bf16 v[84:87], a[96:99], v[252:255], v[84:87]\n"
    "s_waitcnt lgkmcnt(8)\n"
    "v_mfma_f32_16x16x32_bf16 v[88:91], a[160:163], v[252:255], v[88:91]\n"
    "v_mfma_f32_16x16x32_bf16 v[92:95], a[224:227], v[252:255], v[92:95]\n"
    "v_mfma_f32_16x16x32_bf16 v[96:99], a[52:55], v[224:227], v[96:99]\n"
    "ds_read_b64_tr_b16 v[244:245], v5 offset:12288\n"
    "ds_read_b64_tr_b16 v[246:247], v5 offset:12800\n"
    "v_mfma_f32_16x16x32_bf16 v[100:103], a[116:119], v[224:227], v[100:103]\n"
    "s_waitcnt lgkmcnt(8)\n"
    "v_mfma_f32_16x16x32_bf16 v[104:107], a[180:183], v[224:227], v[104:107]\n"
    "s_add_u32 s59, s70, 20480\n"
    "buffer_load_dwordx4 a[220:223], v1, s[44:47], s59 offen offset:3072 sc1\n"
    "v_mfma_f32_16x16x32_bf16 v[108:111], a[244:247], v[224:227], v[108:111]\n"
    "v_mfma_f32_16x16x32_bf16 v[112:115], a[52:55], v[228:231], v[112:115]\n"
    "ds_read_b64_tr_b16 v[248:249], v6 offset:12288\n"
    "ds_read_b64_tr_b16 v[250:251], v6 offset:12800\n"
    "v_mfma_f32_16x16x32_bf16 v[116:119], a[116:119], v[228:231], v[116:119]\n"
    "s_waitcnt lgkmcnt(8)\n"
    "v_mfma_f32_16x16x32_bf16 v[120:123], a[180:183], v[228:231], v[120:123]\n"
    "s_add_u32 s59, s69, 8192\n"
    "buffer_load_dwordx4 a[32:35], v1, s[44:47], s59 offen offset:0 sc1\n"
    "v_mfma_f32_16x16x32_bf16 v[124:127], a[244:247], v[228:231], v[124:127]\n"
    "v_mfma_f32_16x16x32_bf16 v[128:131], a[52:55], v[232:235], v[128:131]\n"
    "ds_read_b64_tr_b16 v[252:253], v8 offset:12288\n"
    "ds_read_b64_tr_b16 v[254:255], v8 offset:12800\n"
    "v_mfma_f32_16x16x32_bf16 v[132:135], a[116:119], v[232:235], v[132:135]\n"
    "s_waitcnt lgkmcnt(8)\n"
    "v_mfma_f32_16x16x32_bf16 v[136:139], a[180:183], v[232:235], v[136:139]\n"
    "s_add_u32 s58, s62, 0x2c00\n"
    "buffer_store_dwordx4 v[216:219], v1, s[44:47], s58 offen sc1\n"
    "v_mfma_f32_16x16x32_bf16 v[140:143], a[244:247], v[232:235], v[140:143]\n"
    "s_waitcnt vmcnt(33)\n"
    "s_waitcnt vmcnt(32)\n"
    "s_barrier\n"
    "v_mfma_f32_16x16x32_bf16 v[144:147], a[52:55], v[236:239], v[144:147]\n"
    "ds_read_b64_tr_b16 v[224:225], v4 offset:49152\n"
    "ds_read_b64_tr_b16 v[226:227], v4 offset:49664\n"
    "v_mfma_f32_16x16x32_bf16 v[148:151], a[116:119], v[236:239], v[148:151]\n"
    "s_waitcnt lgkmcnt(8)\n"
    "v_mfma_f32_16x16x32_bf16 v[152:155], a[180:183], v[236:239], v[152:155]\n"
    "s_add_u32 s59, s69, 24576\n"
    "buffer_load_dwordx4 a[96:99], v1, s[44:47], s59 offen offset:0 sc1\n"
    "v_mfma_f32_16x16x32_bf16 v[156:159], a[244:247], v[236:239], v[156:159]\n"
    "v_mfma_f32_16x16x32_bf16 v[32:35], a[36:39], v[240:243], v[32:35]\n"
    "ds_read_b64_tr_b16 v[228:229], v5 offset:49152\n"
    "ds_read_b64_tr_b16 v[230:231], v5 offset:49664\n"
    "v_mfma_f32_16x16x32_bf16 v[36:39], a[100:103], v[240:243], v[36:39]\n"
    "s_waitcnt lgkmcnt(8)\n"
    "v_mfma_f32_16x16x32_bf16 v[40:43], a[164:167], v[240:243], v[40:43]\n"
    "s_add_u32 s81, s88, 65536\n"
    "buffer_load_dword v31, v15, s[36:39], s81 offen\n"
    "v_mfma_f32_16x16x32_bf16 v[44:47], a[228:231], v[240:243], v[44:47]\n"
    "v_mfma_f32_16x16x32_bf16 v[48:51], a[36:39], v[244:247], v[48:51]\n"
    "ds_read_b64_tr_b16 v[232:233], v6 offset:49152\n"
    "ds_read_b64_tr_b16 v[234:235], v6 offset:49664\n"
    "v_mfma_f32_16x16x32_bf16 v[52:55], a[100:103], v[244:247], v[52:55]\n"
    "s_waitcnt lgkmcnt(8)\n"
    "v_mfma_f32_16x16x32_bf16 v[56:59], a[164:167], v[244:247], v[56:59]\n"
    "s_add_u32 s59, s70, 8192\n"
    "buffer_load_dwordx4 a[160:163], v1, s[44:47], s59 offen offset:0 sc1\n"
    "v_mfma_f32_16x16x32_bf16 v[60:63], a[228:231], v[244:247], v[60:63]\n"
    "v_mfma_f32_16x16x32_bf16 v[64:67], a[36:39], v[248:251], v[64:67]\n"
    "ds_read_b64_tr_b16 v[236:237], v8 offset:49152\n"
    "ds_read_b64_tr_b16 v[238:239], v8 offset:49664\n"
    "v_mfma_f32_16x16x32_bf16 v[68:71], a[100:103], v[248:251], v[68:71]\n"
    "s_waitcnt lgkmcnt(8)\n"
    "v_mfma_f32_16x16x32_bf16 v[72:75], a[164:167], v[248:251], v[72:75]\n"
    "v_mfma_f32_16x16x32_bf16 v[76:79], a[228:231], v[248:251], v[76:79]\n"
    "v_mfma_f32_16x16x32_bf16 v[80:83], a[36:39], v[252:255], v[80:83]\n"
    "ds_read_b64_tr_b16 v[240:241], v4 offset:24576\n"
    "ds_read_b64_tr_b16 v[242:243], v4 offset:25088\n"
    "v_mfma_f32_16x16x32_bf16 v[84:87], a[100:103], v[252:255], v[84:87]\n"
    "s_waitcnt lgkmcnt(8)\n"
    "v_mfma_f32_16x16x32_bf16 v[88:91], a[164:167], v[252:255], v[88:91]\n"
    "v_mfma_f32_16x16x32_bf16 v[92:95], a[228:231], v[252:255], v[92:95]\n"
    "v_mfma_f32_16x16x32_bf16 v[96:99], a[56:59], v[224:227], v[96:99]\n"
    "ds_read_b64_tr_b16 v[244:245], v5 offset:24576\n"
    "ds_read_b64_tr_b16 v[246:247], v5 offset:25088\n"
    "v_mfma_f32_16x16x32_bf16 v[100:103], a[120:123], v[224:227], v[100:103]\n"
    "s_waitcnt lgkmcnt(8)\n"
    "v_mfma_f32_16x16x32_bf16 v[104:107], a[184:187], v[224:227], v[104:107]\n"
    "s_add_u32 s59, s70, 24576\n"
    "buffer_load_dwordx4 a[224:227], v1, s[44:47], s59 offen offset:0 sc1\n"
    "v_mfma_f32_16x16x32_bf16 v[108:111], a[248:251], v[224:227], v[108:111]\n"
    "v_mfma_f32_16x16x32_bf16 v[112:115], a[56:59], v[228:231], v[112:115]\n"
    "ds_read_b64_tr_b16 v[248:249], v6 offset:24576\n"
    "ds_read_b64_tr_b16 v[250:251], v6 offset:25088\n"
    "v_mfma_f32_16x16x32_bf16 v[116:119], a[120:123], v[228:231], v[116:119]\n"
    "s_waitcnt lgkmcnt(8)\n"
    "v_mfma_f32_16x16x32_bf16 v[120:123], a[184:187], v[228:231], v[120:123]\n"
    "s_add_u32 s59, s69, 8192\n"
    "buffer_load_dwordx4 a[36:39], v1, s[44:47], s59 offen offset:1024 sc1\n"
    "v_mfma_f32_16x16x32_bf16 v[124:127], a[248:251], v[228:231], v[124:127]\n"
    "v_mfma_f32_16x16x32_bf16 v[128:131], a[56:59], v[232:235], v[128:131]\n"
    "ds_read_b64_tr_b16 v[252:253], v8 offset:24576\n"
    "ds_read_b64_tr_b16 v[254:255], v8 offset:25088\n"
    "v_mfma_f32_16x16x32_bf16 v[132:135], a[120:123], v[232:235], v[132:135]\n"
    "s_waitcnt lgkmcnt(8)\n"
    "v_mfma_f32_16x16x32_bf16 v[136:139], a[184:187], v[232:235], v[136:139]\n"
    "v_mfma_f32_16x16x32_bf16 v[140:143], a[248:251], v[232:235], v[140:143]\n"
    "s_add_u32 s59, s69, 24576\n"
    "buffer_load_dwordx4 a[100:103], v1, s[44:47], s59 offen offset:1024 sc1\n"
    "v_mfma_f32_16x16x32_bf16 v[144:147], a[56:59], v[236:239], v[144:147]\n"
    "ds_read_b64_tr_b16 v[224:225], v4 offset:53248\n"
    "ds_read_b64_tr_b16 v[226:227], v4 offset:53760\n"
    "v_mfma_f32_16x16x32_bf16 v[148:151], a[120:123], v[236:239], v[148:151]\n"
    "s_waitcnt lgkmcnt(8)\n"
    "v_mfma_f32_16x16x32_bf16 v[152:155], a[184:187], v[236:239], v[152:155]\n"
    "v_mfma_f32_16x16x32_bf16 v[156:159], a[248:251], v[236:239], v[156:159]\n"
    "s_add_u32 s58, s62, 0x3c00\n"
    "buffer_store_dwordx4 v[220:223], v1, s[44:47], s58 offen sc1\n"
    "v_mfma_f32_16x16x32_bf16 v[32:35], a[40:43], v[240:243], v[32:35]\n"
    "ds_read_b64_tr_b16 v[228:229], v5 offset:53248\n"
    "ds_read_b64_tr_b16 v[230:231], v5 offset:53760\n"
    "v_mfma_f32_16x16x32_bf16 v[36:39], a[104:107], v[240:243], v[36:39]\n"
    "s_waitcnt lgkmcnt(8)\n"
    "v_mfma_f32_16x16x32_bf16 v[40:43], a[168:171], v[240:243], v[40:43]\n"
    "v_mfma_f32_16x16x32_bf16 v[44:47], a[232:235], v[240:243], v[44:47]\n"
    "v_mfma_f32_16x16x32_bf16 v[48:51], a[40:43], v[244:247], v[48:51]\n"
    "ds_read_b64_tr_b16 v[232:233], v6 offset:53248\n"
    "ds_read_b64_tr_b16 v[234:235], v6 offset:53760\n"
    "v_mfma_f32_16x16x32_bf16 v[52:55], a[104:107], v[244:247], v[52:55]\n"
    "s_waitcnt lgkmcnt(8)\n"
    "v_mfma_f32_16x16x32_bf16 v[56:59], a[168:171], v[244:247], v[56:59]\n"
    "s_add_u32 s59, s70, 8192\n"
    "buffer_load_dwordx4 a[164:167], v1, s[44:47], s59 offen offset:1024 sc1\n"
    "v_mfma_f32_16x16x32_bf16 v[60:63], a[232:235], v[244:247], v[60:63]\n"
    "v_mfma_f32_16x16x32_bf16 v[64:67], a[40:43], v[248:251], v[64:67]\n"
    "ds_read_b64_tr_b16 v[236:237], v8 offset:53248\n"
    "ds_read_b64_tr_b16 v[238:239], v8 offset:53760\n"
    "v_mfma_f32_16x16x32_bf16 v[68:71], a[104:107], v[248:251], v[68:71]\n"
    "s_waitcnt lgkmcnt(8)\n"
    "v_mfma_f32_16x16x32_bf16 v[72:75], a[168:171], v[248:251], v[72:75]\n"
    "v_mfma_f32_16x16x32_bf16 v[76:79], a[232:235], v[248:251], v[76:79]\n"
    "v_mfma_f32_16x16x32_bf16 v[80:83], a[40:43], v[252:255], v[80:83]\n"
    "ds_read_b64_tr_b16 v[240:241], v4 offset:28672\n"
    "ds_read_b64_tr_b16 v[242:243], v4 offset:29184\n"
    "v_mfma_f32_16x16x32_bf16 v[84:87], a[104:107], v[252:255], v[84:87]\n"
    "s_waitcnt lgkmcnt(8)\n"
    "v_mfma_f32_16x16x32_bf16 v[88:91], a[168:171], v[252:255], v[88:91]\n"
    "v_mfma_f32_16x16x32_bf16 v[92:95], a[232:235], v[252:255], v[92:95]\n"
    "v_mfma_f32_16x16x32_bf16 v[96:99], a[60:63], v[224:227], v[96:99]\n"
    "ds_read_b64_tr_b16 v[244:245], v5 offset:28672\n"
    "ds_read_b64_tr_b16 v[246:247], v5 offset:29184\n"
    "v_mfma_f32_16x16x32_bf16 v[100:103], a[124:127], v[224:227], v[100:103]\n"
    "s_waitcnt lgkmcnt(8)\n"
    "v_mfma_f32_16x16x32_bf16 v[104:107], a[188:191], v[224:227], v[104:107]\n"
    "s_add_u32 s59, s70, 24576\n"
    "buffer_load_dwordx4 a[228:231], v1, s[44:47], s59 offen offset:1024 sc1\n"
    "v_mfma_f32_16x16x32_bf16 v[108:111], a[252:255], v[224:227], v[108:111]\n"
    "v_mfma_f32_16x16x32_bf16 v[112:115], a[60:63], v[228:231], v[112:115]\n"
    "ds_read_b64_tr_b16 v[248:249], v6 offset:28672\n"
    "ds_read_b64_tr_b16 v[250:251], v6 offset:29184\n"
    "v_mfma_f32_16x16x32_bf16 v[116:119], a[124:127], v[228:231], v[116:119]\n"
    "s_waitcnt lgkmcnt(8)\n"
    "v_mfma_f32_16x16x32_bf16 v[120:123], a[188:191], v[228:231], v[120:123]\n"
    "s_add_u32 s59, s69, 8192\n"
    "buffer_load_dwordx4 a[40:43], v1, s[44:47], s59 offen offset:2048 sc1\n"
    "v_mfma_f32_16x16x32_bf16 v[124:127], a[252:255], v[228:231], v[124:127]\n"
    "v_mfma_f32_16x16x32_bf16 v[128:131], a[60:63], v[232:235], v[128:131]\n"
    "ds_read_b64_tr_b16 v[252:253], v8 offset:28672\n"
    "ds_read_b64_tr_b16 v[254:255], v8 offset:29184\n"
    "v_mfma_f32_16x16x32_bf16 v[132:135], a[124:127], v[232:235], v[132:135]\n"
    "s_waitcnt lgkmcnt(8)\n"
    "v_mfma_f32_16x16x32_bf16 v[136:139], a[188:191], v[232:235], v[136:139]\n"
    "v_mfma_f32_16x16x32_bf16 v[140:143], a[252:255], v[232:235], v[140:143]\n"
    "s_add_u32 s59, s69, 24576\n"
    "buffer_load_dwordx4 a[104:107], v1, s[44:47], s59 offen offset:2048 sc1\n"
    "v_xor_b32 v10, 0x10000, v10\n"
    "v_xor_b32 v11, 0x10000, v11\n"
    "s_waitcnt vmcnt(31)\n"
    "s_barrier\n"
    "v_mfma_f32_16x16x32_bf16 v[144:147], a[60:63], v[236:239], v[144:147]\n"
    "ds_read_b64_tr_b16 v[224:225], v10 offset:0\n"
    "ds_read_b64_tr_b16 v[226:227], v10 offset:256\n"
    "v_mfma_f32_16x16x32_bf16 v[148:151], a[124:127], v[236:239], v[148:151]\n"
    "s_waitcnt lgkmcnt(8)\n"
    "v_mfma_f32_16x16x32_bf16 v[152:155], a[188:191], v[236:239], v[152:155]\n"
    "v_mfma_f32_16x16x32_bf16 v[156:159], a[252:255], v[236:239], v[156:159]\n"
    "v_mfma_f32_16x16x32_bf16 v[32:35], a[44:47], v[240:243], v[32:35]\n"
    "ds_read_b64_tr_b16 v[228:229], v11 offset:0\n"
    "ds_read_b64_tr_b16 v[230:231], v11 offset:256\n"
    "v_mfma_f32_16x16x32_bf16 v[36:39], a[108:111], v[240:243], v[36:39]\n"
    "s_waitcnt lgkmcnt(8)\n"
    "v_mfma_f32_16x16x32_bf16 v[40:43], a[172:175], v[240:243], v[40:43]\n"
    "v_cvt_pk_bf16_f32 v96, v96, v97\n"
    "v_cvt_pk_bf16_f32 v97, v98, v99\n"
    "v_mfma_f32_16x16x32_bf16 v[44:47], a[236:239], v[240:243], v[44:47]\n"
    "v_cvt_pk_bf16_f32 v98, v100, v101\n"
    "v_cvt_pk_bf16_f32 v99, v102, v103\n"
    "v_mfma_f32_16x16x32_bf16 v[48:51], a[44:47], v[244:247], v[48:51]\n"
    "ds_read_b64_tr_b16 v[232:233], v10 offset:4096\n"
    "ds_read_b64_tr_b16 v[234:235], v10 offset:4352\n"
    "v_mfma_f32_16x16x32_bf16 v[52:55], a[108:111], v[244:247], v[52:55]\n"
    "s_waitcnt lgkmcnt(8)\n"
    "v_cvt_pk_bf16_f32 v100, v104, v105\n"
    "v_cvt_pk_bf16_f32 v101, v106, v107\n"
    "v_mfma_f32_16x16x32_bf16 v[56:59], a[172:175], v[244:247], v[56:59]\n"
    "s_add_u32 s59, s70, 8192\n"
    "buffer_load_dwordx4 a[168:171], v1, s[44:47], s59 offen offset:2048 sc1\n"
    "v_cvt_pk_bf16_f32 v102, v108, v109\n"
    "v_mfma_f32_16x16x32_bf16 v[60:63], a[236:239], v[244:247], v[60:63]\n"
    "v_cvt_pk_bf16_f32 v103, v110, v111\n"
    "v_cvt_pk_bf16_f32 v104, v112, v113\n"
    "v_mfma_f32_16x16x32_bf16 v[64:67], a[44:47], v[248:251], v[64:67]\n"
    "ds_read_b64_tr_b16 v[236:237], v11 offset:4096\n"
    "ds_read_b64_tr_b16 v[238:239], v11 offset:4352\n"
    "v_mfma_f32_16x16x32_bf16 v[68:71], a[108:111], v[248:251], v[68:71]\n"
    "s_waitcnt lgkmcnt(8)\n"
    "v_cvt_pk_bf16_f32 v105, v114, v115\n"
    "v_cvt_pk_bf16_f32 v106, v116, v117\n"
    "v_mfma_f32_16x16x32_bf16 v[72:75], a[172:175], v[248:251], v[72:75]\n"
    "v_cvt_pk_bf16_f32 v107, v118, v119\n"
    "v_cvt_pk_bf16_f32 v108, v120, v121\n"
    "v_mfma_f32_16x16x32_bf16 v[76:79], a[236:239], v[248:251], v[76:79]\n"
    "v_cvt_pk_bf16_f32 v109, v122, v123\n"
    "v_mfma_f32_16x16x32_bf16 v[80:83], a[44:47], v[252:255], v[80:83]\n"
    "ds_read_b64_tr_b16 v[240:241], v4 offset:40960\n"
    "ds_read_b64_tr_b16 v[242:243], v4 offset:41472\n"
    "v_mfma_f32_16x16x32_bf16 v[84:87], a[108:111], v[252:255], v[84:87]\n"
    "s_waitcnt lgkmcnt(8)\n"
    "v_cvt_pk_bf16_f32 v110, v124, v125\n"
    "v_cvt_pk_bf16_f32 v111, v126, v127\n"
    "v_mfma_f32_16x16x32_bf16 v[88:91], a[172:175], v[252:255], v[88:91]\n"
    "v_cvt_pk_bf16_f32 v112, v128, v129\n"
    "v_cvt_pk_bf16_f32 v113, v130, v131\n"
    "v_mfma_f32_16x16x32_bf16 v[92:95], a[236:239], v[252:255], v[92:95]\n"
    "v_cvt_pk_bf16_f32 v114, v132, v133\n"
    "v_mfma_f32_16x16x32_bf16 v[160:163], v[224:227], a[0:3], 0\n"
    "ds_read_b64_tr_b16 v[244:245], v5 offset:40960\n"
    "ds_read_b64_tr_b16 v[246:247], v5 offset:41472\n"
    "v_mfma_f32_16x16x32_bf16 v[164:167], v[224:227], a[64:67], 0\n"
    "s_waitcnt lgkmcnt(8)\n"
    "v_cvt_pk_bf16_f32 v115, v134, v135\n"
    "v_cvt_pk_bf16_f32 v116, v136, v137\n"
    "v_mfma_f32_16x16x32_bf16 v[168:171], v[224:227], a[128:131], 0\n"
    "s_add_u32 s59, s70, 24576\n"
    "buffer_load_dwordx4 a[232:235], v1, s[44:47], s59 offen offset:2048 sc1\n"
    "v_cvt_pk_bf16_f32 v117, v138, v139\n"
    "v_mfma_f32_16x16x32_bf16 v[172:175], v[224:227], a[192:195], 0\n"
    "v_cvt_pk_bf16_f32 v118, v140, v141\n"
    "v_cvt_pk_bf16_f32 v119, v142, v143\n"
    "v_mfma_f32_16x16x32_bf16 v[176:179], v[228:231], a[0:3], 0\n"
    "ds_read_b64_tr_b16 v[248:249], v6 offset:40960\n"
    "ds_read_b64_tr_b16 v[250:251], v6 offset:41472\n"
    "v_mfma_f32_16x16x32_bf16 v[180:183], v[228:231], a[64:67], 0\n"
    "s_waitcnt lgkmcnt(8)\n"
    "v_cvt_pk_bf16_f32 v120, v144, v145\n"
    "v_cvt_pk_bf16_f32 v121, v146, v147\n"
    "v_mfma_f32_16x16x32_bf16 v[184:187], v[228:231], a[128:131], 0\n"
    "s_add_u32 s59, s69, 8192\n"
    "buffer_load_dwordx4 a[44:47], v1, s[44:47], s59 offen offset:3072 sc1\n"
    "v_cvt_pk_bf16_f32 v122, v148, v149\n"
    "v_mfma_f32_16x16x32_bf16 v[188:191], v[228:231], a[192:195], 0\n"
    "v_cvt_pk_bf16_f32 v123, v150, v151\n"
    "v_cvt_pk_bf16_f32 v124, v152, v153\n"
    "v_mfma_f32_16x16x32_bf16 v[192:195], v[232:235], a[0:3], 0\n"
    "ds_read_b64_tr_b16 v[252:253], v8 offset:40960\n"
    "ds_read_b64_tr_b16 v[254:255], v8 offset:41472\n"
    "v_mfma_f32_16x16x32_bf16 v[196:199], v[232:235], a[64:67], 0\n"
    "s_waitcnt lgkmcnt(8)\n"
    "v_cvt_pk_bf16_f32 v125, v154, v155\n"
    "v_cvt_pk_bf16_f32 v126, v156, v157\n"
    "v_mfma_f32_16x16x32_bf16 v[200:203], v[232:235], a[128:131], 0\n"
    "v_cvt_pk_bf16_f32 v127, v158, v159\n"
    "v_mfma_f32_16x16x32_bf16 v[204:207], v[232:235], a[192:195], 0\n"
    "s_add_u32 s59, s69, 24576\n"
    "buffer_load_dwordx4 a[108:111], v1, s[44:47], s59 offen offset:3072 sc1\n"
    "v_mfma_f32_16x16x32_bf16 v[208:211], v[236:239], a[0:3], 0\n"
    "ds_read_b64_tr_b16 v[224:225], v10 offset:2048\n"
    "ds_read_b64_tr_b16 v[226:227], v10 offset:2304\n"
    "v_mfma_f32_16x16x32_bf16 v[212:215], v[236:239], a[64:67], 0\n"
    "s_waitcnt lgkmcnt(8)\n"
    "v_mfma_f32_16x16x32_bf16 v[216:219], v[236:239], a[128:131], 0\n"
    "v_mfma_f32_16x16x32_bf16 v[220:223], v[236:239], a[192:195], 0\n"
    "v_mfma_f32_16x16x32_bf16 v[32:35], a[48:51], v[240:243], v[32:35]\n"
    "ds_read_b64_tr_b16 v[228:229], v11 offset:2048\n"
    "ds_read_b64_tr_b16 v[230:231], v11 offset:2304\n"
    "v_mfma_f32_16x16x32_bf16 v[36:39], a[112:115], v[240:243], v[36:39]\n"
    "s_waitcnt lgkmcnt(8)\n"
    "v_mfma_f32_16x16x32_bf16 v[40:43], a[176:179], v[240:243], v[40:43]\n"
    "v_mfma_f32_16x16x32_bf16 v[44:47], a[240:243], v[240:243], v[44:47]\n"
    "v_mfma_f32_16x16x32_bf16 v[48:51], a[48:51], v[244:247], v[48:51]\n"
    "ds_read_b64_tr_b16 v[232:233], v10 offset:6144\n"
    "ds_read_b64_tr_b16 v[234:235], v10 offset:6400\n"
    "v_mfma_f32_16x16x32_bf16 v[52:55], a[112:115], v[244:247], v[52:55]\n"
    "s_waitcnt lgkmcnt(8)\n"
    "v_mfma_f32_16x16x32_bf16 v[56:59], a[176:179], v[244:247], v[56:59]\n"
    "s_add_u32 s59, s70, 8192\n"
    "buffer_load_dwordx4 a[172:175], v1, s[44:47], s59 offen offset:3072 sc1\n"
    "v_mfma_f32_16x16x32_bf16 v[60:63], a[240:243], v[244:247], v[60:63]\n"
    "v_mfma_f32_16x16x32_bf16 v[64:67], a[48:51], v[248:251], v[64:67]\n"
    "ds_read_b64_tr_b16 v[236:237], v11 offset:6144\n"
    "ds_read_b64_tr_b16 v[238:239], v11 offset:6400\n"
    "v_mfma_f32_16x16x32_bf16 v[68:71], a[112:115], v[248:251], v[68:71]\n"
    "s_waitcnt lgkmcnt(8)\n"
    "v_mfma_f32_16x16x32_bf16 v[72:75], a[176:179], v[248:251], v[72:75]\n"
    "v_mfma_f32_16x16x32_bf16 v[76:79], a[240:243], v[248:251], v[76:79]\n"
    "v_mfma_f32_16x16x32_bf16 v[80:83], a[48:51], v[252:255], v[80:83]\n"
    "ds_read_b64_tr_b16 v[240:241], v4 offset:45056\n"
    "ds_read_b64_tr_b16 v[242:243], v4 offset:45568\n"
    "v_mfma_f32_16x16x32_bf16 v[84:87], a[112:115], v[252:255], v[84:87]\n"
    "s_waitcnt lgkmcnt(8)\n"
    "v_mfma_f32_16x16x32_bf16 v[88:91], a[176:179], v[252:255], v[88:91]\n"
    "v_mfma_f32_16x16x32_bf16 v[92:95], a[240:243], v[252:255], v[92:95]\n"
    "v_mfma_f32_16x16x32_bf16 v[160:163], v[224:227], a[4:7], v[160:163]\n"
    "ds_read_b64_tr_b16 v[244:245], v5 offset:45056\n"
    "ds_read_b64_tr_b16 v[246:247], v5 offset:45568\n"
    "v_mfma_f32_16x16x32_bf16 v[164:167], v[224:227], a[68:71], v[164:167]\n"
    "s_waitcnt lgkmcnt(8)\n"
    "v_mfma_f32_16x16x32_bf16 v[168:171], v[224:227], a[132:135], v[168:171]\n"
    "s_add_u32 s59, s70, 24576\n"
    "buffer_load_dwordx4 a[236:239], v1, s[44:47], s59 offen offset:3072 sc1\n"
    "v_mfma_f32_16x16x32_bf16 v[172:175], v[224:227], a[196:199], v[172:175]\n"
    "v_mfma_f32_16x16x32_bf16 v[176:179], v[228:231], a[4:7], v[176:179]\n"
    "ds_read_b64_tr_b16 v[248:249], v6 offset:45056\n"
    "ds_read_b64_tr_b16 v[250:251], v6 offset:45568\n"
    "v_mfma_f32_16x16x32_bf16 v[180:183], v[228:231], a[68:71], v[180:183]\n"
    "s_waitcnt lgkmcnt(8)\n"
    "v_mfma_f32_16x16x32_bf16 v[184:187], v[228:231], a[132:135], v[184:187]\n"
    "s_add_u32 s59, s69, 12288\n"
    "buffer_load_dwordx4 a[48:51], v1, s[44:47], s59 offen offset:0 sc1\n"
    "v_mfma_f32_16x16x32_bf16 v[188:191], v[228:231], a[196:199], v[188:191]\n"
    "v_mfma_f32_16x16x32_bf16 v[192:195], v[232:235], a[4:7], v[192:195]\n"
    "ds_read_b64_tr_b16 v[252:253], v8 offset:45056\n"
    "ds_read_b64_tr_b16 v[254:255], v8 offset:45568\n"
    "v_mfma_f32_16x16x32_bf16 v[196:199], v[232:235], a[68:71], v[196:199]\n"
    "s_waitcnt lgkmcnt(8)\n"
    "v_mfma_f32_16x16x32_bf16 v[200:203], v[232:235], a[132:135], v[200:203]\n"
    "v_mfma_f32_16x16x32_bf16 v[204:207], v[232:235], a[196:199], v[204:207]\n"
    "s_add_u32 s59, s69, 28672\n"
    "buffer_load_dwordx4 a[112:115], v1, s[44:47], s59 offen offset:0 sc1\n"
    "s_waitcnt vmcnt(28)\n"
    "s_barrier\n"
    "v_mfma_f32_16x16x32_bf16 v[208:211], v[236:239], a[4:7], v[208:211]\n"
    "ds_read_b64_tr_b16 v[224:225], v10 offset:8192\n"
    "ds_read_b64_tr_b16 v[226:227], v10 offset:8448\n"
    "v_mfma_f32_16x16x32_bf16 v[212:215], v[236:239], a[68:71], v[212:215]\n"
    "s_waitcnt lgkmcnt(8)\n"
    "v_mfma_f32_16x16x32_bf16 v[216:219], v[236:239], a[132:135], v[216:219]\n"
    "v_mfma_f32_16x16x32_bf16 v[220:223], v[236:239], a[196:199], v[220:223]\n"
    "v_mfma_f32_16x16x32_bf16 v[32:35], a[52:55], v[240:243], v[32:35]\n"
    "ds_read_b64_tr_b16 v[228:229], v11 offset:8192\n"
    "ds_read_b64_tr_b16 v[230:231], v11 offset:8448\n"
    "v_mfma_f32_16x16x32_bf16 v[36:39], a[116:119], v[240:243], v[36:39]\n"
    "s_waitcnt lgkmcnt(8)\n"
    "v_mfma_f32_16x16x32_bf16 v[40:43], a[180:183], v[240:243], v[40:43]\n"
    "v_mfma_f32_16x16x32_bf16 v[44:47], a[244:247], v[240:243], v[44:47]\n"
    "v_mfma_f32_16x16x32_bf16 v[48:51], a[52:55], v[244:247], v[48:51]\n"
    "ds_read_b64_tr_b16 v[232:233], v10 offset:12288\n"
    "ds_read_b64_tr_b16 v[234:235], v10 offset:12544\n"
    "v_mfma_f32_16x16x32_bf16 v[52:55], a[116:119], v[244:247], v[52:55]\n"
    "s_waitcnt lgkmcnt(8)\n"
    "v_mfma_f32_16x16x32_bf16 v[56:59], a[180:183], v[244:247], v[56:59]\n"
    "s_add_u32 s59, s70, 12288\n"
    "buffer_load_dwordx4 a[176:179], v1, s[44:47], s59 offen offset:0 sc1\n"
    "v_mfma_f32_16x16x32_bf16 v[60:63], a[244:247], v[244:247], v[60:63]\n"
    "v_mfma_f32_16x16x32_bf16 v[64:67], a[52:55], v[248:251], v[64:67]\n"
    "ds_read_b64_tr_b16 v[236:237], v11 offset:12288\n"
    "ds_read_b64_tr_b16 v[238:239], v11 offset:12544\n"
    "v_mfma_f32_16x16x32_bf16 v[68:71], a[116:119], v[248:251], v[68:71]\n"
    "s_waitcnt lgkmcnt(8)\n"
    "v_mfma_f32_16x16x32_bf16 v[72:75], a[180:183], v[248:251], v[72:75]\n"
    "v_mfma_f32_16x16x32_bf16 v[76:79], a[244:247], v[248:251], v[76:79]\n"
    "v_mfma_f32_16x16x32_bf16 v[80:83], a[52:55], v[252:255], v[80:83]\n"
    "ds_read_b64_tr_b16 v[240:241], v4 offset:57344\n"
    "ds_read_b64_tr_b16 v[242:243], v4 offset:57856\n"
    "v_mfma_f32_16x16x32_bf16 v[84:87], a[116:119], v[252:255], v[84:87]\n"
    "s_waitcnt lgkmcnt(8)\n"
    "v_mfma_f32_16x16x32_bf16 v[88:91], a[180:183], v[252:255], v[88:91]\n"
    "v_mfma_f32_16x16x32_bf16 v[92:95], a[244:247], v[252:255], v[92:95]\n"
    "v_mfma_f32_16x16x32_bf16 v[160:163], v[224:227], a[8:11], v[160:163]\n"
    "ds_read_b64_tr_b16 v[244:245], v5 offset:57344\n"
    "ds_read_b64_tr_b16 v[246:247], v5 offset:57856\n"
    "v_mfma_f32_16x16x32_bf16 v[164:167], v[224:227], a[72:75], v[164:167]\n"
    "s_waitcnt lgkmcnt(8)\n"
    "v_mfma_f32_16x16x32_bf16 v[168:171], v[224:227], a[136:139], v[168:171]\n"
    "s_add_u32 s59, s70, 28672\n"
    "buffer_load_dwordx4 a[240:243], v1, s[44:47], s59 offen offset:0 sc1\n"
    "v_mfma_f32_16x16x32_bf16 v[172:175], v[224:227], a[200:203], v[172:175]\n"
    "v_mfma_f32_16x16x32_bf16 v[176:179], v[228:231], a[8:11], v[176:179]\n"
    "ds_read_b64_tr_b16 v[248:249], v6 offset:57344\n"
    "ds_read_b64_tr_b16 v[250:251], v6 offset:57856\n"
    "v_mfma_f32_16x16x32_bf16 v[180:183], v[228:231], a[72:75], v[180:183]\n"
    "s_waitcnt lgkmcnt(8)\n"
    "v_mfma_f32_16x16x32_bf16 v[184:187], v[228:231], a[136:139], v[184:187]\n"
    "s_add_u32 s59, s69, 12288\n"
    "buffer_load_dwordx4 a[52:55], v1, s[44:47], s59 offen offset:1024 sc1\n"
    "v_mfma_f32_16x16x32_bf16 v[188:191], v[228:231], a[200:203], v[188:191]\n"
    "v_mfma_f32_16x16x32_bf16 v[192:195], v[232:235], a[8:11], v[192:195]\n"
    "ds_read_b64_tr_b16 v[252:253], v8 offset:57344\n"
    "ds_read_b64_tr_b16 v[254:255], v8 offset:57856\n"
    "v_mfma_f32_16x16x32_bf16 v[196:199], v[232:235], a[72:75], v[196:199]\n"
    "s_waitcnt lgkmcnt(8)\n"
    "v_mfma_f32_16x16x32_bf16 v[200:203], v[232:235], a[136:139], v[200:203]\n"
    "v_mfma_f32_16x16x32_bf16 v[204:207], v[232:235], a[200:203], v[204:207]\n"
    "s_add_u32 s59, s69, 28672\n"
    "buffer_load_dwordx4 a[116:119], v1, s[44:47], s59 offen offset:1024 sc1\n"
    "v_mfma_f32_16x16x32_bf16 v[208:211], v[236:239], a[8:11], v[208:211]\n"
    "ds_read_b64_tr_b16 v[224:225], v10 offset:10240\n"
    "ds_read_b64_tr_b16 v[226:227], v10 offset:10496\n"
    "v_mfma_f32_16x16x32_bf16 v[212:215], v[236:239], a[72:75], v[212:215]\n"
    "s_waitcnt lgkmcnt(8)\n"
    "v_mfma_f32_16x16x32_bf16 v[216:219], v[236:239], a[136:139], v[216:219]\n"
    "v_mfma_f32_16x16x32_bf16 v[220:223], v[236:239], a[200:203], v[220:223]\n"
    "v_mfma_f32_16x16x32_bf16 v[32:35], a[56:59], v[240:243], v[32:35]\n"
    "ds_read_b64_tr_b16 v[228:229], v11 offset:10240\n"
    "ds_read_b64_tr_b16 v[230:231], v11 offset:10496\n"
    "v_mfma_f32_16x16x32_bf16 v[36:39], a[120:123], v[240:243], v[36:39]\n"
    "s_waitcnt lgkmcnt(8)\n"
    "v_mfma_f32_16x16x32_bf16 v[40:43], a[184:187], v[240:243], v[40:43]\n"
    "v_mfma_f32_16x16x32_bf16 v[44:47], a[248:251], v[240:243], v[44:47]\n"
    "v_mfma_f32_16x16x32_bf16 v[48:51], a[56:59], v[244:247], v[48:51]\n"
    "ds_read_b64_tr_b16 v[232:233], v10 offset:14336\n"
    "ds_read_b64_tr_b16 v[234:235], v10 offset:14592\n"
    "v_mfma_f32_16x16x32_bf16 v[52:55], a[120:123], v[244:247], v[52:55]\n"
    "s_waitcnt lgkmcnt(8)\n"
    "v_mfma_f32_16x16x32_bf16 v[56:59], a[184:187], v[244:247], v[56:59]\n"
    "s_add_u32 s59, s70, 12288\n"
    "buffer_load_dwordx4 a[180:183], v1, s[44:47], s59 offen offset:1024 sc1\n"
    "v_mfma_f32_16x16x32_bf16 v[60:63], a[248:251], v[244:247], v[60:63]\n"
    "v_mfma_f32_16x16x32_bf16 v[64:67], a[56:59], v[248:251], v[64:67]\n"
    "ds_read_b64_tr_b16 v[236:237], v11 offset:14336\n"
    "ds_read_b64_tr_b16 v[238:239], v11 offset:14592\n"
    "v_mfma_f32_16x16x32_bf16 v[68:71], a[120:123], v[248:251], v[68:71]\n"
    "s_waitcnt lgkmcnt(8)\n"
    "v_mfma_f32_16x16x32_bf16 v[72:75], a[184:187], v[248:251], v[72:75]\n"
    "v_mfma_f32_16x16x32_bf16 v[76:79], a[248:251], v[248:251], v[76:79]\n"
    "v_mfma_f32_16x16x32_bf16 v[80:83], a[56:59], v[252:255], v[80:83]\n"
    "ds_read_b64_tr_b16 v[240:241], v4 offset:61440\n"
    "ds_read_b64_tr_b16 v[242:243], v4 offset:61952\n"
    "v_mfma_f32_16x16x32_bf16 v[84:87], a[120:123], v[252:255], v[84:87]\n"
    "s_waitcnt lgkmcnt(8)\n"
    "v_mfma_f32_16x16x32_bf16 v[88:91], a[184:187], v[252:255], v[88:91]\n"
    "v_mfma_f32_16x16x32_bf16 v[92:95], a[248:251], v[252:255], v[92:95]\n"
    "v_mfma_f32_16x16x32_bf16 v[160:163], v[224:227], a[12:15], v[160:163]\n"
    "ds_read_b64_tr_b16 v[244:245], v5 offset:61440\n"
    "ds_read_b64_tr_b16 v[246:247], v5 offset:61952\n"
    "v_mfma_f32_16x16x32_bf16 v[164:167], v[224:227], a[76:79], v[164:167]\n"
    "s_waitcnt lgkmcnt(8)\n"
    "v_mfma_f32_16x16x32_bf16 v[168:171], v[224:227], a[140:143], v[168:171]\n"
    "s_add_u32 s59, s70, 28672\n"
    "buffer_load_dwordx4 a[244:247], v1, s[44:47], s59 offen offset:1024 sc1\n"
    "v_mfma_f32_16x16x32_bf16 v[172:175], v[224:227], a[204:207], v[172:175]\n"
    "v_mfma_f32_16x16x32_bf16 v[176:179], v[228:231], a[12:15], v[176:179]\n"
    "ds_read_b64_tr_b16 v[248:249], v6 offset:61440\n"
    "ds_read_b64_tr_b16 v[250:251], v6 offset:61952\n"
    "v_mfma_f32_16x16x32_bf16 v[180:183], v[228:231], a[76:79], v[180:183]\n"
    "s_waitcnt lgkmcnt(8)\n"
    "v_mfma_f32_16x16x32_bf16 v[184:187], v[228:231], a[140:143], v[184:187]\n"
    "v_mfma_f32_16x16x32_bf16 v[188:191], v[228:231], a[204:207], v[188:191]\n"
    "v_mfma_f32_16x16x32_bf16 v[192:195], v[232:235], a[12:15], v[192:195]\n"
    "ds_read_b64_tr_b16 v[252:253], v8 offset:61440\n"
    "ds_read_b64_tr_b16 v[254:255], v8 offset:61952\n"
    "v_mfma_f32_16x16x32_bf16 v[196:199], v[232:235], a[76:79], v[196:199]\n"
    "s_waitcnt lgkmcnt(8)\n"
    "v_mfma_f32_16x16x32_bf16 v[200:203], v[232:235], a[140:143], v[200:203]\n"
    "v_mfma_f32_16x16x32_bf16 v[204:207], v[232:235], a[204:207], v[204:207]\n"
    "v_mfma_f32_16x16x32_bf16 v[208:211], v[236:239], a[12:15], v[208:211]\n"
    "ds_read_b64_tr_b16 v[224:225], v10 offset:16384\n"
    "ds_read_b64_tr_b16 v[226:227], v10 offset:16640\n"
    "v_mfma_f32_16x16x32_bf16 v[212:215], v[236:239], a[76:79], v[212:215]\n"
    "s_waitcnt lgkmcnt(8)\n"
    "v_mfma_f32_16x16x32_bf16 v[216:219], v[236:239], a[140:143], v[216:219]\n"
    "v_mfma_f32_16x16x32_bf16 v[220:223], v[236:239], a[204:207], v[220:223]\n"
    "v_mfma_f32_16x16x32_bf16 v[32:35], a[60:63], v[240:243], v[32:35]\n"
    "ds_read_b64_tr_b16 v[228:229], v11 offset:16384\n"
    "ds_read_b64_tr_b16 v[230:231], v11 offset:16640\n"
    "v_mfma_f32_16x16x32_bf16 v[36:39], a[124:127], v[240:243], v[36:39]\n"
    "s_waitcnt lgkmcnt(8)\n"
    "v_mfma_f32_16x16x32_bf16 v[40:43], a[188:191], v[240:243], v[40:43]\n"
    "v_mfma_f32_16x16x32_bf16 v[44:47], a[252:255], v[240:243], v[44:47]\n"
    "v_mfma_f32_16x16x32_bf16 v[48:51], a[60:63], v[244:247], v[48:51]\n"
    "ds_read_b64_tr_b16 v[232:233], v10 offset:20480\n"
    "ds_read_b64_tr_b16 v[234:235], v10 offset:20736\n"
    "v_mfma_f32_16x16x32_bf16 v[52:55], a[124:127], v[244:247], v[52:55]\n"
    "s_waitcnt lgkmcnt(8)\n"
    "v_mfma_f32_16x16x32_bf16 v[56:59], a[188:191], v[244:247], v[56:59]\n"
    "v_mfma_f32_16x16x32_bf16 v[60:63], a[252:255], v[244:247], v[60:63]\n"
    "v_mfma_f32_16x16x32_bf16 v[64:67], a[60:63], v[248:251], v[64:67]\n"
    "ds_read_b64_tr_b16 v[236:237], v11 offset:20480\n"
    "ds_read_b64_tr_b16 v[238:239], v11 offset:20736\n"
    "v_mfma_f32_16x16x32_bf16 v[68:71], a[124:127], v[248:251], v[68:71]\n"
    "s_waitcnt lgkmcnt(8)\n"
    "v_mfma_f32_16x16x32_bf16 v[72:75], a[188:191], v[248:251], v[72:75]\n"
    "v_mfma_f32_16x16x32_bf16 v[76:79], a[252:255], v[248:251], v[76:79]\n"
    "v_mfma_f32_16x16x32_bf16 v[80:83], a[60:63], v[252:255], v[80:83]\n"
    "ds_read_b64_tr_b16 v[240:241], v10 offset:18432\n"
    "ds_read_b64_tr_b16 v[242:243], v10 offset:18688\n"
    "v_mfma_f32_16x16x32_bf16 v[84:87], a[124:127], v[252:255], v[84:87]\n"
    "s_waitcnt lgkmcnt(8)\n"
    "v_mfma_f32_16x16x32_bf16 v[88:91], a[188:191], v[252:255], v[88:91]\n"
    "v_mfma_f32_16x16x32_bf16 v[92:95], a[252:255], v[252:255], v[92:95]\n"
    "s_cmp_eq_u32 s55, 0\n"
    "s_cbranch_scc0 L_pathB_3_%=\n"
    "s_barrier\n"
    "v_mfma_f32_16x16x32_bf16 v[160:163], v[224:227], a[16:19], v[160:163]\n"
    "ds_read_b64_tr_b16 v[244:245], v11 offset:18432\n"
    "ds_read_b64_tr_b16 v[246:247], v11 offset:18688\n"
    "v_mfma_f32_16x16x32_bf16 v[164:167], v[224:227], a[80:83], v[164:167]\n"
    "s_waitcnt lgkmcnt(8)\n"
    "s_add_u32 s56, s66, 0x40\n"
    "v_mfma_f32_16x16x32_bf16 v[168:171], v[224:227], a[144:147], v[168:171]\n"
    "s_add_u32 s59, s69, 12288\n"
    "buffer_load_dwordx4 a[56:59], v1, s[44:47], s59 offen offset:2048 sc1\n"
    "v_mfma_f32_16x16x32_bf16 v[172:175], v[224:227], a[208:211], v[172:175]\n"
    "v_mfma_f32_16x16x32_bf16 v[176:179], v[228:231], a[16:19], v[176:179]\n"
    "ds_read_b64_tr_b16 v[248:249], v10 offset:22528\n"
    "ds_read_b64_tr_b16 v[250:251], v10 offset:22784\n"
    "v_mfma_f32_16x16x32_bf16 v[180:183], v[228:231], a[80:83], v[180:183]\n"
    "s_waitcnt lgkmcnt(8)\n"
    "v_cvt_pk_bf16_f32 v128, v32, v33\n"
    "v_cvt_pk_bf16_f32 v129, v34, v35\n"
    "v_mfma_f32_16x16x32_bf16 v[184:187], v[228:231], a[144:147], v[184:187]\n"
    "s_add_u32 m0, s90, 0\n"
    "s_nop 0\n"
    "buffer_load_dwordx4 v9, s[48:51], s56 offen lds\n"
    "v_cvt_pk_bf16_f32 v130, v36, v37\n"
    "v_mfma_f32_16x16x32_bf16 v[188:191], v[228:231], a[208:211], v[188:191]\n"
    "v_cvt_pk_bf16_f32 v131, v38, v39\n"
    "v_cvt_pk_bf16_f32 v132, v40, v41\n"
    "v_mfma_f32_16x16x32_bf16 v[192:195], v[232:235], a[16:19], v[192:195]\n"
    "ds_read_b64_tr_b16 v[252:253], v11 offset:22528\n"
    "ds_read_b64_tr_b16 v[254:255], v11 offset:22784\n"
    "v_mfma_f32_16x16x32_bf16 v[196:199], v[232:235], a[80:83], v[196:199]\n"
    "s_waitcnt lgkmcnt(8)\n"
    "v_cvt_pk_bf16_f32 v133, v42, v43\n"
    "v_cvt_pk_bf16_f32 v134, v44, v45\n"
    "v_mfma_f32_16x16x32_bf16 v[200:203], v[232:235], a[144:147], v[200:203]\n"
    "s_add_u32 s59, s69, 28672\n"
    "buffer_load_dwordx4 a[120:123], v1, s[44:47], s59 offen offset:2048 sc1\n"
    "v_cvt_pk_bf16_f32 v135, v46, v47\n"
    "v_mfma_f32_16x16x32_bf16 v[204:207], v[232:235], a[208:211], v[204:207]\n"
    "v_cvt_pk_bf16_f32 v136, v48, v49\n"
    "v_cvt_pk_bf16_f32 v137, v50, v51\n"
    "v_mfma_f32_16x16x32_bf16 v[208:211], v[236:239], a[16:19], v[208:211]\n"
    "ds_read_b64_tr_b16 v[224:225], v10 offset:24576\n"
    "ds_read_b64_tr_b16 v[226:227], v10 offset:24832\n"
    "v_mfma_f32_16x16x32_bf16 v[212:215], v[236:239], a[80:83], v[212:215]\n"
    "s_waitcnt lgkmcnt(8)\n"
    "v_cvt_pk_bf16_f32 v138, v52, v53\n"
    "v_cvt_pk_bf16_f32 v139, v54, v55\n"
    "v_mfma_f32_16x16x32_bf16 v[216:219], v[236:239], a[144:147], v[216:219]\n"
    "s_add_u32 s59, s70, 12288\n"
    "buffer_load_dwordx4 a[184:187], v1, s[44:47], s59 offen offset:2048 sc1\n"
    "v_cvt_pk_bf16_f32 v140, v56, v57\n"
    "v_mfma_f32_16x16x32_bf16 v[220:223], v[236:239], a[208:211], v[220:223]\n"
    "v_cvt_pk_bf16_f32 v141, v58, v59\n"
    "v_cvt_pk_bf16_f32 v142, v60, v61\n"
    "v_mfma_f32_16x16x32_bf16 v[160:163], v[240:243], a[20:23], v[160:163]\n"
    "ds_read_b64_tr_b16 v[228:229], v11 offset:24576\n"
    "ds_read_b64_tr_b16 v[230:231], v11 offset:24832\n"
    "v_mfma_f32_16x16x32_bf16 v[164:167], v[240:243], a[84:87], v[164:167]\n"
    "s_waitcnt lgkmcnt(8)\n"
    "v_cvt_pk_bf16_f32 v143, v62, v63\n"
    "v_cvt_pk_bf16_f32 v144, v64, v65\n"
    "v_mfma_f32_16x16x32_bf16 v[168:171], v[240:243], a[148:151], v[168:171]\n"
    "s_add_u32 m0, s90, 4096\n"
    "s_nop 0\n"
    "buffer_load_dwordx4 v9, s[84:87], s56 offen lds\n"
    "v_cvt_pk_bf16_f32 v145, v66, v67\n"
    "v_mfma_f32_16x16x32_bf16 v[172:175], v[240:243], a[212:215], v[172:175]\n"
    "s_add_u32 s56, s66, 0x38040\n"
    "v_cvt_pk_bf16_f32 v146, v68, v69\n"
    "v_cvt_pk_bf16_f32 v147, v70, v71\n"
    "v_mfma_f32_16x16x32_bf16 v[176:179], v[244:247], a[20:23], v[176:179]\n"
    "ds_read_b64_tr_b16 v[232:233], v10 offset:28672\n"
    "ds_read_b64_tr_b16 v[234:235], v10 offset:28928\n"
    "v_mfma_f32_16x16x32_bf16 v[180:183], v[244:247], a[84:87], v[180:183]\n"
    "s_waitcnt lgkmcnt(8)\n"
    "v_cvt_pk_bf16_f32 v148, v72, v73\n"
    "v_cvt_pk_bf16_f32 v149, v74, v75\n"
    "v_mfma_f32_16x16x32_bf16 v[184:187], v[244:247], a[148:151], v[184:187]\n"
    "s_add_u32 s59, s70, 28672\n"
    "buffer_load_dwordx4 a[248:251], v1, s[44:47], s59 offen offset:2048 sc1\n"
    "v_cvt_pk_bf16_f32 v150, v76, v77\n"
    "v_mfma_f32_16x16x32_bf16 v[188:191], v[244:247], a[212:215], v[188:191]\n"
    "v_cvt_pk_bf16_f32 v151, v78, v79\n"
    "v_cvt_pk_bf16_f32 v152, v80, v81\n"
    "v_mfma_f32_16x16x32_bf16 v[192:195], v[248:251], a[20:23], v[192:195]\n"
    "ds_read_b64_tr_b16 v[236:237], v11 offset:28672\n"
    "ds_read_b64_tr_b16 v[238:239], v11 offset:28928\n"
    "v_mfma_f32_16x16x32_bf16 v[196:199], v[248:251], a[84:87], v[196:199]\n"
    "s_waitcnt lgkmcnt(8)\n"
    "v_cvt_pk_bf16_f32 v153, v82, v83\n"
    "v_cvt_pk_bf16_f32 v154, v84, v85\n"
    "v_mfma_f32_16x16x32_bf16 v[200:203], v[248:251], a[148:151], v[200:203]\n"
    "s_add_u32 s59, s69, 12288\n"
    "buffer_load_dwordx4 a[60:63], v1, s[44:47], s59 offen offset:3072 sc1\n"
    "v_cvt_pk_bf16_f32 v155, v86, v87\n"
    "v_mfma_f32_16x16x32_bf16 v[204:207], v[248:251], a[212:215], v[204:207]\n"
    "v_cvt_pk_bf16_f32 v156, v88, v89\n"
    "v_cvt_pk_bf16_f32 v157, v90, v91\n"
    "v_mfma_f32_16x16x32_bf16 v[208:211], v[252:255], a[20:23], v[208:211]\n"
    "ds_read_b64_tr_b16 v[240:241], v10 offset:26624\n"
    "ds_read_b64_tr_b16 v[242:243], v10 offset:26880\n"
    "v_mfma_f32_16x16x32_bf16 v[212:215], v[252:255], a[84:87], v[212:215]\n"
    "s_waitcnt lgkmcnt(8)\n"
    "v_cvt_pk_bf16_f32 v158, v92, v93\n"
    "v_cvt_pk_bf16_f32 v159, v94, v95\n"
    "v_mfma_f32_16x16x32_bf16 v[216:219], v[252:255], a[148:151], v[216:219]\n"
    "s_add_u32 s59, s69, 28672\n"
    "buffer_load_dwordx4 a[124:127], v1, s[44:47], s59 offen offset:3072 sc1\n"
    "v_mfma_f32_16x16x32_bf16 v[220:223], v[252:255], a[212:215], v[220:223]\n"
    "v_mfma_f32_16x16x32_bf16 v[160:163], v[224:227], a[24:27], v[160:163]\n"
    "ds_read_b64_tr_b16 v[244:245], v11 offset:26624\n"
    "ds_read_b64_tr_b16 v[246:247], v11 offset:26880\n"
    "v_mfma_f32_16x16x32_bf16 v[164:167], v[224:227], a[88:91], v[164:167]\n"
    "s_waitcnt lgkmcnt(8)\n"
    "s_waitcnt vmcnt(41)\n"
    "v_mfma_f32_16x16x32_bf16 v[168:171], v[224:227], a[152:155], v[168:171]\n"
    "s_add_u32 s59, s70, 12288\n"
    "buffer_load_dwordx4 a[188:191], v1, s[44:47], s59 offen offset:3072 sc1\n"
    "s_waitcnt vmcnt(40)\n"
    "v_mfma_f32_16x16x32_bf16 v[172:175], v[224:227], a[216:219], v[172:175]\n"
    "v_mfma_f32_16x16x32_bf16 v[176:179], v[228:231], a[24:27], v[176:179]\n"
    "ds_read_b64_tr_b16 v[248:249], v10 offset:30720\n"
    "ds_read_b64_tr_b16 v[250:251], v10 offset:30976\n"
    "v_mfma_f32_16x16x32_bf16 v[180:183], v[228:231], a[88:91], v[180:183]\n"
    "s_waitcnt lgkmcnt(8)\n"
    "v_mfma_f32_16x16x32_bf16 v[184:187], v[228:231], a[152:155], v[184:187]\n"
    "s_add_u32 m0, s90, 8192\n"
    "s_nop 0\n"
    "buffer_load_dwordx4 v9, s[48:51], s56 offen lds\n"
    "v_mfma_f32_16x16x32_bf16 v[188:191], v[228:231], a[216:219], v[188:191]\n"
    "v_mfma_f32_16x16x32_bf16 v[192:195], v[232:235], a[24:27], v[192:195]\n"
    "ds_read_b64_tr_b16 v[252:253], v11 offset:30720\n"
    "ds_read_b64_tr_b16 v[254:255], v11 offset:30976\n"
    "v_mfma_f32_16x16x32_bf16 v[196:199], v[232:235], a[88:91], v[196:199]\n"
    "s_waitcnt lgkmcnt(8)\n"
    "v_mfma_f32_16x16x32_bf16 v[200:203], v[232:235], a[152:155], v[200:203]\n"
    "s_add_u32 s59, s70, 28672\n"
    "buffer_load_dwordx4 a[252:255], v1, s[44:47], s59 offen offset:3072 sc1\n"
    "v_mfma_f32_16x16x32_bf16 v[204:207], v[232:235], a[216:219], v[204:207]\n"
    "v_mfma_f32_16x16x32_bf16 v[208:211], v[236:239], a[24:27], v[208:211]\n"
    "ds_read_b64_tr_b16 v[224:225], v10 offset:32768\n"
    "ds_read_b64_tr_b16 v[226:227], v10 offset:33024\n"
    "v_mfma_f32_16x16x32_bf16 v[212:215], v[236:239], a[88:91], v[212:215]\n"
    "s_waitcnt lgkmcnt(8)\n"
    "v_mfma_f32_16x16x32_bf16 v[216:219], v[236:239], a[152:155], v[216:219]\n"
    "s_add_u32 m0, s90, 12288\n"
    "s_nop 0\n"
    "buffer_load_dwordx4 v9, s[84:87], s56 offen lds\n"
    "v_mfma_f32_16x16x32_bf16 v[220:223], v[236:239], a[216:219], v[220:223]\n"
    "s_add_u32 s56, s66, 0x70040\n"
    "s_waitcnt vmcnt(42)\n"
    "v_mfma_f32_16x16x32_bf16 v[160:163], v[240:243], a[28:31], v[160:163]\n"
    "ds_read_b64_tr_b16 v[228:229], v11 offset:32768\n"
    "ds_read_b64_tr_b16 v[230:231], v11 offset:33024\n"
    "s_waitcnt vmcnt(41)\n"
    "v_mfma_f32_16x16x32_bf16 v[164:167], v[240:243], a[92:95], v[164:167]\n"
    "s_waitcnt lgkmcnt(8)\n"
    "s_waitcnt vmcnt(40)\n"
    "v_mfma_f32_16x16x32_bf16 v[168:171], v[240:243], a[156:159], v[168:171]\n"
    "s_add_u32 m0, s90, 16384\n"
    "s_nop 0\n"
    "buffer_load_dwordx4 v9, s[48:51], s56 offen lds\n"
    "s_waitcnt vmcnt(40)\n"
    "v_mfma_f32_16x16x32_bf16 v[172:175], v[240:243], a[220:223], v[172:175]\n"
    "v_mfma_f32_16x16x32_bf16 v[176:179], v[244:247], a[28:31], v[176:179]\n"
    "ds_read_b64_tr_b16 v[232:233], v10 offset:36864\n"
    "ds_read_b64_tr_b16 v[234:235], v10 offset:37120\n"
    "v_mfma_f32_16x16x32_bf16 v[180:183], v[244:247], a[92:95], v[180:183]\n"
    "s_waitcnt lgkmcnt(8)\n"
    "v_mfma_f32_16x16x32_bf16 v[184:187], v[244:247], a[156:159], v[184:187]\n"
    "s_add_u32 m0, s90, 20480\n"
    "s_nop 0\n"
    "buffer_load_dwordx4 v9, s[84:87], s56 offen lds\n"
    "v_mfma_f32_16x16x32_bf16 v[188:191], v[244:247], a[220:223], v[188:191]\n"
    "s_add_u32 s56, s66, 0xa8040\n"
    "v_mfma_f32_16x16x32_bf16 v[192:195], v[248:251], a[28:31], v[192:195]\n"
    "ds_read_b64_tr_b16 v[236:237], v11 offset:36864\n"
    "ds_read_b64_tr_b16 v[238:239], v11 offset:37120\n"
    "v_mfma_f32_16x16x32_bf16 v[196:199], v[248:251], a[92:95], v[196:199]\n"
    "s_waitcnt lgkmcnt(8)\n"
    "v_mfma_f32_16x16x32_bf16 v[200:203], v[248:251], a[156:159], v[200:203]\n"
    "s_add_u32 m0, s90, 24576\n"
    "s_nop 0\n"
    "buffer_load_dwordx4 v9, s[48:51], s56 offen lds\n"
    "v_mfma_f32_16x16x32_bf16 v[204:207], v[248:251], a[220:223], v[204:207]\n"
    "v_mfma_f32_16x16x32_bf16 v[208:211], v[252:255], a[28:31], v[208:211]\n"
    "ds_read_b64_tr_b16 v[240:241], v10 offset:34816\n"
    "ds_read_b64_tr_b16 v[242:243], v10 offset:35072\n"
    "v_mfma_f32_16x16x32_bf16 v[212:215], v[252:255], a[92:95], v[212:215]\n"
    "s_waitcnt lgkmcnt(8)\n"
    "v_mfma_f32_16x16x32_bf16 v[216:219], v[252:255], a[156:159], v[216:219]\n"
    "s_add_u32 m0, s90, 28672\n"
    "s_nop 0\n"
    "buffer_load_dwordx4 v9, s[84:87], s56 offen lds\n"
    "v_mfma_f32_16x16x32_bf16 v[220:223], v[252:255], a[220:223], v[220:223]\n"
    "s_add_u32 s56, s66, 0xe0040\n"
    "s_waitcnt vmcnt(42)\n"
    "v_mfma_f32_16x16x32_bf16 v[160:163], v[224:227], a[32:35], v[160:163]\n"
    "ds_read_b64_tr_b16 v[244:245], v11 offset:34816\n"
    "ds_read_b64_tr_b16 v[246:247], v11 offset:35072\n"
    "s_waitcnt vmcnt(40)\n"
    "v_mfma_f32_16x16x32_bf16 v[164:167], v[224:227], a[96:99], v[164:167]\n"
    "s_waitcnt lgkmcnt(8)\n"
    "s_waitcnt vmcnt(38)\n"
    "v_mfma_f32_16x16x32_bf16 v[168:171], v[224:227], a[160:163], v[168:171]\n"
    "s_add_u32 m0, s90, 32768\n"
    "s_nop 0\n"
    "buffer_load_dwordx4 v9, s[48:51], s56 offen lds\n"
    "s_waitcnt vmcnt(38)\n"
    "v_mfma_f32_16x16x32_bf16 v[172:175], v[224:227], a[224:227], v[172:175]\n"
    "v_mfma_f32_16x16x32_bf16 v[176:179], v[228:231], a[32:35], v[176:179]\n"
    "ds_read_b64_tr_b16 v[248:249], v10 offset:38912\n"
    "ds_read_b64_tr_b16 v[250:251], v10 offset:39168\n"
    "v_mfma_f32_16x16x32_bf16 v[180:183], v[228:231], a[96:99], v[180:183]\n"
    "s_waitcnt lgkmcnt(8)\n"
    "v_mfma_f32_16x16x32_bf16 v[184:187], v[228:231], a[160:163], v[184:187]\n"
    "s_add_u32 m0, s90, 36864\n"
    "s_nop 0\n"
    "buffer_load_dwordx4 v9, s[84:87], s56 offen lds\n"
    "v_mfma_f32_16x16x32_bf16 v[188:191], v[228:231], a[224:227], v[188:191]\n"
    "s_add_u32 s56, s66, 0x118040\n"
    "v_mfma_f32_16x16x32_bf16 v[192:195], v[232:235], a[32:35], v[192:195]\n"
    "ds_read_b64_tr_b16 v[252:253], v11 offset:38912\n"
    "ds_read_b64_tr_b16 v[254:255], v11 offset:39168\n"
    "v_mfma_f32_16x16x32_bf16 v[196:199], v[232:235], a[96:99], v[196:199]\n"
    "s_waitcnt lgkmcnt(8)\n"
    "v_mfma_f32_16x16x32_bf16 v[200:203], v[232:235], a[160:163], v[200:203]\n"
    "s_add_u32 m0, s90, 40960\n"
    "s_nop 0\n"
    "buffer_load_dwordx4 v9, s[48:51], s56 offen lds\n"
    "v_mfma_f32_16x16x32_bf16 v[204:207], v[232:235], a[224:227], v[204:207]\n"
    "v_mfma_f32_16x16x32_bf16 v[208:211], v[236:239], a[32:35], v[208:211]\n"
    "ds_read_b64_tr_b16 v[224:225], v10 offset:40960\n"
    "ds_read_b64_tr_b16 v[226:227], v10 offset:41216\n"
    "v_mfma_f32_16x16x32_bf16 v[212:215], v[236:239], a[96:99], v[212:215]\n"
    "s_waitcnt lgkmcnt(8)\n"
    "v_mfma_f32_16x16x32_bf16 v[216:219], v[236:239], a[160:163], v[216:219]\n"
    "s_add_u32 m0, s90, 45056\n"
    "s_nop 0\n"
    "buffer_load_dwordx4 v9, s[84:87], s56 offen lds\n"
    "v_mfma_f32_16x16x32_bf16 v[220:223], v[236:239], a[224:227], v[220:223]\n"
    "s_waitcnt vmcnt(40)\n"
    "v_mfma_f32_16x16x32_bf16 v[160:163], v[240:243], a[36:39], v[160:163]\n"
    "ds_read_b64_tr_b16 v[228:229], v11 offset:40960\n"
    "ds_read_b64_tr_b16 v[230:231], v11 offset:41216\n"
    "s_waitcnt vmcnt(39)\n"
    "v_mfma_f32_16x16x32_bf16 v[164:167], v[240:243], a[100:103], v[164:167]\n"
    "s_waitcnt lgkmcnt(8)\n"
    "s_waitcnt vmcnt(37)\n"
    "v_mfma_f32_16x16x32_bf16 v[168:171], v[240:243], a[164:167], v[168:171]\n"
    "s_add_u32 s58, s63, 0x0\n"
    "buffer_store_dwordx4 v[96:99], v1, s[44:47], s58 offen sc1\n"
    "s_waitcnt vmcnt(37)\n"
    "v_mfma_f32_16x16x32_bf16 v[172:175], v[240:243], a[228:231], v[172:175]\n"
    "v_mfma_f32_16x16x32_bf16 v[176:179], v[244:247], a[36:39], v[176:179]\n"
    "ds_read_b64_tr_b16 v[232:233], v10 offset:45056\n"
    "ds_read_b64_tr_b16 v[234:235], v10 offset:45312\n"
    "v_mfma_f32_16x16x32_bf16 v[180:183], v[244:247], a[100:103], v[180:183]\n"
    "s_waitcnt lgkmcnt(8)\n"
    "s_add_u32 s56, s66, 0x150040\n"
    "v_mfma_f32_16x16x32_bf16 v[184:187], v[244:247], a[164:167], v[184:187]\n"
    "s_add_u32 m0, s90, 49152\n"
    "s_nop 0\n"
    "buffer_load_dwordx4 v9, s[48:51], s56 offen lds\n"
    "v_mfma_f32_16x16x32_bf16 v[188:191], v[244:247], a[228:231], v[188:191]\n"
    "v_mfma_f32_16x16x32_bf16 v[192:195], v[248:251], a[36:39], v[192:195]\n"
    "ds_read_b64_tr_b16 v[236:237], v11 offset:45056\n"
    "ds_read_b64_tr_b16 v[238:239], v11 offset:45312\n"
    "v_mfma_f32_16x16x32_bf16 v[196:199], v[248:251], a[100:103], v[196:199]\n"
    "s_waitcnt lgkmcnt(8)\n"
    "v_mfma_f32_16x16x32_bf16 v[200:203], v[248:251], a[164:167], v[200:203]\n"
    "s_add_u32 s58, s63, 0x400\n"
    "buffer_store_dwordx4 v[100:103], v1, s[44:47], s58 offen sc1\n"
    "v_mfma_f32_16x16x32_bf16 v[204:207], v[248:251], a[228:231], v[204:207]\n"
    "v_mfma_f32_16x16x32_bf16 v[208:211], v[252:255], a[36:39], v[208:211]\n"
    "ds_read_b64_tr_b16 v[240:241], v10 offset:43008\n"
    "ds_read_b64_tr_b16 v[242:243], v10 offset:43264\n"
    "v_mfma_f32_16x16x32_bf16 v[212:215], v[252:255], a[100:103], v[212:215]\n"
    "s_waitcnt lgkmcnt(8)\n"
    "v_mfma_f32_16x16x32_bf16 v[216:219], v[252:255], a[164:167], v[216:219]\n"
    "s_add_u32 m0, s90, 53248\n"
    "s_nop 0\n"
    "buffer_load_dwordx4 v9, s[84:87], s56 offen lds\n"
    "v_mfma_f32_16x16x32_bf16 v[220:223], v[252:255], a[228:231], v[220:223]\n"
    "s_waitcnt vmcnt(39)\n"
    "v_mfma_f32_16x16x32_bf16 v[160:163], v[224:227], a[40:43], v[160:163]\n"
    "ds_read_b64_tr_b16 v[244:245], v11 offset:43008\n"
    "ds_read_b64_tr_b16 v[246:247], v11 offset:43264\n"
    "s_waitcnt vmcnt(38)\n"
    "v_mfma_f32_16x16x32_bf16 v[164:167], v[224:227], a[104:107], v[164:167]\n"
    "s_waitcnt lgkmcnt(8)\n"
    "s_waitcnt vmcnt(37)\n"
    "v_mfma_f32_16x16x32_bf16 v[168:171], v[224:227], a[168:171], v[168:171]\n"
    "s_waitcnt vmcnt(36)\n"
    "v_mfma_f32_16x16x32_bf16 v[172:175], v[224:227], a[232:235], v[172:175]\n"
    "s_add_u32 s56, s66, 0x188040\n"
    "v_mfma_f32_16x16x32_bf16 v[176:179], v[228:231], a[40:43], v[176:179]\n"
    "ds_read_b64_tr_b16 v[248:249], v10 offset:47104\n"
    "ds_read_b64_tr_b16 v[250:251], v10 offset:47360\n"
    "v_mfma_f32_16x16x32_bf16 v[180:183], v[228:231], a[104:107], v[180:183]\n"
    "s_waitcnt lgkmcnt(8)\n"
    "v_mfma_f32_16x16x32_bf16 v[184:187], v[228:231], a[168:171], v[184:187]\n"
    "s_add_u32 m0, s90, 57344\n"
    "s_nop 0\n"
    "buffer_load_dwordx4 v9, s[48:51], s56 offen lds\n"
    "v_mfma_f32_16x16x32_bf16 v[188:191], v[228:231], a[232:235], v[188:191]\n"
    "v_mfma_f32_16x16x32_bf16 v[192:195], v[232:235], a[40:43], v[192:195]\n"
    "ds_read_b64_tr_b16 v[252:253], v11 offset:47104\n"
    "ds_read_b64_tr_b16 v[254:255], v11 offset:47360\n"
    "v_mfma_f32_16x16x32_bf16 v[196:199], v[232:235], a[104:107], v[196:199]\n"
    "s_waitcnt lgkmcnt(8)\n"
    "v_mfma_f32_16x16x32_bf16 v[200:203], v[232:235], a[168:171], v[200:203]\n"
    "s_add_u32 m0, s90, 61440\n"
    "s_nop 0\n"
    "buffer_load_dwordx4 v9, s[84:87], s56 offen lds\n"
    "v_mfma_f32_16x16x32_bf16 v[204:207], v[232:235], a[232:235], v[204:207]\n"
    "v_mfma_f32_16x16x32_bf16 v[208:211], v[236:239], a[40:43], v[208:211]\n"
    "ds_read_b64_tr_b16 v[224:225], v10 offset:49152\n"
    "ds_read_b64_tr_b16 v[226:227], v10 offset:49408\n"
    "v_mfma_f32_16x16x32_bf16 v[212:215], v[236:239], a[104:107], v[212:215]\n"
    "s_waitcnt lgkmcnt(8)\n"
    "v_mfma_f32_16x16x32_bf16 v[216:219], v[236:239], a[168:171], v[216:219]\n"
    "s_add_u32 s58, s63, 0x800\n"
    "buffer_store_dwordx4 v[104:107], v1, s[44:47], s58 offen sc1\n"
    "v_mfma_f32_16x16x32_bf16 v[220:223], v[236:239], a[232:235], v[220:223]\n"
    "s_waitcnt vmcnt(38)\n"
    "v_mfma_f32_16x16x32_bf16 v[160:163], v[240:243], a[44:47], v[160:163]\n"
    "ds_read_b64_tr_b16 v[228:229], v11 offset:49152\n"
    "ds_read_b64_tr_b16 v[230:231], v11 offset:49408\n"
    "s_waitcnt vmcnt(37)\n"
    "v_mfma_f32_16x16x32_bf16 v[164:167], v[240:243], a[108:111], v[164:167]\n"
    "s_waitcnt lgkmcnt(8)\n"
    "s_waitcnt vmcnt(36)\n"
    "v_mfma_f32_16x16x32_bf16 v[168:171], v[240:243], a[172:175], v[168:171]\n"
    "s_waitcnt vmcnt(35)\n"
    "v_mfma_f32_16x16x32_bf16 v[172:175], v[240:243], a[236:239], v[172:175]\n"
    "v_mfma_f32_16x16x32_bf16 v[176:179], v[244:247], a[44:47], v[176:179]\n"
    "ds_read_b64_tr_b16 v[232:233], v10 offset:53248\n"
    "ds_read_b64_tr_b16 v[234:235], v10 offset:53504\n"
    "v_mfma_f32_16x16x32_bf16 v[180:183], v[244:247], a[108:111], v[180:183]\n"
    "s_waitcnt lgkmcnt(8)\n"
    "v_mfma_f32_16x16x32_bf16 v[184:187], v[244:247], a[172:175], v[184:187]\n"
    "v_mfma_f32_16x16x32_bf16 v[188:191], v[244:247], a[236:239], v[188:191]\n"
    "v_mfma_f32_16x16x32_bf16 v[192:195], v[248:251], a[44:47], v[192:195]\n"
    "ds_read_b64_tr_b16 v[236:237], v11 offset:53248\n"
    "ds_read_b64_tr_b16 v[238:239], v11 offset:53504\n"
    "v_mfma_f32_16x16x32_bf16 v[196:199], v[248:251], a[108:111], v[196:199]\n"
    "s_waitcnt lgkmcnt(8)\n"
    "v_mfma_f32_16x16x32_bf16 v[200:203], v[248:251], a[172:175], v[200:203]\n"
    "v_mfma_f32_16x16x32_bf16 v[204:207], v[248:251], a[236:239], v[204:207]\n"
    "v_mfma_f32_16x16x32_bf16 v[208:211], v[252:255], a[44:47], v[208:211]\n"
    "ds_read_b64_tr_b16 v[240:241], v10 offset:51200\n"
    "ds_read_b64_tr_b16 v[242:243], v10 offset:51456\n"
    "v_mfma_f32_16x16x32_bf16 v[212:215], v[252:255], a[108:111], v[212:215]\n"
    "s_waitcnt lgkmcnt(8)\n"
    "v_mfma_f32_16x16x32_bf16 v[216:219], v[252:255], a[172:175], v[216:219]\n"
    "v_mfma_f32_16x16x32_bf16 v[220:223], v[252:255], a[236:239], v[220:223]\n"
    "s_waitcnt vmcnt(34)\n"
    "v_mfma_f32_16x16x32_bf16 v[160:163], v[224:227], a[48:51], v[160:163]\n"
    "ds_read_b64_tr_b16 v[244:245], v11 offset:51200\n"
    "ds_read_b64_tr_b16 v[246:247], v11 offset:51456\n"
    "s_waitcnt vmcnt(33)\n"
    "v_mfma_f32_16x16x32_bf16 v[164:167], v[224:227], a[112:115], v[164:167]\n"
    "s_waitcnt lgkmcnt(8)\n"
    "s_waitcnt vmcnt(32)\n"
    "v_mfma_f32_16x16x32_bf16 v[168:171], v[224:227], a[176:179], v[168:171]\n"
    "s_waitcnt vmcnt(31)\n"
    "v_mfma_f32_16x16x32_bf16 v[172:175], v[224:227], a[240:243], v[172:175]\n"
    "v_mfma_f32_16x16x32_bf16 v[176:179], v[228:231], a[48:51], v[176:179]\n"
    "ds_read_b64_tr_b16 v[248:249], v10 offset:55296\n"
    "ds_read_b64_tr_b16 v[250:251], v10 offset:55552\n"
    "v_mfma_f32_16x16x32_bf16 v[180:183], v[228:231], a[112:115], v[180:183]\n"
    "s_waitcnt lgkmcnt(8)\n"
    "v_mfma_f32_16x16x32_bf16 v[184:187], v[228:231], a[176:179], v[184:187]\n"
    "v_mfma_f32_16x16x32_bf16 v[188:191], v[228:231], a[240:243], v[188:191]\n"
    "s_add_u32 s58, s63, 0xc00\n"
    "buffer_store_dwordx4 v[108:111], v1, s[44:47], s58 offen sc1\n"
    "v_mfma_f32_16x16x32_bf16 v[192:195], v[232:235], a[48:51], v[192:195]\n"
    "ds_read_b64_tr_b16 v[252:253], v11 offset:55296\n"
    "ds_read_b64_tr_b16 v[254:255], v11 offset:55552\n"
    "v_mfma_f32_16x16x32_bf16 v[196:199], v[232:235], a[112:115], v[196:199]\n"
    "s_waitcnt lgkmcnt(8)\n"
    "v_mfma_f32_16x16x32_bf16 v[200:203], v[232:235], a[176:179], v[200:203]\n"
    "v_mfma_f32_16x16x32_bf16 v[204:207], v[232:235], a[240:243], v[204:207]\n"
    "v_mfma_f32_16x16x32_bf16 v[208:211], v[236:239], a[48:51], v[208:211]\n"
    "ds_read_b64_tr_b16 v[224:225], v10 offset:57344\n"
    "ds_read_b64_tr_b16 v[226:227], v10 offset:57600\n"
    "v_mfma_f32_16x16x32_bf16 v[212:215], v[236:239], a[112:115], v[212:215]\n"
    "s_waitcnt lgkmcnt(8)\n"
    "v_mfma_f32_16x16x32_bf16 v[216:219], v[236:239], a[176:179], v[216:219]\n"
    "v_mfma_f32_16x16x32_bf16 v[220:223], v[236:239], a[240:243], v[220:223]\n"
    "s_waitcnt vmcnt(31)\n"
    "v_mfma_f32_16x16x32_bf16 v[160:163], v[240:243], a[52:55], v[160:163]\n"
    "ds_read_b64_tr_b16 v[228:229], v11 offset:57344\n"
    "ds_read_b64_tr_b16 v[230:231], v11 offset:57600\n"
    "s_waitcnt vmcnt(30)\n"
    "v_mfma_f32_16x16x32_bf16 v[164:167], v[240:243], a[116:119], v[164:167]\n"
    "s_waitcnt lgkmcnt(8)\n"
    "s_waitcnt vmcnt(29)\n"
    "v_mfma_f32_16x16x32_bf16 v[168:171], v[240:243], a[180:183], v[168:171]\n"
    "s_waitcnt vmcnt(28)\n"
    "v_mfma_f32_16x16x32_bf16 v[172:175], v[240:243], a[244:247], v[172:175]\n"
    "v_mfma_f32_16x16x32_bf16 v[176:179], v[244:247], a[52:55], v[176:179]\n"
    "ds_read_b64_tr_b16 v[232:233], v10 offset:61440\n"
    "ds_read_b64_tr_b16 v[234:235], v10 offset:61696\n"
    "v_mfma_f32_16x16x32_bf16 v[180:183], v[244:247], a[116:119], v[180:183]\n"
    "s_waitcnt lgkmcnt(8)\n"
    "v_mfma_f32_16x16x32_bf16 v[184:187], v[244:247], a[180:183], v[184:187]\n"
    "v_mfma_f32_16x16x32_bf16 v[188:191], v[244:247], a[244:247], v[188:191]\n"
    "v_mfma_f32_16x16x32_bf16 v[192:195], v[248:251], a[52:55], v[192:195]\n"
    "ds_read_b64_tr_b16 v[236:237], v11 offset:61440\n"
    "ds_read_b64_tr_b16 v[238:239], v11 offset:61696\n"
    "v_mfma_f32_16x16x32_bf16 v[196:199], v[248:251], a[116:119], v[196:199]\n"
    "s_waitcnt lgkmcnt(8)\n"
    "v_mfma_f32_16x16x32_bf16 v[200:203], v[248:251], a[180:183], v[200:203]\n"
    "v_mfma_f32_16x16x32_bf16 v[204:207], v[248:251], a[244:247], v[204:207]\n"
    "v_mfma_f32_16x16x32_bf16 v[208:211], v[252:255], a[52:55], v[208:211]\n"
    "ds_read_b64_tr_b16 v[240:241], v10 offset:59392\n"
    "ds_read_b64_tr_b16 v[242:243], v10 offset:59648\n"
    "v_mfma_f32_16x16x32_bf16 v[212:215], v[252:255], a[116:119], v[212:215]\n"
    "s_waitcnt lgkmcnt(8)\n"
    "v_mfma_f32_16x16x32_bf16 v[216:219], v[252:255], a[180:183], v[216:219]\n"
    "v_mfma_f32_16x16x32_bf16 v[220:223], v[252:255], a[244:247], v[220:223]\n"
    "s_waitcnt vmcnt(27)\n"
    "v_mfma_f32_16x16x32_bf16 v[160:163], v[224:227], a[56:59], v[160:163]\n"
    "ds_read_b64_tr_b16 v[244:245], v11 offset:59392\n"
    "ds_read_b64_tr_b16 v[246:247], v11 offset:59648\n"
    "s_waitcnt vmcnt(25)\n"
    "v_mfma_f32_16x16x32_bf16 v[164:167], v[224:227], a[120:123], v[164:167]\n"
    "s_waitcnt lgkmcnt(8)\n"
    "s_waitcnt vmcnt(24)\n"
    "v_mfma_f32_16x16x32_bf16 v[168:171], v[224:227], a[184:187], v[168:171]\n"
    "s_waitcnt vmcnt(22)\n"
    "v_mfma_f32_16x16x32_bf16 v[172:175], v[224:227], a[248:251], v[172:175]\n"
    "v_mfma_f32_16x16x32_bf16 v[176:179], v[228:231], a[56:59], v[176:179]\n"
    "ds_read_b64_tr_b16 v[248:249], v10 offset:63488\n"
    "ds_read_b64_tr_b16 v[250:251], v10 offset:63744\n"
    "v_mfma_f32_16x16x32_bf16 v[180:183], v[228:231], a[120:123], v[180:183]\n"
    "s_waitcnt lgkmcnt(8)\n"
    "v_mfma_f32_16x16x32_bf16 v[184:187], v[228:231], a[184:187], v[184:187]\n"
    "v_mfma_f32_16x16x32_bf16 v[188:191], v[228:231], a[248:251], v[188:191]\n"
    "s_add_u32 s58, s63, 0x1000\n"
    "buffer_store_dwordx4 v[112:115], v1, s[44:47], s58 offen sc1\n"
    "v_mfma_f32_16x16x32_bf16 v[192:195], v[232:235], a[56:59], v[192:195]\n"
    "ds_read_b64_tr_b16 v[252:253], v11 offset:63488\n"
    "ds_read_b64_tr_b16 v[254:255], v11 offset:63744\n"
    "v_mfma_f32_16x16x32_bf16 v[196:199], v[232:235], a[120:123], v[196:199]\n"
    "s_waitcnt lgkmcnt(8)\n"
    "v_mfma_f32_16x16x32_bf16 v[200:203], v[232:235], a[184:187], v[200:203]\n"
    "v_mfma_f32_16x16x32_bf16 v[204:207], v[232:235], a[248:251], v[204:207]\n"
    "v_xor_b32 v10, 0x10000, v10\n"
    "v_xor_b32 v11, 0x10000, v11\n"
    "s_waitcnt vmcnt(3)\n"
    "s_barrier\n"
    "v_mfma_f32_16x16x32_bf16 v[208:211], v[236:239], a[56:59], v[208:211]\n"
    "ds_read_b64_tr_b16 v[224:225], v10 offset:0\n"
    "ds_read_b64_tr_b16 v[226:227], v10 offset:256\n"
    "v_mfma_f32_16x16x32_bf16 v[212:215], v[236:239], a[120:123], v[212:215]\n"
    "s_waitcnt lgkmcnt(8)\n"
    "s_add_u32 s56, s61, 0x0\n"
    "v_mfma_f32_16x16x32_bf16 v[216:219], v[236:239], a[184:187], v[216:219]\n"
    "s_add_u32 m0, s60, 65536\n"
    "s_nop 0\n"
    "buffer_load_dwordx4 v2, s[36:39], s56 offen lds\n"
    "v_mfma_f32_16x16x32_bf16 v[220:223], v[236:239], a[248:251], v[220:223]\n"
    "v_mfma_f32_16x16x32_bf16 v[160:163], v[240:243], a[60:63], v[160:163]\n"
    "ds_read_b64_tr_b16 v[228:229], v11 offset:0\n"
    "ds_read_b64_tr_b16 v[230:231], v11 offset:256\n"
    "v_mfma_f32_16x16x32_bf16 v[164:167], v[240:243], a[124:127], v[164:167]\n"
    "s_waitcnt lgkmcnt(8)\n"
    "v_mfma_f32_16x16x32_bf16 v[168:171], v[240:243], a[188:191], v[168:171]\n"
    "v_mfma_f32_16x16x32_bf16 v[172:175], v[240:243], a[252:255], v[172:175]\n"
    "v_mfma_f32_16x16x32_bf16 v[176:179], v[244:247], a[60:63], v[176:179]\n"
    "ds_read_b64_tr_b16 v[232:233], v10 offset:4096\n"
    "ds_read_b64_tr_b16 v[234:235], v10 offset:4352\n"
    "v_mfma_f32_16x16x32_bf16 v[180:183], v[244:247], a[124:127], v[180:183]\n"
    "s_waitcnt lgkmcnt(8)\n"
    "v_mfma_f32_16x16x32_bf16 v[184:187], v[244:247], a[188:191], v[184:187]\n"
    "v_mfma_f32_16x16x32_bf16 v[188:191], v[244:247], a[252:255], v[188:191]\n"
    "v_mfma_f32_16x16x32_bf16 v[192:195], v[248:251], a[60:63], v[192:195]\n"
    "ds_read_b64_tr_b16 v[236:237], v11 offset:4096\n"
    "ds_read_b64_tr_b16 v[238:239], v11 offset:4352\n"
    "v_mfma_f32_16x16x32_bf16 v[196:199], v[248:251], a[124:127], v[196:199]\n"
    "s_waitcnt lgkmcnt(8)\n"
    "v_mfma_f32_16x16x32_bf16 v[200:203], v[248:251], a[188:191], v[200:203]\n"
    "s_add_u32 m0, s60, 66560\n"
    "s_nop 0\n"
    "buffer_load_dwordx4 v3, s[36:39], s56 offen lds\n"
    "v_mfma_f32_16x16x32_bf16 v[204:207], v[248:251], a[252:255], v[204:207]\n"
    "v_mfma_f32_16x16x32_bf16 v[208:211], v[252:255], a[60:63], v[208:211]\n"
    "ds_read_b64_tr_b16 v[240:241], v10 offset:2048\n"
    "ds_read_b64_tr_b16 v[242:243], v10 offset:2304\n"
    "v_mfma_f32_16x16x32_bf16 v[212:215], v[252:255], a[124:127], v[212:215]\n"
    "s_waitcnt lgkmcnt(8)\n"
    "v_mfma_f32_16x16x32_bf16 v[216:219], v[252:255], a[188:191], v[216:219]\n"
    "v_mfma_f32_16x16x32_bf16 v[220:223], v[252:255], a[252:255], v[220:223]\n"
    "v_mfma_f32_16x16x32_bf16 v[32:35], v[224:227], a[0:3], 0\n"
    "ds_read_b64_tr_b16 v[244:245], v11 offset:2048\n"
    "ds_read_b64_tr_b16 v[246:247], v11 offset:2304\n"
    "v_mfma_f32_16x16x32_bf16 v[36:39], v[224:227], a[64:67], 0\n"
    "s_waitcnt lgkmcnt(8)\n"
    "v_mfma_f32_16x16x32_bf16 v[40:43], v[224:227], a[128:131], 0\n"
    "v_mul_f32 v25, 0xbfb8aa3b, v160\n"
    "v_mul_f32 v26, 0xbfb8aa3b, v161\n"
    "v_mfma_f32_16x16x32_bf16 v[44:47], v[224:227], a[192:195], 0\n"
    "v_mul_f32 v27, 0xbfb8aa3b, v162\n"
    "v_mul_f32 v28, 0xbfb8aa3b, v163\n"
    "v_mfma_f32_16x16x32_bf16 v[48:51], v[228:231], a[0:3], 0\n"
    "ds_read_b64_tr_b16 v[248:249], v10 offset:6144\n"
    "ds_read_b64_tr_b16 v[250:251], v10 offset:6400\n"
    "v_mfma_f32_16x16x32_bf16 v[52:55], v[228:231], a[64:67], 0\n"
    "s_waitcnt lgkmcnt(8)\n"
    "v_exp_f32 v25, v25\n"
    "v_mfma_f32_16x16x32_bf16 v[56:59], v[228:231], a[128:131], 0\n"
    "s_add_u32 s58, s63, 0x1400\n"
    "buffer_store_dwordx4 v[116:119], v1, s[44:47], s58 offen sc1\n"
    "v_mfma_f32_16x16x32_bf16 v[60:63], v[228:231], a[192:195], 0\n"
    "s_add_u32 s56, s61, 0x80000\n"
    "v_exp_f32 v26, v26\n"
    "v_mfma_f32_16x16x32_bf16 v[64:67], v[232:235], a[0:3], 0\n"
    "ds_read_b64_tr_b16 v[252:253], v11 offset:6144\n"
    "ds_read_b64_tr_b16 v[254:255], v11 offset:6400\n"
    "v_mfma_f32_16x16x32_bf16 v[68:71], v[232:235], a[64:67], 0\n"
    "s_waitcnt lgkmcnt(8)\n"
    "v_exp_f32 v27, v27\n"
    "v_mfma_f32_16x16x32_bf16 v[72:75], v[232:235], a[128:131], 0\n"
    "s_add_u32 m0, s60, 73728\n"
    "s_nop 0\n"
    "buffer_load_dwordx4 v2, s[36:39], s56 offen lds\n"
    "v_mfma_f32_16x16x32_bf16 v[76:79], v[232:235], a[192:195], 0\n"
    "v_exp_f32 v28, v28\n"
    "v_mfma_f32_16x16x32_bf16 v[80:83], v[236:239], a[0:3], 0\n"
    "ds_read_b64_tr_b16 v[224:225], v10 offset:8192\n"
    "ds_read_b64_tr_b16 v[226:227], v10 offset:8448\n"
    "v_mfma_f32_16x16x32_bf16 v[84:87], v[236:239], a[64:67], 0\n"
    "s_waitcnt lgkmcnt(8)\n"
    "v_mul_f32 v160, v160, v192\n"
    "v_mul_f32 v161, v161, v193\n"
    "v_mfma_f32_16x16x32_bf16 v[88:91], v[236:239], a[128:131], 0\n"
    "v_mul_f32 v162, v162, v194\n"
    "v_mul_f32 v163, v163, v195\n"
    "v_mfma_f32_16x16x32_bf16 v[92:95], v[236:239], a[192:195], 0\n"
    "v_add_f32 v25, 1.0, v25\n"
    "v_mfma_f32_16x16x32_bf16 v[32:35], v[240:243], a[4:7], v[32:35]\n"
    "ds_read_b64_tr_b16 v[228:229], v11 offset:8192\n"
    "ds_read_b64_tr_b16 v[230:231], v11 offset:8448\n"
    "v_mfma_f32_16x16x32_bf16 v[36:39], v[240:243], a[68:71], v[36:39]\n"
    "s_waitcnt lgkmcnt(8)\n"
    "v_add_f32 v26, 1.0, v26\n"
    "v_add_f32 v27, 1.0, v27\n"
    "v_mfma_f32_16x16x32_bf16 v[40:43], v[240:243], a[132:135], v[40:43]\n"
    "s_add_u32 m0, s60, 74752\n"
    "s_nop 0\n"
    "buffer_load_dwordx4 v3, s[36:39], s56 offen lds\n"
    "v_add_f32 v28, 1.0, v28\n"
    "v_mfma_f32_16x16x32_bf16 v[44:47], v[240:243], a[196:199], v[44:47]\n"
    "v_rcp_f32 v25, v25\n"
    "v_mfma_f32_16x16x32_bf16 v[48:51], v[244:247], a[4:7], v[48:51]\n"
    "ds_read_b64_tr_b16 v[232:233], v10 offset:12288\n"
    "ds_read_b64_tr_b16 v[234:235], v10 offset:12544\n"
    "v_mfma_f32_16x16x32_bf16 v[52:55], v[244:247], a[68:71], v[52:55]\n"
    "s_waitcnt lgkmcnt(8)\n"
    "v_rcp_f32 v26, v26\n"
    "v_mfma_f32_16x16x32_bf16 v[56:59], v[244:247], a[132:135], v[56:59]\n"
    "v_rcp_f32 v27, v27\n"
    "v_mfma_f32_16x16x32_bf16 v[60:63], v[244:247], a[196:199], v[60:63]\n"
    "v_rcp_f32 v28, v28\n"
    "v_mfma_f32_16x16x32_bf16 v[64:67], v[248:251], a[4:7], v[64:67]\n"
    "ds_read_b64_tr_b16 v[236:237], v11 offset:12288\n"
    "ds_read_b64_tr_b16 v[238:239], v11 offset:12544\n"
    "v_mfma_f32_16x16x32_bf16 v[68:71], v[248:251], a[68:71], v[68:71]\n"
    "s_waitcnt lgkmcnt(8)\n"
    "v_mul_f32 v160, v160, v25\n"
    "v_mul_f32 v161, v161, v26\n"
    "v_mfma_f32_16x16x32_bf16 v[72:75], v[248:251], a[132:135], v[72:75]\n"
    "v_mul_f32 v162, v162, v27\n"
    "v_mul_f32 v163, v163, v28\n"
    "v_mfma_f32_16x16x32_bf16 v[76:79], v[248:251], a[196:199], v[76:79]\n"
    "v_cvt_pk_bf16_f32 v16, v160, v161\n"
    "v_mfma_f32_16x16x32_bf16 v[80:83], v[252:255], a[4:7], v[80:83]\n"
    "ds_read_b64_tr_b16 v[240:241], v10 offset:10240\n"
    "ds_read_b64_tr_b16 v[242:243], v10 offset:10496\n"
    "v_mfma_f32_16x16x32_bf16 v[84:87], v[252:255], a[68:71], v[84:87]\n"
    "s_waitcnt lgkmcnt(8)\n"
    "v_cvt_pk_bf16_f32 v17, v162, v163\n"
    "v_mul_f32 v25, 0xbfb8aa3b, v176\n"
    "v_mfma_f32_16x16x32_bf16 v[88:91], v[252:255], a[132:135], v[88:91]\n"
    "s_add_u32 s56, s61, 0x100000\n"
    "v_mul_f32 v26, 0xbfb8aa3b, v177\n"
    "v_mul_f32 v27, 0xbfb8aa3b, v178\n"
    "v_mfma_f32_16x16x32_bf16 v[92:95], v[252:255], a[196:199], v[92:95]\n"
    "s_add_u32 m0, s60, 81920\n"
    "s_nop 0\n"
    "buffer_load_dwordx4 v2, s[36:39], s56 offen lds\n"
    "v_mul_f32 v28, 0xbfb8aa3b, v179\n"
    "v_mfma_f32_16x16x32_bf16 v[32:35], v[224:227], a[8:11], v[32:35]\n"
    "ds_read_b64_tr_b16 v[244:245], v11 offset:10240\n"
    "ds_read_b64_tr_b16 v[246:247], v11 offset:10496\n"
    "v_mfma_f32_16x16x32_bf16 v[36:39], v[224:227], a[72:75], v[36:39]\n"
    "s_waitcnt lgkmcnt(8)\n"
    "v_exp_f32 v25, v25\n"
    "v_mfma_f32_16x16x32_bf16 v[40:43], v[224:227], a[136:139], v[40:43]\n"
    "v_exp_f32 v26, v26\n"
    "v_mfma_f32_16x16x32_bf16 v[44:47], v[224:227], a[200:203], v[44:47]\n"
    "v_exp_f32 v27, v27\n"
    "v_mfma_f32_16x16x32_bf16 v[48:51], v[228:231], a[8:11], v[48:51]\n"
    "ds_read_b64_tr_b16 v[248:249], v10 offset:14336\n"
    "ds_read_b64_tr_b16 v[250:251], v10 offset:14592\n"
    "v_mfma_f32_16x16x32_bf16 v[52:55], v[228:231], a[72:75], v[52:55]\n"
    "s_waitcnt lgkmcnt(8)\n"
    "v_exp_f32 v28, v28\n"
    "v_mfma_f32_16x16x32_bf16 v[56:59], v[228:231], a[136:139], v[56:59]\n"
    "s_add_u32 s58, s63, 0x1800\n"
    "buffer_store_dwordx4 v[120:123], v1, s[44:47], s58 offen sc1\n"
    "v_mul_f32 v176, v176, v208\n"
    "v_mfma_f32_16x16x32_bf16 v[60:63], v[228:231], a[200:203], v[60:63]\n"
    "v_mul_f32 v177, v177, v209\n"
    "v_mul_f32 v178, v178, v210\n"
    "v_mfma_f32_16x16x32_bf16 v[64:67], v[232:235], a[8:11], v[64:67]\n"
    "ds_read_b64_tr_b16 v[252:253], v11 offset:14336\n"
    "ds_read_b64_tr_b16 v[254:255], v11 offset:14592\n"
    "v_mfma_f32_16x16x32_bf16 v[68:71], v[232:235], a[72:75], v[68:71]\n"
    "s_waitcnt lgkmcnt(8)\n"
    "v_mul_f32 v179, v179, v211\n"
    "v_add_f32 v25, 1.0, v25\n"
    "v_mfma_f32_16x16x32_bf16 v[72:75], v[232:235], a[136:139], v[72:75]\n"
    "s_add_u32 m0, s60, 82944\n"
    "s_nop 0\n"
    "buffer_load_dwordx4 v3, s[36:39], s56 offen lds\n"
    "v_add_f32 v26, 1.0, v26\n"
    "v_mfma_f32_16x16x32_bf16 v[76:79], v[232:235], a[200:203], v[76:79]\n"
    "v_add_f32 v27, 1.0, v27\n"
    "v_add_f32 v28, 1.0, v28\n"
    "v_mfma_f32_16x16x32_bf16 v[80:83], v[236:239], a[8:11], v[80:83]\n"
    "ds_read_b64_tr_b16 v[224:225], v10 offset:16384\n"
    "ds_read_b64_tr_b16 v[226:227], v10 offset:16640\n"
    "v_mfma_f32_16x16x32_bf16 v[84:87], v[236:239], a[72:75], v[84:87]\n"
    "s_waitcnt lgkmcnt(8)\n"
    "v_rcp_f32 v25, v25\n"
    "v_mfma_f32_16x16x32_bf16 v[88:91], v[236:239], a[136:139], v[88:91]\n"
    "v_rcp_f32 v26, v26\n"
    "v_mfma_f32_16x16x32_bf16 v[92:95], v[236:239], a[200:203], v[92:95]\n"
    "v_rcp_f32 v27, v27\n"
    "v_mfma_f32_16x16x32_bf16 v[32:35], v[240:243], a[12:15], v[32:35]\n"
    "ds_read_b64_tr_b16 v[228:229], v11 offset:16384\n"
    "ds_read_b64_tr_b16 v[230:231], v11 offset:16640\n"
    "v_mfma_f32_16x16x32_bf16 v[36:39], v[240:243], a[76:79], v[36:39]\n"
    "s_waitcnt lgkmcnt(8)\n"
    "v_rcp_f32 v28, v28\n"
    "v_mfma_f32_16x16x32_bf16 v[40:43], v[240:243], a[140:143], v[40:43]\n"
    "v_mul_f32 v176, v176, v25\n"
    "v_mul_f32 v177, v177, v26\n"
    "v_mfma_f32_16x16x32_bf16 v[44:47], v[240:243], a[204:207], v[44:47]\n"
    "v_mul_f32 v178, v178, v27\n"
    "v_mul_f32 v179, v179, v28\n"
    "v_mfma_f32_16x16x32_bf16 v[48:51], v[244:247], a[12:15], v[48:51]\n"
    "ds_read_b64_tr_b16 v[232:233], v10 offset:20480\n"
    "ds_read_b64_tr_b16 v[234:235], v10 offset:20736\n"
    "v_mfma_f32_16x16x32_bf16 v[52:55], v[244:247], a[76:79], v[52:55]\n"
    "s_waitcnt lgkmcnt(8)\n"
    "v_cvt_pk_bf16_f32 v18, v176, v177\n"
    "v_cvt_pk_bf16_f32 v19, v178, v179\n"
    "v_mfma_f32_16x16x32_bf16 v[56:59], v[244:247], a[140:143], v[56:59]\n"
    "s_add_u32 s58, s67, 0x0\n"
    "buffer_store_dwordx4 v[16:19], v1, s[44:47], s58 offen sc1\n"
    "v_mul_f32 v25, 0xbfb8aa3b, v164\n"
    "v_mfma_f32_16x16x32_bf16 v[60:63], v[244:247], a[204:207], v[60:63]\n"
    "s_add_u32 s56, s61, 0x180000\n"
    "v_mul_f32 v26, 0xbfb8aa3b, v165\n"
    "v_mul_f32 v27, 0xbfb8aa3b, v166\n"
    "v_mfma_f32_16x16x32_bf16 v[64:67], v[248:251], a[12:15], v[64:67]\n"
    "ds_read_b64_tr_b16 v[236:237], v11 offset:20480\n"
    "ds_read_b64_tr_b16 v[238:239], v11 offset:20736\n"
    "v_mfma_f32_16x16x32_bf16 v[68:71], v[248:251], a[76:79], v[68:71]\n"
    "s_waitcnt lgkmcnt(8)\n"
    "v_mul_f32 v28, 0xbfb8aa3b, v167\n"
    "v_mfma_f32_16x16x32_bf16 v[72:75], v[248:251], a[140:143], v[72:75]\n"
    "s_add_u32 m0, s60, 90112\n"
    "s_nop 0\n"
    "buffer_load_dwordx4 v2, s[36:39], s56 offen lds\n"
    "v_mfma_f32_16x16x32_bf16 v[76:79], v[248:251], a[204:207], v[76:79]\n"
    "v_exp_f32 v25, v25\n"
    "v_mfma_f32_16x16x32_bf16 v[80:83], v[252:255], a[12:15], v[80:83]\n"
    "ds_read_b64_tr_b16 v[240:241], v10 offset:18432\n"
    "ds_read_b64_tr_b16 v[242:243], v10 offset:18688\n"
    "v_mfma_f32_16x16x32_bf16 v[84:87], v[252:255], a[76:79], v[84:87]\n"
    "s_waitcnt lgkmcnt(8)\n"
    "v_exp_f32 v26, v26\n"
    "v_mfma_f32_16x16x32_bf16 v[88:91], v[252:255], a[140:143], v[88:91]\n"
    "v_exp_f32 v27, v27\n"
    "v_mfma_f32_16x16x32_bf16 v[92:95], v[252:255], a[204:207], v[92:95]\n"
    "v_exp_f32 v28, v28\n"
    "v_mfma_f32_16x16x32_bf16 v[32:35], v[224:227], a[16:19], v[32:35]\n"
    "ds_read_b64_tr_b16 v[244:245], v11 offset:18432\n"
    "ds_read_b64_tr_b16 v[246:247], v11 offset:18688\n"
    "v_mfma_f32_16x16x32_bf16 v[36:39], v[224:227], a[80:83], v[36:39]\n"
    "s_waitcnt lgkmcnt(8)\n"
    "v_mul_f32 v164, v164, v196\n"
    "v_mul_f32 v165, v165, v197\n"
    "v_mfma_f32_16x16x32_bf16 v[40:43], v[224:227], a[144:147], v[40:43]\n"
    "v_mul_f32 v166, v166, v198\n"
    "v_mul_f32 v167, v167, v199\n"
    "v_mfma_f32_16x16x32_bf16 v[44:47], v[224:227], a[208:211], v[44:47]\n"
    "s_add_u32 m0, s60, 91136\n"
    "s_nop 0\n"
    "buffer_load_dwordx4 v3, s[36:39], s56 offen lds\n"
    "v_add_f32 v25, 1.0, v25\n"
    "v_mfma_f32_16x16x32_bf16 v[48:51], v[228:231], a[16:19], v[48:51]\n"
    "ds_read_b64_tr_b16 v[248:249], v10 offset:22528\n"
    "ds_read_b64_tr_b16 v[250:251], v10 offset:22784\n"
    "v_mfma_f32_16x16x32_bf16 v[52:55], v[228:231], a[80:83], v[52:55]\n"
    "s_waitcnt lgkmcnt(8)\n"
    "v_add_f32 v26, 1.0, v26\n"
    "v_add_f32 v27, 1.0, v27\n"
    "v_mfma_f32_16x16x32_bf16 v[56:59], v[228:231], a[144:147], v[56:59]\n"
    "v_add_f32 v28, 1.0, v28\n"
    "v_mfma_f32_16x16x32_bf16 v[60:63], v[228:231], a[208:211], v[60:63]\n"
    "s_add_u32 s58, s63, 0x1c00\n"
    "buffer_store_dwordx4 v[124:127], v1, s[44:47], s58 offen sc1\n"
    "v_mfma_f32_16x16x32_bf16 v[64:67], v[232:235], a[16:19], v[64:67]\n"
    "ds_read_b64_tr_b16 v[252:253], v11 offset:22528\n"
    "ds_read_b64_tr_b16 v[254:255], v11 offset:22784\n"
    "v_mfma_f32_16x16x32_bf16 v[68:71], v[232:235], a[80:83], v[68:71]\n"
    "s_waitcnt lgkmcnt(8)\n"
    "v_rcp_f32 v25, v25\n"
    "v_mfma_f32_16x16x32_bf16 v[72:75], v[232:235], a[144:147], v[72:75]\n"
    "v_rcp_f32 v26, v26\n"
    "v_mfma_f32_16x16x32_bf16 v[76:79], v[232:235], a[208:211], v[76:79]\n"
    "v_rcp_f32 v27, v27\n"
    "v_mfma_f32_16x16x32_bf16 v[80:83], v[236:239], a[16:19], v[80:83]\n"
    "ds_read_b64_tr_b16 v[224:225], v10 offset:24576\n"
    "ds_read_b64_tr_b16 v[226:227], v10 offset:24832\n"
    "v_mfma_f32_16x16x32_bf16 v[84:87], v[236:239], a[80:83], v[84:87]\n"
    "s_waitcnt lgkmcnt(8)\n"
    "v_rcp_f32 v28, v28\n"
    "v_mfma_f32_16x16x32_bf16 v[88:91], v[236:239], a[144:147], v[88:91]\n"
    "s_add_u32 s56, s61, 0x200000\n"
    "v_mul_f32 v164, v164, v25\n"
    "v_mul_f32 v165, v165, v26\n"
    "v_mfma_f32_16x16x32_bf16 v[92:95], v[236:239], a[208:211], v[92:95]\n"
    "s_add_u32 m0, s60, 98304\n"
    "s_nop 0\n"
    "buffer_load_dwordx4 v2, s[36:39], s56 offen lds\n"
    "v_mul_f32 v166, v166, v27\n"
    "v_mfma_f32_16x16x32_bf16 v[32:35], v[240:243], a[20:23], v[32:35]\n"
    "ds_read_b64_tr_b16 v[228:229], v11 offset:24576\n"
    "ds_read_b64_tr_b16 v[230:231], v11 offset:24832\n"
    "v_mfma_f32_16x16x32_bf16 v[36:39], v[240:243], a[84:87], v[36:39]\n"
    "s_waitcnt lgkmcnt(8)\n"
    "v_mul_f32 v167, v167, v28\n"
    "v_cvt_pk_bf16_f32 v20, v164, v165\n"
    "v_mfma_f32_16x16x32_bf16 v[40:43], v[240:243], a[148:151], v[40:43]\n"
    "v_cvt_pk_bf16_f32 v21, v166, v167\n"
    "v_mul_f32 v25, 0xbfb8aa3b, v180\n"
    "v_mfma_f32_16x16x32_bf16 v[44:47], v[240:243], a[212:215], v[44:47]\n"
    "v_mul_f32 v26, 0xbfb8aa3b, v181\n"
    "v_mfma_f32_16x16x32_bf16 v[48:51], v[244:247], a[20:23], v[48:51]\n"
    "ds_read_b64_tr_b16 v[232:233], v10 offset:28672\n"
    "ds_read_b64_tr_b16 v[234:235], v10 offset:28928\n"
    "v_mfma_f32_16x16x32_bf16 v[52:55], v[244:247], a[84:87], v[52:55]\n"
    "s_waitcnt lgkmcnt(8)\n"
    "v_mul_f32 v27, 0xbfb8aa3b, v182\n"
    "v_mul_f32 v28, 0xbfb8aa3b, v183\n"
    "v_mfma_f32_16x16x32_bf16 v[56:59], v[244:247], a[148:151], v[56:59]\n"
    "v_exp_f32 v25, v25\n"
    "v_mfma_f32_16x16x32_bf16 v[60:63], v[244:247], a[212:215], v[60:63]\n"
    "v_exp_f32 v26, v26\n"
    "v_mfma_f32_16x16x32_bf16 v[64:67], v[248:251], a[20:23], v[64:67]\n"
    "ds_read_b64_tr_b16 v[236:237], v11 offset:28672\n"
    "ds_read_b64_tr_b16 v[238:239], v11 offset:28928\n"
    "v_mfma_f32_16x16x32_bf16 v[68:71], v[248:251], a[84:87], v[68:71]\n"
    "s_waitcnt lgkmcnt(8)\n"
    "v_exp_f32 v27, v27\n"
    "v_mfma_f32_16x16x32_bf16 v[72:75], v[248:251], a[148:151], v[72:75]\n"
    "v_exp_f32 v28, v28\n"
    "v_mfma_f32_16x16x32_bf16 v[76:79], v[248:251], a[212:215], v[76:79]\n"
    "s_add_u32 m0, s60, 99328\n"
    "s_nop 0\n"
    "buffer_load_dwordx4 v3, s[36:39], s56 offen lds\n"
    "v_mul_f32 v180, v180, v212\n"
    "v_mfma_f32_16x16x32_bf16 v[80:83], v[252:255], a[20:23], v[80:83]\n"
    "ds_read_b64_tr_b16 v[240:241], v10 offset:26624\n"
    "ds_read_b64_tr_b16 v[242:243], v10 offset:26880\n"
    "v_mfma_f32_16x16x32_bf16 v[84:87], v[252:255], a[84:87], v[84:87]\n"
    "s_waitcnt lgkmcnt(8)\n"
    "v_mul_f32 v181, v181, v213\n"
    "v_mul_f32 v182, v182, v214\n"
    "v_mfma_f32_16x16x32_bf16 v[88:91], v[252:255], a[148:151], v[88:91]\n"
    "v_mul_f32 v183, v183, v215\n"
    "v_add_f32 v25, 1.0, v25\n"
    "v_mfma_f32_16x16x32_bf16 v[92:95], v[252:255], a[212:215], v[92:95]\n"
    "v_add_f32 v26, 1.0, v26\n"
    "v_mfma_f32_16x16x32_bf16 v[32:35], v[224:227], a[24:27], v[32:35]\n"
    "ds_read_b64_tr_b16 v[244:245], v11 offset:26624\n"
    "ds_read_b64_tr_b16 v[246:247], v11 offset:26880\n"
    "v_mfma_f32_16x16x32_bf16 v[36:39], v[224:227], a[88:91], v[36:39]\n"
    "s_waitcnt lgkmcnt(8)\n"
    "v_add_f32 v27, 1.0, v27\n"
    "v_add_f32 v28, 1.0, v28\n"
    "v_mfma_f32_16x16x32_bf16 v[40:43], v[224:227], a[152:155], v[40:43]\n"
    "v_rcp_f32 v25, v25\n"
    "v_mfma_f32_16x16x32_bf16 v[44:47], v[224:227], a[216:219], v[44:47]\n"
    "v_rcp_f32 v26, v26\n"
    "v_mfma_f32_16x16x32_bf16 v[48:51], v[228:231], a[24:27], v[48:51]\n"
    "ds_read_b64_tr_b16 v[248:249], v10 offset:30720\n"
    "ds_read_b64_tr_b16 v[250:251], v10 offset:30976\n"
    "v_mfma_f32_16x16x32_bf16 v[52:55], v[228:231], a[88:91], v[52:55]\n"
    "s_waitcnt lgkmcnt(8)\n"
    "v_rcp_f32 v27, v27\n"
    "v_mfma_f32_16x16x32_bf16 v[56:59], v[228:231], a[152:155], v[56:59]\n"
    "s_add_u32 s58, s63, 0x2000\n"
    "buffer_store_dwordx4 v[128:131], v1, s[44:47], s58 offen sc1\n"
    "v_mfma_f32_16x16x32_bf16 v[60:63], v[228:231], a[216:219], v[60:63]\n"
    "s_add_u32 s56, s61, 0x280000\n"
    "v_rcp_f32 v28, v28\n"
    "v_mfma_f32_16x16x32_bf16 v[64:67], v[232:235], a[24:27], v[64:67]\n"
    "ds_read_b64_tr_b16 v[252:253], v11 offset:30720\n"
    "ds_read_b64_tr_b16 v[254:255], v11 offset:30976\n"
    "v_mfma_f32_16x16x32_bf16 v[68:71], v[232:235], a[88:91], v[68:71]\n"
    "s_waitcnt lgkmcnt(8)\n"
    "v_mul_f32 v180, v180, v25\n"
    "v_mul_f32 v181, v181, v26\n"
    "v_mfma_f32_16x16x32_bf16 v[72:75], v[232:235], a[152:155], v[72:75]\n"
    "s_add_u32 m0, s60, 106496\n"
    "s_nop 0\n"
    "buffer_load_dwordx4 v2, s[36:39], s56 offen lds\n"
    "v_mul_f32 v182, v182, v27\n"
    "v_mfma_f32_16x16x32_bf16 v[76:79], v[232:235], a[216:219], v[76:79]\n"
    "v_mul_f32 v183, v183, v28\n"
    "v_cvt_pk_bf16_f32 v22, v180, v181\n"
    "v_mfma_f32_16x16x32_bf16 v[80:83], v[236:239], a[24:27], v[80:83]\n"
    "ds_read_b64_tr_b16 v[224:225], v10 offset:32768\n"
    "ds_read_b64_tr_b16 v[226:227], v10 offset:33024\n"
    "v_mfma_f32_16x16x32_bf16 v[84:87], v[236:239], a[88:91], v[84:87]\n"
    "s_waitcnt lgkmcnt(8)\n"
    "v_cvt_pk_bf16_f32 v23, v182, v183\n"
    "v_mfma_f32_16x16x32_bf16 v[88:91], v[236:239], a[152:155], v[88:91]\n"
    "s_add_u32 s58, s67, 0xe000\n"
    "buffer_store_dwordx4 v[20:23], v1, s[44:47], s58 offen sc1\n"
    "v_mul_f32 v25, 0xbfb8aa3b, v168\n"
    "v_mfma_f32_16x16x32_bf16 v[92:95], v[236:239], a[216:219], v[92:95]\n"
    "v_mul_f32 v26, 0xbfb8aa3b, v169\n"
    "v_mul_f32 v27, 0xbfb8aa3b, v170\n"
    "v_mfma_f32_16x16x32_bf16 v[32:35], v[240:243], a[28:31], v[32:35]\n"
    "ds_read_b64_tr_b16 v[228:229], v11 offset:32768\n"
    "ds_read_b64_tr_b16 v[230:231], v11 offset:33024\n"
    "v_mfma_f32_16x16x32_bf16 v[36:39], v[240:243], a[92:95], v[36:39]\n"
    "s_waitcnt lgkmcnt(8)\n"
    "v_mul_f32 v28, 0xbfb8aa3b, v171\n"
    "v_mfma_f32_16x16x32_bf16 v[40:43], v[240:243], a[156:159], v[40:43]\n"
    "v_exp_f32 v25, v25\n"
    "v_mfma_f32_16x16x32_bf16 v[44:47], v[240:243], a[220:223], v[44:47]\n"
    "v_exp_f32 v26, v26\n"
    "v_mfma_f32_16x16x32_bf16 v[48:51], v[244:247], a[28:31], v[48:51]\n"
    "ds_read_b64_tr_b16 v[232:233], v10 offset:36864\n"
    "ds_read_b64_tr_b16 v[234:235], v10 offset:37120\n"
    "v_mfma_f32_16x16x32_bf16 v[52:55], v[244:247], a[92:95], v[52:55]\n"
    "s_waitcnt lgkmcnt(8)\n"
    "v_exp_f32 v27, v27\n"
    "v_mfma_f32_16x16x32_bf16 v[56:59], v[244:247], a[156:159], v[56:59]\n"
    "s_add_u32 m0, s60, 107520\n"
    "s_nop 0\n"
    "buffer_load_dwordx4 v3, s[36:39], s56 offen lds\n"
    "v_mfma_f32_16x16x32_bf16 v[60:63], v[244:247], a[220:223], v[60:63]\n"
    "v_exp_f32 v28, v28\n"
    "v_mfma_f32_16x16x32_bf16 v[64:67], v[248:251], a[28:31], v[64:67]\n"
    "ds_read_b64_tr_b16 v[236:237], v11 offset:36864\n"
    "ds_read_b64_tr_b16 v[238:239], v11 offset:37120\n"
    "v_mfma_f32_16x16x32_bf16 v[68:71], v[248:251], a[92:95], v[68:71]\n"
    "s_waitcnt lgkmcnt(8)\n"
    "v_mul_f32 v168, v168, v200\n"
    "v_mul_f32 v169, v169, v201\n"
    "v_mfma_f32_16x16x32_bf16 v[72:75], v[248:251], a[156:159], v[72:75]\n"
    "v_mul_f32 v170, v170, v202\n"
    "v_mul_f32 v171, v171, v203\n"
    "v_mfma_f32_16x16x32_bf16 v[76:79], v[248:251], a[220:223], v[76:79]\n"
    "v_add_f32 v25, 1.0, v25\n"
    "v_mfma_f32_16x16x32_bf16 v[80:83], v[252:255], a[28:31], v[80:83]\n"
    "ds_read_b64_tr_b16 v[240:241], v10 offset:34816\n"
    "ds_read_b64_tr_b16 v[242:243], v10 offset:35072\n"
    "v_mfma_f32_16x16x32_bf16 v[84:87], v[252:255], a[92:95], v[84:87]\n"
    "s_waitcnt lgkmcnt(8)\n"
    "v_add_f32 v26, 1.0, v26\n"
    "v_add_f32 v27, 1.0, v27\n"
    "v_mfma_f32_16x16x32_bf16 v[88:91], v[252:255], a[156:159], v[88:91]\n"
    "v_add_f32 v28, 1.0, v28\n"
    "v_mfma_f32_16x16x32_bf16 v[92:95], v[252:255], a[220:223], v[92:95]\n"
    "v_rcp_f32 v25, v25\n"
    "v_mfma_f32_16x16x32_bf16 v[32:35], v[224:227], a[32:35], v[32:35]\n"
    "ds_read_b64_tr_b16 v[244:245], v11 offset:34816\n"
    "ds_read_b64_tr_b16 v[246:247], v11 offset:35072\n"
    "v_mfma_f32_16x16x32_bf16 v[36:39], v[224:227], a[96:99], v[36:39]\n"
    "s_waitcnt lgkmcnt(8)\n"
    "s_add_u32 s56, s61, 0x300000\n"
    "v_mfma_f32_16x16x32_bf16 v[40:43], v[224:227], a[160:163], v[40:43]\n"
    "s_add_u32 m0, s60, 114688\n"
    "s_nop 0\n"
    "buffer_load_dwordx4 v2, s[36:39], s56 offen lds\n"
    "v_mfma_f32_16x16x32_bf16 v[44:47], v[224:227], a[224:227], v[44:47]\n"
    "v_rcp_f32 v26, v26\n"
    "v_mfma_f32_16x16x32_bf16 v[48:51], v[228:231], a[32:35], v[48:51]\n"
    "ds_read_b64_tr_b16 v[248:249], v10 offset:38912\n"
    "ds_read_b64_tr_b16 v[250:251], v10 offset:39168\n"
    "v_mfma_f32_16x16x32_bf16 v[52:55], v[228:231], a[96:99], v[52:55]\n"
    "s_waitcnt lgkmcnt(8)\n"
    "v_rcp_f32 v27, v27\n"
    "v_mfma_f32_16x16x32_bf16 v[56:59], v[228:231], a[160:163], v[56:59]\n"
    "s_add_u32 s58, s63, 0x2400\n"
    "buffer_store_dwordx4 v[132:135], v1, s[44:47], s58 offen sc1\n"
    "v_mfma_f32_16x16x32_bf16 v[60:63], v[228:231], a[224:227], v[60:63]\n"
    "v_rcp_f32 v28, v28\n"
    "v_mfma_f32_16x16x32_bf16 v[64:67], v[232:235], a[32:35], v[64:67]\n"
    "ds_read_b64_tr_b16 v[252:253], v11 offset:38912\n"
    "ds_read_b64_tr_b16 v[254:255], v11 offset:39168\n"
    "v_mfma_f32_16x16x32_bf16 v[68:71], v[232:235], a[96:99], v[68:71]\n"
    "s_waitcnt lgkmcnt(8)\n"
    "v_mul_f32 v168, v168, v25\n"
    "v_mul_f32 v169, v169, v26\n"
    "v_mfma_f32_16x16x32_bf16 v[72:75], v[232:235], a[160:163], v[72:75]\n"
    "v_mul_f32 v170, v170, v27\n"
    "v_mul_f32 v171, v171, v28\n"
    "v_mfma_f32_16x16x32_bf16 v[76:79], v[232:235], a[224:227], v[76:79]\n"
    "v_cvt_pk_bf16_f32 v16, v168, v169\n"
    "v_mfma_f32_16x16x32_bf16 v[80:83], v[236:239], a[32:35], v[80:83]\n"
    "ds_read_b64_tr_b16 v[224:225], v10 offset:40960\n"
    "ds_read_b64_tr_b16 v[226:227], v10 offset:41216\n"
    "v_mfma_f32_16x16x32_bf16 v[84:87], v[236:239], a[96:99], v[84:87]\n"
    "s_waitcnt lgkmcnt(8)\n"
    "v_cvt_pk_bf16_f32 v17, v170, v171\n"
    "v_mul_f32 v25, 0xbfb8aa3b, v184\n"
    "v_mfma_f32_16x16x32_bf16 v[88:91], v[236:239], a[160:163], v[88:91]\n"
    "s_add_u32 m0, s60, 115712\n"
    "s_nop 0\n"
    "buffer_load_dwordx4 v3, s[36:39], s56 offen lds\n"
    "v_mul_f32 v26, 0xbfb8aa3b, v185\n"
    "v_mfma_f32_16x16x32_bf16 v[92:95], v[236:239], a[224:227], v[92:95]\n"
    "v_mul_f32 v27, 0xbfb8aa3b, v186\n"
    "v_mul_f32 v28, 0xbfb8aa3b, v187\n"
    "v_mfma_f32_16x16x32_bf16 v[32:35], v[240:243], a[36:39], v[32:35]\n"
    "ds_read_b64_tr_b16 v[228:229], v11 offset:40960\n"
    "ds_read_b64_tr_b16 v[230:231], v11 offset:41216\n"
    "v_mfma_f32_16x16x32_bf16 v[36:39], v[240:243], a[100:103], v[36:39]\n"
    "s_waitcnt lgkmcnt(8)\n"
    "v_exp_f32 v25, v25\n"
    "v_mfma_f32_16x16x32_bf16 v[40:43], v[240:243], a[164:167], v[40:43]\n"
    "v_exp_f32 v26, v26\n"
    "v_mfma_f32_16x16x32_bf16 v[44:47], v[240:243], a[228:231], v[44:47]\n"
    "v_exp_f32 v27, v27\n"
    "v_mfma_f32_16x16x32_bf16 v[48:51], v[244:247], a[36:39], v[48:51]\n"
    "ds_read_b64_tr_b16 v[232:233], v10 offset:45056\n"
    "ds_read_b64_tr_b16 v[234:235], v10 offset:45312\n"
    "v_mfma_f32_16x16x32_bf16 v[52:55], v[244:247], a[100:103], v[52:55]\n"
    "s_waitcnt lgkmcnt(8)\n"
    "v_exp_f32 v28, v28\n"
    "v_mfma_f32_16x16x32_bf16 v[56:59], v[244:247], a[164:167], v[56:59]\n"
    "v_mul_f32 v184, v184, v216\n"
    "v_mul_f32 v185, v185, v217\n"
    "v_mfma_f32_16x16x32_bf16 v[60:63], v[244:247], a[228:231], v[60:63]\n"
    "v_mul_f32 v186, v186, v218\n"
    "v_mul_f32 v187, v187, v219\n"
    "v_mfma_f32_16x16x32_bf16 v[64:67], v[248:251], a[36:39], v[64:67]\n"
    "ds_read_b64_tr_b16 v[236:237], v11 offset:45056\n"
    "ds_read_b64_tr_b16 v[238:239], v11 offset:45312\n"
    "v_mfma_f32_16x16x32_bf16 v[68:71], v[248:251], a[100:103], v[68:71]\n"
    "s_waitcnt lgkmcnt(8)\n"
    "s_add_u32 s56, s61, 0x380000\n"
    "v_mfma_f32_16x16x32_bf16 v[72:75], v[248:251], a[164:167], v[72:75]\n"
    "s_add_u32 m0, s60, 122880\n"
    "s_nop 0\n"
    "buffer_load_dwordx4 v2, s[36:39], s56 offen lds\n"
    "v_add_f32 v25, 1.0, v25\n"
    "v_mfma_f32_16x16x32_bf16 v[76:79], v[248:251], a[228:231], v[76:79]\n"
    "v_add_f32 v26, 1.0, v26\n"
    "v_add_f32 v27, 1.0, v27\n"
    "v_mfma_f32_16x16x32_bf16 v[80:83], v[252:255], a[36:39], v[80:83]\n"
    "ds_read_b64_tr_b16 v[240:241], v10 offset:43008\n"
    "ds_read_b64_tr_b16 v[242:243], v10 offset:43264\n"
    "v_mfma_f32_16x16x32_bf16 v[84:87], v[252:255], a[100:103], v[84:87]\n"
    "s_waitcnt lgkmcnt(8)\n"
    "v_add_f32 v28, 1.0, v28\n"
    "v_mfma_f32_16x16x32_bf16 v[88:91], v[252:255], a[164:167], v[88:91]\n"
    "v_rcp_f32 v25, v25\n"
    "v_mfma_f32_16x16x32_bf16 v[92:95], v[252:255], a[228:231], v[92:95]\n"
    "v_rcp_f32 v26, v26\n"
    "v_mfma_f32_16x16x32_bf16 v[32:35], v[224:227], a[40:43], v[32:35]\n"
    "ds_read_b64_tr_b16 v[244:245], v11 offset:43008\n"
    "ds_read_b64_tr_b16 v[246:247], v11 offset:43264\n"
    "v_mfma_f32_16x16x32_bf16 v[36:39], v[224:227], a[104:107], v[36:39]\n"
    "s_waitcnt lgkmcnt(8)\n"
    "v_rcp_f32 v27, v27\n"
    "v_mfma_f32_16x16x32_bf16 v[40:43], v[224:227], a[168:171], v[40:43]\n"
    "v_rcp_f32 v28, v28\n"
    "v_mfma_f32_16x16x32_bf16 v[44:47], v[224:227], a[232:235], v[44:47]\n"
    "v_mul_f32 v184, v184, v25\n"
    "v_mul_f32 v185, v185, v26\n"
    "v_mfma_f32_16x16x32_bf16 v[48:51], v[228:231], a[40:43], v[48:51]\n"
    "ds_read_b64_tr_b16 v[248:249], v10 offset:47104\n"
    "ds_read_b64_tr_b16 v[250:251], v10 offset:47360\n"
    "v_mfma_f32_16x16x32_bf16 v[52:55], v[228:231], a[104:107], v[52:55]\n"
    "s_waitcnt lgkmcnt(8)\n"
    "v_mul_f32 v186, v186, v27\n"
    "v_mul_f32 v187, v187, v28\n"
    "v_mfma_f32_16x16x32_bf16 v[56:59], v[228:231], a[168:171], v[56:59]\n"
    "s_add_u32 m0, s60, 123904\n"
    "s_nop 0\n"
    "buffer_load_dwordx4 v3, s[36:39], s56 offen lds\n"
    "v_cvt_pk_bf16_f32 v18, v184, v185\n"
    "v_mfma_f32_16x16x32_bf16 v[60:63], v[228:231], a[232:235], v[60:63]\n"
    "v_cvt_pk_bf16_f32 v19, v186, v187\n"
    "v_mfma_f32_16x16x32_bf16 v[64:67], v[232:235], a[40:43], v[64:67]\n"
    "ds_read_b64_tr_b16 v[252:253], v11 offset:47104\n"
    "ds_read_b64_tr_b16 v[254:255], v11 offset:47360\n"
    "v_mfma_f32_16x16x32_bf16 v[68:71], v[232:235], a[104:107], v[68:71]\n"
    "s_waitcnt lgkmcnt(8)\n"
    "v_mfma_f32_16x16x32_bf16 v[72:75], v[232:235], a[168:171], v[72:75]\n"
    "s_add_u32 s58, s68, 0x0\n"
    "buffer_store_dwordx4 v[16:19], v1, s[44:47], s58 offen sc1\n"
    "v_mul_f32 v25, 0xbfb8aa3b, v172\n"
    "v_mfma_f32_16x16x32_bf16 v[76:79], v[232:235], a[232:235], v[76:79]\n"
    "v_mul_f32 v26, 0xbfb8aa3b, v173\n"
    "v_mul_f32 v27, 0xbfb8aa3b, v174\n"
    "v_mfma_f32_16x16x32_bf16 v[80:83], v[236:239], a[40:43], v[80:83]\n"
    "ds_read_b64_tr_b16 v[224:225], v10 offset:49152\n"
    "ds_read_b64_tr_b16 v[226:227], v10 offset:49408\n"
    "v_mfma_f32_16x16x32_bf16 v[84:87], v[236:239], a[104:107], v[84:87]\n"
    "s_waitcnt lgkmcnt(8)\n"
    "v_mul_f32 v28, 0xbfb8aa3b, v175\n"
    "v_mfma_f32_16x16x32_bf16 v[88:91], v[236:239], a[168:171], v[88:91]\n"
    "s_add_u32 s58, s63, 0x2800\n"
    "buffer_store_dwordx4 v[136:139], v1, s[44:47], s58 offen sc1\n"
    "v_mfma_f32_16x16x32_bf16 v[92:95], v[236:239], a[232:235], v[92:95]\n"
    "v_exp_f32 v25, v25\n"
    "v_mfma_f32_16x16x32_bf16 v[32:35], v[240:243], a[44:47], v[32:35]\n"
    "ds_read_b64_tr_b16 v[228:229], v11 offset:49152\n"
    "ds_read_b64_tr_b16 v[230:231], v11 offset:49408\n"
    "v_mfma_f32_16x16x32_bf16 v[36:39], v[240:243], a[108:111], v[36:39]\n"
    "s_waitcnt lgkmcnt(8)\n"
    "v_exp_f32 v26, v26\n"
    "v_mfma_f32_16x16x32_bf16 v[40:43], v[240:243], a[172:175], v[40:43]\n"
    "v_exp_f32 v27, v27\n"
    "v_mfma_f32_16x16x32_bf16 v[44:47], v[240:243], a[236:239], v[44:47]\n"
    "v_exp_f32 v28, v28\n"
    "v_mfma_f32_16x16x32_bf16 v[48:51], v[244:247], a[44:47], v[48:51]\n"
    "ds_read_b64_tr_b16 v[232:233], v10 offset:53248\n"
    "ds_read_b64_tr_b16 v[234:235], v10 offset:53504\n"
    "v_mfma_f32_16x16x32_bf16 v[52:55], v[244:247], a[108:111], v[52:55]\n"
    "s_waitcnt lgkmcnt(8)\n"
    "v_mul_f32 v172, v172, v204\n"
    "v_mul_f32 v173, v173, v205\n"
    "v_mfma_f32_16x16x32_bf16 v[56:59], v[244:247], a[172:175], v[56:59]\n"
    "v_mul_f32 v174, v174, v206\n"
    "v_mul_f32 v175, v175, v207\n"
    "v_mfma_f32_16x16x32_bf16 v[60:63], v[244:247], a[236:239], v[60:63]\n"
    "v_add_f32 v25, 1.0, v25\n"
    "v_mfma_f32_16x16x32_bf16 v[64:67], v[248:251], a[44:47], v[64:67]\n"
    "ds_read_b64_tr_b16 v[236:237], v11 offset:53248\n"
    "ds_read_b64_tr_b16 v[238:239], v11 offset:53504\n"
    "v_mfma_f32_16x16x32_bf16 v[68:71], v[248:251], a[108:111], v[68:71]\n"
    "s_waitcnt lgkmcnt(8)\n"
    "v_add_f32 v26, 1.0, v26\n"
    "v_add_f32 v27, 1.0, v27\n"
    "v_mfma_f32_16x16x32_bf16 v[72:75], v[248:251], a[172:175], v[72:75]\n"
    "v_add_f32 v28, 1.0, v28\n"
    "v_mfma_f32_16x16x32_bf16 v[76:79], v[248:251], a[236:239], v[76:79]\n"
    "v_rcp_f32 v25, v25\n"
    "v_mfma_f32_16x16x32_bf16 v[80:83], v[252:255], a[44:47], v[80:83]\n"
    "ds_read_b64_tr_b16 v[240:241], v10 offset:51200\n"
    "ds_read_b64_tr_b16 v[242:243], v10 offset:51456\n"
    "v_mfma_f32_16x16x32_bf16 v[84:87], v[252:255], a[108:111], v[84:87]\n"
    "s_waitcnt lgkmcnt(8)\n"
    "v_rcp_f32 v26, v26\n"
    "v_mfma_f32_16x16x32_bf16 v[88:91], v[252:255], a[172:175], v[88:91]\n"
    "v_rcp_f32 v27, v27\n"
    "v_mfma_f32_16x16x32_bf16 v[92:95], v[252:255], a[236:239], v[92:95]\n"
    "v_rcp_f32 v28, v28\n"
    "v_mfma_f32_16x16x32_bf16 v[32:35], v[224:227], a[48:51], v[32:35]\n"
    "ds_read_b64_tr_b16 v[244:245], v11 offset:51200\n"
    "ds_read_b64_tr_b16 v[246:247], v11 offset:51456\n"
    "v_mfma_f32_16x16x32_bf16 v[36:39], v[224:227], a[112:115], v[36:39]\n"
    "s_waitcnt lgkmcnt(8)\n"
    "v_mul_f32 v172, v172, v25\n"
    "v_mul_f32 v173, v173, v26\n"
    "v_mfma_f32_16x16x32_bf16 v[40:43], v[224:227], a[176:179], v[40:43]\n"
    "v_mul_f32 v174, v174, v27\n"
    "v_mul_f32 v175, v175, v28\n"
    "v_mfma_f32_16x16x32_bf16 v[44:47], v[224:227], a[240:243], v[44:47]\n"
    "v_cvt_pk_bf16_f32 v20, v172, v173\n"
    "v_mfma_f32_16x16x32_bf16 v[48:51], v[228:231], a[48:51], v[48:51]\n"
    "ds_read_b64_tr_b16 v[248:249], v10 offset:55296\n"
    "ds_read_b64_tr_b16 v[250:251], v10 offset:55552\n"
    "v_mfma_f32_16x16x32_bf16 v[52:55], v[228:231], a[112:115], v[52:55]\n"
    "s_waitcnt lgkmcnt(8)\n"
    "v_cvt_pk_bf16_f32 v21, v174, v175\n"
    "v_mul_f32 v25, 0xbfb8aa3b, v188\n"
    "v_mfma_f32_16x16x32_bf16 v[56:59], v[228:231], a[176:179], v[56:59]\n"
    "s_add_u32 s58, s63, 0x2c00\n"
    "buffer_store_dwordx4 v[140:143], v1, s[44:47], s58 offen sc1\n"
    "v_mul_f32 v26, 0xbfb8aa3b, v189\n"
    "v_mfma_f32_16x16x32_bf16 v[60:63], v[228:231], a[240:243], v[60:63]\n"
    "v_mul_f32 v27, 0xbfb8aa3b, v190\n"
    "v_mul_f32 v28, 0xbfb8aa3b, v191\n"
    "v_mfma_f32_16x16x32_bf16 v[64:67], v[232:235], a[48:51], v[64:67]\n"
    "ds_read_b64_tr_b16 v[252:253], v11 offset:55296\n"
    "ds_read_b64_tr_b16 v[254:255], v11 offset:55552\n"
    "v_mfma_f32_16x16x32_bf16 v[68:71], v[232:235], a[112:115], v[68:71]\n"
    "s_waitcnt lgkmcnt(8)\n"
    "v_exp_f32 v25, v25\n"
    "v_mfma_f32_16x16x32_bf16 v[72:75], v[232:235], a[176:179], v[72:75]\n"
    "v_exp_f32 v26, v26\n"
    "v_mfma_f32_16x16x32_bf16 v[76:79], v[232:235], a[240:243], v[76:79]\n"
    "v_exp_f32 v27, v27\n"
    "v_mfma_f32_16x16x32_bf16 v[80:83], v[236:239], a[48:51], v[80:83]\n"
    "ds_read_b64_tr_b16 v[224:225], v10 offset:57344\n"
    "ds_read_b64_tr_b16 v[226:227], v10 offset:57600\n"
    "v_mfma_f32_16x16x32_bf16 v[84:87], v[236:239], a[112:115], v[84:87]\n"
    "s_waitcnt lgkmcnt(8)\n"
    "v_exp_f32 v28, v28\n"
    "v_mfma_f32_16x16x32_bf16 v[88:91], v[236:239], a[176:179], v[88:91]\n"
    "v_mul_f32 v188, v188, v220\n"
    "v_mul_f32 v189, v189, v221\n"
    "v_mfma_f32_16x16x32_bf16 v[92:95], v[236:239], a[240:243], v[92:95]\n"
    "v_mul_f32 v190, v190, v222\n"
    "v_mul_f32 v191, v191, v223\n"
    "v_mfma_f32_16x16x32_bf16 v[32:35], v[240:243], a[52:55], v[32:35]\n"
    "ds_read_b64_tr_b16 v[228:229], v11 offset:57344\n"
    "ds_read_b64_tr_b16 v[230:231], v11 offset:57600\n"
    "v_mfma_f32_16x16x32_bf16 v[36:39], v[240:243], a[116:119], v[36:39]\n"
    "s_waitcnt lgkmcnt(8)\n"
    "v_add_f32 v25, 1.0, v25\n"
    "v_add_f32 v26, 1.0, v26\n"
    "v_mfma_f32_16x16x32_bf16 v[40:43], v[240:243], a[180:183], v[40:43]\n"
    "v_add_f32 v27, 1.0, v27\n"
    "v_add_f32 v28, 1.0, v28\n"
    "v_mfma_f32_16x16x32_bf16 v[44:47], v[240:243], a[244:247], v[44:47]\n"
    "v_rcp_f32 v25, v25\n"
    "v_mfma_f32_16x16x32_bf16 v[48:51], v[244:247], a[52:55], v[48:51]\n"
    "ds_read_b64_tr_b16 v[232:233], v10 offset:61440\n"
    "ds_read_b64_tr_b16 v[234:235], v10 offset:61696\n"
    "v_mfma_f32_16x16x32_bf16 v[52:55], v[244:247], a[116:119], v[52:55]\n"
    "s_waitcnt lgkmcnt(8)\n"
    "v_rcp_f32 v26, v26\n"
    "v_mfma_f32_16x16x32_bf16 v[56:59], v[244:247], a[180:183], v[56:59]\n"
    "v_rcp_f32 v27, v27\n"
    "v_mfma_f32_16x16x32_bf16 v[60:63], v[244:247], a[244:247], v[60:63]\n"
    "v_rcp_f32 v28, v28\n"
    "v_mfma_f32_16x16x32_bf16 v[64:67], v[248:251], a[52:55], v[64:67]\n"
    "ds_read_b64_tr_b16 v[236:237], v11 offset:61440\n"
    "ds_read_b64_tr_b16 v[238:239], v11 offset:61696\n"
    "v_mfma_f32_16x16x32_bf16 v[68:71], v[248:251], a[116:119], v[68:71]\n"
    "s_waitcnt lgkmcnt(8)\n"
    "v_mul_f32 v188, v188, v25\n"
    "v_mul_f32 v189, v189, v26\n"
    "v_mfma_f32_16x16x32_bf16 v[72:75], v[248:251], a[180:183], v[72:75]\n"
    "v_mul_f32 v190, v190, v27\n"
    "v_mul_f32 v191, v191, v28\n"
    "v_mfma_f32_16x16x32_bf16 v[76:79], v[248:251], a[244:247], v[76:79]\n"
    "v_cvt_pk_bf16_f32 v22, v188, v189\n"
    "v_mfma_f32_16x16x32_bf16 v[80:83], v[252:255], a[52:55], v[80:83]\n"
    "ds_read_b64_tr_b16 v[240:241], v10 offset:59392\n"
    "ds_read_b64_tr_b16 v[242:243], v10 offset:59648\n"
    "v_mfma_f32_16x16x32_bf16 v[84:87], v[252:255], a[116:119], v[84:87]\n"
    "s_waitcnt lgkmcnt(8)\n"
    "v_cvt_pk_bf16_f32 v23, v190, v191\n"
    "v_mfma_f32_16x16x32_bf16 v[88:91], v[252:255], a[180:183], v[88:91]\n"
    "s_add_u32 s58, s68, 0xe000\n"
    "buffer_store_dwordx4 v[20:23], v1, s[44:47], s58 offen sc1\n"
    "v_mfma_f32_16x16x32_bf16 v[92:95], v[252:255], a[244:247], v[92:95]\n"
    "v_mfma_f32_16x16x32_bf16 v[32:35], v[224:227], a[56:59], v[32:35]\n"
    "ds_read_b64_tr_b16 v[244:245], v11 offset:59392\n"
    "ds_read_b64_tr_b16 v[246:247], v11 offset:59648\n"
    "v_mfma_f32_16x16x32_bf16 v[36:39], v[224:227], a[120:123], v[36:39]\n"
    "s_waitcnt lgkmcnt(8)\n"
    "v_mfma_f32_16x16x32_bf16 v[40:43], v[224:227], a[184:187], v[40:43]\n"
    "v_mfma_f32_16x16x32_bf16 v[44:47], v[224:227], a[248:251], v[44:47]\n"
    "v_mfma_f32_16x16x32_bf16 v[48:51], v[228:231], a[56:59], v[48:51]\n"
    "ds_read_b64_tr_b16 v[248:249], v10 offset:63488\n"
    "ds_read_b64_tr_b16 v[250:251], v10 offset:63744\n"
    "v_mfma_f32_16x16x32_bf16 v[52:55], v[228:231], a[120:123], v[52:55]\n"
    "s_waitcnt lgkmcnt(8)\n"
    "v_mfma_f32_16x16x32_bf16 v[56:59], v[228:231], a[184:187], v[56:59]\n"
    "s_add_u32 s58, s63, 0x3000\n"
    "buffer_store_dwordx4 v[144:147], v1, s[44:47], s58 offen sc1\n"
    "v_mfma_f32_16x16x32_bf16 v[60:63], v[228:231], a[248:251], v[60:63]\n"
    "v_mfma_f32_16x16x32_bf16 v[64:67], v[232:235], a[56:59], v[64:67]\n"
    "ds_read_b64_tr_b16 v[252:253], v11 offset:63488\n"
    "ds_read_b64_tr_b16 v[254:255], v11 offset:63744\n"
    "v_mfma_f32_16x16x32_bf16 v[68:71], v[232:235], a[120:123], v[68:71]\n"
    "s_waitcnt lgkmcnt(8)\n"
    "v_mfma_f32_16x16x32_bf16 v[72:75], v[232:235], a[184:187], v[72:75]\n"
    "v_mfma_f32_16x16x32_bf16 v[76:79], v[232:235], a[248:251], v[76:79]\n"
    "v_xor_b32 v4, 0x10000, v4\n"
    "v_xor_b32 v5, 0x10000, v5\n"
    "v_xor_b32 v6, 0x10000, v6\n"
    "v_xor_b32 v8, 0x10000, v8\n"
    "s_waitcnt vmcnt(5)\n"
    "s_barrier\n"
    "v_mfma_f32_16x16x32_bf16 v[80:83], v[236:239], a[56:59], v[80:83]\n"
    "ds_read_b64_tr_b16 v[224:225], v4 offset:0\n"
    "ds_read_b64_tr_b16 v[226:227], v4 offset:512\n"
    "v_mfma_f32_16x16x32_bf16 v[84:87], v[236:239], a[120:123], v[84:87]\n"
    "s_waitcnt lgkmcnt(8)\n"
    "s_add_u32 s56, s61, 0x80\n"
    "v_mfma_f32_16x16x32_bf16 v[88:91], v[236:239], a[184:187], v[88:91]\n"
    "s_add_u32 m0, s60, 0\n"
    "s_nop 0\n"
    "buffer_load_dwordx4 v2, s[36:39], s56 offen lds\n"
    "v_mfma_f32_16x16x32_bf16 v[92:95], v[236:239], a[248:251], v[92:95]\n"
    "v_mfma_f32_16x16x32_bf16 v[32:35], v[240:243], a[60:63], v[32:35]\n"
    "ds_read_b64_tr_b16 v[228:229], v5 offset:0\n"
    "ds_read_b64_tr_b16 v[230:231], v5 offset:512\n"
    "v_mfma_f32_16x16x32_bf16 v[36:39], v[240:243], a[124:127], v[36:39]\n"
    "s_waitcnt lgkmcnt(8)\n"
    "v_mfma_f32_16x16x32_bf16 v[40:43], v[240:243], a[188:191], v[40:43]\n"
    "v_mfma_f32_16x16x32_bf16 v[44:47], v[240:243], a[252:255], v[44:47]\n"
    "v_mfma_f32_16x16x32_bf16 v[48:51], v[244:247], a[60:63], v[48:51]\n"
    "ds_read_b64_tr_b16 v[232:233], v6 offset:0\n"
    "ds_read_b64_tr_b16 v[234:235], v6 offset:512\n"
    "v_mfma_f32_16x16x32_bf16 v[52:55], v[244:247], a[124:127], v[52:55]\n"
    "s_waitcnt lgkmcnt(8)\n"
    "v_mfma_f32_16x16x32_bf16 v[56:59], v[244:247], a[188:191], v[56:59]\n"
    "v_mfma_f32_16x16x32_bf16 v[60:63], v[244:247], a[252:255], v[60:63]\n"
    "v_mfma_f32_16x16x32_bf16 v[64:67], v[248:251], a[60:63], v[64:67]\n"
    "ds_read_b64_tr_b16 v[236:237], v8 offset:0\n"
    "ds_read_b64_tr_b16 v[238:239], v8 offset:512\n"
    "v_mfma_f32_16x16x32_bf16 v[68:71], v[248:251], a[124:127], v[68:71]\n"
    "s_waitcnt lgkmcnt(8)\n"
    "v_mfma_f32_16x16x32_bf16 v[72:75], v[248:251], a[188:191], v[72:75]\n"
    "s_add_u32 m0, s60, 1024\n"
    "s_nop 0\n"
    "buffer_load_dwordx4 v3, s[36:39], s56 offen lds\n"
    "v_mfma_f32_16x16x32_bf16 v[76:79], v[248:251], a[252:255], v[76:79]\n"
    "v_mfma_f32_16x16x32_bf16 v[80:83], v[252:255], a[60:63], v[80:83]\n"
    "ds_read_b64_tr_b16 v[240:241], v4 offset:4096\n"
    "ds_read_b64_tr_b16 v[242:243], v4 offset:4608\n"
    "v_mfma_f32_16x16x32_bf16 v[84:87], v[252:255], a[124:127], v[84:87]\n"
    "s_waitcnt lgkmcnt(8)\n"
    "v_mfma_f32_16x16x32_bf16 v[88:91], v[252:255], a[188:191], v[88:91]\n"
    "v_mfma_f32_16x16x32_bf16 v[92:95], v[252:255], a[252:255], v[92:95]\n"
    "v_mfma_f32_16x16x32_bf16 v[160:163], v[224:227], a[0:3], 0\n"
    "ds_read_b64_tr_b16 v[244:245], v5 offset:4096\n"
    "ds_read_b64_tr_b16 v[246:247], v5 offset:4608\n"
    "v_mfma_f32_16x16x32_bf16 v[164:167], v[224:227], a[64:67], 0\n"
    "s_waitcnt lgkmcnt(8)\n"
    "v_mfma_f32_16x16x32_bf16 v[168:171], v[224:227], a[128:131], 0\n"
    "v_mul_f32 v25, 0xbfb8aa3b, v32\n"
    "v_mul_f32 v26, 0xbfb8aa3b, v33\n"
    "v_mfma_f32_16x16x32_bf16 v[172:175], v[224:227], a[192:195], 0\n"
    "v_mul_f32 v27, 0xbfb8aa3b, v34\n"
    "v_mul_f32 v28, 0xbfb8aa3b, v35\n"
    "v_mfma_f32_16x16x32_bf16 v[176:179], v[228:231], a[0:3], 0\n"
    "ds_read_b64_tr_b16 v[248:249], v6 offset:4096\n"
    "ds_read_b64_tr_b16 v[250:251], v6 offset:4608\n"
    "v_mfma_f32_16x16x32_bf16 v[180:183], v[228:231], a[64:67], 0\n"
    "s_waitcnt lgkmcnt(8)\n"
    "v_exp_f32 v25, v25\n"
    "v_mfma_f32_16x16x32_bf16 v[184:187], v[228:231], a[128:131], 0\n"
    "s_add_u32 s58, s63, 0x3400\n"
    "buffer_store_dwordx4 v[148:151], v1, s[44:47], s58 offen sc1\n"
    "v_mfma_f32_16x16x32_bf16 v[188:191], v[228:231], a[192:195], 0\n"
    "s_add_u32 s56, s61, 0x80080\n"
    "v_exp_f32 v26, v26\n"
    "v_mfma_f32_16x16x32_bf16 v[192:195], v[232:235], a[0:3], 0\n"
    "ds_read_b64_tr_b16 v[252:253], v8 offset:4096\n"
    "ds_read_b64_tr_b16 v[254:255], v8 offset:4608\n"
    "v_mfma_f32_16x16x32_bf16 v[196:199], v[232:235], a[64:67], 0\n"
    "s_waitcnt lgkmcnt(8)\n"
    "v_exp_f32 v27, v27\n"
    "v_mfma_f32_16x16x32_bf16 v[200:203], v[232:235], a[128:131], 0\n"
    "s_add_u32 m0, s60, 8192\n"
    "s_nop 0\n"
    "buffer_load_dwordx4 v2, s[36:39], s56 offen lds\n"
    "v_mfma_f32_16x16x32_bf16 v[204:207], v[232:235], a[192:195], 0\n"
    "v_exp_f32 v28, v28\n"
    "v_mfma_f32_16x16x32_bf16 v[208:211], v[236:239], a[0:3], 0\n"
    "ds_read_b64_tr_b16 v[224:225], v4 offset:8192\n"
    "ds_read_b64_tr_b16 v[226:227], v4 offset:8704\n"
    "v_mfma_f32_16x16x32_bf16 v[212:215], v[236:239], a[64:67], 0\n"
    "s_waitcnt lgkmcnt(8)\n"
    "v_mul_f32 v32, v32, v64\n"
    "v_mul_f32 v33, v33, v65\n"
    "v_mfma_f32_16x16x32_bf16 v[216:219], v[236:239], a[128:131], 0\n"
    "v_mul_f32 v34, v34, v66\n"
    "v_mul_f32 v35, v35, v67\n"
    "v_mfma_f32_16x16x32_bf16 v[220:223], v[236:239], a[192:195], 0\n"
    "v_add_f32 v25, 1.0, v25\n"
    "v_mfma_f32_16x16x32_bf16 v[160:163], v[240:243], a[4:7], v[160:163]\n"
    "ds_read_b64_tr_b16 v[228:229], v5 offset:8192\n"
    "ds_read_b64_tr_b16 v[230:231], v5 offset:8704\n"
    "v_mfma_f32_16x16x32_bf16 v[164:167], v[240:243], a[68:71], v[164:167]\n"
    "s_waitcnt lgkmcnt(8)\n"
    "v_add_f32 v26, 1.0, v26\n"
    "v_add_f32 v27, 1.0, v27\n"
    "v_mfma_f32_16x16x32_bf16 v[168:171], v[240:243], a[132:135], v[168:171]\n"
    "s_add_u32 m0, s60, 9216\n"
    "s_nop 0\n"
    "buffer_load_dwordx4 v3, s[36:39], s56 offen lds\n"
    "v_add_f32 v28, 1.0, v28\n"
    "v_mfma_f32_16x16x32_bf16 v[172:175], v[240:243], a[196:199], v[172:175]\n"
    "v_rcp_f32 v25, v25\n"
    "v_mfma_f32_16x16x32_bf16 v[176:179], v[244:247], a[4:7], v[176:179]\n"
    "ds_read_b64_tr_b16 v[232:233], v6 offset:8192\n"
    "ds_read_b64_tr_b16 v[234:235], v6 offset:8704\n"
    "v_mfma_f32_16x16x32_bf16 v[180:183], v[244:247], a[68:71], v[180:183]\n"
    "s_waitcnt lgkmcnt(8)\n"
    "v_rcp_f32 v26, v26\n"
    "v_mfma_f32_16x16x32_bf16 v[184:187], v[244:247], a[132:135], v[184:187]\n"
    "v_rcp_f32 v27, v27\n"
    "v_mfma_f32_16x16x32_bf16 v[188:191], v[244:247], a[196:199], v[188:191]\n"
    "v_rcp_f32 v28, v28\n"
    "v_mfma_f32_16x16x32_bf16 v[192:195], v[248:251], a[4:7], v[192:195]\n"
    "ds_read_b64_tr_b16 v[236:237], v8 offset:8192\n"
    "ds_read_b64_tr_b16 v[238:239], v8 offset:8704\n"
    "v_mfma_f32_16x16x32_bf16 v[196:199], v[248:251], a[68:71], v[196:199]\n"
    "s_waitcnt lgkmcnt(8)\n"
    "v_mul_f32 v32, v32, v25\n"
    "v_mul_f32 v33, v33, v26\n"
    "v_mfma_f32_16x16x32_bf16 v[200:203], v[248:251], a[132:135], v[200:203]\n"
    "v_mul_f32 v34, v34, v27\n"
    "v_mul_f32 v35, v35, v28\n"
    "v_mfma_f32_16x16x32_bf16 v[204:207], v[248:251], a[196:199], v[204:207]\n"
    "v_cvt_pk_bf16_f32 v16, v32, v33\n"
    "v_mfma_f32_16x16x32_bf16 v[208:211], v[252:255], a[4:7], v[208:211]\n"
    "ds_read_b64_tr_b16 v[240:241], v4 offset:12288\n"
    "ds_read_b64_tr_b16 v[242:243], v4 offset:12800\n"
    "v_mfma_f32_16x16x32_bf16 v[212:215], v[252:255], a[68:71], v[212:215]\n"
    "s_waitcnt lgkmcnt(8)\n"
    "v_cvt_pk_bf16_f32 v17, v34, v35\n"
    "v_mul_f32 v25, 0xbfb8aa3b, v48\n"
    "v_mfma_f32_16x16x32_bf16 v[216:219], v[252:255], a[132:135], v[216:219]\n"
    "s_add_u32 s56, s61, 0x100080\n"
    "v_mul_f32 v26, 0xbfb8aa3b, v49\n"
    "v_mul_f32 v27, 0xbfb8aa3b, v50\n"
    "v_mfma_f32_16x16x32_bf16 v[220:223], v[252:255], a[196:199], v[220:223]\n"
    "s_add_u32 m0, s60, 16384\n"
    "s_nop 0\n"
    "buffer_load_dwordx4 v2, s[36:39], s56 offen lds\n"
    "v_mul_f32 v28, 0xbfb8aa3b, v51\n"
    "v_mfma_f32_16x16x32_bf16 v[160:163], v[224:227], a[8:11], v[160:163]\n"
    "ds_read_b64_tr_b16 v[244:245], v5 offset:12288\n"
    "ds_read_b64_tr_b16 v[246:247], v5 offset:12800\n"
    "v_mfma_f32_16x16x32_bf16 v[164:167], v[224:227], a[72:75], v[164:167]\n"
    "s_waitcnt lgkmcnt(8)\n"
    "v_exp_f32 v25, v25\n"
    "v_mfma_f32_16x16x32_bf16 v[168:171], v[224:227], a[136:139], v[168:171]\n"
    "v_exp_f32 v26, v26\n"
    "v_mfma_f32_16x16x32_bf16 v[172:175], v[224:227], a[200:203], v[172:175]\n"
    "v_exp_f32 v27, v27\n"
    "v_mfma_f32_16x16x32_bf16 v[176:179], v[228:231], a[8:11], v[176:179]\n"
    "ds_read_b64_tr_b16 v[248:249], v6 offset:12288\n"
    "ds_read_b64_tr_b16 v[250:251], v6 offset:12800\n"
    "v_mfma_f32_16x16x32_bf16 v[180:183], v[228:231], a[72:75], v[180:183]\n"
    "s_waitcnt lgkmcnt(8)\n"
    "v_exp_f32 v28, v28\n"
    "v_mfma_f32_16x16x32_bf16 v[184:187], v[228:231], a[136:139], v[184:187]\n"
    "s_add_u32 s58, s63, 0x3800\n"
    "buffer_store_dwordx4 v[152:155], v1, s[44:47], s58 offen sc1\n"
    "v_mul_f32 v48, v48, v80\n"
    "v_mfma_f32_16x16x32_bf16 v[188:191], v[228:231], a[200:203], v[188:191]\n"
    "v_mul_f32 v49, v49, v81\n"
    "v_mul_f32 v50, v50, v82\n"
    "v_mfma_f32_16x16x32_bf16 v[192:195], v[232:235], a[8:11], v[192:195]\n"
    "ds_read_b64_tr_b16 v[252:253], v8 offset:12288\n"
    "ds_read_b64_tr_b16 v[254:255], v8 offset:12800\n"
    "v_mfma_f32_16x16x32_bf16 v[196:199], v[232:235], a[72:75], v[196:199]\n"
    "s_waitcnt lgkmcnt(8)\n"
    "v_mul_f32 v51, v51, v83\n"
    "v_add_f32 v25, 1.0, v25\n"
    "v_mfma_f32_16x16x32_bf16 v[200:203], v[232:235], a[136:139], v[200:203]\n"
    "s_add_u32 m0, s60, 17408\n"
    "s_nop 0\n"
    "buffer_load_dwordx4 v3, s[36:39], s56 offen lds\n"
    "v_add_f32 v26, 1.0, v26\n"
    "v_mfma_f32_16x16x32_bf16 v[204:207], v[232:235], a[200:203], v[204:207]\n"
    "v_add_f32 v27, 1.0, v27\n"
    "v_add_f32 v28, 1.0, v28\n"
    "v_mfma_f32_16x16x32_bf16 v[208:211], v[236:239], a[8:11], v[208:211]\n"
    "ds_read_b64_tr_b16 v[224:225], v4 offset:16384\n"
    "ds_read_b64_tr_b16 v[226:227], v4 offset:16896\n"
    "v_mfma_f32_16x16x32_bf16 v[212:215], v[236:239], a[72:75], v[212:215]\n"
    "s_waitcnt lgkmcnt(8)\n"
    "v_rcp_f32 v25, v25\n"
    "v_mfma_f32_16x16x32_bf16 v[216:219], v[236:239], a[136:139], v[216:219]\n"
    "v_rcp_f32 v26, v26\n"
    "v_mfma_f32_16x16x32_bf16 v[220:223], v[236:239], a[200:203], v[220:223]\n"
    "v_rcp_f32 v27, v27\n"
    "v_mfma_f32_16x16x32_bf16 v[160:163], v[240:243], a[12:15], v[160:163]\n"
    "ds_read_b64_tr_b16 v[228:229], v5 offset:16384\n"
    "ds_read_b64_tr_b16 v[230:231], v5 offset:16896\n"
    "v_mfma_f32_16x16x32_bf16 v[164:167], v[240:243], a[76:79], v[164:167]\n"
    "s_waitcnt lgkmcnt(8)\n"
    "v_rcp_f32 v28, v28\n"
    "v_mfma_f32_16x16x32_bf16 v[168:171], v[240:243], a[140:143], v[168:171]\n"
    "v_mul_f32 v48, v48, v25\n"
    "v_mul_f32 v49, v49, v26\n"
    "v_mfma_f32_16x16x32_bf16 v[172:175], v[240:243], a[204:207], v[172:175]\n"
    "v_mul_f32 v50, v50, v27\n"
    "v_mul_f32 v51, v51, v28\n"
    "v_mfma_f32_16x16x32_bf16 v[176:179], v[244:247], a[12:15], v[176:179]\n"
    "ds_read_b64_tr_b16 v[232:233], v6 offset:16384\n"
    "ds_read_b64_tr_b16 v[234:235], v6 offset:16896\n"
    "v_mfma_f32_16x16x32_bf16 v[180:183], v[244:247], a[76:79], v[180:183]\n"
    "s_waitcnt lgkmcnt(8)\n"
    "v_cvt_pk_bf16_f32 v18, v48, v49\n"
    "v_cvt_pk_bf16_f32 v19, v50, v51\n"
    "v_mfma_f32_16x16x32_bf16 v[184:187], v[244:247], a[140:143], v[184:187]\n"
    "s_add_u32 s58, s67, 0x400\n"
    "buffer_store_dwordx4 v[16:19], v1, s[44:47], s58 offen sc1\n"
    "v_mul_f32 v25, 0xbfb8aa3b, v36\n"
    "v_mfma_f32_16x16x32_bf16 v[188:191], v[244:247], a[204:207], v[188:191]\n"
    "s_add_u32 s56, s61, 0x180080\n"
    "v_mul_f32 v26, 0xbfb8aa3b, v37\n"
    "v_mul_f32 v27, 0xbfb8aa3b, v38\n"
    "v_mfma_f32_16x16x32_bf16 v[192:195], v[248:251], a[12:15], v[192:195]\n"
    "ds_read_b64_tr_b16 v[236:237], v8 offset:16384\n"
    "ds_read_b64_tr_b16 v[238:239], v8 offset:16896\n"
    "v_mfma_f32_16x16x32_bf16 v[196:199], v[248:251], a[76:79], v[196:199]\n"
    "s_waitcnt lgkmcnt(8)\n"
    "v_mul_f32 v28, 0xbfb8aa3b, v39\n"
    "v_mfma_f32_16x16x32_bf16 v[200:203], v[248:251], a[140:143], v[200:203]\n"
    "s_add_u32 m0, s60, 24576\n"
    "s_nop 0\n"
    "buffer_load_dwordx4 v2, s[36:39], s56 offen lds\n"
    "v_mfma_f32_16x16x32_bf16 v[204:207], v[248:251], a[204:207], v[204:207]\n"
    "v_exp_f32 v25, v25\n"
    "v_mfma_f32_16x16x32_bf16 v[208:211], v[252:255], a[12:15], v[208:211]\n"
    "ds_read_b64_tr_b16 v[240:241], v4 offset:20480\n"
    "ds_read_b64_tr_b16 v[242:243], v4 offset:20992\n"
    "v_mfma_f32_16x16x32_bf16 v[212:215], v[252:255], a[76:79], v[212:215]\n"
    "s_waitcnt lgkmcnt(8)\n"
    "v_exp_f32 v26, v26\n"
    "v_mfma_f32_16x16x32_bf16 v[216:219], v[252:255], a[140:143], v[216:219]\n"
    "v_exp_f32 v27, v27\n"
    "v_mfma_f32_16x16x32_bf16 v[220:223], v[252:255], a[204:207], v[220:223]\n"
    "v_exp_f32 v28, v28\n"
    "v_mfma_f32_16x16x32_bf16 v[160:163], v[224:227], a[16:19], v[160:163]\n"
    "ds_read_b64_tr_b16 v[244:245], v5 offset:20480\n"
    "ds_read_b64_tr_b16 v[246:247], v5 offset:20992\n"
    "v_mfma_f32_16x16x32_bf16 v[164:167], v[224:227], a[80:83], v[164:167]\n"
    "s_waitcnt lgkmcnt(8)\n"
    "v_mul_f32 v36, v36, v68\n"
    "v_mul_f32 v37, v37, v69\n"
    "v_mfma_f32_16x16x32_bf16 v[168:171], v[224:227], a[144:147], v[168:171]\n"
    "v_mul_f32 v38, v38, v70\n"
    "v_mul_f32 v39, v39, v71\n"
    "v_mfma_f32_16x16x32_bf16 v[172:175], v[224:227], a[208:211], v[172:175]\n"
    "s_add_u32 m0, s60, 25600\n"
    "s_nop 0\n"
    "buffer_load_dwordx4 v3, s[36:39], s56 offen lds\n"
    "v_add_f32 v25, 1.0, v25\n"
    "v_mfma_f32_16x16x32_bf16 v[176:179], v[228:231], a[16:19], v[176:179]\n"
    "ds_read_b64_tr_b16 v[248:249], v6 offset:20480\n"
    "ds_read_b64_tr_b16 v[250:251], v6 offset:20992\n"
    "v_mfma_f32_16x16x32_bf16 v[180:183], v[228:231], a[80:83], v[180:183]\n"
    "s_waitcnt lgkmcnt(8)\n"
    "v_add_f32 v26, 1.0, v26\n"
    "v_add_f32 v27, 1.0, v27\n"
    "v_mfma_f32_16x16x32_bf16 v[184:187], v[228:231], a[144:147], v[184:187]\n"
    "v_add_f32 v28, 1.0, v28\n"
    "v_mfma_f32_16x16x32_bf16 v[188:191], v[228:231], a[208:211], v[188:191]\n"
    "s_add_u32 s58, s63, 0x3c00\n"
    "buffer_store_dwordx4 v[156:159], v1, s[44:47], s58 offen sc1\n"
    "v_mfma_f32_16x16x32_bf16 v[192:195], v[232:235], a[16:19], v[192:195]\n"
    "ds_read_b64_tr_b16 v[252:253], v8 offset:20480\n"
    "ds_read_b64_tr_b16 v[254:255], v8 offset:20992\n"
    "v_mfma_f32_16x16x32_bf16 v[196:199], v[232:235], a[80:83], v[196:199]\n"
    "s_waitcnt lgkmcnt(8)\n"
    "v_rcp_f32 v25, v25\n"
    "v_mfma_f32_16x16x32_bf16 v[200:203], v[232:235], a[144:147], v[200:203]\n"
    "v_rcp_f32 v26, v26\n"
    "v_mfma_f32_16x16x32_bf16 v[204:207], v[232:235], a[208:211], v[204:207]\n"
    "v_rcp_f32 v27, v27\n"
    "v_mfma_f32_16x16x32_bf16 v[208:211], v[236:239], a[16:19], v[208:211]\n"
    "ds_read_b64_tr_b16 v[224:225], v4 offset:24576\n"
    "ds_read_b64_tr_b16 v[226:227], v4 offset:25088\n"
    "v_mfma_f32_16x16x32_bf16 v[212:215], v[236:239], a[80:83], v[212:215]\n"
    "s_waitcnt lgkmcnt(8)\n"
    "v_rcp_f32 v28, v28\n"
    "v_mfma_f32_16x16x32_bf16 v[216:219], v[236:239], a[144:147], v[216:219]\n"
    "s_add_u32 s56, s61, 0x200080\n"
    "v_mul_f32 v36, v36, v25\n"
    "v_mul_f32 v37, v37, v26\n"
    "v_mfma_f32_16x16x32_bf16 v[220:223], v[236:239], a[208:211], v[220:223]\n"
    "s_add_u32 m0, s60, 32768\n"
    "s_nop 0\n"
    "buffer_load_dwordx4 v2, s[36:39], s56 offen lds\n"
    "v_mul_f32 v38, v38, v27\n"
    "v_mfma_f32_16x16x32_bf16 v[160:163], v[240:243], a[20:23], v[160:163]\n"
    "ds_read_b64_tr_b16 v[228:229], v5 offset:24576\n"
    "ds_read_b64_tr_b16 v[230:231], v5 offset:25088\n"
    "v_mfma_f32_16x16x32_bf16 v[164:167], v[240:243], a[84:87], v[164:167]\n"
    "s_waitcnt lgkmcnt(8)\n"
    "v_mul_f32 v39, v39, v28\n"
    "v_cvt_pk_bf16_f32 v20, v36, v37\n"
    "v_mfma_f32_16x16x32_bf16 v[168:171], v[240:243], a[148:151], v[168:171]\n"
    "v_cvt_pk_bf16_f32 v21, v38, v39\n"
    "v_mul_f32 v25, 0xbfb8aa3b, v52\n"
    "v_mfma_f32_16x16x32_bf16 v[172:175], v[240:243], a[212:215], v[172:175]\n"
    "v_mul_f32 v26, 0xbfb8aa3b, v53\n"
    "v_mfma_f32_16x16x32_bf16 v[176:179], v[244:247], a[20:23], v[176:179]\n"
    "ds_read_b64_tr_b16 v[232:233], v6 offset:24576\n"
    "ds_read_b64_tr_b16 v[234:235], v6 offset:25088\n"
    "v_mfma_f32_16x16x32_bf16 v[180:183], v[244:247], a[84:87], v[180:183]\n"
    "s_waitcnt lgkmcnt(8)\n"
    "v_mul_f32 v27, 0xbfb8aa3b, v54\n"
    "v_mul_f32 v28, 0xbfb8aa3b, v55\n"
    "v_mfma_f32_16x16x32_bf16 v[184:187], v[244:247], a[148:151], v[184:187]\n"
    "v_exp_f32 v25, v25\n"
    "v_mfma_f32_16x16x32_bf16 v[188:191], v[244:247], a[212:215], v[188:191]\n"
    "v_exp_f32 v26, v26\n"
    "v_mfma_f32_16x16x32_bf16 v[192:195], v[248:251], a[20:23], v[192:195]\n"
    "ds_read_b64_tr_b16 v[236:237], v8 offset:24576\n"
    "ds_read_b64_tr_b16 v[238:239], v8 offset:25088\n"
    "v_mfma_f32_16x16x32_bf16 v[196:199], v[248:251], a[84:87], v[196:199]\n"
    "s_waitcnt lgkmcnt(8)\n"
    "v_exp_f32 v27, v27\n"
    "v_mfma_f32_16x16x32_bf16 v[200:203], v[248:251], a[148:151], v[200:203]\n"
    "v_exp_f32 v28, v28\n"
    "v_mfma_f32_16x16x32_bf16 v[204:207], v[248:251], a[212:215], v[204:207]\n"
    "s_add_u32 m0, s60, 33792\n"
    "s_nop 0\n"
    "buffer_load_dwordx4 v3, s[36:39], s56 offen lds\n"
    "v_mul_f32 v52, v52, v84\n"
    "v_mfma_f32_16x16x32_bf16 v[208:211], v[252:255], a[20:23], v[208:211]\n"
    "ds_read_b64_tr_b16 v[240:241], v4 offset:28672\n"
    "ds_read_b64_tr_b16 v[242:243], v4 offset:29184\n"
    "v_mfma_f32_16x16x32_bf16 v[212:215], v[252:255], a[84:87], v[212:215]\n"
    "s_waitcnt lgkmcnt(8)\n"
    "v_mul_f32 v53, v53, v85\n"
    "v_mul_f32 v54, v54, v86\n"
    "v_mfma_f32_16x16x32_bf16 v[216:219], v[252:255], a[148:151], v[216:219]\n"
    "v_mul_f32 v55, v55, v87\n"
    "v_add_f32 v25, 1.0, v25\n"
    "v_mfma_f32_16x16x32_bf16 v[220:223], v[252:255], a[212:215], v[220:223]\n"
    "v_add_f32 v26, 1.0, v26\n"
    "v_mfma_f32_16x16x32_bf16 v[160:163], v[224:227], a[24:27], v[160:163]\n"
    "ds_read_b64_tr_b16 v[244:245], v5 offset:28672\n"
    "ds_read_b64_tr_b16 v[246:247], v5 offset:29184\n"
    "v_mfma_f32_16x16x32_bf16 v[164:167], v[224:227], a[88:91], v[164:167]\n"
    "s_waitcnt lgkmcnt(8)\n"
    "v_add_f32 v27, 1.0, v27\n"
    "v_add_f32 v28, 1.0, v28\n"
    "v_mfma_f32_16x16x32_bf16 v[168:171], v[224:227], a[152:155], v[168:171]\n"
    "v_rcp_f32 v25, v25\n"
    "v_mfma_f32_16x16x32_bf16 v[172:175], v[224:227], a[216:219], v[172:175]\n"
    "v_rcp_f32 v26, v26\n"
    "v_mfma_f32_16x16x32_bf16 v[176:179], v[228:231], a[24:27], v[176:179]\n"
    "ds_read_b64_tr_b16 v[248:249], v6 offset:28672\n"
    "ds_read_b64_tr_b16 v[250:251], v6 offset:29184\n"
    "v_mfma_f32_16x16x32_bf16 v[180:183], v[228:231], a[88:91], v[180:183]\n"
    "s_waitcnt lgkmcnt(8)\n"
    "v_rcp_f32 v27, v27\n"
    "v_mfma_f32_16x16x32_bf16 v[184:187], v[228:231], a[152:155], v[184:187]\n"
    "s_sub_u32 s58, 15, s54\n"
    "s_lshl_b32 s58, s58, 7\n"
    "s_lshl_b32 s59, s52, 5\n"
    "s_add_u32 s78, s58, s59\n"
    "s_lshl_b32 s58, s54, 7\n"
    "s_add_u32 s79, s58, s59\n"
    "s_and_b32 s83, s52, 1\n"
    "s_lshl_b32 s83, s83, 14\n"
    "s_lshl_b32 s58, s53, 19\n"
    "s_lshl_b32 s59, s52, 12\n"
    "s_add_u32 s58, s58, s59\n"
    "s_add_u32 s71, s58, 0x400000\n"
    "s_add_u32 s72, s58, 0x1400000\n"
    "s_add_u32 s75, s59, 0x10000\n"
    "s_add_u32 s76, s59, 0x18000\n"
    "s_mov_b32 s73, 0x4000\n"
    "s_mov_b32 s74, 0x4000\n"
    "s_lshl_b32 s58, s78, 13\n"
    "s_lshl_b32 s59, s53, 12\n"
    "s_add_u32 s58, s58, s59\n"
    "s_add_u32 s81, s58, 0x2c00000\n"
    "s_lshl_b32 s58, s79, 13\n"
    "s_lshl_b32 s59, s53, 12\n"
    "s_add_u32 s58, s58, s59\n"
    "s_add_u32 s82, s58, 0x2c00000\n"
    "v_mfma_f32_16x16x32_bf16 v[188:191], v[228:231], a[216:219], v[188:191]\n"
    "s_add_u32 s56, s61, 0x280080\n"
    "v_rcp_f32 v28, v28\n"
    "v_mfma_f32_16x16x32_bf16 v[192:195], v[232:235], a[24:27], v[192:195]\n"
    "ds_read_b64_tr_b16 v[252:253], v8 offset:28672\n"
    "ds_read_b64_tr_b16 v[254:255], v8 offset:29184\n"
    "v_mfma_f32_16x16x32_bf16 v[196:199], v[232:235], a[88:91], v[196:199]\n"
    "s_waitcnt lgkmcnt(8)\n"
    "v_mul_f32 v52, v52, v25\n"
    "v_mul_f32 v53, v53, v26\n"
    "v_mfma_f32_16x16x32_bf16 v[200:203], v[232:235], a[152:155], v[200:203]\n"
    "s_add_u32 m0, s60, 40960\n"
    "s_nop 0\n"
    "buffer_load_dwordx4 v2, s[36:39], s56 offen lds\n"
    "v_mul_f32 v54, v54, v27\n"
    "v_mfma_f32_16x16x32_bf16 v[204:207], v[232:235], a[216:219], v[204:207]\n"
    "v_mul_f32 v55, v55, v28\n"
    "v_cvt_pk_bf16_f32 v22, v52, v53\n"
    "v_mfma_f32_16x16x32_bf16 v[208:211], v[236:239], a[24:27], v[208:211]\n"
    "ds_read_b64_tr_b16 v[224:225], v4 offset:32768\n"
    "ds_read_b64_tr_b16 v[226:227], v4 offset:33280\n"
    "v_mfma_f32_16x16x32_bf16 v[212:215], v[236:239], a[88:91], v[212:215]\n"
    "s_waitcnt lgkmcnt(8)\n"
    "v_cvt_pk_bf16_f32 v23, v54, v55\n"
    "v_mfma_f32_16x16x32_bf16 v[216:219], v[236:239], a[152:155], v[216:219]\n"
    "s_add_u32 s58, s67, 0xe400\n"
    "buffer_store_dwordx4 v[20:23], v1, s[44:47], s58 offen sc1\n"
    "v_mul_f32 v25, 0xbfb8aa3b, v40\n"
    "v_mfma_f32_16x16x32_bf16 v[220:223], v[236:239], a[216:219], v[220:223]\n"
    "v_mul_f32 v26, 0xbfb8aa3b, v41\n"
    "v_mul_f32 v27, 0xbfb8aa3b, v42\n"
    "v_mfma_f32_16x16x32_bf16 v[160:163], v[240:243], a[28:31], v[160:163]\n"
    "ds_read_b64_tr_b16 v[228:229], v5 offset:32768\n"
    "ds_read_b64_tr_b16 v[230:231], v5 offset:33280\n"
    "v_mfma_f32_16x16x32_bf16 v[164:167], v[240:243], a[92:95], v[164:167]\n"
    "s_waitcnt lgkmcnt(8)\n"
    "v_mul_f32 v28, 0xbfb8aa3b, v43\n"
    "v_mfma_f32_16x16x32_bf16 v[168:171], v[240:243], a[156:159], v[168:171]\n"
    "v_exp_f32 v25, v25\n"
    "v_mfma_f32_16x16x32_bf16 v[172:175], v[240:243], a[220:223], v[172:175]\n"
    "v_exp_f32 v26, v26\n"
    "v_mfma_f32_16x16x32_bf16 v[176:179], v[244:247], a[28:31], v[176:179]\n"
    "ds_read_b64_tr_b16 v[232:233], v6 offset:32768\n"
    "ds_read_b64_tr_b16 v[234:235], v6 offset:33280\n"
    "v_mfma_f32_16x16x32_bf16 v[180:183], v[244:247], a[92:95], v[180:183]\n"
    "s_waitcnt lgkmcnt(8)\n"
    "v_exp_f32 v27, v27\n"
    "v_mfma_f32_16x16x32_bf16 v[184:187], v[244:247], a[156:159], v[184:187]\n"
    "s_add_u32 m0, s60, 41984\n"
    "s_nop 0\n"
    "buffer_load_dwordx4 v3, s[36:39], s56 offen lds\n"
    "v_mfma_f32_16x16x32_bf16 v[188:191], v[244:247], a[220:223], v[188:191]\n"
    "v_exp_f32 v28, v28\n"
    "v_mfma_f32_16x16x32_bf16 v[192:195], v[248:251], a[28:31], v[192:195]\n"
    "ds_read_b64_tr_b16 v[236:237], v8 offset:32768\n"
    "ds_read_b64_tr_b16 v[238:239], v8 offset:33280\n"
    "v_mfma_f32_16x16x32_bf16 v[196:199], v[248:251], a[92:95], v[196:199]\n"
    "s_waitcnt lgkmcnt(8)\n"
    "v_mul_f32 v40, v40, v72\n"
    "v_mul_f32 v41, v41, v73\n"
    "v_mfma_f32_16x16x32_bf16 v[200:203], v[248:251], a[156:159], v[200:203]\n"
    "v_mul_f32 v42, v42, v74\n"
    "v_mul_f32 v43, v43, v75\n"
    "v_mfma_f32_16x16x32_bf16 v[204:207], v[248:251], a[220:223], v[204:207]\n"
    "v_add_f32 v25, 1.0, v25\n"
    "v_mfma_f32_16x16x32_bf16 v[208:211], v[252:255], a[28:31], v[208:211]\n"
    "ds_read_b64_tr_b16 v[240:241], v4 offset:36864\n"
    "ds_read_b64_tr_b16 v[242:243], v4 offset:37376\n"
    "v_mfma_f32_16x16x32_bf16 v[212:215], v[252:255], a[92:95], v[212:215]\n"
    "s_waitcnt lgkmcnt(8)\n"
    "v_add_f32 v26, 1.0, v26\n"
    "v_add_f32 v27, 1.0, v27\n"
    "v_mfma_f32_16x16x32_bf16 v[216:219], v[252:255], a[156:159], v[216:219]\n"
    "v_add_f32 v28, 1.0, v28\n"
    "v_mfma_f32_16x16x32_bf16 v[220:223], v[252:255], a[220:223], v[220:223]\n"
    "v_rcp_f32 v25, v25\n"
    "v_mfma_f32_16x16x32_bf16 v[160:163], v[224:227], a[32:35], v[160:163]\n"
    "ds_read_b64_tr_b16 v[244:245], v5 offset:36864\n"
    "ds_read_b64_tr_b16 v[246:247], v5 offset:37376\n"
    "v_mfma_f32_16x16x32_bf16 v[164:167], v[224:227], a[96:99], v[164:167]\n"
    "s_waitcnt lgkmcnt(8)\n"
    "s_add_u32 s56, s61, 0x300080\n"
    "v_mfma_f32_16x16x32_bf16 v[168:171], v[224:227], a[160:163], v[168:171]\n"
    "s_add_u32 m0, s60, 49152\n"
    "s_nop 0\n"
    "buffer_load_dwordx4 v2, s[36:39], s56 offen lds\n"
    "v_mfma_f32_16x16x32_bf16 v[172:175], v[224:227], a[224:227], v[172:175]\n"
    "v_rcp_f32 v26, v26\n"
    "v_mfma_f32_16x16x32_bf16 v[176:179], v[228:231], a[32:35], v[176:179]\n"
    "ds_read_b64_tr_b16 v[248:249], v6 offset:36864\n"
    "ds_read_b64_tr_b16 v[250:251], v6 offset:37376\n"
    "v_mfma_f32_16x16x32_bf16 v[180:183], v[228:231], a[96:99], v[180:183]\n"
    "s_waitcnt lgkmcnt(8)\n"
    "v_rcp_f32 v27, v27\n"
    "v_mfma_f32_16x16x32_bf16 v[184:187], v[228:231], a[160:163], v[184:187]\n"
    "v_rcp_f32 v28, v28\n"
    "v_mfma_f32_16x16x32_bf16 v[188:191], v[228:231], a[224:227], v[188:191]\n"
    "v_mul_f32 v40, v40, v25\n"
    "v_mul_f32 v41, v41, v26\n"
    "v_mfma_f32_16x16x32_bf16 v[192:195], v[232:235], a[32:35], v[192:195]\n"
    "ds_read_b64_tr_b16 v[252:253], v8 offset:36864\n"
    "ds_read_b64_tr_b16 v[254:255], v8 offset:37376\n"
    "v_mfma_f32_16x16x32_bf16 v[196:199], v[232:235], a[96:99], v[196:199]\n"
    "s_waitcnt lgkmcnt(8)\n"
    "v_mul_f32 v42, v42, v27\n"
    "v_mul_f32 v43, v43, v28\n"
    "v_mfma_f32_16x16x32_bf16 v[200:203], v[232:235], a[160:163], v[200:203]\n"
    "v_cvt_pk_bf16_f32 v16, v40, v41\n"
    "v_cvt_pk_bf16_f32 v17, v42, v43\n"
    "v_mfma_f32_16x16x32_bf16 v[204:207], v[232:235], a[224:227], v[204:207]\n"
    "v_mul_f32 v25, 0xbfb8aa3b, v56\n"
    "v_mfma_f32_16x16x32_bf16 v[208:211], v[236:239], a[32:35], v[208:211]\n"
    "ds_read_b64_tr_b16 v[224:225], v4 offset:40960\n"
    "ds_read_b64_tr_b16 v[226:227], v4 offset:41472\n"
    "v_mfma_f32_16x16x32_bf16 v[212:215], v[236:239], a[96:99], v[212:215]\n"
    "s_waitcnt lgkmcnt(8)\n"
    "v_mul_f32 v26, 0xbfb8aa3b, v57\n"
    "v_mul_f32 v27, 0xbfb8aa3b, v58\n"
    "v_mfma_f32_16x16x32_bf16 v[216:219], v[236:239], a[160:163], v[216:219]\n"
    "s_add_u32 m0, s60, 50176\n"
    "s_nop 0\n"
    "buffer_load_dwordx4 v3, s[36:39], s56 offen lds\n"
    "v_mul_f32 v28, 0xbfb8aa3b, v59\n"
    "v_mfma_f32_16x16x32_bf16 v[220:223], v[236:239], a[224:227], v[220:223]\n"
    "v_exp_f32 v25, v25\n"
    "v_mfma_f32_16x16x32_bf16 v[160:163], v[240:243], a[36:39], v[160:163]\n"
    "ds_read_b64_tr_b16 v[228:229], v5 offset:40960\n"
    "ds_read_b64_tr_b16 v[230:231], v5 offset:41472\n"
    "v_mfma_f32_16x16x32_bf16 v[164:167], v[240:243], a[100:103], v[164:167]\n"
    "s_waitcnt lgkmcnt(8)\n"
    "v_exp_f32 v26, v26\n"
    "v_mfma_f32_16x16x32_bf16 v[168:171], v[240:243], a[164:167], v[168:171]\n"
    "v_exp_f32 v27, v27\n"
    "v_mfma_f32_16x16x32_bf16 v[172:175], v[240:243], a[228:231], v[172:175]\n"
    "v_exp_f32 v28, v28\n"
    "v_mfma_f32_16x16x32_bf16 v[176:179], v[244:247], a[36:39], v[176:179]\n"
    "ds_read_b64_tr_b16 v[232:233], v6 offset:40960\n"
    "ds_read_b64_tr_b16 v[234:235], v6 offset:41472\n"
    "v_mfma_f32_16x16x32_bf16 v[180:183], v[244:247], a[100:103], v[180:183]\n"
    "s_waitcnt lgkmcnt(8)\n"
    "v_mul_f32 v56, v56, v88\n"
    "v_mul_f32 v57, v57, v89\n"
    "v_mfma_f32_16x16x32_bf16 v[184:187], v[244:247], a[164:167], v[184:187]\n"
    "v_mul_f32 v58, v58, v90\n"
    "v_mul_f32 v59, v59, v91\n"
    "v_mfma_f32_16x16x32_bf16 v[188:191], v[244:247], a[228:231], v[188:191]\n"
    "v_add_f32 v25, 1.0, v25\n"
    "v_mfma_f32_16x16x32_bf16 v[192:195], v[248:251], a[36:39], v[192:195]\n"
    "ds_read_b64_tr_b16 v[236:237], v8 offset:40960\n"
    "ds_read_b64_tr_b16 v[238:239], v8 offset:41472\n"
    "v_mfma_f32_16x16x32_bf16 v[196:199], v[248:251], a[100:103], v[196:199]\n"
    "s_waitcnt lgkmcnt(8)\n"
    "s_add_u32 s56, s61, 0x380080\n"
    "v_mfma_f32_16x16x32_bf16 v[200:203], v[248:251], a[164:167], v[200:203]\n"
    "s_add_u32 m0, s60, 57344\n"
    "s_nop 0\n"
    "buffer_load_dwordx4 v2, s[36:39], s56 offen lds\n"
    "v_add_f32 v26, 1.0, v26\n"
    "v_mfma_f32_16x16x32_bf16 v[204:207], v[248:251], a[228:231], v[204:207]\n"
    "v_add_f32 v27, 1.0, v27\n"
    "v_add_f32 v28, 1.0, v28\n"
    "v_mfma_f32_16x16x32_bf16 v[208:211], v[252:255], a[36:39], v[208:211]\n"
    "ds_read_b64_tr_b16 v[240:241], v4 offset:45056\n"
    "ds_read_b64_tr_b16 v[242:243], v4 offset:45568\n"
    "v_mfma_f32_16x16x32_bf16 v[212:215], v[252:255], a[100:103], v[212:215]\n"
    "s_waitcnt lgkmcnt(8)\n"
    "v_rcp_f32 v25, v25\n"
    "v_mfma_f32_16x16x32_bf16 v[216:219], v[252:255], a[164:167], v[216:219]\n"
    "v_rcp_f32 v26, v26\n"
    "v_mfma_f32_16x16x32_bf16 v[220:223], v[252:255], a[228:231], v[220:223]\n"
    "v_rcp_f32 v27, v27\n"
    "v_mfma_f32_16x16x32_bf16 v[160:163], v[224:227], a[40:43], v[160:163]\n"
    "ds_read_b64_tr_b16 v[244:245], v5 offset:45056\n"
    "ds_read_b64_tr_b16 v[246:247], v5 offset:45568\n"
    "v_mfma_f32_16x16x32_bf16 v[164:167], v[224:227], a[104:107], v[164:167]\n"
    "s_waitcnt lgkmcnt(8)\n"
    "v_rcp_f32 v28, v28\n"
    "v_mfma_f32_16x16x32_bf16 v[168:171], v[224:227], a[168:171], v[168:171]\n"
    "s_waitcnt vmcnt(8)\n"
    "s_lshl_b32 s96, s54, 2\n"
    "s_add_u32 s96, s96, s52\n"
    "s_lshl_b32 s96, s96, 2\n"
    "s_lshl_b32 s95, s53, 7\n"
    "s_add_u32 s96, s96, s95\n"
    "s_add_u32 s96, s96, 0x2000\n"
    "v_mov_b32 v24, s97\n"
    "buffer_store_dword v24, v7, s[44:47], s96 offen sc1\n"
    "v_mfma_f32_16x16x32_bf16 v[172:175], v[224:227], a[232:235], v[172:175]\n"
    "v_mul_f32 v56, v56, v25\n"
    "v_mul_f32 v57, v57, v26\n"
    "v_mfma_f32_16x16x32_bf16 v[176:179], v[228:231], a[40:43], v[176:179]\n"
    "ds_read_b64_tr_b16 v[248:249], v6 offset:45056\n"
    "ds_read_b64_tr_b16 v[250:251], v6 offset:45568\n"
    "v_mfma_f32_16x16x32_bf16 v[180:183], v[228:231], a[104:107], v[180:183]\n"
    "s_waitcnt lgkmcnt(8)\n"
    "v_mul_f32 v58, v58, v27\n"
    "v_mul_f32 v59, v59, v28\n"
    "v_mfma_f32_16x16x32_bf16 v[184:187], v[228:231], a[168:171], v[184:187]\n"
    "s_add_u32 m0, s60, 58368\n"
    "s_nop 0\n"
    "buffer_load_dwordx4 v3, s[36:39], s56 offen lds\n"
    "v_cvt_pk_bf16_f32 v18, v56, v57\n"
    "v_mfma_f32_16x16x32_bf16 v[188:191], v[228:231], a[232:235], v[188:191]\n"
    "v_cvt_pk_bf16_f32 v19, v58, v59\n"
    "v_mfma_f32_16x16x32_bf16 v[192:195], v[232:235], a[40:43], v[192:195]\n"
    "ds_read_b64_tr_b16 v[252:253], v8 offset:45056\n"
    "ds_read_b64_tr_b16 v[254:255], v8 offset:45568\n"
    "v_mfma_f32_16x16x32_bf16 v[196:199], v[232:235], a[104:107], v[196:199]\n"
    "s_waitcnt lgkmcnt(8)\n"
    "v_mfma_f32_16x16x32_bf16 v[200:203], v[232:235], a[168:171], v[200:203]\n"
    "s_add_u32 s58, s68, 0x400\n"
    "buffer_store_dwordx4 v[16:19], v1, s[44:47], s58 offen sc1\n"
    "v_mul_f32 v25, 0xbfb8aa3b, v44\n"
    "v_mfma_f32_16x16x32_bf16 v[204:207], v[232:235], a[232:235], v[204:207]\n"
    "v_mul_f32 v26, 0xbfb8aa3b, v45\n"
    "v_mul_f32 v27, 0xbfb8aa3b, v46\n"
    "v_mfma_f32_16x16x32_bf16 v[208:211], v[236:239], a[40:43], v[208:211]\n"
    "ds_read_b64_tr_b16 v[224:225], v4 offset:49152\n"
    "ds_read_b64_tr_b16 v[226:227], v4 offset:49664\n"
    "v_mfma_f32_16x16x32_bf16 v[212:215], v[236:239], a[104:107], v[212:215]\n"
    "s_waitcnt lgkmcnt(8)\n"
    "v_mul_f32 v28, 0xbfb8aa3b, v47\n"
    "v_mfma_f32_16x16x32_bf16 v[216:219], v[236:239], a[168:171], v[216:219]\n"
    "v_exp_f32 v25, v25\n"
    "v_mfma_f32_16x16x32_bf16 v[220:223], v[236:239], a[232:235], v[220:223]\n"
    "v_exp_f32 v26, v26\n"
    "v_mfma_f32_16x16x32_bf16 v[160:163], v[240:243], a[44:47], v[160:163]\n"
    "ds_read_b64_tr_b16 v[228:229], v5 offset:49152\n"
    "ds_read_b64_tr_b16 v[230:231], v5 offset:49664\n"
    "v_mfma_f32_16x16x32_bf16 v[164:167], v[240:243], a[108:111], v[164:167]\n"
    "s_waitcnt lgkmcnt(8)\n"
    "v_exp_f32 v27, v27\n"
    "v_mfma_f32_16x16x32_bf16 v[168:171], v[240:243], a[172:175], v[168:171]\n"
    "v_exp_f32 v28, v28\n"
    "v_mfma_f32_16x16x32_bf16 v[172:175], v[240:243], a[236:239], v[172:175]\n"
    "v_mul_f32 v44, v44, v76\n"
    "v_mul_f32 v45, v45, v77\n"
    "v_mfma_f32_16x16x32_bf16 v[176:179], v[244:247], a[44:47], v[176:179]\n"
    "ds_read_b64_tr_b16 v[232:233], v6 offset:49152\n"
    "ds_read_b64_tr_b16 v[234:235], v6 offset:49664\n"
    "v_mfma_f32_16x16x32_bf16 v[180:183], v[244:247], a[108:111], v[180:183]\n"
    "s_waitcnt lgkmcnt(8)\n"
    "v_mul_f32 v46, v46, v78\n"
    "v_mul_f32 v47, v47, v79\n"
    "v_mfma_f32_16x16x32_bf16 v[184:187], v[244:247], a[172:175], v[184:187]\n"
    "v_add_f32 v25, 1.0, v25\n"
    "v_add_f32 v26, 1.0, v26\n"
    "v_mfma_f32_16x16x32_bf16 v[188:191], v[244:247], a[236:239], v[188:191]\n"
    "v_add_f32 v27, 1.0, v27\n"
    "v_mfma_f32_16x16x32_bf16 v[192:195], v[248:251], a[44:47], v[192:195]\n"
    "ds_read_b64_tr_b16 v[236:237], v8 offset:49152\n"
    "ds_read_b64_tr_b16 v[238:239], v8 offset:49664\n"
    "v_mfma_f32_16x16x32_bf16 v[196:199], v[248:251], a[108:111], v[196:199]\n"
    "s_waitcnt lgkmcnt(8)\n"
    "v_add_f32 v28, 1.0, v28\n"
    "v_mfma_f32_16x16x32_bf16 v[200:203], v[248:251], a[172:175], v[200:203]\n"
    "v_rcp_f32 v25, v25\n"
    "v_mfma_f32_16x16x32_bf16 v[204:207], v[248:251], a[236:239], v[204:207]\n"
    "v_rcp_f32 v26, v26\n"
    "v_mfma_f32_16x16x32_bf16 v[208:211], v[252:255], a[44:47], v[208:211]\n"
    "ds_read_b64_tr_b16 v[240:241], v4 offset:53248\n"
    "ds_read_b64_tr_b16 v[242:243], v4 offset:53760\n"
    "v_mfma_f32_16x16x32_bf16 v[212:215], v[252:255], a[108:111], v[212:215]\n"
    "s_waitcnt lgkmcnt(8)\n"
    "v_rcp_f32 v27, v27\n"
    "v_mfma_f32_16x16x32_bf16 v[216:219], v[252:255], a[172:175], v[216:219]\n"
    "v_rcp_f32 v28, v28\n"
    "v_mfma_f32_16x16x32_bf16 v[220:223], v[252:255], a[236:239], v[220:223]\n"
    "v_mul_f32 v44, v44, v25\n"
    "v_mul_f32 v45, v45, v26\n"
    "v_mfma_f32_16x16x32_bf16 v[160:163], v[224:227], a[48:51], v[160:163]\n"
    "ds_read_b64_tr_b16 v[244:245], v5 offset:53248\n"
    "ds_read_b64_tr_b16 v[246:247], v5 offset:53760\n"
    "v_mfma_f32_16x16x32_bf16 v[164:167], v[224:227], a[112:115], v[164:167]\n"
    "s_waitcnt lgkmcnt(8)\n"
    "v_mul_f32 v46, v46, v27\n"
    "v_mul_f32 v47, v47, v28\n"
    "v_mfma_f32_16x16x32_bf16 v[168:171], v[224:227], a[176:179], v[168:171]\n"
    "v_cvt_pk_bf16_f32 v20, v44, v45\n"
    "v_cvt_pk_bf16_f32 v21, v46, v47\n"
    "v_mfma_f32_16x16x32_bf16 v[172:175], v[224:227], a[240:243], v[172:175]\n"
    "v_mul_f32 v25, 0xbfb8aa3b, v60\n"
    "v_mfma_f32_16x16x32_bf16 v[176:179], v[228:231], a[48:51], v[176:179]\n"
    "ds_read_b64_tr_b16 v[248:249], v6 offset:53248\n"
    "ds_read_b64_tr_b16 v[250:251], v6 offset:53760\n"
    "v_mfma_f32_16x16x32_bf16 v[180:183], v[228:231], a[112:115], v[180:183]\n"
    "s_waitcnt lgkmcnt(8)\n"
    "v_mul_f32 v26, 0xbfb8aa3b, v61\n"
    "v_mul_f32 v27, 0xbfb8aa3b, v62\n"
    "v_mfma_f32_16x16x32_bf16 v[184:187], v[228:231], a[176:179], v[184:187]\n"
    "v_mul_f32 v28, 0xbfb8aa3b, v63\n"
    "v_mfma_f32_16x16x32_bf16 v[188:191], v[228:231], a[240:243], v[188:191]\n"
    "v_exp_f32 v25, v25\n"
    "v_mfma_f32_16x16x32_bf16 v[192:195], v[232:235], a[48:51], v[192:195]\n"
    "ds_read_b64_tr_b16 v[252:253], v8 offset:53248\n"
    "ds_read_b64_tr_b16 v[254:255], v8 offset:53760\n"
    "v_mfma_f32_16x16x32_bf16 v[196:199], v[232:235], a[112:115], v[196:199]\n"
    "s_waitcnt lgkmcnt(8)\n"
    "v_exp_f32 v26, v26\n"
    "v_mfma_f32_16x16x32_bf16 v[200:203], v[232:235], a[176:179], v[200:203]\n"
    "v_exp_f32 v27, v27\n"
    "v_mfma_f32_16x16x32_bf16 v[204:207], v[232:235], a[240:243], v[204:207]\n"
    "v_exp_f32 v28, v28\n"
    "v_mfma_f32_16x16x32_bf16 v[208:211], v[236:239], a[48:51], v[208:211]\n"
    "ds_read_b64_tr_b16 v[224:225], v4 offset:57344\n"
    "ds_read_b64_tr_b16 v[226:227], v4 offset:57856\n"
    "v_mfma_f32_16x16x32_bf16 v[212:215], v[236:239], a[112:115], v[212:215]\n"
    "s_waitcnt lgkmcnt(8)\n"
    "v_mul_f32 v60, v60, v92\n"
    "v_mul_f32 v61, v61, v93\n"
    "v_mfma_f32_16x16x32_bf16 v[216:219], v[236:239], a[176:179], v[216:219]\n"
    "v_mul_f32 v62, v62, v94\n"
    "v_mul_f32 v63, v63, v95\n"
    "v_mfma_f32_16x16x32_bf16 v[220:223], v[236:239], a[240:243], v[220:223]\n"
    "v_add_f32 v25, 1.0, v25\n"
    "v_mfma_f32_16x16x32_bf16 v[160:163], v[240:243], a[52:55], v[160:163]\n"
    "ds_read_b64_tr_b16 v[228:229], v5 offset:57344\n"
    "ds_read_b64_tr_b16 v[230:231], v5 offset:57856\n"
    "v_mfma_f32_16x16x32_bf16 v[164:167], v[240:243], a[116:119], v[164:167]\n"
    "s_waitcnt lgkmcnt(8)\n"
    "v_add_f32 v26, 1.0, v26\n"
    "v_add_f32 v27, 1.0, v27\n"
    "v_mfma_f32_16x16x32_bf16 v[168:171], v[240:243], a[180:183], v[168:171]\n"
    "v_add_f32 v28, 1.0, v28\n"
    "v_mfma_f32_16x16x32_bf16 v[172:175], v[240:243], a[244:247], v[172:175]\n"
    "v_rcp_f32 v25, v25\n"
    "v_mfma_f32_16x16x32_bf16 v[176:179], v[244:247], a[52:55], v[176:179]\n"
    "ds_read_b64_tr_b16 v[232:233], v6 offset:57344\n"
    "ds_read_b64_tr_b16 v[234:235], v6 offset:57856\n"
    "v_mfma_f32_16x16x32_bf16 v[180:183], v[244:247], a[116:119], v[180:183]\n"
    "s_waitcnt lgkmcnt(8)\n"
    "v_rcp_f32 v26, v26\n"
    "v_mfma_f32_16x16x32_bf16 v[184:187], v[244:247], a[180:183], v[184:187]\n"
    "v_rcp_f32 v27, v27\n"
    "v_mfma_f32_16x16x32_bf16 v[188:191], v[244:247], a[244:247], v[188:191]\n"
    "v_rcp_f32 v28, v28\n"
    "v_mfma_f32_16x16x32_bf16 v[192:195], v[248:251], a[52:55], v[192:195]\n"
    "ds_read_b64_tr_b16 v[236:237], v8 offset:57344\n"
    "ds_read_b64_tr_b16 v[238:239], v8 offset:57856\n"
    "v_mfma_f32_16x16x32_bf16 v[196:199], v[248:251], a[116:119], v[196:199]\n"
    "s_waitcnt lgkmcnt(8)\n"
    "v_mul_f32 v60, v60, v25\n"
    "v_mul_f32 v61, v61, v26\n"
    "v_mfma_f32_16x16x32_bf16 v[200:203], v[248:251], a[180:183], v[200:203]\n"
    "v_mul_f32 v62, v62, v27\n"
    "v_mul_f32 v63, v63, v28\n"
    "v_mfma_f32_16x16x32_bf16 v[204:207], v[248:251], a[244:247], v[204:207]\n"
    "v_cvt_pk_bf16_f32 v22, v60, v61\n"
    "v_mfma_f32_16x16x32_bf16 v[208:211], v[252:255], a[52:55], v[208:211]\n"
    "ds_read_b64_tr_b16 v[240:241], v4 offset:61440\n"
    "ds_read_b64_tr_b16 v[242:243], v4 offset:61952\n"
    "v_mfma_f32_16x16x32_bf16 v[212:215], v[252:255], a[116:119], v[212:215]\n"
    "s_waitcnt lgkmcnt(8)\n"
    "v_cvt_pk_bf16_f32 v23, v62, v63\n"
    "v_mfma_f32_16x16x32_bf16 v[216:219], v[252:255], a[180:183], v[216:219]\n"
    "s_add_u32 s58, s68, 0xe400\n"
    "buffer_store_dwordx4 v[20:23], v1, s[44:47], s58 offen sc1\n"
    "v_mfma_f32_16x16x32_bf16 v[220:223], v[252:255], a[244:247], v[220:223]\n"
    "v_mfma_f32_16x16x32_bf16 v[160:163], v[224:227], a[56:59], v[160:163]\n"
    "ds_read_b64_tr_b16 v[244:245], v5 offset:61440\n"
    "ds_read_b64_tr_b16 v[246:247], v5 offset:61952\n"
    "v_mfma_f32_16x16x32_bf16 v[164:167], v[224:227], a[120:123], v[164:167]\n"
    "s_waitcnt lgkmcnt(8)\n"
    "v_mfma_f32_16x16x32_bf16 v[168:171], v[224:227], a[184:187], v[168:171]\n"
    "v_mfma_f32_16x16x32_bf16 v[172:175], v[224:227], a[248:251], v[172:175]\n"
    "v_mfma_f32_16x16x32_bf16 v[176:179], v[228:231], a[56:59], v[176:179]\n"
    "ds_read_b64_tr_b16 v[248:249], v6 offset:61440\n"
    "ds_read_b64_tr_b16 v[250:251], v6 offset:61952\n"
    "v_mfma_f32_16x16x32_bf16 v[180:183], v[228:231], a[120:123], v[180:183]\n"
    "s_waitcnt lgkmcnt(8)\n"
    "v_mfma_f32_16x16x32_bf16 v[184:187], v[228:231], a[184:187], v[184:187]\n"
    "v_mfma_f32_16x16x32_bf16 v[188:191], v[228:231], a[248:251], v[188:191]\n"
    "v_mfma_f32_16x16x32_bf16 v[192:195], v[232:235], a[56:59], v[192:195]\n"
    "ds_read_b64_tr_b16 v[252:253], v8 offset:61440\n"
    "ds_read_b64_tr_b16 v[254:255], v8 offset:61952\n"
    "v_mfma_f32_16x16x32_bf16 v[196:199], v[232:235], a[120:123], v[196:199]\n"
    "s_waitcnt lgkmcnt(8)\n"
    "v_mfma_f32_16x16x32_bf16 v[200:203], v[232:235], a[184:187], v[200:203]\n"
    "v_mfma_f32_16x16x32_bf16 v[204:207], v[232:235], a[248:251], v[204:207]\n"
    "v_xor_b32 v4, 0x10000, v4\n"
    "v_xor_b32 v5, 0x10000, v5\n"
    "v_xor_b32 v6, 0x10000, v6\n"
    "v_xor_b32 v8, 0x10000, v8\n"
    "s_waitcnt vmcnt(2)\n"
    "s_barrier\n"
    "v_mfma_f32_16x16x32_bf16 v[208:211], v[236:239], a[56:59], v[208:211]\n"
    "ds_read_b64_tr_b16 v[224:225], v4 offset:0\n"
    "ds_read_b64_tr_b16 v[226:227], v4 offset:512\n"
    "v_mfma_f32_16x16x32_bf16 v[212:215], v[236:239], a[120:123], v[212:215]\n"
    "s_waitcnt lgkmcnt(8)\n"
    "v_mfma_f32_16x16x32_bf16 v[216:219], v[236:239], a[184:187], v[216:219]\n"
    "v_mfma_f32_16x16x32_bf16 v[220:223], v[236:239], a[248:251], v[220:223]\n"
    "v_mfma_f32_16x16x32_bf16 v[160:163], v[240:243], a[60:63], v[160:163]\n"
    "ds_read_b64_tr_b16 v[228:229], v5 offset:0\n"
    "ds_read_b64_tr_b16 v[230:231], v5 offset:512\n"
    "v_mfma_f32_16x16x32_bf16 v[164:167], v[240:243], a[124:127], v[164:167]\n"
    "s_waitcnt lgkmcnt(8)\n"
    "v_mfma_f32_16x16x32_bf16 v[168:171], v[240:243], a[188:191], v[168:171]\n"
    "v_mfma_f32_16x16x32_bf16 v[172:175], v[240:243], a[252:255], v[172:175]\n"
    "v_mfma_f32_16x16x32_bf16 v[176:179], v[244:247], a[60:63], v[176:179]\n"
    "ds_read_b64_tr_b16 v[232:233], v6 offset:0\n"
    "ds_read_b64_tr_b16 v[234:235], v6 offset:512\n"
    "v_mfma_f32_16x16x32_bf16 v[180:183], v[244:247], a[124:127], v[180:183]\n"
    "s_waitcnt lgkmcnt(8)\n"
    "v_mfma_f32_16x16x32_bf16 v[184:187], v[244:247], a[188:191], v[184:187]\n"
    "v_mfma_f32_16x16x32_bf16 v[188:191], v[244:247], a[252:255], v[188:191]\n"
    "v_mfma_f32_16x16x32_bf16 v[192:195], v[248:251], a[60:63], v[192:195]\n"
    "ds_read_b64_tr_b16 v[236:237], v8 offset:0\n"
    "ds_read_b64_tr_b16 v[238:239], v8 offset:512\n"
    "v_mfma_f32_16x16x32_bf16 v[196:199], v[248:251], a[124:127], v[196:199]\n"
    "s_waitcnt lgkmcnt(8)\n"
    "v_mfma_f32_16x16x32_bf16 v[200:203], v[248:251], a[188:191], v[200:203]\n"
    "v_mfma_f32_16x16x32_bf16 v[204:207], v[248:251], a[252:255], v[204:207]\n"
    "v_mfma_f32_16x16x32_bf16 v[208:211], v[252:255], a[60:63], v[208:211]\n"
    "ds_read_b64_tr_b16 v[240:241], v4 offset:4096\n"
    "ds_read_b64_tr_b16 v[242:243], v4 offset:4608\n"
    "v_mfma_f32_16x16x32_bf16 v[212:215], v[252:255], a[124:127], v[212:215]\n"
    "s_waitcnt lgkmcnt(8)\n"
    "v_mfma_f32_16x16x32_bf16 v[216:219], v[252:255], a[188:191], v[216:219]\n"
    "v_mfma_f32_16x16x32_bf16 v[220:223], v[252:255], a[252:255], v[220:223]\n"
    "v_mfma_f32_16x16x32_bf16 v[32:35], v[224:227], a[0:3], 0\n"
    "ds_read_b64_tr_b16 v[244:245], v5 offset:4096\n"
    "ds_read_b64_tr_b16 v[246:247], v5 offset:4608\n"
    "v_mfma_f32_16x16x32_bf16 v[36:39], v[224:227], a[64:67], 0\n"
    "s_waitcnt lgkmcnt(8)\n"
    "v_mfma_f32_16x16x32_bf16 v[40:43], v[224:227], a[128:131], 0\n"
    "v_lshrrev_b32 v29, 2, v1\n"
    "v_and_b32 v29, 0x7c, v29\n"
    "s_lshl_b32 s96, s53, 7\n"
    "s_add_u32 s96, s96, 0x2000\n"
    "buffer_load_dword v24, v29, s[44:47], s96 offen sc1\n"
    "v_mfma_f32_16x16x32_bf16 v[44:47], v[224:227], a[192:195], 0\n"
    "v_mul_f32 v160, s80, v160\n"
    "v_mul_f32 v161, s80, v161\n"
    "v_mfma_f32_16x16x32_bf16 v[48:51], v[228:231], a[0:3], 0\n"
    "ds_read_b64_tr_b16 v[248:249], v6 offset:4096\n"
    "ds_read_b64_tr_b16 v[250:251], v6 offset:4608\n"
    "v_mfma_f32_16x16x32_bf16 v[52:55], v[228:231], a[64:67], 0\n"
    "s_waitcnt lgkmcnt(8)\n"
    "v_mul_f32 v162, s80, v162\n"
    "v_mul_f32 v163, s80, v163\n"
    "v_mfma_f32_16x16x32_bf16 v[56:59], v[228:231], a[128:131], 0\n"
    "v_mul_f32 v176, s80, v176\n"
    "v_mul_f32 v177, s80, v177\n"
    "v_mfma_f32_16x16x32_bf16 v[60:63], v[228:231], a[192:195], 0\n"
    "v_mul_f32 v178, s80, v178\n"
    "v_mfma_f32_16x16x32_bf16 v[64:67], v[232:235], a[0:3], 0\n"
    "ds_read_b64_tr_b16 v[252:253], v8 offset:4096\n"
    "ds_read_b64_tr_b16 v[254:255], v8 offset:4608\n"
    "v_mfma_f32_16x16x32_bf16 v[68:71], v[232:235], a[64:67], 0\n"
    "s_waitcnt lgkmcnt(8)\n"
    "v_mul_f32 v179, s80, v179\n"
    "v_cvt_pk_bf16_f32 v96, v160, v161\n"
    "v_mfma_f32_16x16x32_bf16 v[72:75], v[232:235], a[128:131], 0\n"
    "v_cvt_pk_bf16_f32 v97, v162, v163\n"
    "v_cvt_pk_bf16_f32 v98, v176, v177\n"
    "v_mfma_f32_16x16x32_bf16 v[76:79], v[232:235], a[192:195], 0\n"
    "v_cvt_pk_bf16_f32 v99, v178, v179\n"
    "v_mfma_f32_16x16x32_bf16 v[80:83], v[236:239], a[0:3], 0\n"
    "ds_read_b64_tr_b16 v[224:225], v4 offset:8192\n"
    "ds_read_b64_tr_b16 v[226:227], v4 offset:8704\n"
    "v_mfma_f32_16x16x32_bf16 v[84:87], v[236:239], a[64:67], 0\n"
    "s_waitcnt lgkmcnt(8)\n"
    "v_mul_f32 v192, s80, v192\n"
    "v_mul_f32 v193, s80, v193\n"
    "v_mfma_f32_16x16x32_bf16 v[88:91], v[236:239], a[128:131], 0\n"
    "v_mul_f32 v194, s80, v194\n"
    "v_mul_f32 v195, s80, v195\n"
    "v_mfma_f32_16x16x32_bf16 v[92:95], v[236:239], a[192:195], 0\n"
    "v_mul_f32 v208, s80, v208\n"
    "v_mfma_f32_16x16x32_bf16 v[32:35], v[240:243], a[4:7], v[32:35]\n"
    "ds_read_b64_tr_b16 v[228:229], v5 offset:8192\n"
    "ds_read_b64_tr_b16 v[230:231], v5 offset:8704\n"
    "v_mfma_f32_16x16x32_bf16 v[36:39], v[240:243], a[68:71], v[36:39]\n"
    "s_waitcnt lgkmcnt(8)\n"
    "v_mul_f32 v209, s80, v209\n"
    "v_mul_f32 v210, s80, v210\n"
    "v_mfma_f32_16x16x32_bf16 v[40:43], v[240:243], a[132:135], v[40:43]\n"
    "v_mul_f32 v211, s80, v211\n"
    "v_cvt_pk_bf16_f32 v100, v192, v193\n"
    "v_mfma_f32_16x16x32_bf16 v[44:47], v[240:243], a[196:199], v[44:47]\n"
    "v_cvt_pk_bf16_f32 v101, v194, v195\n"
    "v_mfma_f32_16x16x32_bf16 v[48:51], v[244:247], a[4:7], v[48:51]\n"
    "ds_read_b64_tr_b16 v[232:233], v6 offset:8192\n"
    "ds_read_b64_tr_b16 v[234:235], v6 offset:8704\n"
    "v_mfma_f32_16x16x32_bf16 v[52:55], v[244:247], a[68:71], v[52:55]\n"
    "s_waitcnt lgkmcnt(8)\n"
    "v_cvt_pk_bf16_f32 v102, v208, v209\n"
    "v_cvt_pk_bf16_f32 v103, v210, v211\n"
    "v_mfma_f32_16x16x32_bf16 v[56:59], v[244:247], a[132:135], v[56:59]\n"
    "v_mul_f32 v164, s80, v164\n"
    "v_mul_f32 v165, s80, v165\n"
    "v_mfma_f32_16x16x32_bf16 v[60:63], v[244:247], a[196:199], v[60:63]\n"
    "v_mul_f32 v166, s80, v166\n"
    "v_mfma_f32_16x16x32_bf16 v[64:67], v[248:251], a[4:7], v[64:67]\n"
    "ds_read_b64_tr_b16 v[236:237], v8 offset:8192\n"
    "ds_read_b64_tr_b16 v[238:239], v8 offset:8704\n"
    "v_mfma_f32_16x16x32_bf16 v[68:71], v[248:251], a[68:71], v[68:71]\n"
    "s_waitcnt lgkmcnt(8)\n"
    "v_mul_f32 v167, s80, v167\n"
    "v_mul_f32 v180, s80, v180\n"
    "v_mfma_f32_16x16x32_bf16 v[72:75], v[248:251], a[132:135], v[72:75]\n"
    "v_mul_f32 v181, s80, v181\n"
    "v_mul_f32 v182, s80, v182\n"
    "v_mfma_f32_16x16x32_bf16 v[76:79], v[248:251], a[196:199], v[76:79]\n"
    "v_mul_f32 v183, s80, v183\n"
    "v_mfma_f32_16x16x32_bf16 v[80:83], v[252:255], a[4:7], v[80:83]\n"
    "ds_read_b64_tr_b16 v[240:241], v4 offset:12288\n"
    "ds_read_b64_tr_b16 v[242:243], v4 offset:12800\n"
    "v_mfma_f32_16x16x32_bf16 v[84:87], v[252:255], a[68:71], v[84:87]\n"
    "s_waitcnt lgkmcnt(8)\n"
    "v_cvt_pk_bf16_f32 v112, v164, v165\n"
    "v_cvt_pk_bf16_f32 v113, v166, v167\n"
    "v_mfma_f32_16x16x32_bf16 v[88:91], v[252:255], a[132:135], v[88:91]\n"
    "v_cvt_pk_bf16_f32 v114, v180, v181\n"
    "v_cvt_pk_bf16_f32 v115, v182, v183\n"
    "v_mfma_f32_16x16x32_bf16 v[92:95], v[252:255], a[196:199], v[92:95]\n"
    "v_mul_f32 v196, s80, v196\n"
    "v_mfma_f32_16x16x32_bf16 v[32:35], v[224:227], a[8:11], v[32:35]\n"
    "ds_read_b64_tr_b16 v[244:245], v5 offset:12288\n"
    "ds_read_b64_tr_b16 v[246:247], v5 offset:12800\n"
    "v_mfma_f32_16x16x32_bf16 v[36:39], v[224:227], a[72:75], v[36:39]\n"
    "s_waitcnt lgkmcnt(8)\n"
    "v_mul_f32 v197, s80, v197\n"
    "v_mul_f32 v198, s80, v198\n"
    "v_mfma_f32_16x16x32_bf16 v[40:43], v[224:227], a[136:139], v[40:43]\n"
    "v_mul_f32 v199, s80, v199\n"
    "v_mul_f32 v212, s80, v212\n"
    "v_mfma_f32_16x16x32_bf16 v[44:47], v[224:227], a[200:203], v[44:47]\n"
    "v_mul_f32 v213, s80, v213\n"
    "v_mfma_f32_16x16x32_bf16 v[48:51], v[228:231], a[8:11], v[48:51]\n"
    "ds_read_b64_tr_b16 v[248:249], v6 offset:12288\n"
    "ds_read_b64_tr_b16 v[250:251], v6 offset:12800\n"
    "v_mfma_f32_16x16x32_bf16 v[52:55], v[228:231], a[72:75], v[52:55]\n"
    "s_waitcnt lgkmcnt(8)\n"
    "v_mul_f32 v214, s80, v214\n"
    "v_mul_f32 v215, s80, v215\n"
    "v_mfma_f32_16x16x32_bf16 v[56:59], v[228:231], a[136:139], v[56:59]\n"
    "v_cvt_pk_bf16_f32 v116, v196, v197\n"
    "v_cvt_pk_bf16_f32 v117, v198, v199\n"
    "v_mfma_f32_16x16x32_bf16 v[60:63], v[228:231], a[200:203], v[60:63]\n"
    "v_cvt_pk_bf16_f32 v118, v212, v213\n"
    "v_mfma_f32_16x16x32_bf16 v[64:67], v[232:235], a[8:11], v[64:67]\n"
    "ds_read_b64_tr_b16 v[252:253], v8 offset:12288\n"
    "ds_read_b64_tr_b16 v[254:255], v8 offset:12800\n"
    "v_mfma_f32_16x16x32_bf16 v[68:71], v[232:235], a[72:75], v[68:71]\n"
    "s_waitcnt lgkmcnt(8)\n"
    "v_cvt_pk_bf16_f32 v119, v214, v215\n"
    "v_mul_f32 v168, s80, v168\n"
    "v_mfma_f32_16x16x32_bf16 v[72:75], v[232:235], a[136:139], v[72:75]\n"
    "v_mul_f32 v169, s80, v169\n"
    "v_mul_f32 v170, s80, v170\n"
    "v_mfma_f32_16x16x32_bf16 v[76:79], v[232:235], a[200:203], v[76:79]\n"
    "v_mul_f32 v171, s80, v171\n"
    "v_mfma_f32_16x16x32_bf16 v[80:83], v[236:239], a[8:11], v[80:83]\n"
    "ds_read_b64_tr_b16 v[224:225], v4 offset:16384\n"
    "ds_read_b64_tr_b16 v[226:227], v4 offset:16896\n"
    "v_mfma_f32_16x16x32_bf16 v[84:87], v[236:239], a[72:75], v[84:87]\n"
    "s_waitcnt lgkmcnt(8)\n"
    "v_mul_f32 v184, s80, v184\n"
    "v_mul_f32 v185, s80, v185\n"
    "v_mfma_f32_16x16x32_bf16 v[88:91], v[236:239], a[136:139], v[88:91]\n"
    "v_mul_f32 v186, s80, v186\n"
    "v_mul_f32 v187, s80, v187\n"
    "v_mfma_f32_16x16x32_bf16 v[92:95], v[236:239], a[200:203], v[92:95]\n"
    "v_cvt_pk_bf16_f32 v128, v168, v169\n"
    "v_mfma_f32_16x16x32_bf16 v[32:35], v[240:243], a[12:15], v[32:35]\n"
    "ds_read_b64_tr_b16 v[228:229], v5 offset:16384\n"
    "ds_read_b64_tr_b16 v[230:231], v5 offset:16896\n"
    "v_mfma_f32_16x16x32_bf16 v[36:39], v[240:243], a[76:79], v[36:39]\n"
    "s_waitcnt lgkmcnt(8)\n"
    "v_cvt_pk_bf16_f32 v129, v170, v171\n"
    "v_cvt_pk_bf16_f32 v130, v184, v185\n"
    "v_mfma_f32_16x16x32_bf16 v[40:43], v[240:243], a[140:143], v[40:43]\n"
    "v_cvt_pk_bf16_f32 v131, v186, v187\n"
    "v_mul_f32 v200, s80, v200\n"
    "v_mfma_f32_16x16x32_bf16 v[44:47], v[240:243], a[204:207], v[44:47]\n"
    "v_mul_f32 v201, s80, v201\n"
    "v_mfma_f32_16x16x32_bf16 v[48:51], v[244:247], a[12:15], v[48:51]\n"
    "ds_read_b64_tr_b16 v[232:233], v6 offset:16384\n"
    "ds_read_b64_tr_b16 v[234:235], v6 offset:16896\n"
    "v_mfma_f32_16x16x32_bf16 v[52:55], v[244:247], a[76:79], v[52:55]\n"
    "s_waitcnt lgkmcnt(8)\n"
    "v_mul_f32 v202, s80, v202\n"
    "v_mul_f32 v203, s80, v203\n"
    "v_mfma_f32_16x16x32_bf16 v[56:59], v[244:247], a[140:143], v[56:59]\n"
    "v_mul_f32 v216, s80, v216\n"
    "v_mul_f32 v217, s80, v217\n"
    "v_mfma_f32_16x16x32_bf16 v[60:63], v[244:247], a[204:207], v[60:63]\n"
    "v_mul_f32 v218, s80, v218\n"
    "v_mfma_f32_16x16x32_bf16 v[64:67], v[248:251], a[12:15], v[64:67]\n"
    "ds_read_b64_tr_b16 v[236:237], v8 offset:16384\n"
    "ds_read_b64_tr_b16 v[238:239], v8 offset:16896\n"
    "v_mfma_f32_16x16x32_bf16 v[68:71], v[248:251], a[76:79], v[68:71]\n"
    "s_waitcnt lgkmcnt(8)\n"
    "v_mul_f32 v219, s80, v219\n"
    "v_cvt_pk_bf16_f32 v132, v200, v201\n"
    "v_mfma_f32_16x16x32_bf16 v[72:75], v[248:251], a[140:143], v[72:75]\n"
    "v_cvt_pk_bf16_f32 v133, v202, v203\n"
    "v_cvt_pk_bf16_f32 v134, v216, v217\n"
    "v_mfma_f32_16x16x32_bf16 v[76:79], v[248:251], a[204:207], v[76:79]\n"
    "v_cvt_pk_bf16_f32 v135, v218, v219\n"
    "v_mfma_f32_16x16x32_bf16 v[80:83], v[252:255], a[12:15], v[80:83]\n"
    "ds_read_b64_tr_b16 v[240:241], v4 offset:20480\n"
    "ds_read_b64_tr_b16 v[242:243], v4 offset:20992\n"
    "v_mfma_f32_16x16x32_bf16 v[84:87], v[252:255], a[76:79], v[84:87]\n"
    "s_waitcnt lgkmcnt(8)\n"
    "v_mul_f32 v172, s80, v172\n"
    "v_mul_f32 v173, s80, v173\n"
    "v_mfma_f32_16x16x32_bf16 v[88:91], v[252:255], a[140:143], v[88:91]\n"
    "s_waitcnt vmcnt(0)\n"
    "v_cmp_eq_u32 vcc, s97, v24\n"
    "s_nop 0\n"
    "s_cmp_eq_u64 vcc, -1\n"
    "s_cbranch_scc1 L_pok_5_%=\n"
    "s_lshl_b32 s96, s53, 7\n"
    "s_add_u32 s96, s96, 0x2000\n"
    "s_mov_b32 s94, 2000000\n"
    "L_psp_6_%=:\n"
    "s_sleep 2\n"
    "buffer_load_dword v24, v29, s[44:47], s96 offen sc1\n"
    "s_waitcnt vmcnt(0)\n"
    "v_cmp_eq_u32 vcc, s97, v24\n"
    "s_nop 4\n"
    "s_cmp_eq_u64 vcc, -1\n"
    "s_cbranch_scc1 L_pok_5_%=\n"
    "s_sub_u32 s94, s94, 1\n"
    "s_cmp_lg_u32 s94, 0\n"
    "s_cbranch_scc1 L_psp_6_%=\n"
    "L_pok_5_%=:\n"
    "v_mfma_f32_16x16x32_bf16 v[92:95], v[252:255], a[204:207], v[92:95]\n"
    "v_mul_f32 v174, s80, v174\n"
    "v_mul_f32 v175, s80, v175\n"
    "v_mfma_f32_16x16x32_bf16 v[32:35], v[224:227], a[16:19], v[32:35]\n"
    "ds_read_b64_tr_b16 v[244:245], v5 offset:20480\n"
    "ds_read_b64_tr_b16 v[246:247], v5 offset:20992\n"
    "v_mfma_f32_16x16x32_bf16 v[36:39], v[224:227], a[80:83], v[36:39]\n"
    "s_waitcnt lgkmcnt(8)\n"
    "v_mul_f32 v188, s80, v188\n"
    "v_mul_f32 v189, s80, v189\n"
    "v_mfma_f32_16x16x32_bf16 v[40:43], v[224:227], a[144:147], v[40:43]\n"
    "v_mul_f32 v190, s80, v190\n"
    "v_mul_f32 v191, s80, v191\n"
    "v_mfma_f32_16x16x32_bf16 v[44:47], v[224:227], a[208:211], v[44:47]\n"
    "v_cvt_pk_bf16_f32 v144, v172, v173\n"
    "v_mfma_f32_16x16x32_bf16 v[48:51], v[228:231], a[16:19], v[48:51]\n"
    "ds_read_b64_tr_b16 v[248:249], v6 offset:20480\n"
    "ds_read_b64_tr_b16 v[250:251], v6 offset:20992\n"
    "v_mfma_f32_16x16x32_bf16 v[52:55], v[228:231], a[80:83], v[52:55]\n"
    "s_waitcnt lgkmcnt(8)\n"
    "s_add_u32 s56, s71, 0\n"
    "v_mfma_f32_16x16x32_bf16 v[56:59], v[228:231], a[144:147], v[56:59]\n"
    "s_add_u32 m0, s75, 0\n"
    "s_nop 0\n"
    "buffer_load_dwordx4 v1, s[44:47], s56 offen sc1 lds\n"
    "v_cvt_pk_bf16_f32 v145, v174, v175\n"
    "v_mfma_f32_16x16x32_bf16 v[60:63], v[228:231], a[208:211], v[60:63]\n"
    "v_cvt_pk_bf16_f32 v146, v188, v189\n"
    "v_cvt_pk_bf16_f32 v147, v190, v191\n"
    "v_mfma_f32_16x16x32_bf16 v[64:67], v[232:235], a[16:19], v[64:67]\n"
    "ds_read_b64_tr_b16 v[252:253], v8 offset:20480\n"
    "ds_read_b64_tr_b16 v[254:255], v8 offset:20992\n"
    "v_mfma_f32_16x16x32_bf16 v[68:71], v[232:235], a[80:83], v[68:71]\n"
    "s_waitcnt lgkmcnt(8)\n"
    "v_mul_f32 v204, s80, v204\n"
    "v_mul_f32 v205, s80, v205\n"
    "v_mfma_f32_16x16x32_bf16 v[72:75], v[232:235], a[144:147], v[72:75]\n"
    "v_mul_f32 v206, s80, v206\n"
    "v_mul_f32 v207, s80, v207\n"
    "v_mfma_f32_16x16x32_bf16 v[76:79], v[232:235], a[208:211], v[76:79]\n"
    "v_mul_f32 v220, s80, v220\n"
    "v_mfma_f32_16x16x32_bf16 v[80:83], v[236:239], a[16:19], v[80:83]\n"
    "ds_read_b64_tr_b16 v[224:225], v4 offset:24576\n"
    "ds_read_b64_tr_b16 v[226:227], v4 offset:25088\n"
    "v_mfma_f32_16x16x32_bf16 v[84:87], v[236:239], a[80:83], v[84:87]\n"
    "s_waitcnt lgkmcnt(8)\n"
    "buffer_load_dwordx4 v1, s[44:47], s56 offen offset:1024 sc1 lds\n"
    "v_mul_f32 v221, s80, v221\n"
    "v_mfma_f32_16x16x32_bf16 v[88:91], v[236:239], a[144:147], v[88:91]\n"
    "v_mul_f32 v222, s80, v222\n"
    "v_mul_f32 v223, s80, v223\n"
    "v_mfma_f32_16x16x32_bf16 v[92:95], v[236:239], a[208:211], v[92:95]\n"
    "v_cvt_pk_bf16_f32 v148, v204, v205\n"
    "v_cvt_pk_bf16_f32 v149, v206, v207\n"
    "v_mfma_f32_16x16x32_bf16 v[32:35], v[240:243], a[20:23], v[32:35]\n"
    "ds_read_b64_tr_b16 v[228:229], v5 offset:24576\n"
    "ds_read_b64_tr_b16 v[230:231], v5 offset:25088\n"
    "v_mfma_f32_16x16x32_bf16 v[36:39], v[240:243], a[84:87], v[36:39]\n"
    "s_waitcnt lgkmcnt(8)\n"
    "v_cvt_pk_bf16_f32 v150, v220, v221\n"
    "v_cvt_pk_bf16_f32 v151, v222, v223\n"
    "v_mfma_f32_16x16x32_bf16 v[40:43], v[240:243], a[148:151], v[40:43]\n"
    "v_mfma_f32_16x16x32_bf16 v[44:47], v[240:243], a[212:215], v[44:47]\n"
    "buffer_load_dwordx4 v1, s[44:47], s56 offen offset:2048 sc1 lds\n"
    "v_mfma_f32_16x16x32_bf16 v[48:51], v[244:247], a[20:23], v[48:51]\n"
    "ds_read_b64_tr_b16 v[232:233], v6 offset:24576\n"
    "ds_read_b64_tr_b16 v[234:235], v6 offset:25088\n"
    "v_mfma_f32_16x16x32_bf16 v[52:55], v[244:247], a[84:87], v[52:55]\n"
    "s_waitcnt lgkmcnt(8)\n"
    "v_mfma_f32_16x16x32_bf16 v[56:59], v[244:247], a[148:151], v[56:59]\n"
    "v_mfma_f32_16x16x32_bf16 v[60:63], v[244:247], a[212:215], v[60:63]\n"
    "v_mfma_f32_16x16x32_bf16 v[64:67], v[248:251], a[20:23], v[64:67]\n"
    "ds_read_b64_tr_b16 v[236:237], v8 offset:24576\n"
    "ds_read_b64_tr_b16 v[238:239], v8 offset:25088\n"
    "v_mfma_f32_16x16x32_bf16 v[68:71], v[248:251], a[84:87], v[68:71]\n"
    "s_waitcnt lgkmcnt(8)\n"
    "v_mfma_f32_16x16x32_bf16 v[72:75], v[248:251], a[148:151], v[72:75]\n"
    "v_mfma_f32_16x16x32_bf16 v[76:79], v[248:251], a[212:215], v[76:79]\n"
    "buffer_load_dwordx4 v1, s[44:47], s56 offen offset:3072 sc1 lds\n"
    "v_mfma_f32_16x16x32_bf16 v[80:83], v[252:255], a[20:23], v[80:83]\n"
    "ds_read_b64_tr_b16 v[240:241], v4 offset:28672\n"
    "ds_read_b64_tr_b16 v[242:243], v4 offset:29184\n"
    "v_mfma_f32_16x16x32_bf16 v[84:87], v[252:255], a[84:87], v[84:87]\n"
    "s_waitcnt lgkmcnt(8)\n"
    "v_mfma_f32_16x16x32_bf16 v[88:91], v[252:255], a[148:151], v[88:91]\n"
    "v_mfma_f32_16x16x32_bf16 v[92:95], v[252:255], a[212:215], v[92:95]\n"
    "v_mfma_f32_16x16x32_bf16 v[32:35], v[224:227], a[24:27], v[32:35]\n"
    "ds_read_b64_tr_b16 v[244:245], v5 offset:28672\n"
    "ds_read_b64_tr_b16 v[246:247], v5 offset:29184\n"
    "v_mfma_f32_16x16x32_bf16 v[36:39], v[224:227], a[88:91], v[36:39]\n"
    "s_waitcnt lgkmcnt(8)\n"
    "v_mfma_f32_16x16x32_bf16 v[40:43], v[224:227], a[152:155], v[40:43]\n"
    "s_add_u32 s71, s71, s73\n"
    "s_xor_b32 s75, s75, 0x10000\n"
    "v_mfma_f32_16x16x32_bf16 v[44:47], v[224:227], a[216:219], v[44:47]\n"
    "s_add_u32 s57, s72, 0\n"
    "v_mfma_f32_16x16x32_bf16 v[48:51], v[228:231], a[24:27], v[48:51]\n"
    "ds_read_b64_tr_b16 v[248:249], v6 offset:28672\n"
    "ds_read_b64_tr_b16 v[250:251], v6 offset:29184\n"
    "v_mfma_f32_16x16x32_bf16 v[52:55], v[228:231], a[88:91], v[52:55]\n"
    "s_waitcnt lgkmcnt(8)\n"
    "v_mfma_f32_16x16x32_bf16 v[56:59], v[228:231], a[152:155], v[56:59]\n"
    "s_add_u32 m0, s76, 0\n"
    "s_nop 0\n"
    "buffer_load_dwordx4 v1, s[44:47], s57 offen sc1 lds\n"
    "v_mfma_f32_16x16x32_bf16 v[60:63], v[228:231], a[216:219], v[60:63]\n"
    "s_lshl_b32 s96, s54, 2\n"
    "s_lshr_b32 s95, s53, 3\n"
    "s_add_u32 s96, s96, s95\n"
    "s_lshl_b32 s96, s96, 7\n"
    "s_add_u32 s96, s96, 0x3000\n"
    "s_and_b32 s95, s53, 7\n"
    "s_lshl_b32 s95, s95, 2\n"
    "s_add_u32 s95, s95, s52\n"
    "s_lshl_b32 s95, s95, 2\n"
    "s_add_u32 s96, s96, s95\n"
    "v_mov_b32 v24, s97\n"
    "buffer_store_dword v24, v7, s[44:47], s96 offen sc1\n"
    "v_mfma_f32_16x16x32_bf16 v[64:67], v[232:235], a[24:27], v[64:67]\n"
    "ds_read_b64_tr_b16 v[252:253], v8 offset:28672\n"
    "ds_read_b64_tr_b16 v[254:255], v8 offset:29184\n"
    "v_mfma_f32_16x16x32_bf16 v[68:71], v[232:235], a[88:91], v[68:71]\n"
    "s_waitcnt lgkmcnt(8)\n"
    "v_mfma_f32_16x16x32_bf16 v[72:75], v[232:235], a[152:155], v[72:75]\n"
    "v_mfma_f32_16x16x32_bf16 v[76:79], v[232:235], a[216:219], v[76:79]\n"
    "buffer_load_dwordx4 v1, s[44:47], s57 offen offset:1024 sc1 lds\n"
    "v_mfma_f32_16x16x32_bf16 v[80:83], v[236:239], a[24:27], v[80:83]\n"
    "ds_read_b64_tr_b16 v[224:225], v4 offset:32768\n"
    "ds_read_b64_tr_b16 v[226:227], v4 offset:33280\n"
    "v_mfma_f32_16x16x32_bf16 v[84:87], v[236:239], a[88:91], v[84:87]\n"
    "s_waitcnt lgkmcnt(8)\n"
    "v_mfma_f32_16x16x32_bf16 v[88:91], v[236:239], a[152:155], v[88:91]\n"
    "v_mfma_f32_16x16x32_bf16 v[92:95], v[236:239], a[216:219], v[92:95]\n"
    "v_mfma_f32_16x16x32_bf16 v[32:35], v[240:243], a[28:31], v[32:35]\n"
    "ds_read_b64_tr_b16 v[228:229], v5 offset:32768\n"
    "ds_read_b64_tr_b16 v[230:231], v5 offset:33280\n"
    "v_mfma_f32_16x16x32_bf16 v[36:39], v[240:243], a[92:95], v[36:39]\n"
    "s_waitcnt lgkmcnt(8)\n"
    "v_mfma_f32_16x16x32_bf16 v[40:43], v[240:243], a[156:159], v[40:43]\n"
    "v_mfma_f32_16x16x32_bf16 v[44:47], v[240:243], a[220:223], v[44:47]\n"
    "buffer_load_dwordx4 v1, s[44:47], s57 offen offset:2048 sc1 lds\n"
    "v_mfma_f32_16x16x32_bf16 v[48:51], v[244:247], a[28:31], v[48:51]\n"
    "ds_read_b64_tr_b16 v[232:233], v6 offset:32768\n"
    "ds_read_b64_tr_b16 v[234:235], v6 offset:33280\n"
    "v_mfma_f32_16x16x32_bf16 v[52:55], v[244:247], a[92:95], v[52:55]\n"
    "s_waitcnt lgkmcnt(8)\n"
    "v_mfma_f32_16x16x32_bf16 v[56:59], v[244:247], a[156:159], v[56:59]\n"
    "v_mfma_f32_16x16x32_bf16 v[60:63], v[244:247], a[220:223], v[60:63]\n"
    "v_mfma_f32_16x16x32_bf16 v[64:67], v[248:251], a[28:31], v[64:67]\n"
    "ds_read_b64_tr_b16 v[236:237], v8 offset:32768\n"
    "ds_read_b64_tr_b16 v[238:239], v8 offset:33280\n"
    "v_mfma_f32_16x16x32_bf16 v[68:71], v[248:251], a[92:95], v[68:71]\n"
    "s_waitcnt lgkmcnt(8)\n"
    "v_mfma_f32_16x16x32_bf16 v[72:75], v[248:251], a[156:159], v[72:75]\n"
    "buffer_load_dwordx4 v1, s[44:47], s57 offen offset:3072 sc1 lds\n"
    "v_mfma_f32_16x16x32_bf16 v[76:79], v[248:251], a[220:223], v[76:79]\n"
    "v_mfma_f32_16x16x32_bf16 v[80:83], v[252:255], a[28:31], v[80:83]\n"
    "ds_read_b64_tr_b16 v[240:241], v4 offset:36864\n"
    "ds_read_b64_tr_b16 v[242:243], v4 offset:37376\n"
    "v_mfma_f32_16x16x32_bf16 v[84:87], v[252:255], a[92:95], v[84:87]\n"
    "s_waitcnt lgkmcnt(8)\n"
    "v_mfma_f32_16x16x32_bf16 v[88:91], v[252:255], a[156:159], v[88:91]\n"
    "v_mfma_f32_16x16x32_bf16 v[92:95], v[252:255], a[220:223], v[92:95]\n"
    "v_mfma_f32_16x16x32_bf16 v[32:35], v[224:227], a[32:35], v[32:35]\n"
    "ds_read_b64_tr_b16 v[244:245], v5 offset:36864\n"
    "ds_read_b64_tr_b16 v[246:247], v5 offset:37376\n"
    "v_mfma_f32_16x16x32_bf16 v[36:39], v[224:227], a[96:99], v[36:39]\n"
    "s_waitcnt lgkmcnt(8)\n"
    "v_mfma_f32_16x16x32_bf16 v[40:43], v[224:227], a[160:163], v[40:43]\n"
    "s_add_u32 s72, s72, s74\n"
    "s_xor_b32 s76, s76, 0x10000\n"
    "s_lshl_b32 s59, s53, 19\n"
    "v_mfma_f32_16x16x32_bf16 v[44:47], v[224:227], a[224:227], v[44:47]\n"
    "s_add_u32 s59, s59, 0x400000\n"
    "v_mfma_f32_16x16x32_bf16 v[48:51], v[228:231], a[32:35], v[48:51]\n"
    "ds_read_b64_tr_b16 v[248:249], v6 offset:36864\n"
    "ds_read_b64_tr_b16 v[250:251], v6 offset:37376\n"
    "v_mfma_f32_16x16x32_bf16 v[52:55], v[228:231], a[96:99], v[52:55]\n"
    "s_waitcnt lgkmcnt(8)\n"
    "buffer_load_dwordx4 v[160:163], v1, s[44:47], s59 offen offset:0 sc1\n"
    "v_mfma_f32_16x16x32_bf16 v[56:59], v[228:231], a[160:163], v[56:59]\n"
    "v_mfma_f32_16x16x32_bf16 v[60:63], v[228:231], a[224:227], v[60:63]\n"
    "v_mfma_f32_16x16x32_bf16 v[64:67], v[232:235], a[32:35], v[64:67]\n"
    "ds_read_b64_tr_b16 v[252:253], v8 offset:36864\n"
    "ds_read_b64_tr_b16 v[254:255], v8 offset:37376\n"
    "v_mfma_f32_16x16x32_bf16 v[68:71], v[232:235], a[96:99], v[68:71]\n"
    "s_waitcnt lgkmcnt(8)\n"
    "v_mfma_f32_16x16x32_bf16 v[72:75], v[232:235], a[160:163], v[72:75]\n"
    "v_mfma_f32_16x16x32_bf16 v[76:79], v[232:235], a[224:227], v[76:79]\n"
    "v_mfma_f32_16x16x32_bf16 v[80:83], v[236:239], a[32:35], v[80:83]\n"
    "ds_read_b64_tr_b16 v[224:225], v4 offset:40960\n"
    "ds_read_b64_tr_b16 v[226:227], v4 offset:41472\n"
    "v_mfma_f32_16x16x32_bf16 v[84:87], v[236:239], a[96:99], v[84:87]\n"
    "s_waitcnt lgkmcnt(8)\n"
    "v_mfma_f32_16x16x32_bf16 v[88:91], v[236:239], a[160:163], v[88:91]\n"
    "v_mfma_f32_16x16x32_bf16 v[92:95], v[236:239], a[224:227], v[92:95]\n"
    "v_mfma_f32_16x16x32_bf16 v[32:35], v[240:243], a[36:39], v[32:35]\n"
    "ds_read_b64_tr_b16 v[228:229], v5 offset:40960\n"
    "ds_read_b64_tr_b16 v[230:231], v5 offset:41472\n"
    "v_mfma_f32_16x16x32_bf16 v[36:39], v[240:243], a[100:103], v[36:39]\n"
    "s_waitcnt lgkmcnt(8)\n"
    "buffer_load_dwordx4 v[164:167], v1, s[44:47], s59 offen offset:1024 sc1\n"
    "v_mfma_f32_16x16x32_bf16 v[40:43], v[240:243], a[164:167], v[40:43]\n"
    "v_mfma_f32_16x16x32_bf16 v[44:47], v[240:243], a[228:231], v[44:47]\n"
    "v_mfma_f32_16x16x32_bf16 v[48:51], v[244:247], a[36:39], v[48:51]\n"
    "ds_read_b64_tr_b16 v[232:233], v6 offset:40960\n"
    "ds_read_b64_tr_b16 v[234:235], v6 offset:41472\n"
    "v_mfma_f32_16x16x32_bf16 v[52:55], v[244:247], a[100:103], v[52:55]\n"
    "s_waitcnt lgkmcnt(8)\n"
    "v_mfma_f32_16x16x32_bf16 v[56:59], v[244:247], a[164:167], v[56:59]\n"
    "v_mfma_f32_16x16x32_bf16 v[60:63], v[244:247], a[228:231], v[60:63]\n"
    "v_mfma_f32_16x16x32_bf16 v[64:67], v[248:251], a[36:39], v[64:67]\n"
    "ds_read_b64_tr_b16 v[236:237], v8 offset:40960\n"
    "ds_read_b64_tr_b16 v[238:239], v8 offset:41472\n"
    "v_mfma_f32_16x16x32_bf16 v[68:71], v[248:251], a[100:103], v[68:71]\n"
    "s_waitcnt lgkmcnt(8)\n"
    "v_mfma_f32_16x16x32_bf16 v[72:75], v[248:251], a[164:167], v[72:75]\n"
    "v_mfma_f32_16x16x32_bf16 v[76:79], v[248:251], a[228:231], v[76:79]\n"
    "v_mfma_f32_16x16x32_bf16 v[80:83], v[252:255], a[36:39], v[80:83]\n"
    "ds_read_b64_tr_b16 v[240:241], v4 offset:45056\n"
    "ds_read_b64_tr_b16 v[242:243], v4 offset:45568\n"
    "v_mfma_f32_16x16x32_bf16 v[84:87], v[252:255], a[100:103], v[84:87]\n"
    "s_waitcnt lgkmcnt(8)\n"
    "buffer_load_dwordx4 v[168:171], v1, s[44:47], s59 offen offset:2048 sc1\n"
    "v_mfma_f32_16x16x32_bf16 v[88:91], v[252:255], a[164:167], v[88:91]\n"
    "v_mfma_f32_16x16x32_bf16 v[92:95], v[252:255], a[228:231], v[92:95]\n"
    "v_mfma_f32_16x16x32_bf16 v[32:35], v[224:227], a[40:43], v[32:35]\n"
    "ds_read_b64_tr_b16 v[244:245], v5 offset:45056\n"
    "ds_read_b64_tr_b16 v[246:247], v5 offset:45568\n"
    "v_mfma_f32_16x16x32_bf16 v[36:39], v[224:227], a[104:107], v[36:39]\n"
    "s_waitcnt lgkmcnt(8)\n"
    "v_mfma_f32_16x16x32_bf16 v[40:43], v[224:227], a[168:171], v[40:43]\n"
    "v_mfma_f32_16x16x32_bf16 v[44:47], v[224:227], a[232:235], v[44:47]\n"
    "v_mfma_f32_16x16x32_bf16 v[48:51], v[228:231], a[40:43], v[48:51]\n"
    "ds_read_b64_tr_b16 v[248:249], v6 offset:45056\n"
    "ds_read_b64_tr_b16 v[250:251], v6 offset:45568\n"
    "v_mfma_f32_16x16x32_bf16 v[52:55], v[228:231], a[104:107], v[52:55]\n"
    "s_waitcnt lgkmcnt(8)\n"
    "v_mfma_f32_16x16x32_bf16 v[56:59], v[228:231], a[168:171], v[56:59]\n"
    "v_mfma_f32_16x16x32_bf16 v[60:63], v[228:231], a[232:235], v[60:63]\n"
    "v_mfma_f32_16x16x32_bf16 v[64:67], v[232:235], a[40:43], v[64:67]\n"
    "ds_read_b64_tr_b16 v[252:253], v8 offset:45056\n"
    "ds_read_b64_tr_b16 v[254:255], v8 offset:45568\n"
    "v_mfma_f32_16x16x32_bf16 v[68:71], v[232:235], a[104:107], v[68:71]\n"
    "s_waitcnt lgkmcnt(8)\n"
    "buffer_load_dwordx4 v[172:175], v1, s[44:47], s59 offen offset:3072 sc1\n"
    "v_mfma_f32_16x16x32_bf16 v[72:75], v[232:235], a[168:171], v[72:75]\n"
    "v_mfma_f32_16x16x32_bf16 v[76:79], v[232:235], a[232:235], v[76:79]\n"
    "v_mfma_f32_16x16x32_bf16 v[80:83], v[236:239], a[40:43], v[80:83]\n"
    "ds_read_b64_tr_b16 v[224:225], v4 offset:49152\n"
    "ds_read_b64_tr_b16 v[226:227], v4 offset:49664\n"
    "v_mfma_f32_16x16x32_bf16 v[84:87], v[236:239], a[104:107], v[84:87]\n"
    "s_waitcnt lgkmcnt(8)\n"
    "v_mfma_f32_16x16x32_bf16 v[88:91], v[236:239], a[168:171], v[88:91]\n"
    "v_mfma_f32_16x16x32_bf16 v[92:95], v[236:239], a[232:235], v[92:95]\n"
    "v_mfma_f32_16x16x32_bf16 v[32:35], v[240:243], a[44:47], v[32:35]\n"
    "ds_read_b64_tr_b16 v[228:229], v5 offset:49152\n"
    "ds_read_b64_tr_b16 v[230:231], v5 offset:49664\n"
    "v_mfma_f32_16x16x32_bf16 v[36:39], v[240:243], a[108:111], v[36:39]\n"
    "s_waitcnt lgkmcnt(8)\n"
    "v_mfma_f32_16x16x32_bf16 v[40:43], v[240:243], a[172:175], v[40:43]\n"
    "v_mfma_f32_16x16x32_bf16 v[44:47], v[240:243], a[236:239], v[44:47]\n"
    "v_mfma_f32_16x16x32_bf16 v[48:51], v[244:247], a[44:47], v[48:51]\n"
    "ds_read_b64_tr_b16 v[232:233], v6 offset:49152\n"
    "ds_read_b64_tr_b16 v[234:235], v6 offset:49664\n"
    "v_mfma_f32_16x16x32_bf16 v[52:55], v[244:247], a[108:111], v[52:55]\n"
    "s_waitcnt lgkmcnt(8)\n"
    "v_mfma_f32_16x16x32_bf16 v[56:59], v[244:247], a[172:175], v[56:59]\n"
    "v_mfma_f32_16x16x32_bf16 v[60:63], v[244:247], a[236:239], v[60:63]\n"
    "v_mfma_f32_16x16x32_bf16 v[64:67], v[248:251], a[44:47], v[64:67]\n"
    "ds_read_b64_tr_b16 v[236:237], v8 offset:49152\n"
    "ds_read_b64_tr_b16 v[238:239], v8 offset:49664\n"
    "v_mfma_f32_16x16x32_bf16 v[68:71], v[248:251], a[108:111], v[68:71]\n"
    "s_waitcnt lgkmcnt(8)\n"
    "v_mfma_f32_16x16x32_bf16 v[72:75], v[248:251], a[172:175], v[72:75]\n"
    "v_mfma_f32_16x16x32_bf16 v[76:79], v[248:251], a[236:239], v[76:79]\n"
    "v_mfma_f32_16x16x32_bf16 v[80:83], v[252:255], a[44:47], v[80:83]\n"
    "ds_read_b64_tr_b16 v[240:241], v4 offset:53248\n"
    "ds_read_b64_tr_b16 v[242:243], v4 offset:53760\n"
    "v_mfma_f32_16x16x32_bf16 v[84:87], v[252:255], a[108:111], v[84:87]\n"
    "s_waitcnt lgkmcnt(8)\n"
    "v_mfma_f32_16x16x32_bf16 v[88:91], v[252:255], a[172:175], v[88:91]\n"
    "v_mfma_f32_16x16x32_bf16 v[92:95], v[252:255], a[236:239], v[92:95]\n"
    "v_mfma_f32_16x16x32_bf16 v[32:35], v[224:227], a[48:51], v[32:35]\n"
    "ds_read_b64_tr_b16 v[244:245], v5 offset:53248\n"
    "ds_read_b64_tr_b16 v[246:247], v5 offset:53760\n"
    "v_mfma_f32_16x16x32_bf16 v[36:39], v[224:227], a[112:115], v[36:39]\n"
    "s_waitcnt lgkmcnt(8)\n"
    "v_mfma_f32_16x16x32_bf16 v[40:43], v[224:227], a[176:179], v[40:43]\n"
    "v_mfma_f32_16x16x32_bf16 v[44:47], v[224:227], a[240:243], v[44:47]\n"
    "v_mfma_f32_16x16x32_bf16 v[48:51], v[228:231], a[48:51], v[48:51]\n"
    "ds_read_b64_tr_b16 v[248:249], v6 offset:53248\n"
    "ds_read_b64_tr_b16 v[250:251], v6 offset:53760\n"
    "v_mfma_f32_16x16x32_bf16 v[52:55], v[228:231], a[112:115], v[52:55]\n"
    "s_waitcnt lgkmcnt(8)\n"
    "v_mfma_f32_16x16x32_bf16 v[56:59], v[228:231], a[176:179], v[56:59]\n"
    "v_mfma_f32_16x16x32_bf16 v[60:63], v[228:231], a[240:243], v[60:63]\n"
    "v_mfma_f32_16x16x32_bf16 v[64:67], v[232:235], a[48:51], v[64:67]\n"
    "ds_read_b64_tr_b16 v[252:253], v8 offset:53248\n"
    "ds_read_b64_tr_b16 v[254:255], v8 offset:53760\n"
    "v_mfma_f32_16x16x32_bf16 v[68:71], v[232:235], a[112:115], v[68:71]\n"
    "s_waitcnt lgkmcnt(8)\n"
    "v_mfma_f32_16x16x32_bf16 v[72:75], v[232:235], a[176:179], v[72:75]\n"
    "v_mfma_f32_16x16x32_bf16 v[76:79], v[232:235], a[240:243], v[76:79]\n"
    "v_mfma_f32_16x16x32_bf16 v[80:83], v[236:239], a[48:51], v[80:83]\n"
    "ds_read_b64_tr_b16 v[224:225], v4 offset:57344\n"
    "ds_read_b64_tr_b16 v[226:227], v4 offset:57856\n"
    "v_mfma_f32_16x16x32_bf16 v[84:87], v[236:239], a[112:115], v[84:87]\n"
    "s_waitcnt lgkmcnt(8)\n"
    "v_mfma_f32_16x16x32_bf16 v[88:91], v[236:239], a[176:179], v[88:91]\n"
    "v_mfma_f32_16x16x32_bf16 v[92:95], v[236:239], a[240:243], v[92:95]\n"
    "v_mfma_f32_16x16x32_bf16 v[32:35], v[240:243], a[52:55], v[32:35]\n"
    "ds_read_b64_tr_b16 v[228:229], v5 offset:57344\n"
    "ds_read_b64_tr_b16 v[230:231], v5 offset:57856\n"
    "v_mfma_f32_16x16x32_bf16 v[36:39], v[240:243], a[116:119], v[36:39]\n"
    "s_waitcnt lgkmcnt(8)\n"
    "v_mfma_f32_16x16x32_bf16 v[40:43], v[240:243], a[180:183], v[40:43]\n"
    "v_mfma_f32_16x16x32_bf16 v[44:47], v[240:243], a[244:247], v[44:47]\n"
    "v_mfma_f32_16x16x32_bf16 v[48:51], v[244:247], a[52:55], v[48:51]\n"
    "ds_read_b64_tr_b16 v[232:233], v6 offset:57344\n"
    "ds_read_b64_tr_b16 v[234:235], v6 offset:57856\n"
    "v_mfma_f32_16x16x32_bf16 v[52:55], v[244:247], a[116:119], v[52:55]\n"
    "s_waitcnt lgkmcnt(8)\n"
    "v_mfma_f32_16x16x32_bf16 v[56:59], v[244:247], a[180:183], v[56:59]\n"
    "v_mfma_f32_16x16x32_bf16 v[60:63], v[244:247], a[244:247], v[60:63]\n"
    "v_mfma_f32_16x16x32_bf16 v[64:67], v[248:251], a[52:55], v[64:67]\n"
    "ds_read_b64_tr_b16 v[236:237], v8 offset:57344\n"
    "ds_read_b64_tr_b16 v[238:239], v8 offset:57856\n"
    "v_mfma_f32_16x16x32_bf16 v[68:71], v[248:251], a[116:119], v[68:71]\n"
    "s_waitcnt lgkmcnt(8)\n"
    "v_mfma_f32_16x16x32_bf16 v[72:75], v[248:251], a[180:183], v[72:75]\n"
    "v_mfma_f32_16x16x32_bf16 v[76:79], v[248:251], a[244:247], v[76:79]\n"
    "v_mfma_f32_16x16x32_bf16 v[80:83], v[252:255], a[52:55], v[80:83]\n"
    "ds_read_b64_tr_b16 v[240:241], v4 offset:61440\n"
    "ds_read_b64_tr_b16 v[242:243], v4 offset:61952\n"
    "v_mfma_f32_16x16x32_bf16 v[84:87], v[252:255], a[116:119], v[84:87]\n"
    "s_waitcnt lgkmcnt(8)\n"
    "v_mfma_f32_16x16x32_bf16 v[88:91], v[252:255], a[180:183], v[88:91]\n"
    "v_mfma_f32_16x16x32_bf16 v[92:95], v[252:255], a[244:247], v[92:95]\n"
    "v_mfma_f32_16x16x32_bf16 v[32:35], v[224:227], a[56:59], v[32:35]\n"
    "ds_read_b64_tr_b16 v[244:245], v5 offset:61440\n"
    "ds_read_b64_tr_b16 v[246:247], v5 offset:61952\n"
    "v_mfma_f32_16x16x32_bf16 v[36:39], v[224:227], a[120:123], v[36:39]\n"
    "s_waitcnt lgkmcnt(8)\n"
    "v_mfma_f32_16x16x32_bf16 v[40:43], v[224:227], a[184:187], v[40:43]\n"
    "v_mfma_f32_16x16x32_bf16 v[44:47], v[224:227], a[248:251], v[44:47]\n"
    "v_mfma_f32_16x16x32_bf16 v[48:51], v[228:231], a[56:59], v[48:51]\n"
    "ds_read_b64_tr_b16 v[248:249], v6 offset:61440\n"
    "ds_read_b64_tr_b16 v[250:251], v6 offset:61952\n"
    "v_mfma_f32_16x16x32_bf16 v[52:55], v[228:231], a[120:123], v[52:55]\n"
    "s_waitcnt lgkmcnt(8)\n"
    "v_mfma_f32_16x16x32_bf16 v[56:59], v[228:231], a[184:187], v[56:59]\n"
    "v_mfma_f32_16x16x32_bf16 v[60:63], v[228:231], a[248:251], v[60:63]\n"
    "v_mfma_f32_16x16x32_bf16 v[64:67], v[232:235], a[56:59], v[64:67]\n"
    "ds_read_b64_tr_b16 v[252:253], v8 offset:61440\n"
    "ds_read_b64_tr_b16 v[254:255], v8 offset:61952\n"
    "v_mfma_f32_16x16x32_bf16 v[68:71], v[232:235], a[120:123], v[68:71]\n"
    "s_waitcnt lgkmcnt(8)\n"
    "v_mfma_f32_16x16x32_bf16 v[72:75], v[232:235], a[184:187], v[72:75]\n"
    "v_mfma_f32_16x16x32_bf16 v[76:79], v[232:235], a[248:251], v[76:79]\n"
    "v_mfma_f32_16x16x32_bf16 v[80:83], v[236:239], a[56:59], v[80:83]\n"
    "v_mfma_f32_16x16x32_bf16 v[84:87], v[236:239], a[120:123], v[84:87]\n"
    "s_waitcnt lgkmcnt(6)\n"
    "v_mfma_f32_16x16x32_bf16 v[88:91], v[236:239], a[184:187], v[88:91]\n"
    "v_mfma_f32_16x16x32_bf16 v[92:95], v[236:239], a[248:251], v[92:95]\n"
    "v_mfma_f32_16x16x32_bf16 v[32:35], v[240:243], a[60:63], v[32:35]\n"
    "v_mfma_f32_16x16x32_bf16 v[36:39], v[240:243], a[124:127], v[36:39]\n"
    "s_waitcnt lgkmcnt(4)\n"
    "v_mfma_f32_16x16x32_bf16 v[40:43], v[240:243], a[188:191], v[40:43]\n"
    "v_mfma_f32_16x16x32_bf16 v[44:47], v[240:243], a[252:255], v[44:47]\n"
    "v_mfma_f32_16x16x32_bf16 v[48:51], v[244:247], a[60:63], v[48:51]\n"
    "v_mfma_f32_16x16x32_bf16 v[52:55], v[244:247], a[124:127], v[52:55]\n"
    "s_waitcnt lgkmcnt(2)\n"
    "v_mfma_f32_16x16x32_bf16 v[56:59], v[244:247], a[188:191], v[56:59]\n"
    "v_mfma_f32_16x16x32_bf16 v[60:63], v[244:247], a[252:255], v[60:63]\n"
    "v_mfma_f32_16x16x32_bf16 v[64:67], v[248:251], a[60:63], v[64:67]\n"
    "v_mfma_f32_16x16x32_bf16 v[68:71], v[248:251], a[124:127], v[68:71]\n"
    "s_waitcnt lgkmcnt(0)\n"
    "v_mfma_f32_16x16x32_bf16 v[72:75], v[248:251], a[188:191], v[72:75]\n"
    "v_mfma_f32_16x16x32_bf16 v[76:79], v[248:251], a[252:255], v[76:79]\n"
    "v_mfma_f32_16x16x32_bf16 v[80:83], v[252:255], a[60:63], v[80:83]\n"
    "v_mfma_f32_16x16x32_bf16 v[84:87], v[252:255], a[124:127], v[84:87]\n"
    "v_mfma_f32_16x16x32_bf16 v[88:91], v[252:255], a[188:191], v[88:91]\n"
    "v_mfma_f32_16x16x32_bf16 v[92:95], v[252:255], a[252:255], v[92:95]\n"
    "s_barrier\n"
    "s_load_dwordx2 s[60:61], %11, 0x28\n"
    "s_load_dwordx2 s[62:63], %11, 0x40\n"
    "s_add_u32 s56, s71, 0\n"
    "s_add_u32 m0, s75, 0\n"
    "s_nop 0\n"
    "buffer_load_dwordx4 v1, s[44:47], s56 offen sc1 lds\n"
    "buffer_load_dwordx4 v1, s[44:47], s56 offen offset:1024 sc1 lds\n"
    "buffer_load_dwordx4 v1, s[44:47], s56 offen offset:2048 sc1 lds\n"
    "buffer_load_dwordx4 v1, s[44:47], s56 offen offset:3072 sc1 lds\n"
    "s_add_u32 s71, s71, s73\n"
    "s_xor_b32 s75, s75, 0x10000\n"
    "v_mov_b32 v16, 0\n"
    "v_mov_b32 v17, 0\n"
    "v_mov_b32 v18, 0\n"
    "v_mov_b32 v19, 0\n"
    "v_mul_f32 v32, s80, v32\n"
    "v_mul_f32 v33, s80, v33\n"
    "v_mul_f32 v34, s80, v34\n"
    "v_mul_f32 v35, s80, v35\n"
    "v_mul_f32 v48, s80, v48\n"
    "v_mul_f32 v49, s80, v49\n"
    "v_mul_f32 v50, s80, v50\n"
    "v_mul_f32 v51, s80, v51\n"
    "v_cvt_pk_bf16_f32 v104, v32, v33\n"
    "v_cvt_pk_bf16_f32 v105, v34, v35\n"
    "v_cvt_pk_bf16_f32 v106, v48, v49\n"
    "v_cvt_pk_bf16_f32 v107, v50, v51\n"
    "v_mfma_f32_32x32x16_bf16 a[0:15], v[16:19], v[16:19], 0\n"
    "v_mul_f32 v64, s80, v64\n"
    "v_mul_f32 v65, s80, v65\n"
    "v_mul_f32 v66, s80, v66\n"
    "v_mul_f32 v67, s80, v67\n"
    "v_mul_f32 v80, s80, v80\n"
    "v_mul_f32 v81, s80, v81\n"
    "v_mul_f32 v82, s80, v82\n"
    "v_mul_f32 v83, s80, v83\n"
    "v_cvt_pk_bf16_f32 v108, v64, v65\n"
    "v_cvt_pk_bf16_f32 v109, v66, v67\n"
    "v_cvt_pk_bf16_f32 v110, v80, v81\n"
    "v_cvt_pk_bf16_f32 v111, v82, v83\n"
    "v_mfma_f32_32x32x16_bf16 a[16:31], v[16:19], v[16:19], 0\n"
    "v_mul_f32 v36, s80, v36\n"
    "v_mul_f32 v37, s80, v37\n"
    "v_mul_f32 v38, s80, v38\n"
    "v_mul_f32 v39, s80, v39\n"
    "v_mul_f32 v52, s80, v52\n"
    "v_mul_f32 v53, s80, v53\n"
    "v_mul_f32 v54, s80, v54\n"
    "v_mul_f32 v55, s80, v55\n"
    "v_cvt_pk_bf16_f32 v120, v36, v37\n"
    "v_cvt_pk_bf16_f32 v121, v38, v39\n"
    "v_cvt_pk_bf16_f32 v122, v52, v53\n"
    "v_cvt_pk_bf16_f32 v123, v54, v55\n"
    "v_mfma_f32_32x32x16_bf16 a[32:47], v[16:19], v[16:19], 0\n"
    "v_mul_f32 v68, s80, v68\n"
    "v_mul_f32 v69, s80, v69\n"
    "v_mul_f32 v70, s80, v70\n"
    "v_mul_f32 v71, s80, v71\n"
    "v_mul_f32 v84, s80, v84\n"
    "v_mul_f32 v85, s80, v85\n"
    "v_mul_f32 v86, s80, v86\n"
    "v_mul_f32 v87, s80, v87\n"
    "v_cvt_pk_bf16_f32 v124, v68, v69\n"
    "v_cvt_pk_bf16_f32 v125, v70, v71\n"
    "v_cvt_pk_bf16_f32 v126, v84, v85\n"
    "v_cvt_pk_bf16_f32 v127, v86, v87\n"
    "v_mfma_f32_32x32x16_bf16 a[48:63], v[16:19], v[16:19], 0\n"
    "v_mul_f32 v40, s80, v40\n"
    "v_mul_f32 v41, s80, v41\n"
    "v_mul_f32 v42, s80, v42\n"
    "v_mul_f32 v43, s80, v43\n"
    "v_mul_f32 v56, s80, v56\n"
    "v_mul_f32 v57, s80, v57\n"
    "v_mul_f32 v58, s80, v58\n"
    "v_mul_f32 v59, s80, v59\n"
    "v_cvt_pk_bf16_f32 v136, v40, v41\n"
    "v_cvt_pk_bf16_f32 v137, v42, v43\n"
    "v_cvt_pk_bf16_f32 v138, v56, v57\n"
    "v_cvt_pk_bf16_f32 v139, v58, v59\n"
    "v_mfma_f32_32x32x16_bf16 a[64:79], v[16:19], v[16:19], 0\n"
    "v_mul_f32 v72, s80, v72\n"
    "v_mul_f32 v73, s80, v73\n"
    "v_mul_f32 v74, s80, v74\n"
    "v_mul_f32 v75, s80, v75\n"
    "v_mul_f32 v88, s80, v88\n"
    "v_mul_f32 v89, s80, v89\n"
    "v_mul_f32 v90, s80, v90\n"
    "v_mul_f32 v91, s80, v91\n"
    "v_cvt_pk_bf16_f32 v140, v72, v73\n"
    "v_cvt_pk_bf16_f32 v141, v74, v75\n"
    "v_cvt_pk_bf16_f32 v142, v88, v89\n"
    "v_cvt_pk_bf16_f32 v143, v90, v91\n"
    "v_mfma_f32_32x32x16_bf16 a[80:95], v[16:19], v[16:19], 0\n"
    "v_mul_f32 v44, s80, v44\n"
    "v_mul_f32 v45, s80, v45\n"
    "v_mul_f32 v46, s80, v46\n"
    "v_mul_f32 v47, s80, v47\n"
    "v_mul_f32 v60, s80, v60\n"
    "v_mul_f32 v61, s80, v61\n"
    "v_mul_f32 v62, s80, v62\n"
    "v_mul_f32 v63, s80, v63\n"
    "v_cvt_pk_bf16_f32 v152, v44, v45\n"
    "v_cvt_pk_bf16_f32 v153, v46, v47\n"
    "v_cvt_pk_bf16_f32 v154, v60, v61\n"
    "v_cvt_pk_bf16_f32 v155, v62, v63\n"
    "v_mfma_f32_32x32x16_bf16 a[96:111], v[16:19], v[16:19], 0\n"
    "v_mul_f32 v76, s80, v76\n"
    "v_mul_f32 v77, s80, v77\n"
    "v_mul_f32 v78, s80, v78\n"
    "v_mul_f32 v79, s80, v79\n"
    "v_mul_f32 v92, s80, v92\n"
    "v_mul_f32 v93, s80, v93\n"
    "v_mul_f32 v94, s80, v94\n"
    "v_mul_f32 v95, s80, v95\n"
    "v_cvt_pk_bf16_f32 v156, v76, v77\n"
    "v_cvt_pk_bf16_f32 v157, v78, v79\n"
    "v_cvt_pk_bf16_f32 v158, v92, v93\n"
    "v_cvt_pk_bf16_f32 v159, v94, v95\n"
    "v_mfma_f32_32x32x16_bf16 a[112:127], v[16:19], v[16:19], 0\n"
    "s_waitcnt vmcnt(7)\n"
    "v_mfma_f32_16x16x32_bf16 v[32:35], v[160:163], v[96:99], 0\n"
    "s_waitcnt vmcnt(6)\n"
    "v_mfma_f32_16x16x32_bf16 v[32:35], v[164:167], v[100:103], v[32:35]\n"
    "s_waitcnt vmcnt(5)\n"
    "v_mfma_f32_16x16x32_bf16 v[32:35], v[168:171], v[104:107], v[32:35]\n"
    "s_waitcnt vmcnt(4)\n"
    "v_mfma_f32_16x16x32_bf16 v[32:35], v[172:175], v[108:111], v[32:35]\n"
    "v_mfma_f32_16x16x32_bf16 v[36:39], v[160:163], v[112:115], 0\n"
    "v_mfma_f32_16x16x32_bf16 v[36:39], v[164:167], v[116:119], v[36:39]\n"
    "v_mfma_f32_16x16x32_bf16 v[36:39], v[168:171], v[120:123], v[36:39]\n"
    "v_mfma_f32_16x16x32_bf16 v[36:39], v[172:175], v[124:127], v[36:39]\n"
    "v_mfma_f32_16x16x32_bf16 v[40:43], v[160:163], v[128:131], 0\n"
    "v_mfma_f32_16x16x32_bf16 v[40:43], v[164:167], v[132:135], v[40:43]\n"
    "v_mfma_f32_16x16x32_bf16 v[40:43], v[168:171], v[136:139], v[40:43]\n"
    "v_mfma_f32_16x16x32_bf16 v[40:43], v[172:175], v[140:143], v[40:43]\n"
    "v_mfma_f32_16x16x32_bf16 v[44:47], v[160:163], v[144:147], 0\n"
    "v_mfma_f32_16x16x32_bf16 v[44:47], v[164:167], v[148:151], v[44:47]\n"
    "v_mfma_f32_16x16x32_bf16 v[44:47], v[168:171], v[152:155], v[44:47]\n"
    "v_mfma_f32_16x16x32_bf16 v[44:47], v[172:175], v[156:159], v[44:47]\n"
    "v_and_b32 v21, 63, v0\n"
    "v_and_b32 v22, 15, v21\n"
    "v_lshlrev_b32 v20, 2, v22\n"
    "ds_bpermute_b32 v12, v20, v32\n"
    "ds_bpermute_b32 v13, v20, v36\n"
    "ds_bpermute_b32 v14, v20, v40\n"
    "s_nop 1\n"
    "ds_bpermute_b32 v15, v20, v44\n"
    "v_lshrrev_b32 v23, 4, v21\n"
    "v_lshlrev_b32 v23, 2, v23\n"
    "v_sub_u32 v5, v22, v23\n"
    "v_mov_b32 v4, 0xff800000\n"
    "v_add_u32 v2, 0x10000, v1\n"
    "v_add_u32 v3, 0x18000, v1\n"
    "s_waitcnt lgkmcnt(0)\n"
    "v_sub_f32 v12, 0, v12\n"
    "v_sub_f32 v13, 0, v13\n"
    "v_sub_f32 v14, 0, v14\n"
    "v_sub_f32 v15, 0, v15\n"
    "s_lshl_b32 s56, s52, 13\n"
    "s_add_u32 s56, s56, 131072\n"
    "v_add_u32 v22, s56, v1\n"
    "ds_write_b128 v22, v[96:99] offset:0\n"
    "ds_write_b128 v22, v[100:103] offset:1024\n"
    "ds_write_b128 v22, v[104:107] offset:2048\n"
    "ds_write_b128 v22, v[108:111] offset:3072\n"
    "ds_write_b128 v22, v[112:115] offset:4096\n"
    "ds_write_b128 v22, v[116:119] offset:5120\n"
    "ds_write_b128 v22, v[120:123] offset:6144\n"
    "ds_write_b128 v22, v[124:127] offset:7168\n"
    "s_lshl_b32 s56, s52, 9\n"
    "s_add_u32 s56, s56, 0x14000\n"
    "v_lshrrev_b32 v23, 2, v1\n"
    "v_add_u32 v23, s56, v23\n"
    "ds_write_b32 v23, v12\n"
    "ds_write_b32 v23, v13 offset:256\n"
    "v_mov_b32 v32, v12\n"
    "v_mov_b32 v33, v12\n"
    "v_mov_b32 v34, v12\n"
    "v_mov_b32 v35, v12\n"
    "v_mov_b32 v36, v12\n"
    "v_mov_b32 v37, v12\n"
    "v_mov_b32 v38, v12\n"
    "v_mov_b32 v39, v12\n"
    "v_mov_b32 v40, v12\n"
    "v_mov_b32 v41, v12\n"
    "v_mov_b32 v42, v12\n"
    "v_mov_b32 v43, v12\n"
    "v_mov_b32 v44, v12\n"
    "v_mov_b32 v45, v12\n"
    "v_mov_b32 v46, v12\n"
    "v_mov_b32 v47, v12\n"
    "v_mov_b32 v48, v13\n"
    "v_mov_b32 v49, v13\n"
    "v_mov_b32 v50, v13\n"
    "v_mov_b32 v51, v13\n"
    "v_mov_b32 v52, v13\n"
    "v_mov_b32 v53, v13\n"
    "v_mov_b32 v54, v13\n"
    "v_mov_b32 v55, v13\n"
    "v_mov_b32 v56, v13\n"
    "v_mov_b32 v57, v13\n"
    "v_mov_b32 v58, v13\n"
    "v_mov_b32 v59, v13\n"
    "v_mov_b32 v60, v13\n"
    "v_mov_b32 v61, v13\n"
    "v_mov_b32 v62, v13\n"
    "v_mov_b32 v63, v13\n"
    "v_mov_b32 v64, v14\n"
    "v_mov_b32 v65, v14\n"
    "v_mov_b32 v66, v14\n"
    "v_mov_b32 v67, v14\n"
    "v_mov_b32 v68, v14\n"
    "v_mov_b32 v69, v14\n"
    "v_mov_b32 v70, v14\n"
    "v_mov_b32 v71, v14\n"
    "v_mov_b32 v72, v14\n"
    "v_mov_b32 v73, v14\n"
    "v_mov_b32 v74, v14\n"
    "v_mov_b32 v75, v14\n"
    "v_mov_b32 v76, v14\n"
    "v_mov_b32 v77, v14\n"
    "v_mov_b32 v78, v14\n"
    "v_mov_b32 v79, v14\n"
    "v_mov_b32 v80, v15\n"
    "v_mov_b32 v81, v15\n"
    "v_mov_b32 v82, v15\n"
    "v_mov_b32 v83, v15\n"
    "v_mov_b32 v84, v15\n"
    "v_mov_b32 v85, v15\n"
    "v_mov_b32 v86, v15\n"
    "v_mov_b32 v87, v15\n"
    "v_mov_b32 v88, v15\n"
    "v_mov_b32 v89, v15\n"
    "v_mov_b32 v90, v15\n"
    "v_mov_b32 v91, v15\n"
    "v_mov_b32 v92, v15\n"
    "v_mov_b32 v93, v15\n"
    "v_mov_b32 v94, v15\n"
    "v_mov_b32 v95, v15\n"
    "v_mov_b32 v8, 0\n"
    "v_mov_b32 v9, 0\n"
    "v_mov_b32 v10, 0\n"
    "v_mov_b32 v11, 0\n"
    "v_mov_b32 v200, 0\n"
    "v_mov_b32 v201, 0\n"
    "v_mov_b32 v202, 0\n"
    "v_mov_b32 v203, 0\n"
    "v_mov_b32 v204, 0\n"
    "v_mov_b32 v205, 0\n"
    "v_mov_b32 v206, 0\n"
    "v_mov_b32 v207, 0\n"
    "v_mov_b32 v216, 0\n"
    "v_mov_b32 v217, 0\n"
    "v_mov_b32 v218, 0\n"
    "v_mov_b32 v219, 0\n"
    "v_mov_b32 v220, 0\n"
    "v_mov_b32 v221, 0\n"
    "v_mov_b32 v222, 0\n"
    "v_mov_b32 v223, 0\n"
    "s_waitcnt lgkmcnt(0)\n"
    "s_barrier\n"
    "v_xor_b32 v23, 512, v23\n"
    "ds_read_b32 v26, v23\n"
    "ds_read_b32 v27, v23 offset:256\n"
    "ds_read_b128 a[128:131], v2 offset:0\n"
    "ds_read_b128 a[132:135], v2 offset:1024\n"
    "ds_read_b128 a[136:139], v2 offset:2048\n"
    "ds_read_b128 a[140:143], v2 offset:3072\n"
    "ds_read_b128 a[144:147], v2 offset:4096\n"
    "ds_read_b128 a[148:151], v2 offset:5120\n"
    "ds_read_b128 a[152:155], v2 offset:6144\n"
    "ds_read_b128 a[156:159], v2 offset:7168\n"
    "ds_read_b128 a[160:163], v2 offset:8192\n"
    "ds_read_b128 a[164:167], v2 offset:9216\n"
    "ds_read_b128 a[168:171], v2 offset:10240\n"
    "ds_read_b128 a[172:175], v2 offset:11264\n"
    "ds_read_b128 a[176:179], v2 offset:12288\n"
    "s_waitcnt lgkmcnt(8)\n"
    "ds_read_b128 a[180:183], v2 offset:13312\n"
    "ds_read_b128 a[184:187], v2 offset:14336\n"
    "ds_read_b128 a[188:191], v2 offset:15360\n"
    "s_waitcnt lgkmcnt(0)\n"
    "s_branch L_join_4_%=\n"
    "L_pathB_3_%=:\n"
    "s_barrier\n"
    "v_mfma_f32_16x16x32_bf16 v[160:163], v[224:227], a[16:19], v[160:163]\n"
    "ds_read_b64_tr_b16 v[244:245], v11 offset:18432\n"
    "ds_read_b64_tr_b16 v[246:247], v11 offset:18688\n"
    "v_mfma_f32_16x16x32_bf16 v[164:167], v[224:227], a[80:83], v[164:167]\n"
    "s_waitcnt lgkmcnt(8)\n"
    "s_add_u32 s56, s61, 0x0\n"
    "v_mfma_f32_16x16x32_bf16 v[168:171], v[224:227], a[144:147], v[168:171]\n"
    "s_add_u32 s59, s69, 12288\n"
    "buffer_load_dwordx4 a[56:59], v1, s[44:47], s59 offen offset:2048 sc1\n"
    "v_mfma_f32_16x16x32_bf16 v[172:175], v[224:227], a[208:211], v[172:175]\n"
    "v_mfma_f32_16x16x32_bf16 v[176:179], v[228:231], a[16:19], v[176:179]\n"
    "ds_read_b64_tr_b16 v[248:249], v10 offset:22528\n"
    "ds_read_b64_tr_b16 v[250:251], v10 offset:22784\n"
    "v_mfma_f32_16x16x32_bf16 v[180:183], v[228:231], a[80:83], v[180:183]\n"
    "s_waitcnt lgkmcnt(8)\n"
    "v_cvt_pk_bf16_f32 v128, v32, v33\n"
    "v_cvt_pk_bf16_f32 v129, v34, v35\n"
    "v_mfma_f32_16x16x32_bf16 v[184:187], v[228:231], a[144:147], v[184:187]\n"
    "s_add_u32 m0, s60, 0\n"
    "s_nop 0\n"
    "buffer_load_dwordx4 v2, s[36:39], s56 offen lds\n"
    "v_cvt_pk_bf16_f32 v130, v36, v37\n"
    "v_mfma_f32_16x16x32_bf16 v[188:191], v[228:231], a[208:211], v[188:191]\n"
    "v_cvt_pk_bf16_f32 v131, v38, v39\n"
    "v_cvt_pk_bf16_f32 v132, v40, v41\n"
    "v_mfma_f32_16x16x32_bf16 v[192:195], v[232:235], a[16:19], v[192:195]\n"
    "ds_read_b64_tr_b16 v[252:253], v11 offset:22528\n"
    "ds_read_b64_tr_b16 v[254:255], v11 offset:22784\n"
    "v_mfma_f32_16x16x32_bf16 v[196:199], v[232:235], a[80:83], v[196:199]\n"
    "s_waitcnt lgkmcnt(8)\n"
    "v_cvt_pk_bf16_f32 v133, v42, v43\n"
    "v_cvt_pk_bf16_f32 v134, v44, v45\n"
    "v_mfma_f32_16x16x32_bf16 v[200:203], v[232:235], a[144:147], v[200:203]\n"
    "s_add_u32 s59, s69, 28672\n"
    "buffer_load_dwordx4 a[120:123], v1, s[44:47], s59 offen offset:2048 sc1\n"
    "v_cvt_pk_bf16_f32 v135, v46, v47\n"
    "v_mfma_f32_16x16x32_bf16 v[204:207], v[232:235], a[208:211], v[204:207]\n"
    "v_cvt_pk_bf16_f32 v136, v48, v49\n"
    "v_cvt_pk_bf16_f32 v137, v50, v51\n"
    "v_mfma_f32_16x16x32_bf16 v[208:211], v[236:239], a[16:19], v[208:211]\n"
    "ds_read_b64_tr_b16 v[224:225], v10 offset:24576\n"
    "ds_read_b64_tr_b16 v[226:227], v10 offset:24832\n"
    "v_mfma_f32_16x16x32_bf16 v[212:215], v[236:239], a[80:83], v[212:215]\n"
    "s_waitcnt lgkmcnt(8)\n"
    "v_cvt_pk_bf16_f32 v138, v52, v53\n"
    "v_cvt_pk_bf16_f32 v139, v54, v55\n"
    "v_mfma_f32_16x16x32_bf16 v[216:219], v[236:239], a[144:147], v[216:219]\n"
    "s_add_u32 s59, s70, 12288\n"
    "buffer_load_dwordx4 a[184:187], v1, s[44:47], s59 offen offset:2048 sc1\n"
    "v_cvt_pk_bf16_f32 v140, v56, v57\n"
    "v_mfma_f32_16x16x32_bf16 v[220:223], v[236:239], a[208:211], v[220:223]\n"
    "v_cvt_pk_bf16_f32 v141, v58, v59\n"
    "v_cvt_pk_bf16_f32 v142, v60, v61\n"
    "v_mfma_f32_16x16x32_bf16 v[160:163], v[240:243], a[20:23], v[160:163]\n"
    "ds_read_b64_tr_b16 v[228:229], v11 offset:24576\n"
    "ds_read_b64_tr_b16 v[230:231], v11 offset:24832\n"
    "v_mfma_f32_16x16x32_bf16 v[164:167], v[240:243], a[84:87], v[164:167]\n"
    "s_waitcnt lgkmcnt(8)\n"
    "v_cvt_pk_bf16_f32 v143, v62, v63\n"
    "v_cvt_pk_bf16_f32 v144, v64, v65\n"
    "v_mfma_f32_16x16x32_bf16 v[168:171], v[240:243], a[148:151], v[168:171]\n"
    "s_add_u32 m0, s60, 1024\n"
    "s_nop 0\n"
    "buffer_load_dwordx4 v3, s[36:39], s56 offen lds\n"
    "v_cvt_pk_bf16_f32 v145, v66, v67\n"
    "v_mfma_f32_16x16x32_bf16 v[172:175], v[240:243], a[212:215], v[172:175]\n"
    "s_add_u32 s56, s61, 0x80000\n"
    "v_cvt_pk_bf16_f32 v146, v68, v69\n"
    "v_cvt_pk_bf16_f32 v147, v70, v71\n"
    "v_mfma_f32_16x16x32_bf16 v[176:179], v[244:247], a[20:23], v[176:179]\n"
    "ds_read_b64_tr_b16 v[232:233], v10 offset:28672\n"
    "ds_read_b64_tr_b16 v[234:235], v10 offset:28928\n"
    "v_mfma_f32_16x16x32_bf16 v[180:183], v[244:247], a[84:87], v[180:183]\n"
    "s_waitcnt lgkmcnt(8)\n"
    "v_cvt_pk_bf16_f32 v148, v72, v73\n"
    "v_cvt_pk_bf16_f32 v149, v74, v75\n"
    "v_mfma_f32_16x16x32_bf16 v[184:187], v[244:247], a[148:151], v[184:187]\n"
    "s_add_u32 s59, s70, 28672\n"
    "buffer_load_dwordx4 a[248:251], v1, s[44:47], s59 offen offset:2048 sc1\n"
    "v_cvt_pk_bf16_f32 v150, v76, v77\n"
    "v_mfma_f32_16x16x32_bf16 v[188:191], v[244:247], a[212:215], v[188:191]\n"
    "v_cvt_pk_bf16_f32 v151, v78, v79\n"
    "v_cvt_pk_bf16_f32 v152, v80, v81\n"
    "v_mfma_f32_16x16x32_bf16 v[192:195], v[248:251], a[20:23], v[192:195]\n"
    "ds_read_b64_tr_b16 v[236:237], v11 offset:28672\n"
    "ds_read_b64_tr_b16 v[238:239], v11 offset:28928\n"
    "v_mfma_f32_16x16x32_bf16 v[196:199], v[248:251], a[84:87], v[196:199]\n"
    "s_waitcnt lgkmcnt(8)\n"
    "v_cvt_pk_bf16_f32 v153, v82, v83\n"
    "v_cvt_pk_bf16_f32 v154, v84, v85\n"
    "v_mfma_f32_16x16x32_bf16 v[200:203], v[248:251], a[148:151], v[200:203]\n"
    "s_add_u32 s59, s69, 12288\n"
    "buffer_load_dwordx4 a[60:63], v1, s[44:47], s59 offen offset:3072 sc1\n"
    "v_cvt_pk_bf16_f32 v155, v86, v87\n"
    "v_mfma_f32_16x16x32_bf16 v[204:207], v[248:251], a[212:215], v[204:207]\n"
    "v_cvt_pk_bf16_f32 v156, v88, v89\n"
    "v_cvt_pk_bf16_f32 v157, v90, v91\n"
    "v_mfma_f32_16x16x32_bf16 v[208:211], v[252:255], a[20:23], v[208:211]\n"
    "ds_read_b64_tr_b16 v[240:241], v10 offset:26624\n"
    "ds_read_b64_tr_b16 v[242:243], v10 offset:26880\n"
    "v_mfma_f32_16x16x32_bf16 v[212:215], v[252:255], a[84:87], v[212:215]\n"
    "s_waitcnt lgkmcnt(8)\n"
    "v_cvt_pk_bf16_f32 v158, v92, v93\n"
    "v_cvt_pk_bf16_f32 v159, v94, v95\n"
    "v_mfma_f32_16x16x32_bf16 v[216:219], v[252:255], a[148:151], v[216:219]\n"
    "s_add_u32 s59, s69, 28672\n"
    "buffer_load_dwordx4 a[124:127], v1, s[44:47], s59 offen offset:3072 sc1\n"
    "v_mfma_f32_16x16x32_bf16 v[220:223], v[252:255], a[212:215], v[220:223]\n"
    "v_mfma_f32_16x16x32_bf16 v[160:163], v[224:227], a[24:27], v[160:163]\n"
    "ds_read_b64_tr_b16 v[244:245], v11 offset:26624\n"
    "ds_read_b64_tr_b16 v[246:247], v11 offset:26880\n"
    "v_mfma_f32_16x16x32_bf16 v[164:167], v[224:227], a[88:91], v[164:167]\n"
    "s_waitcnt lgkmcnt(8)\n"
    "s_waitcnt vmcnt(41)\n"
    "v_mfma_f32_16x16x32_bf16 v[168:171], v[224:227], a[152:155], v[168:171]\n"
    "s_add_u32 s59, s70, 12288\n"
    "buffer_load_dwordx4 a[188:191], v1, s[44:47], s59 offen offset:3072 sc1\n"
    "s_waitcnt vmcnt(40)\n"
    "v_mfma_f32_16x16x32_bf16 v[172:175], v[224:227], a[216:219], v[172:175]\n"
    "v_mfma_f32_16x16x32_bf16 v[176:179], v[228:231], a[24:27], v[176:179]\n"
    "ds_read_b64_tr_b16 v[248:249], v10 offset:30720\n"
    "ds_read_b64_tr_b16 v[250:251], v10 offset:30976\n"
    "v_mfma_f32_16x16x32_bf16 v[180:183], v[228:231], a[88:91], v[180:183]\n"
    "s_waitcnt lgkmcnt(8)\n"
    "v_mfma_f32_16x16x32_bf16 v[184:187], v[228:231], a[152:155], v[184:187]\n"
    "s_add_u32 m0, s60, 8192\n"
    "s_nop 0\n"
    "buffer_load_dwordx4 v2, s[36:39], s56 offen lds\n"
    "v_mfma_f32_16x16x32_bf16 v[188:191], v[228:231], a[216:219], v[188:191]\n"
    "v_mfma_f32_16x16x32_bf16 v[192:195], v[232:235], a[24:27], v[192:195]\n"
    "ds_read_b64_tr_b16 v[252:253], v11 offset:30720\n"
    "ds_read_b64_tr_b16 v[254:255], v11 offset:30976\n"
    "v_mfma_f32_16x16x32_bf16 v[196:199], v[232:235], a[88:91], v[196:199]\n"
    "s_waitcnt lgkmcnt(8)\n"
    "v_mfma_f32_16x16x32_bf16 v[200:203], v[232:235], a[152:155], v[200:203]\n"
    "s_add_u32 s59, s70, 28672\n"
    "buffer_load_dwordx4 a[252:255], v1, s[44:47], s59 offen offset:3072 sc1\n"
    "v_mfma_f32_16x16x32_bf16 v[204:207], v[232:235], a[216:219], v[204:207]\n"
    "v_mfma_f32_16x16x32_bf16 v[208:211], v[236:239], a[24:27], v[208:211]\n"
    "ds_read_b64_tr_b16 v[224:225], v10 offset:32768\n"
    "ds_read_b64_tr_b16 v[226:227], v10 offset:33024\n"
    "v_mfma_f32_16x16x32_bf16 v[212:215], v[236:239], a[88:91], v[212:215]\n"
    "s_waitcnt lgkmcnt(8)\n"
    "v_mfma_f32_16x16x32_bf16 v[216:219], v[236:239], a[152:155], v[216:219]\n"
    "s_add_u32 m0, s60, 9216\n"
    "s_nop 0\n"
    "buffer_load_dwordx4 v3, s[36:39], s56 offen lds\n"
    "v_mfma_f32_16x16x32_bf16 v[220:223], v[236:239], a[216:219], v[220:223]\n"
    "s_add_u32 s56, s61, 0x100000\n"
    "s_waitcnt vmcnt(42)\n"
    "v_mfma_f32_16x16x32_bf16 v[160:163], v[240:243], a[28:31], v[160:163]\n"
    "ds_read_b64_tr_b16 v[228:229], v11 offset:32768\n"
    "ds_read_b64_tr_b16 v[230:231], v11 offset:33024\n"
    "s_waitcnt vmcnt(41)\n"
    "v_mfma_f32_16x16x32_bf16 v[164:167], v[240:243], a[92:95], v[164:167]\n"
    "s_waitcnt lgkmcnt(8)\n"
    "s_waitcnt vmcnt(40)\n"
    "v_mfma_f32_16x16x32_bf16 v[168:171], v[240:243], a[156:159], v[168:171]\n"
    "s_add_u32 m0, s60, 16384\n"
    "s_nop 0\n"
    "buffer_load_dwordx4 v2, s[36:39], s56 offen lds\n"
    "s_waitcnt vmcnt(40)\n"
    "v_mfma_f32_16x16x32_bf16 v[172:175], v[240:243], a[220:223], v[172:175]\n"
    "v_mfma_f32_16x16x32_bf16 v[176:179], v[244:247], a[28:31], v[176:179]\n"
    "ds_read_b64_tr_b16 v[232:233], v10 offset:36864\n"
    "ds_read_b64_tr_b16 v[234:235], v10 offset:37120\n"
    "v_mfma_f32_16x16x32_bf16 v[180:183], v[244:247], a[92:95], v[180:183]\n"
    "s_waitcnt lgkmcnt(8)\n"
    "v_mfma_f32_16x16x32_bf16 v[184:187], v[244:247], a[156:159], v[184:187]\n"
    "s_add_u32 m0, s60, 17408\n"
    "s_nop 0\n"
    "buffer_load_dwordx4 v3, s[36:39], s56 offen lds\n"
    "v_mfma_f32_16x16x32_bf16 v[188:191], v[244:247], a[220:223], v[188:191]\n"
    "s_add_u32 s56, s61, 0x180000\n"
    "v_mfma_f32_16x16x32_bf16 v[192:195], v[248:251], a[28:31], v[192:195]\n"
    "ds_read_b64_tr_b16 v[236:237], v11 offset:36864\n"
    "ds_read_b64_tr_b16 v[238:239], v11 offset:37120\n"
    "v_mfma_f32_16x16x32_bf16 v[196:199], v[248:251], a[92:95], v[196:199]\n"
    "s_waitcnt lgkmcnt(8)\n"
    "v_mfma_f32_16x16x32_bf16 v[200:203], v[248:251], a[156:159], v[200:203]\n"
    "s_add_u32 m0, s60, 24576\n"
    "s_nop 0\n"
    "buffer_load_dwordx4 v2, s[36:39], s56 offen lds\n"
    "v_mfma_f32_16x16x32_bf16 v[204:207], v[248:251], a[220:223], v[204:207]\n"
    "v_mfma_f32_16x16x32_bf16 v[208:211], v[252:255], a[28:31], v[208:211]\n"
    "ds_read_b64_tr_b16 v[240:241], v10 offset:34816\n"
    "ds_read_b64_tr_b16 v[242:243], v10 offset:35072\n"
    "v_mfma_f32_16x16x32_bf16 v[212:215], v[252:255], a[92:95], v[212:215]\n"
    "s_waitcnt lgkmcnt(8)\n"
    "v_mfma_f32_16x16x32_bf16 v[216:219], v[252:255], a[156:159], v[216:219]\n"
    "s_add_u32 m0, s60, 25600\n"
    "s_nop 0\n"
    "buffer_load_dwordx4 v3, s[36:39], s56 offen lds\n"
    "v_mfma_f32_16x16x32_bf16 v[220:223], v[252:255], a[220:223], v[220:223]\n"
    "s_add_u32 s56, s61, 0x200000\n"
    "s_waitcnt vmcnt(42)\n"
    "v_mfma_f32_16x16x32_bf16 v[160:163], v[224:227], a[32:35], v[160:163]\n"
    "ds_read_b64_tr_b16 v[244:245], v11 offset:34816\n"
    "ds_read_b64_tr_b16 v[246:247], v11 offset:35072\n"
    "s_waitcnt vmcnt(40)\n"
    "v_mfma_f32_16x16x32_bf16 v[164:167], v[224:227], a[96:99], v[164:167]\n"
    "s_waitcnt lgkmcnt(8)\n"
    "s_waitcnt vmcnt(38)\n"
    "v_mfma_f32_16x16x32_bf16 v[168:171], v[224:227], a[160:163], v[168:171]\n"
    "s_add_u32 m0, s60, 32768\n"
    "s_nop 0\n"
    "buffer_load_dwordx4 v2, s[36:39], s56 offen lds\n"
    "s_waitcnt vmcnt(38)\n"
    "v_mfma_f32_16x16x32_bf16 v[172:175], v[224:227], a[224:227], v[172:175]\n"
    "v_mfma_f32_16x16x32_bf16 v[176:179], v[228:231], a[32:35], v[176:179]\n"
    "ds_read_b64_tr_b16 v[248:249], v10 offset:38912\n"
    "ds_read_b64_tr_b16 v[250:251], v10 offset:39168\n"
    "v_mfma_f32_16x16x32_bf16 v[180:183], v[228:231], a[96:99], v[180:183]\n"
    "s_waitcnt lgkmcnt(8)\n"
    "v_mfma_f32_16x16x32_bf16 v[184:187], v[228:231], a[160:163], v[184:187]\n"
    "s_add_u32 m0, s60, 33792\n"
    "s_nop 0\n"
    "buffer_load_dwordx4 v3, s[36:39], s56 offen lds\n"
    "v_mfma_f32_16x16x32_bf16 v[188:191], v[228:231], a[224:227], v[188:191]\n"
    "s_add_u32 s56, s61, 0x280000\n"
    "v_mfma_f32_16x16x32_bf16 v[192:195], v[232:235], a[32:35], v[192:195]\n"
    "ds_read_b64_tr_b16 v[252:253], v11 offset:38912\n"
    "ds_read_b64_tr_b16 v[254:255], v11 offset:39168\n"
    "v_mfma_f32_16x16x32_bf16 v[196:199], v[232:235], a[96:99], v[196:199]\n"
    "s_waitcnt lgkmcnt(8)\n"
    "v_mfma_f32_16x16x32_bf16 v[200:203], v[232:235], a[160:163], v[200:203]\n"
    "s_add_u32 m0, s60, 40960\n"
    "s_nop 0\n"
    "buffer_load_dwordx4 v2, s[36:39], s56 offen lds\n"
    "v_mfma_f32_16x16x32_bf16 v[204:207], v[232:235], a[224:227], v[204:207]\n"
    "v_mfma_f32_16x16x32_bf16 v[208:211], v[236:239], a[32:35], v[208:211]\n"
    "ds_read_b64_tr_b16 v[224:225], v10 offset:40960\n"
    "ds_read_b64_tr_b16 v[226:227], v10 offset:41216\n"
    "v_mfma_f32_16x16x32_bf16 v[212:215], v[236:239], a[96:99], v[212:215]\n"
    "s_waitcnt lgkmcnt(8)\n"
    "v_mfma_f32_16x16x32_bf16 v[216:219], v[236:239], a[160:163], v[216:219]\n"
    "s_add_u32 m0, s60, 41984\n"
    "s_nop 0\n"
    "buffer_load_dwordx4 v3, s[36:39], s56 offen lds\n"
    "v_mfma_f32_16x16x32_bf16 v[220:223], v[236:239], a[224:227], v[220:223]\n"
    "s_waitcnt vmcnt(40)\n"
    "v_mfma_f32_16x16x32_bf16 v[160:163], v[240:243], a[36:39], v[160:163]\n"
    "ds_read_b64_tr_b16 v[228:229], v11 offset:40960\n"
    "ds_read_b64_tr_b16 v[230:231], v11 offset:41216\n"
    "s_waitcnt vmcnt(39)\n"
    "v_mfma_f32_16x16x32_bf16 v[164:167], v[240:243], a[100:103], v[164:167]\n"
    "s_waitcnt lgkmcnt(8)\n"
    "s_waitcnt vmcnt(37)\n"
    "v_mfma_f32_16x16x32_bf16 v[168:171], v[240:243], a[164:167], v[168:171]\n"
    "s_add_u32 s58, s63, 0x0\n"
    "buffer_store_dwordx4 v[96:99], v1, s[44:47], s58 offen sc1\n"
    "s_waitcnt vmcnt(37)\n"
    "v_mfma_f32_16x16x32_bf16 v[172:175], v[240:243], a[228:231], v[172:175]\n"
    "v_mfma_f32_16x16x32_bf16 v[176:179], v[244:247], a[36:39], v[176:179]\n"
    "ds_read_b64_tr_b16 v[232:233], v10 offset:45056\n"
    "ds_read_b64_tr_b16 v[234:235], v10 offset:45312\n"
    "v_mfma_f32_16x16x32_bf16 v[180:183], v[244:247], a[100:103], v[180:183]\n"
    "s_waitcnt lgkmcnt(8)\n"
    "s_add_u32 s56, s61, 0x300000\n"
    "v_mfma_f32_16x16x32_bf16 v[184:187], v[244:247], a[164:167], v[184:187]\n"
    "s_add_u32 m0, s60, 49152\n"
    "s_nop 0\n"
    "buffer_load_dwordx4 v2, s[36:39], s56 offen lds\n"
    "v_mfma_f32_16x16x32_bf16 v[188:191], v[244:247], a[228:231], v[188:191]\n"
    "v_mfma_f32_16x16x32_bf16 v[192:195], v[248:251], a[36:39], v[192:195]\n"
    "ds_read_b64_tr_b16 v[236:237], v11 offset:45056\n"
    "ds_read_b64_tr_b16 v[238:239], v11 offset:45312\n"
    "v_mfma_f32_16x16x32_bf16 v[196:199], v[248:251], a[100:103], v[196:199]\n"
    "s_waitcnt lgkmcnt(8)\n"
    "v_mfma_f32_16x16x32_bf16 v[200:203], v[248:251], a[164:167], v[200:203]\n"
    "s_add_u32 s58, s63, 0x400\n"
    "buffer_store_dwordx4 v[100:103], v1, s[44:47], s58 offen sc1\n"
    "v_mfma_f32_16x16x32_bf16 v[204:207], v[248:251], a[228:231], v[204:207]\n"
    "v_mfma_f32_16x16x32_bf16 v[208:211], v[252:255], a[36:39], v[208:211]\n"
    "ds_read_b64_tr_b16 v[240:241], v10 offset:43008\n"
    "ds_read_b64_tr_b16 v[242:243], v10 offset:43264\n"
    "v_mfma_f32_16x16x32_bf16 v[212:215], v[252:255], a[100:103], v[212:215]\n"
    "s_waitcnt lgkmcnt(8)\n"
    "v_mfma_f32_16x16x32_bf16 v[216:219], v[252:255], a[164:167], v[216:219]\n"
    "s_add_u32 m0, s60, 50176\n"
    "s_nop 0\n"
    "buffer_load_dwordx4 v3, s[36:39], s56 offen lds\n"
    "v_mfma_f32_16x16x32_bf16 v[220:223], v[252:255], a[228:231], v[220:223]\n"
    "s_waitcnt vmcnt(39)\n"
    "v_mfma_f32_16x16x32_bf16 v[160:163], v[224:227], a[40:43], v[160:163]\n"
    "ds_read_b64_tr_b16 v[244:245], v11 offset:43008\n"
    "ds_read_b64_tr_b16 v[246:247], v11 offset:43264\n"
    "s_waitcnt vmcnt(38)\n"
    "v_mfma_f32_16x16x32_bf16 v[164:167], v[224:227], a[104:107], v[164:167]\n"
    "s_waitcnt lgkmcnt(8)\n"
    "s_waitcnt vmcnt(37)\n"
    "v_mfma_f32_16x16x32_bf16 v[168:171], v[224:227], a[168:171], v[168:171]\n"
    "s_add_u32 s58, s63, 0x800\n"
    "buffer_store_dwordx4 v[104:107], v1, s[44:47], s58 offen sc1\n"
    "s_waitcnt vmcnt(37)\n"
    "v_mfma_f32_16x16x32_bf16 v[172:175], v[224:227], a[232:235], v[172:175]\n"
    "s_add_u32 s56, s61, 0x380000\n"
    "v_mfma_f32_16x16x32_bf16 v[176:179], v[228:231], a[40:43], v[176:179]\n"
    "ds_read_b64_tr_b16 v[248:249], v10 offset:47104\n"
    "ds_read_b64_tr_b16 v[250:251], v10 offset:47360\n"
    "v_mfma_f32_16x16x32_bf16 v[180:183], v[228:231], a[104:107], v[180:183]\n"
    "s_waitcnt lgkmcnt(8)\n"
    "v_mfma_f32_16x16x32_bf16 v[184:187], v[228:231], a[168:171], v[184:187]\n"
    "s_add_u32 m0, s60, 57344\n"
    "s_nop 0\n"
    "buffer_load_dwordx4 v2, s[36:39], s56 offen lds\n"
    "v_mfma_f32_16x16x32_bf16 v[188:191], v[228:231], a[232:235], v[188:191]\n"
    "v_mfma_f32_16x16x32_bf16 v[192:195], v[232:235], a[40:43], v[192:195]\n"
    "ds_read_b64_tr_b16 v[252:253], v11 offset:47104\n"
    "ds_read_b64_tr_b16 v[254:255], v11 offset:47360\n"
    "v_mfma_f32_16x16x32_bf16 v[196:199], v[232:235], a[104:107], v[196:199]\n"
    "s_waitcnt lgkmcnt(8)\n"
    "v_mfma_f32_16x16x32_bf16 v[200:203], v[232:235], a[168:171], v[200:203]\n"
    "s_add_u32 m0, s60, 58368\n"
    "s_nop 0\n"
    "buffer_load_dwordx4 v3, s[36:39], s56 offen lds\n"
    "v_mfma_f32_16x16x32_bf16 v[204:207], v[232:235], a[232:235], v[204:207]\n"
    "v_mfma_f32_16x16x32_bf16 v[208:211], v[236:239], a[40:43], v[208:211]\n"
    "ds_read_b64_tr_b16 v[224:225], v10 offset:49152\n"
    "ds_read_b64_tr_b16 v[226:227], v10 offset:49408\n"
    "v_mfma_f32_16x16x32_bf16 v[212:215], v[236:239], a[104:107], v[212:215]\n"
    "s_waitcnt lgkmcnt(8)\n"
    "v_mfma_f32_16x16x32_bf16 v[216:219], v[236:239], a[168:171], v[216:219]\n"
    "s_add_u32 s58, s63, 0xc00\n"
    "buffer_store_dwordx4 v[108:111], v1, s[44:47], s58 offen sc1\n"
    "v_mfma_f32_16x16x32_bf16 v[220:223], v[236:239], a[232:235], v[220:223]\n"
    "s_waitcnt vmcnt(39)\n"
    "v_mfma_f32_16x16x32_bf16 v[160:163], v[240:243], a[44:47], v[160:163]\n"
    "ds_read_b64_tr_b16 v[228:229], v11 offset:49152\n"
    "ds_read_b64_tr_b16 v[230:231], v11 offset:49408\n"
    "s_waitcnt vmcnt(38)\n"
    "v_mfma_f32_16x16x32_bf16 v[164:167], v[240:243], a[108:111], v[164:167]\n"
    "s_waitcnt lgkmcnt(8)\n"
    "s_waitcnt vmcnt(37)\n"
    "v_mfma_f32_16x16x32_bf16 v[168:171], v[240:243], a[172:175], v[168:171]\n"
    "s_waitcnt vmcnt(36)\n"
    "v_mfma_f32_16x16x32_bf16 v[172:175], v[240:243], a[236:239], v[172:175]\n"
    "s_add_u32 s58, s63, 0x1000\n"
    "buffer_store_dwordx4 v[112:115], v1, s[44:47], s58 offen sc1\n"
    "v_mfma_f32_16x16x32_bf16 v[176:179], v[244:247], a[44:47], v[176:179]\n"
    "ds_read_b64_tr_b16 v[232:233], v10 offset:53248\n"
    "ds_read_b64_tr_b16 v[234:235], v10 offset:53504\n"
    "v_mfma_f32_16x16x32_bf16 v[180:183], v[244:247], a[108:111], v[180:183]\n"
    "s_waitcnt lgkmcnt(8)\n"
    "v_mfma_f32_16x16x32_bf16 v[184:187], v[244:247], a[172:175], v[184:187]\n"
    "v_mfma_f32_16x16x32_bf16 v[188:191], v[244:247], a[236:239], v[188:191]\n"
    "v_mfma_f32_16x16x32_bf16 v[192:195], v[248:251], a[44:47], v[192:195]\n"
    "ds_read_b64_tr_b16 v[236:237], v11 offset:53248\n"
    "ds_read_b64_tr_b16 v[238:239], v11 offset:53504\n"
    "v_mfma_f32_16x16x32_bf16 v[196:199], v[248:251], a[108:111], v[196:199]\n"
    "s_waitcnt lgkmcnt(8)\n"
    "v_mfma_f32_16x16x32_bf16 v[200:203], v[248:251], a[172:175], v[200:203]\n"
    "v_mfma_f32_16x16x32_bf16 v[204:207], v[248:251], a[236:239], v[204:207]\n"
    "v_mfma_f32_16x16x32_bf16 v[208:211], v[252:255], a[44:47], v[208:211]\n"
    "ds_read_b64_tr_b16 v[240:241], v10 offset:51200\n"
    "ds_read_b64_tr_b16 v[242:243], v10 offset:51456\n"
    "v_mfma_f32_16x16x32_bf16 v[212:215], v[252:255], a[108:111], v[212:215]\n"
    "s_waitcnt lgkmcnt(8)\n"
    "v_mfma_f32_16x16x32_bf16 v[216:219], v[252:255], a[172:175], v[216:219]\n"
    "v_mfma_f32_16x16x32_bf16 v[220:223], v[252:255], a[236:239], v[220:223]\n"
    "s_waitcnt vmcnt(36)\n"
    "v_mfma_f32_16x16x32_bf16 v[160:163], v[224:227], a[48:51], v[160:163]\n"
    "ds_read_b64_tr_b16 v[244:245], v11 offset:51200\n"
    "ds_read_b64_tr_b16 v[246:247], v11 offset:51456\n"
    "s_waitcnt vmcnt(35)\n"
    "v_mfma_f32_16x16x32_bf16 v[164:167], v[224:227], a[112:115], v[164:167]\n"
    "s_waitcnt lgkmcnt(8)\n"
    "s_waitcnt vmcnt(34)\n"
    "v_mfma_f32_16x16x32_bf16 v[168:171], v[224:227], a[176:179], v[168:171]\n"
    "s_waitcnt vmcnt(33)\n"
    "v_mfma_f32_16x16x32_bf16 v[172:175], v[224:227], a[240:243], v[172:175]\n"
    "v_mfma_f32_16x16x32_bf16 v[176:179], v[228:231], a[48:51], v[176:179]\n"
    "ds_read_b64_tr_b16 v[248:249], v10 offset:55296\n"
    "ds_read_b64_tr_b16 v[250:251], v10 offset:55552\n"
    "v_mfma_f32_16x16x32_bf16 v[180:183], v[228:231], a[112:115], v[180:183]\n"
    "s_waitcnt lgkmcnt(8)\n"
    "v_mfma_f32_16x16x32_bf16 v[184:187], v[228:231], a[176:179], v[184:187]\n"
    "s_add_u32 s58, s63, 0x1400\n"
    "buffer_store_dwordx4 v[116:119], v1, s[44:47], s58 offen sc1\n"
    "v_mfma_f32_16x16x32_bf16 v[188:191], v[228:231], a[240:243], v[188:191]\n"
    "v_mfma_f32_16x16x32_bf16 v[192:195], v[232:235], a[48:51], v[192:195]\n"
    "ds_read_b64_tr_b16 v[252:253], v11 offset:55296\n"
    "ds_read_b64_tr_b16 v[254:255], v11 offset:55552\n"
    "v_mfma_f32_16x16x32_bf16 v[196:199], v[232:235], a[112:115], v[196:199]\n"
    "s_waitcnt lgkmcnt(8)\n"
    "v_mfma_f32_16x16x32_bf16 v[200:203], v[232:235], a[176:179], v[200:203]\n"
    "v_mfma_f32_16x16x32_bf16 v[204:207], v[232:235], a[240:243], v[204:207]\n"
    "v_mfma_f32_16x16x32_bf16 v[208:211], v[236:239], a[48:51], v[208:211]\n"
    "ds_read_b64_tr_b16 v[224:225], v10 offset:57344\n"
    "ds_read_b64_tr_b16 v[226:227], v10 offset:57600\n"
    "v_mfma_f32_16x16x32_bf16 v[212:215], v[236:239], a[112:115], v[212:215]\n"
    "s_waitcnt lgkmcnt(8)\n"
    "v_mfma_f32_16x16x32_bf16 v[216:219], v[236:239], a[176:179], v[216:219]\n"
    "v_mfma_f32_16x16x32_bf16 v[220:223], v[236:239], a[240:243], v[220:223]\n"
    "s_waitcnt vmcnt(33)\n"
    "v_mfma_f32_16x16x32_bf16 v[160:163], v[240:243], a[52:55], v[160:163]\n"
    "ds_read_b64_tr_b16 v[228:229], v11 offset:57344\n"
    "ds_read_b64_tr_b16 v[230:231], v11 offset:57600\n"
    "s_waitcnt vmcnt(32)\n"
    "v_mfma_f32_16x16x32_bf16 v[164:167], v[240:243], a[116:119], v[164:167]\n"
    "s_waitcnt lgkmcnt(8)\n"
    "s_waitcnt vmcnt(31)\n"
    "v_mfma_f32_16x16x32_bf16 v[168:171], v[240:243], a[180:183], v[168:171]\n"
    "s_waitcnt vmcnt(30)\n"
    "v_mfma_f32_16x16x32_bf16 v[172:175], v[240:243], a[244:247], v[172:175]\n"
    "v_mfma_f32_16x16x32_bf16 v[176:179], v[244:247], a[52:55], v[176:179]\n"
    "ds_read_b64_tr_b16 v[232:233], v10 offset:61440\n"
    "ds_read_b64_tr_b16 v[234:235], v10 offset:61696\n"
    "v_mfma_f32_16x16x32_bf16 v[180:183], v[244:247], a[116:119], v[180:183]\n"
    "s_waitcnt lgkmcnt(8)\n"
    "v_mfma_f32_16x16x32_bf16 v[184:187], v[244:247], a[180:183], v[184:187]\n"
    "v_mfma_f32_16x16x32_bf16 v[188:191], v[244:247], a[244:247], v[188:191]\n"
    "v_mfma_f32_16x16x32_bf16 v[192:195], v[248:251], a[52:55], v[192:195]\n"
    "ds_read_b64_tr_b16 v[236:237], v11 offset:61440\n"
    "ds_read_b64_tr_b16 v[238:239], v11 offset:61696\n"
    "v_mfma_f32_16x16x32_bf16 v[196:199], v[248:251], a[116:119], v[196:199]\n"
    "s_waitcnt lgkmcnt(8)\n"
    "v_mfma_f32_16x16x32_bf16 v[200:203], v[248:251], a[180:183], v[200:203]\n"
    "s_add_u32 s58, s63, 0x1800\n"
    "buffer_store_dwordx4 v[120:123], v1, s[44:47], s58 offen sc1\n"
    "v_mfma_f32_16x16x32_bf16 v[204:207], v[248:251], a[244:247], v[204:207]\n"
    "v_mfma_f32_16x16x32_bf16 v[208:211], v[252:255], a[52:55], v[208:211]\n"
    "ds_read_b64_tr_b16 v[240:241], v10 offset:59392\n"
    "ds_read_b64_tr_b16 v[242:243], v10 offset:59648\n"
    "v_mfma_f32_16x16x32_bf16 v[212:215], v[252:255], a[116:119], v[212:215]\n"
    "s_waitcnt lgkmcnt(8)\n"
    "v_mfma_f32_16x16x32_bf16 v[216:219], v[252:255], a[180:183], v[216:219]\n"
    "v_mfma_f32_16x16x32_bf16 v[220:223], v[252:255], a[244:247], v[220:223]\n"
    "s_waitcnt vmcnt(30)\n"
    "v_mfma_f32_16x16x32_bf16 v[160:163], v[224:227], a[56:59], v[160:163]\n"
    "ds_read_b64_tr_b16 v[244:245], v11 offset:59392\n"
    "ds_read_b64_tr_b16 v[246:247], v11 offset:59648\n"
    "s_waitcnt vmcnt(28)\n"
    "v_mfma_f32_16x16x32_bf16 v[164:167], v[224:227], a[120:123], v[164:167]\n"
    "s_waitcnt lgkmcnt(8)\n"
    "s_waitcnt vmcnt(27)\n"
    "v_mfma_f32_16x16x32_bf16 v[168:171], v[224:227], a[184:187], v[168:171]\n"
    "s_waitcnt vmcnt(25)\n"
    "v_mfma_f32_16x16x32_bf16 v[172:175], v[224:227], a[248:251], v[172:175]\n"
    "v_mfma_f32_16x16x32_bf16 v[176:179], v[228:231], a[56:59], v[176:179]\n"
    "ds_read_b64_tr_b16 v[248:249], v10 offset:63488\n"
    "ds_read_b64_tr_b16 v[250:251], v10 offset:63744\n"
    "v_mfma_f32_16x16x32_bf16 v[180:183], v[228:231], a[120:123], v[180:183]\n"
    "s_waitcnt lgkmcnt(8)\n"
    "v_mfma_f32_16x16x32_bf16 v[184:187], v[228:231], a[184:187], v[184:187]\n"
    "v_mfma_f32_16x16x32_bf16 v[188:191], v[228:231], a[248:251], v[188:191]\n"
    "v_mfma_f32_16x16x32_bf16 v[192:195], v[232:235], a[56:59], v[192:195]\n"
    "ds_read_b64_tr_b16 v[252:253], v11 offset:63488\n"
    "ds_read_b64_tr_b16 v[254:255], v11 offset:63744\n"
    "v_mfma_f32_16x16x32_bf16 v[196:199], v[232:235], a[120:123], v[196:199]\n"
    "s_waitcnt lgkmcnt(8)\n"
    "v_mfma_f32_16x16x32_bf16 v[200:203], v[232:235], a[184:187], v[200:203]\n"
    "v_mfma_f32_16x16x32_bf16 v[204:207], v[232:235], a[248:251], v[204:207]\n"
    "s_waitcnt vmcnt(4)\n"
    "s_barrier\n"
    "v_mfma_f32_16x16x32_bf16 v[208:211], v[236:239], a[56:59], v[208:211]\n"
    "ds_read_b64_tr_b16 v[224:225], v4 offset:0\n"
    "ds_read_b64_tr_b16 v[226:227], v4 offset:512\n"
    "v_mfma_f32_16x16x32_bf16 v[212:215], v[236:239], a[120:123], v[212:215]\n"
    "s_waitcnt lgkmcnt(8)\n"
    "s_add_u32 s56, s61, 0x80\n"
    "v_mfma_f32_16x16x32_bf16 v[216:219], v[236:239], a[184:187], v[216:219]\n"
    "s_add_u32 m0, s60, 65536\n"
    "s_nop 0\n"
    "buffer_load_dwordx4 v2, s[36:39], s56 offen lds\n"
    "v_mfma_f32_16x16x32_bf16 v[220:223], v[236:239], a[248:251], v[220:223]\n"
    "v_mfma_f32_16x16x32_bf16 v[160:163], v[240:243], a[60:63], v[160:163]\n"
    "ds_read_b64_tr_b16 v[228:229], v5 offset:0\n"
    "ds_read_b64_tr_b16 v[230:231], v5 offset:512\n"
    "v_mfma_f32_16x16x32_bf16 v[164:167], v[240:243], a[124:127], v[164:167]\n"
    "s_waitcnt lgkmcnt(8)\n"
    "v_mfma_f32_16x16x32_bf16 v[168:171], v[240:243], a[188:191], v[168:171]\n"
    "s_add_u32 s58, s63, 0x1c00\n"
    "buffer_store_dwordx4 v[124:127], v1, s[44:47], s58 offen sc1\n"
    "v_mfma_f32_16x16x32_bf16 v[172:175], v[240:243], a[252:255], v[172:175]\n"
    "v_mfma_f32_16x16x32_bf16 v[176:179], v[244:247], a[60:63], v[176:179]\n"
    "ds_read_b64_tr_b16 v[232:233], v6 offset:0\n"
    "ds_read_b64_tr_b16 v[234:235], v6 offset:512\n"
    "v_mfma_f32_16x16x32_bf16 v[180:183], v[244:247], a[124:127], v[180:183]\n"
    "s_waitcnt lgkmcnt(8)\n"
    "v_mfma_f32_16x16x32_bf16 v[184:187], v[244:247], a[188:191], v[184:187]\n"
    "v_mfma_f32_16x16x32_bf16 v[188:191], v[244:247], a[252:255], v[188:191]\n"
    "v_mfma_f32_16x16x32_bf16 v[192:195], v[248:251], a[60:63], v[192:195]\n"
    "ds_read_b64_tr_b16 v[236:237], v8 offset:0\n"
    "ds_read_b64_tr_b16 v[238:239], v8 offset:512\n"
    "v_mfma_f32_16x16x32_bf16 v[196:199], v[248:251], a[124:127], v[196:199]\n"
    "s_waitcnt lgkmcnt(8)\n"
    "v_mfma_f32_16x16x32_bf16 v[200:203], v[248:251], a[188:191], v[200:203]\n"
    "s_add_u32 m0, s60, 66560\n"
    "s_nop 0\n"
    "buffer_load_dwordx4 v3, s[36:39], s56 offen lds\n"
    "v_mfma_f32_16x16x32_bf16 v[204:207], v[248:251], a[252:255], v[204:207]\n"
    "v_mfma_f32_16x16x32_bf16 v[208:211], v[252:255], a[60:63], v[208:211]\n"
    "ds_read_b64_tr_b16 v[240:241], v4 offset:4096\n"
    "ds_read_b64_tr_b16 v[242:243], v4 offset:4608\n"
    "v_mfma_f32_16x16x32_bf16 v[212:215], v[252:255], a[124:127], v[212:215]\n"
    "s_waitcnt lgkmcnt(8)\n"
    "v_mfma_f32_16x16x32_bf16 v[216:219], v[252:255], a[188:191], v[216:219]\n"
    "v_mfma_f32_16x16x32_bf16 v[220:223], v[252:255], a[252:255], v[220:223]\n"
    "s_add_u32 s58, s63, 0x2000\n"
    "buffer_store_dwordx4 v[128:131], v1, s[44:47], s58 offen sc1\n"
    "v_mfma_f32_16x16x32_bf16 v[32:35], v[224:227], a[0:3], 0\n"
    "ds_read_b64_tr_b16 v[244:245], v5 offset:4096\n"
    "ds_read_b64_tr_b16 v[246:247], v5 offset:4608\n"
    "v_mfma_f32_16x16x32_bf16 v[36:39], v[224:227], a[64:67], 0\n"
    "s_waitcnt lgkmcnt(8)\n"
    "v_mfma_f32_16x16x32_bf16 v[40:43], v[224:227], a[128:131], 0\n"
    "v_mul_f32 v25, 0xbfb8aa3b, v160\n"
    "v_mul_f32 v26, 0xbfb8aa3b, v161\n"
    "v_mfma_f32_16x16x32_bf16 v[44:47], v[224:227], a[192:195], 0\n"
    "v_mul_f32 v27, 0xbfb8aa3b, v162\n"
    "v_mul_f32 v28, 0xbfb8aa3b, v163\n"
    "v_mfma_f32_16x16x32_bf16 v[48:51], v[228:231], a[0:3], 0\n"
    "ds_read_b64_tr_b16 v[248:249], v6 offset:4096\n"
    "ds_read_b64_tr_b16 v[250:251], v6 offset:4608\n"
    "v_mfma_f32_16x16x32_bf16 v[52:55], v[228:231], a[64:67], 0\n"
    "s_waitcnt lgkmcnt(8)\n"
    "v_exp_f32 v25, v25\n"
    "v_mfma_f32_16x16x32_bf16 v[56:59], v[228:231], a[128:131], 0\n"
    "s_add_u32 s56, s61, 0x80080\n"
    "v_exp_f32 v26, v26\n"
    "v_mfma_f32_16x16x32_bf16 v[60:63], v[228:231], a[192:195], 0\n"
    "s_add_u32 m0, s60, 73728\n"
    "s_nop 0\n"
    "buffer_load_dwordx4 v2, s[36:39], s56 offen lds\n"
    "v_mfma_f32_16x16x32_bf16 v[64:67], v[232:235], a[0:3], 0\n"
    "ds_read_b64_tr_b16 v[252:253], v8 offset:4096\n"
    "ds_read_b64_tr_b16 v[254:255], v8 offset:4608\n"
    "v_mfma_f32_16x16x32_bf16 v[68:71], v[232:235], a[64:67], 0\n"
    "s_waitcnt lgkmcnt(8)\n"
    "v_exp_f32 v27, v27\n"
    "v_mfma_f32_16x16x32_bf16 v[72:75], v[232:235], a[128:131], 0\n"
    "v_exp_f32 v28, v28\n"
    "v_mfma_f32_16x16x32_bf16 v[76:79], v[232:235], a[192:195], 0\n"
    "v_mul_f32 v160, v160, v192\n"
    "v_mul_f32 v161, v161, v193\n"
    "v_mfma_f32_16x16x32_bf16 v[80:83], v[236:239], a[0:3], 0\n"
    "ds_read_b64_tr_b16 v[224:225], v4 offset:8192\n"
    "ds_read_b64_tr_b16 v[226:227], v4 offset:8704\n"
    "v_mfma_f32_16x16x32_bf16 v[84:87], v[236:239], a[64:67], 0\n"
    "s_waitcnt lgkmcnt(8)\n"
    "v_mul_f32 v162, v162, v194\n"
    "v_mul_f32 v163, v163, v195\n"
    "v_mfma_f32_16x16x32_bf16 v[88:91], v[236:239], a[128:131], 0\n"
    "v_add_f32 v25, 1.0, v25\n"
    "v_add_f32 v26, 1.0, v26\n"
    "v_mfma_f32_16x16x32_bf16 v[92:95], v[236:239], a[192:195], 0\n"
    "v_add_f32 v27, 1.0, v27\n"
    "v_mfma_f32_16x16x32_bf16 v[32:35], v[240:243], a[4:7], v[32:35]\n"
    "ds_read_b64_tr_b16 v[228:229], v5 offset:8192\n"
    "ds_read_b64_tr_b16 v[230:231], v5 offset:8704\n"
    "v_mfma_f32_16x16x32_bf16 v[36:39], v[240:243], a[68:71], v[36:39]\n"
    "s_waitcnt lgkmcnt(8)\n"
    "v_add_f32 v28, 1.0, v28\n"
    "v_mfma_f32_16x16x32_bf16 v[40:43], v[240:243], a[132:135], v[40:43]\n"
    "s_add_u32 m0, s60, 74752\n"
    "s_nop 0\n"
    "buffer_load_dwordx4 v3, s[36:39], s56 offen lds\n"
    "v_mfma_f32_16x16x32_bf16 v[44:47], v[240:243], a[196:199], v[44:47]\n"
    "v_rcp_f32 v25, v25\n"
    "v_mfma_f32_16x16x32_bf16 v[48:51], v[244:247], a[4:7], v[48:51]\n"
    "ds_read_b64_tr_b16 v[232:233], v6 offset:8192\n"
    "ds_read_b64_tr_b16 v[234:235], v6 offset:8704\n"
    "v_mfma_f32_16x16x32_bf16 v[52:55], v[244:247], a[68:71], v[52:55]\n"
    "s_waitcnt lgkmcnt(8)\n"
    "v_rcp_f32 v26, v26\n"
    "v_mfma_f32_16x16x32_bf16 v[56:59], v[244:247], a[132:135], v[56:59]\n"
    "s_add_u32 s58, s63, 0x2400\n"
    "buffer_store_dwordx4 v[132:135], v1, s[44:47], s58 offen sc1\n"
    "v_mfma_f32_16x16x32_bf16 v[60:63], v[244:247], a[196:199], v[60:63]\n"
    "v_rcp_f32 v27, v27\n"
    "v_mfma_f32_16x16x32_bf16 v[64:67], v[248:251], a[4:7], v[64:67]\n"
    "ds_read_b64_tr_b16 v[236:237], v8 offset:8192\n"
    "ds_read_b64_tr_b16 v[238:239], v8 offset:8704\n"
    "v_mfma_f32_16x16x32_bf16 v[68:71], v[248:251], a[68:71], v[68:71]\n"
    "s_waitcnt lgkmcnt(8)\n"
    "v_rcp_f32 v28, v28\n"
    "v_mfma_f32_16x16x32_bf16 v[72:75], v[248:251], a[132:135], v[72:75]\n"
    "v_mul_f32 v160, v160, v25\n"
    "v_mul_f32 v161, v161, v26\n"
    "v_mfma_f32_16x16x32_bf16 v[76:79], v[248:251], a[196:199], v[76:79]\n"
    "v_mul_f32 v162, v162, v27\n"
    "v_mul_f32 v163, v163, v28\n"
    "v_mfma_f32_16x16x32_bf16 v[80:83], v[252:255], a[4:7], v[80:83]\n"
    "ds_read_b64_tr_b16 v[240:241], v4 offset:12288\n"
    "ds_read_b64_tr_b16 v[242:243], v4 offset:12800\n"
    "v_mfma_f32_16x16x32_bf16 v[84:87], v[252:255], a[68:71], v[84:87]\n"
    "s_waitcnt lgkmcnt(8)\n"
    "v_cvt_pk_bf16_f32 v16, v160, v161\n"
    "v_cvt_pk_bf16_f32 v17, v162, v163\n"
    "v_mfma_f32_16x16x32_bf16 v[88:91], v[252:255], a[132:135], v[88:91]\n"
    "s_add_u32 s56, s61, 0x100080\n"
    "v_mul_f32 v25, 0xbfb8aa3b, v176\n"
    "v_mul_f32 v26, 0xbfb8aa3b, v177\n"
    "v_mfma_f32_16x16x32_bf16 v[92:95], v[252:255], a[196:199], v[92:95]\n"
    "s_add_u32 m0, s60, 81920\n"
    "s_nop 0\n"
    "buffer_load_dwordx4 v2, s[36:39], s56 offen lds\n"
    "v_mul_f32 v27, 0xbfb8aa3b, v178\n"
    "v_mfma_f32_16x16x32_bf16 v[32:35], v[224:227], a[8:11], v[32:35]\n"
    "ds_read_b64_tr_b16 v[244:245], v5 offset:12288\n"
    "ds_read_b64_tr_b16 v[246:247], v5 offset:12800\n"
    "v_mfma_f32_16x16x32_bf16 v[36:39], v[224:227], a[72:75], v[36:39]\n"
    "s_waitcnt lgkmcnt(8)\n"
    "v_mul_f32 v28, 0xbfb8aa3b, v179\n"
    "v_mfma_f32_16x16x32_bf16 v[40:43], v[224:227], a[136:139], v[40:43]\n"
    "v_exp_f32 v25, v25\n"
    "v_mfma_f32_16x16x32_bf16 v[44:47], v[224:227], a[200:203], v[44:47]\n"
    "v_exp_f32 v26, v26\n"
    "v_mfma_f32_16x16x32_bf16 v[48:51], v[228:231], a[8:11], v[48:51]\n"
    "ds_read_b64_tr_b16 v[248:249], v6 offset:12288\n"
    "ds_read_b64_tr_b16 v[250:251], v6 offset:12800\n"
    "v_mfma_f32_16x16x32_bf16 v[52:55], v[228:231], a[72:75], v[52:55]\n"
    "s_waitcnt lgkmcnt(8)\n"
    "v_exp_f32 v27, v27\n"
    "v_mfma_f32_16x16x32_bf16 v[56:59], v[228:231], a[136:139], v[56:59]\n"
    "s_add_u32 s58, s63, 0x2800\n"
    "buffer_store_dwordx4 v[136:139], v1, s[44:47], s58 offen sc1\n"
    "v_mfma_f32_16x16x32_bf16 v[60:63], v[228:231], a[200:203], v[60:63]\n"
    "v_exp_f32 v28, v28\n"
    "v_mfma_f32_16x16x32_bf16 v[64:67], v[232:235], a[8:11], v[64:67]\n"
    "ds_read_b64_tr_b16 v[252:253], v8 offset:12288\n"
    "ds_read_b64_tr_b16 v[254:255], v8 offset:12800\n"
    "v_mfma_f32_16x16x32_bf16 v[68:71], v[232:235], a[72:75], v[68:71]\n"
    "s_waitcnt lgkmcnt(8)\n"
    "v_mul_f32 v176, v176, v208\n"
    "v_mul_f32 v177, v177, v209\n"
    "v_mfma_f32_16x16x32_bf16 v[72:75], v[232:235], a[136:139], v[72:75]\n"
    "s_add_u32 m0, s60, 82944\n"
    "s_nop 0\n"
    "buffer_load_dwordx4 v3, s[36:39], s56 offen lds\n"
    "v_mul_f32 v178, v178, v210\n"
    "v_mfma_f32_16x16x32_bf16 v[76:79], v[232:235], a[200:203], v[76:79]\n"
    "v_mul_f32 v179, v179, v211\n"
    "v_add_f32 v25, 1.0, v25\n"
    "v_mfma_f32_16x16x32_bf16 v[80:83], v[236:239], a[8:11], v[80:83]\n"
    "ds_read_b64_tr_b16 v[224:225], v4 offset:16384\n"
    "ds_read_b64_tr_b16 v[226:227], v4 offset:16896\n"
    "v_mfma_f32_16x16x32_bf16 v[84:87], v[236:239], a[72:75], v[84:87]\n"
    "s_waitcnt lgkmcnt(8)\n"
    "v_add_f32 v26, 1.0, v26\n"
    "v_add_f32 v27, 1.0, v27\n"
    "v_mfma_f32_16x16x32_bf16 v[88:91], v[236:239], a[136:139], v[88:91]\n"
    "v_add_f32 v28, 1.0, v28\n"
    "v_mfma_f32_16x16x32_bf16 v[92:95], v[236:239], a[200:203], v[92:95]\n"
    "v_rcp_f32 v25, v25\n"
    "v_mfma_f32_16x16x32_bf16 v[32:35], v[240:243], a[12:15], v[32:35]\n"
    "ds_read_b64_tr_b16 v[228:229], v5 offset:16384\n"
    "ds_read_b64_tr_b16 v[230:231], v5 offset:16896\n"
    "v_mfma_f32_16x16x32_bf16 v[36:39], v[240:243], a[76:79], v[36:39]\n"
    "s_waitcnt lgkmcnt(8)\n"
    "v_rcp_f32 v26, v26\n"
    "v_mfma_f32_16x16x32_bf16 v[40:43], v[240:243], a[140:143], v[40:43]\n"
    "v_rcp_f32 v27, v27\n"
    "v_mfma_f32_16x16x32_bf16 v[44:47], v[240:243], a[204:207], v[44:47]\n"
    "v_rcp_f32 v28, v28\n"
    "v_mfma_f32_16x16x32_bf16 v[48:51], v[244:247], a[12:15], v[48:51]\n"
    "ds_read_b64_tr_b16 v[232:233], v6 offset:16384\n"
    "ds_read_b64_tr_b16 v[234:235], v6 offset:16896\n"
    "v_mfma_f32_16x16x32_bf16 v[52:55], v[244:247], a[76:79], v[52:55]\n"
    "s_waitcnt lgkmcnt(8)\n"
    "v_mul_f32 v176, v176, v25\n"
    "v_mul_f32 v177, v177, v26\n"
    "v_mfma_f32_16x16x32_bf16 v[56:59], v[244:247], a[140:143], v[56:59]\n"
    "s_add_u32 s56, s61, 0x180080\n"
    "v_mul_f32 v178, v178, v27\n"
    "v_mul_f32 v179, v179, v28\n"
    "v_mfma_f32_16x16x32_bf16 v[60:63], v[244:247], a[204:207], v[60:63]\n"
    "s_add_u32 m0, s60, 90112\n"
    "s_nop 0\n"
    "buffer_load_dwordx4 v2, s[36:39], s56 offen lds\n"
    "v_cvt_pk_bf16_f32 v18, v176, v177\n"
    "v_mfma_f32_16x16x32_bf16 v[64:67], v[248:251], a[12:15], v[64:67]\n"
    "ds_read_b64_tr_b16 v[236:237], v8 offset:16384\n"
    "ds_read_b64_tr_b16 v[238:239], v8 offset:16896\n"
    "v_mfma_f32_16x16x32_bf16 v[68:71], v[248:251], a[76:79], v[68:71]\n"
    "s_waitcnt lgkmcnt(8)\n"
    "v_cvt_pk_bf16_f32 v19, v178, v179\n"
    "v_mfma_f32_16x16x32_bf16 v[72:75], v[248:251], a[140:143], v[72:75]\n"
    "v_mfma_f32_16x16x32_bf16 v[76:79], v[248:251], a[204:207], v[76:79]\n"
    "s_add_u32 s58, s67, 0x0\n"
    "buffer_store_dwordx4 v[16:19], v1, s[44:47], s58 offen sc1\n"
    "v_mul_f32 v25, 0xbfb8aa3b, v164\n"
    "v_mfma_f32_16x16x32_bf16 v[80:83], v[252:255], a[12:15], v[80:83]\n"
    "ds_read_b64_tr_b16 v[240:241], v4 offset:20480\n"
    "ds_read_b64_tr_b16 v[242:243], v4 offset:20992\n"
    "v_mfma_f32_16x16x32_bf16 v[84:87], v[252:255], a[76:79], v[84:87]\n"
    "s_waitcnt lgkmcnt(8)\n"
    "v_mul_f32 v26, 0xbfb8aa3b, v165\n"
    "v_mul_f32 v27, 0xbfb8aa3b, v166\n"
    "v_mfma_f32_16x16x32_bf16 v[88:91], v[252:255], a[140:143], v[88:91]\n"
    "v_mul_f32 v28, 0xbfb8aa3b, v167\n"
    "v_mfma_f32_16x16x32_bf16 v[92:95], v[252:255], a[204:207], v[92:95]\n"
    "s_add_u32 s58, s63, 0x2c00\n"
    "buffer_store_dwordx4 v[140:143], v1, s[44:47], s58 offen sc1\n"
    "v_mfma_f32_16x16x32_bf16 v[32:35], v[224:227], a[16:19], v[32:35]\n"
    "ds_read_b64_tr_b16 v[244:245], v5 offset:20480\n"
    "ds_read_b64_tr_b16 v[246:247], v5 offset:20992\n"
    "v_mfma_f32_16x16x32_bf16 v[36:39], v[224:227], a[80:83], v[36:39]\n"
    "s_waitcnt lgkmcnt(8)\n"
    "v_exp_f32 v25, v25\n"
    "v_mfma_f32_16x16x32_bf16 v[40:43], v[224:227], a[144:147], v[40:43]\n"
    "v_exp_f32 v26, v26\n"
    "v_mfma_f32_16x16x32_bf16 v[44:47], v[224:227], a[208:211], v[44:47]\n"
    "s_add_u32 m0, s60, 91136\n"
    "s_nop 0\n"
    "buffer_load_dwordx4 v3, s[36:39], s56 offen lds\n"
    "v_mfma_f32_16x16x32_bf16 v[48:51], v[228:231], a[16:19], v[48:51]\n"
    "ds_read_b64_tr_b16 v[248:249], v6 offset:20480\n"
    "ds_read_b64_tr_b16 v[250:251], v6 offset:20992\n"
    "v_mfma_f32_16x16x32_bf16 v[52:55], v[228:231], a[80:83], v[52:55]\n"
    "s_waitcnt lgkmcnt(8)\n"
    "v_exp_f32 v27, v27\n"
    "v_mfma_f32_16x16x32_bf16 v[56:59], v[228:231], a[144:147], v[56:59]\n"
    "v_exp_f32 v28, v28\n"
    "v_mfma_f32_16x16x32_bf16 v[60:63], v[228:231], a[208:211], v[60:63]\n"
    "v_mul_f32 v164, v164, v196\n"
    "v_mul_f32 v165, v165, v197\n"
    "v_mfma_f32_16x16x32_bf16 v[64:67], v[232:235], a[16:19], v[64:67]\n"
    "ds_read_b64_tr_b16 v[252:253], v8 offset:20480\n"
    "ds_read_b64_tr_b16 v[254:255], v8 offset:20992\n"
    "v_mfma_f32_16x16x32_bf16 v[68:71], v[232:235], a[80:83], v[68:71]\n"
    "s_waitcnt lgkmcnt(8)\n"
    "v_mul_f32 v166, v166, v198\n"
    "v_mul_f32 v167, v167, v199\n"
    "v_mfma_f32_16x16x32_bf16 v[72:75], v[232:235], a[144:147], v[72:75]\n"
    "s_add_u32 s58, s63, 0x3000\n"
    "buffer_store_dwordx4 v[144:147], v1, s[44:47], s58 offen sc1\n"
    "v_add_f32 v25, 1.0, v25\n"
    "v_mfma_f32_16x16x32_bf16 v[76:79], v[232:235], a[208:211], v[76:79]\n"
    "v_add_f32 v26, 1.0, v26\n"
    "v_add_f32 v27, 1.0, v27\n"
    "v_mfma_f32_16x16x32_bf16 v[80:83], v[236:239], a[16:19], v[80:83]\n"
    "ds_read_b64_tr_b16 v[224:225], v4 offset:24576\n"
    "ds_read_b64_tr_b16 v[226:227], v4 offset:25088\n"
    "v_mfma_f32_16x16x32_bf16 v[84:87], v[236:239], a[80:83], v[84:87]\n"
    "s_waitcnt lgkmcnt(8)\n"
    "v_add_f32 v28, 1.0, v28\n"
    "v_mfma_f32_16x16x32_bf16 v[88:91], v[236:239], a[144:147], v[88:91]\n"
    "s_add_u32 s56, s61, 0x200080\n"
    "v_rcp_f32 v25, v25\n"
    "v_mfma_f32_16x16x32_bf16 v[92:95], v[236:239], a[208:211], v[92:95]\n"
    "s_add_u32 m0, s60, 98304\n"
    "s_nop 0\n"
    "buffer_load_dwordx4 v2, s[36:39], s56 offen lds\n"
    "v_mfma_f32_16x16x32_bf16 v[32:35], v[240:243], a[20:23], v[32:35]\n"
    "ds_read_b64_tr_b16 v[228:229], v5 offset:24576\n"
    "ds_read_b64_tr_b16 v[230:231], v5 offset:25088\n"
    "v_mfma_f32_16x16x32_bf16 v[36:39], v[240:243], a[84:87], v[36:39]\n"
    "s_waitcnt lgkmcnt(8)\n"
    "v_rcp_f32 v26, v26\n"
    "v_mfma_f32_16x16x32_bf16 v[40:43], v[240:243], a[148:151], v[40:43]\n"
    "v_rcp_f32 v27, v27\n"
    "v_mfma_f32_16x16x32_bf16 v[44:47], v[240:243], a[212:215], v[44:47]\n"
    "v_rcp_f32 v28, v28\n"
    "v_mfma_f32_16x16x32_bf16 v[48:51], v[244:247], a[20:23], v[48:51]\n"
    "ds_read_b64_tr_b16 v[232:233], v6 offset:24576\n"
    "ds_read_b64_tr_b16 v[234:235], v6 offset:25088\n"
    "v_mfma_f32_16x16x32_bf16 v[52:55], v[244:247], a[84:87], v[52:55]\n"
    "s_waitcnt lgkmcnt(8)\n"
    "v_mul_f32 v164, v164, v25\n"
    "v_mul_f32 v165, v165, v26\n"
    "v_mfma_f32_16x16x32_bf16 v[56:59], v[244:247], a[148:151], v[56:59]\n"
    "v_mul_f32 v166, v166, v27\n"
    "v_mul_f32 v167, v167, v28\n"
    "v_mfma_f32_16x16x32_bf16 v[60:63], v[244:247], a[212:215], v[60:63]\n"
    "v_cvt_pk_bf16_f32 v20, v164, v165\n"
    "v_mfma_f32_16x16x32_bf16 v[64:67], v[248:251], a[20:23], v[64:67]\n"
    "ds_read_b64_tr_b16 v[236:237], v8 offset:24576\n"
    "ds_read_b64_tr_b16 v[238:239], v8 offset:25088\n"
    "v_mfma_f32_16x16x32_bf16 v[68:71], v[248:251], a[84:87], v[68:71]\n"
    "s_waitcnt lgkmcnt(8)\n"
    "v_cvt_pk_bf16_f32 v21, v166, v167\n"
    "v_mul_f32 v25, 0xbfb8aa3b, v180\n"
    "v_mfma_f32_16x16x32_bf16 v[72:75], v[248:251], a[148:151], v[72:75]\n"
    "v_mul_f32 v26, 0xbfb8aa3b, v181\n"
    "v_mul_f32 v27, 0xbfb8aa3b, v182\n"
    "v_mfma_f32_16x16x32_bf16 v[76:79], v[248:251], a[212:215], v[76:79]\n"
    "s_add_u32 m0, s60, 99328\n"
    "s_nop 0\n"
    "buffer_load_dwordx4 v3, s[36:39], s56 offen lds\n"
    "v_mul_f32 v28, 0xbfb8aa3b, v183\n"
    "v_mfma_f32_16x16x32_bf16 v[80:83], v[252:255], a[20:23], v[80:83]\n"
    "ds_read_b64_tr_b16 v[240:241], v4 offset:28672\n"
    "ds_read_b64_tr_b16 v[242:243], v4 offset:29184\n"
    "v_mfma_f32_16x16x32_bf16 v[84:87], v[252:255], a[84:87], v[84:87]\n"
    "s_waitcnt lgkmcnt(8)\n"
    "v_exp_f32 v25, v25\n"
    "v_mfma_f32_16x16x32_bf16 v[88:91], v[252:255], a[148:151], v[88:91]\n"
    "v_exp_f32 v26, v26\n"
    "v_mfma_f32_16x16x32_bf16 v[92:95], v[252:255], a[212:215], v[92:95]\n"
    "s_add_u32 s58, s63, 0x3400\n"
    "buffer_store_dwordx4 v[148:151], v1, s[44:47], s58 offen sc1\n"
    "v_mfma_f32_16x16x32_bf16 v[32:35], v[224:227], a[24:27], v[32:35]\n"
    "ds_read_b64_tr_b16 v[244:245], v5 offset:28672\n"
    "ds_read_b64_tr_b16 v[246:247], v5 offset:29184\n"
    "v_mfma_f32_16x16x32_bf16 v[36:39], v[224:227], a[88:91], v[36:39]\n"
    "s_waitcnt lgkmcnt(8)\n"
    "v_exp_f32 v27, v27\n"
    "v_mfma_f32_16x16x32_bf16 v[40:43], v[224:227], a[152:155], v[40:43]\n"
    "v_exp_f32 v28, v28\n"
    "v_mfma_f32_16x16x32_bf16 v[44:47], v[224:227], a[216:219], v[44:47]\n"
    "v_mul_f32 v180, v180, v212\n"
    "v_mul_f32 v181, v181, v213\n"
    "v_mfma_f32_16x16x32_bf16 v[48:51], v[228:231], a[24:27], v[48:51]\n"
    "ds_read_b64_tr_b16 v[248:249], v6 offset:28672\n"
    "ds_read_b64_tr_b16 v[250:251], v6 offset:29184\n"
    "v_mfma_f32_16x16x32_bf16 v[52:55], v[228:231], a[88:91], v[52:55]\n"
    "s_waitcnt lgkmcnt(8)\n"
    "v_mul_f32 v182, v182, v214\n"
    "v_mul_f32 v183, v183, v215\n"
    "v_mfma_f32_16x16x32_bf16 v[56:59], v[228:231], a[152:155], v[56:59]\n"
    "s_sub_u32 s58, 15, s54\n"
    "s_lshl_b32 s58, s58, 7\n"
    "s_lshl_b32 s59, s52, 5\n"
    "s_add_u32 s78, s58, s59\n"
    "s_lshl_b32 s58, s54, 7\n"
    "s_add_u32 s79, s58, s59\n"
    "s_and_b32 s83, s52, 1\n"
    "s_lshl_b32 s83, s83, 14\n"
    "s_lshl_b32 s58, s53, 19\n"
    "s_lshl_b32 s59, s52, 12\n"
    "s_add_u32 s58, s58, s59\n"
    "s_add_u32 s71, s58, 0x400000\n"
    "s_add_u32 s72, s58, 0x1400000\n"
    "s_add_u32 s75, s59, 0x0\n"
    "s_add_u32 s76, s59, 0x8000\n"
    "s_mov_b32 s73, 0x4000\n"
    "s_mov_b32 s74, 0x4000\n"
    "s_lshl_b32 s58, s78, 13\n"
    "s_lshl_b32 s59, s53, 12\n"
    "s_add_u32 s58, s58, s59\n"
    "s_add_u32 s81, s58, 0x2c00000\n"
    "s_lshl_b32 s58, s79, 13\n"
    "s_lshl_b32 s59, s53, 12\n"
    "s_add_u32 s58, s58, s59\n"
    "s_add_u32 s82, s58, 0x2c00000\n"
    "v_mfma_f32_16x16x32_bf16 v[60:63], v[228:231], a[216:219], v[60:63]\n"
    "s_add_u32 s56, s61, 0x280080\n"
    "v_add_f32 v25, 1.0, v25\n"
    "v_add_f32 v26, 1.0, v26\n"
    "v_mfma_f32_16x16x32_bf16 v[64:67], v[232:235], a[24:27], v[64:67]\n"
    "ds_read_b64_tr_b16 v[252:253], v8 offset:28672\n"
    "ds_read_b64_tr_b16 v[254:255], v8 offset:29184\n"
    "v_mfma_f32_16x16x32_bf16 v[68:71], v[232:235], a[88:91], v[68:71]\n"
    "s_waitcnt lgkmcnt(8)\n"
    "v_add_f32 v27, 1.0, v27\n"
    "v_add_f32 v28, 1.0, v28\n"
    "v_mfma_f32_16x16x32_bf16 v[72:75], v[232:235], a[152:155], v[72:75]\n"
    "s_add_u32 m0, s60, 106496\n"
    "s_nop 0\n"
    "buffer_load_dwordx4 v2, s[36:39], s56 offen lds\n"
    "v_mfma_f32_16x16x32_bf16 v[76:79], v[232:235], a[216:219], v[76:79]\n"
    "v_rcp_f32 v25, v25\n"
    "v_mfma_f32_16x16x32_bf16 v[80:83], v[236:239], a[24:27], v[80:83]\n"
    "ds_read_b64_tr_b16 v[224:225], v4 offset:32768\n"
    "ds_read_b64_tr_b16 v[226:227], v4 offset:33280\n"
    "v_mfma_f32_16x16x32_bf16 v[84:87], v[236:239], a[88:91], v[84:87]\n"
    "s_waitcnt lgkmcnt(8)\n"
    "v_rcp_f32 v26, v26\n"
    "v_mfma_f32_16x16x32_bf16 v[88:91], v[236:239], a[152:155], v[88:91]\n"
    "v_rcp_f32 v27, v27\n"
    "v_mfma_f32_16x16x32_bf16 v[92:95], v[236:239], a[216:219], v[92:95]\n"
    "v_rcp_f32 v28, v28\n"
    "v_mfma_f32_16x16x32_bf16 v[32:35], v[240:243], a[28:31], v[32:35]\n"
    "ds_read_b64_tr_b16 v[228:229], v5 offset:32768\n"
    "ds_read_b64_tr_b16 v[230:231], v5 offset:33280\n"
    "v_mfma_f32_16x16x32_bf16 v[36:39], v[240:243], a[92:95], v[36:39]\n"
    "s_waitcnt lgkmcnt(8)\n"
    "v_mul_f32 v180, v180, v25\n"
    "v_mul_f32 v181, v181, v26\n"
    "v_mfma_f32_16x16x32_bf16 v[40:43], v[240:243], a[156:159], v[40:43]\n"
    "s_add_u32 s58, s63, 0x3800\n"
    "buffer_store_dwordx4 v[152:155], v1, s[44:47], s58 offen sc1\n"
    "v_mul_f32 v182, v182, v27\n"
    "v_mfma_f32_16x16x32_bf16 v[44:47], v[240:243], a[220:223], v[44:47]\n"
    "v_mul_f32 v183, v183, v28\n"
    "v_cvt_pk_bf16_f32 v22, v180, v181\n"
    "v_mfma_f32_16x16x32_bf16 v[48:51], v[244:247], a[28:31], v[48:51]\n"
    "ds_read_b64_tr_b16 v[232:233], v6 offset:32768\n"
    "ds_read_b64_tr_b16 v[234:235], v6 offset:33280\n"
    "v_mfma_f32_16x16x32_bf16 v[52:55], v[244:247], a[92:95], v[52:55]\n"
    "s_waitcnt lgkmcnt(8)\n"
    "v_cvt_pk_bf16_f32 v23, v182, v183\n"
    "v_mfma_f32_16x16x32_bf16 v[56:59], v[244:247], a[156:159], v[56:59]\n"
    "s_add_u32 s58, s67, 0xe000\n"
    "buffer_store_dwordx4 v[20:23], v1, s[44:47], s58 offen sc1\n"
    "v_mul_f32 v25, 0xbfb8aa3b, v168\n"
    "v_mfma_f32_16x16x32_bf16 v[60:63], v[244:247], a[220:223], v[60:63]\n"
    "v_mul_f32 v26, 0xbfb8aa3b, v169\n"
    "v_mul_f32 v27, 0xbfb8aa3b, v170\n"
    "v_mfma_f32_16x16x32_bf16 v[64:67], v[248:251], a[28:31], v[64:67]\n"
    "ds_read_b64_tr_b16 v[236:237], v8 offset:32768\n"
    "ds_read_b64_tr_b16 v[238:239], v8 offset:33280\n"
    "v_mfma_f32_16x16x32_bf16 v[68:71], v[248:251], a[92:95], v[68:71]\n"
    "s_waitcnt lgkmcnt(8)\n"
    "v_mul_f32 v28, 0xbfb8aa3b, v171\n"
    "v_mfma_f32_16x16x32_bf16 v[72:75], v[248:251], a[156:159], v[72:75]\n"
    "s_add_u32 m0, s60, 107520\n"
    "s_nop 0\n"
    "buffer_load_dwordx4 v3, s[36:39], s56 offen lds\n"
    "v_mfma_f32_16x16x32_bf16 v[76:79], v[248:251], a[220:223], v[76:79]\n"
    "v_exp_f32 v25, v25\n"
    "v_mfma_f32_16x16x32_bf16 v[80:83], v[252:255], a[28:31], v[80:83]\n"
    "ds_read_b64_tr_b16 v[240:241], v4 offset:36864\n"
    "ds_read_b64_tr_b16 v[242:243], v4 offset:37376\n"
    "v_mfma_f32_16x16x32_bf16 v[84:87], v[252:255], a[92:95], v[84:87]\n"
    "s_waitcnt lgkmcnt(8)\n"
    "v_exp_f32 v26, v26\n"
    "v_mfma_f32_16x16x32_bf16 v[88:91], v[252:255], a[156:159], v[88:91]\n"
    "v_exp_f32 v27, v27\n"
    "v_mfma_f32_16x16x32_bf16 v[92:95], v[252:255], a[220:223], v[92:95]\n"
    "v_exp_f32 v28, v28\n"
    "v_mfma_f32_16x16x32_bf16 v[32:35], v[224:227], a[32:35], v[32:35]\n"
    "ds_read_b64_tr_b16 v[244:245], v5 offset:36864\n"
    "ds_read_b64_tr_b16 v[246:247], v5 offset:37376\n"
    "v_mfma_f32_16x16x32_bf16 v[36:39], v[224:227], a[96:99], v[36:39]\n"
    "s_waitcnt lgkmcnt(8)\n"
    "s_add_u32 s56, s61, 0x300080\n"
    "v_mfma_f32_16x16x32_bf16 v[40:43], v[224:227], a[160:163], v[40:43]\n"
    "s_add_u32 m0, s60, 114688\n"
    "s_nop 0\n"
    "buffer_load_dwordx4 v2, s[36:39], s56 offen lds\n"
    "v_mul_f32 v168, v168, v200\n"
    "v_mfma_f32_16x16x32_bf16 v[44:47], v[224:227], a[224:227], v[44:47]\n"
    "v_mul_f32 v169, v169, v201\n"
    "v_mul_f32 v170, v170, v202\n"
    "v_mfma_f32_16x16x32_bf16 v[48:51], v[228:231], a[32:35], v[48:51]\n"
    "ds_read_b64_tr_b16 v[248:249], v6 offset:36864\n"
    "ds_read_b64_tr_b16 v[250:251], v6 offset:37376\n"
    "v_mfma_f32_16x16x32_bf16 v[52:55], v[228:231], a[96:99], v[52:55]\n"
    "s_waitcnt lgkmcnt(8)\n"
    "v_mul_f32 v171, v171, v203\n"
    "v_add_f32 v25, 1.0, v25\n"
    "v_mfma_f32_16x16x32_bf16 v[56:59], v[228:231], a[160:163], v[56:59]\n"
    "s_add_u32 s58, s63, 0x3c00\n"
    "buffer_store_dwordx4 v[156:159], v1, s[44:47], s58 offen sc1\n"
    "v_add_f32 v26, 1.0, v26\n"
    "v_mfma_f32_16x16x32_bf16 v[60:63], v[228:231], a[224:227], v[60:63]\n"
    "v_add_f32 v27, 1.0, v27\n"
    "v_add_f32 v28, 1.0, v28\n"
    "v_mfma_f32_16x16x32_bf16 v[64:67], v[232:235], a[32:35], v[64:67]\n"
    "ds_read_b64_tr_b16 v[252:253], v8 offset:36864\n"
    "ds_read_b64_tr_b16 v[254:255], v8 offset:37376\n"
    "v_mfma_f32_16x16x32_bf16 v[68:71], v[232:235], a[96:99], v[68:71]\n"
    "s_waitcnt lgkmcnt(8)\n"
    "v_rcp_f32 v25, v25\n"
    "v_mfma_f32_16x16x32_bf16 v[72:75], v[232:235], a[160:163], v[72:75]\n"
    "v_rcp_f32 v26, v26\n"
    "v_mfma_f32_16x16x32_bf16 v[76:79], v[232:235], a[224:227], v[76:79]\n"
    "v_rcp_f32 v27, v27\n"
    "v_mfma_f32_16x16x32_bf16 v[80:83], v[236:239], a[32:35], v[80:83]\n"
    "ds_read_b64_tr_b16 v[224:225], v4 offset:40960\n"
    "ds_read_b64_tr_b16 v[226:227], v4 offset:41472\n"
    "v_mfma_f32_16x16x32_bf16 v[84:87], v[236:239], a[96:99], v[84:87]\n"
    "s_waitcnt lgkmcnt(8)\n"
    "v_rcp_f32 v28, v28\n"
    "v_mfma_f32_16x16x32_bf16 v[88:91], v[236:239], a[160:163], v[88:91]\n"
    "s_add_u32 m0, s60, 115712\n"
    "s_nop 0\n"
    "buffer_load_dwordx4 v3, s[36:39], s56 offen lds\n"
    "v_mul_f32 v168, v168, v25\n"
    "v_mfma_f32_16x16x32_bf16 v[92:95], v[236:239], a[224:227], v[92:95]\n"
    "v_mul_f32 v169, v169, v26\n"
    "v_mul_f32 v170, v170, v27\n"
    "v_mfma_f32_16x16x32_bf16 v[32:35], v[240:243], a[36:39], v[32:35]\n"
    "ds_read_b64_tr_b16 v[228:229], v5 offset:40960\n"
    "ds_read_b64_tr_b16 v[230:231], v5 offset:41472\n"
    "v_mfma_f32_16x16x32_bf16 v[36:39], v[240:243], a[100:103], v[36:39]\n"
    "s_waitcnt lgkmcnt(8)\n"
    "v_mul_f32 v171, v171, v28\n"
    "v_cvt_pk_bf16_f32 v16, v168, v169\n"
    "v_mfma_f32_16x16x32_bf16 v[40:43], v[240:243], a[164:167], v[40:43]\n"
    "v_cvt_pk_bf16_f32 v17, v170, v171\n"
    "v_mul_f32 v25, 0xbfb8aa3b, v184\n"
    "v_mfma_f32_16x16x32_bf16 v[44:47], v[240:243], a[228:231], v[44:47]\n"
    "v_mul_f32 v26, 0xbfb8aa3b, v185\n"
    "v_mfma_f32_16x16x32_bf16 v[48:51], v[244:247], a[36:39], v[48:51]\n"
    "ds_read_b64_tr_b16 v[232:233], v6 offset:40960\n"
    "ds_read_b64_tr_b16 v[234:235], v6 offset:41472\n"
    "v_mfma_f32_16x16x32_bf16 v[52:55], v[244:247], a[100:103], v[52:55]\n"
    "s_waitcnt lgkmcnt(8)\n"
    "v_mul_f32 v27, 0xbfb8aa3b, v186\n"
    "v_mul_f32 v28, 0xbfb8aa3b, v187\n"
    "v_mfma_f32_16x16x32_bf16 v[56:59], v[244:247], a[164:167], v[56:59]\n"
    "v_exp_f32 v25, v25\n"
    "v_mfma_f32_16x16x32_bf16 v[60:63], v[244:247], a[228:231], v[60:63]\n"
    "v_exp_f32 v26, v26\n"
    "v_mfma_f32_16x16x32_bf16 v[64:67], v[248:251], a[36:39], v[64:67]\n"
    "ds_read_b64_tr_b16 v[236:237], v8 offset:40960\n"
    "ds_read_b64_tr_b16 v[238:239], v8 offset:41472\n"
    "v_mfma_f32_16x16x32_bf16 v[68:71], v[248:251], a[100:103], v[68:71]\n"
    "s_waitcnt lgkmcnt(8)\n"
    "s_add_u32 s56, s61, 0x380080\n"
    "v_mfma_f32_16x16x32_bf16 v[72:75], v[248:251], a[164:167], v[72:75]\n"
    "s_add_u32 m0, s60, 122880\n"
    "s_nop 0\n"
    "buffer_load_dwordx4 v2, s[36:39], s56 offen lds\n"
    "v_mfma_f32_16x16x32_bf16 v[76:79], v[248:251], a[228:231], v[76:79]\n"
    "v_exp_f32 v27, v27\n"
    "v_mfma_f32_16x16x32_bf16 v[80:83], v[252:255], a[36:39], v[80:83]\n"
    "ds_read_b64_tr_b16 v[240:241], v4 offset:45056\n"
    "ds_read_b64_tr_b16 v[242:243], v4 offset:45568\n"
    "v_mfma_f32_16x16x32_bf16 v[84:87], v[252:255], a[100:103], v[84:87]\n"
    "s_waitcnt lgkmcnt(8)\n"
    "v_exp_f32 v28, v28\n"
    "v_mfma_f32_16x16x32_bf16 v[88:91], v[252:255], a[164:167], v[88:91]\n"
    "v_mul_f32 v184, v184, v216\n"
    "v_mul_f32 v185, v185, v217\n"
    "v_mfma_f32_16x16x32_bf16 v[92:95], v[252:255], a[228:231], v[92:95]\n"
    "v_mul_f32 v186, v186, v218\n"
    "v_mul_f32 v187, v187, v219\n"
    "v_mfma_f32_16x16x32_bf16 v[32:35], v[224:227], a[40:43], v[32:35]\n"
    "ds_read_b64_tr_b16 v[244:245], v5 offset:45056\n"
    "ds_read_b64_tr_b16 v[246:247], v5 offset:45568\n"
    "v_mfma_f32_16x16x32_bf16 v[36:39], v[224:227], a[104:107], v[36:39]\n"
    "s_waitcnt lgkmcnt(8)\n"
    "v_add_f32 v25, 1.0, v25\n"
    "v_add_f32 v26, 1.0, v26\n"
    "v_mfma_f32_16x16x32_bf16 v[40:43], v[224:227], a[168:171], v[40:43]\n"
    "v_add_f32 v27, 1.0, v27\n"
    "v_add_f32 v28, 1.0, v28\n"
    "v_mfma_f32_16x16x32_bf16 v[44:47], v[224:227], a[232:235], v[44:47]\n"
    "v_rcp_f32 v25, v25\n"
    "v_mfma_f32_16x16x32_bf16 v[48:51], v[228:231], a[40:43], v[48:51]\n"
    "ds_read_b64_tr_b16 v[248:249], v6 offset:45056\n"
    "ds_read_b64_tr_b16 v[250:251], v6 offset:45568\n"
    "v_mfma_f32_16x16x32_bf16 v[52:55], v[228:231], a[104:107], v[52:55]\n"
    "s_waitcnt lgkmcnt(8)\n"
    "v_rcp_f32 v26, v26\n"
    "v_mfma_f32_16x16x32_bf16 v[56:59], v[228:231], a[168:171], v[56:59]\n"
    "s_add_u32 m0, s60, 123904\n"
    "s_nop 0\n"
    "buffer_load_dwordx4 v3, s[36:39], s56 offen lds\n"
    "v_mfma_f32_16x16x32_bf16 v[60:63], v[228:231], a[232:235], v[60:63]\n"
    "v_rcp_f32 v27, v27\n"
    "v_mfma_f32_16x16x32_bf16 v[64:67], v[232:235], a[40:43], v[64:67]\n"
    "ds_read_b64_tr_b16 v[252:253], v8 offset:45056\n"
    "ds_read_b64_tr_b16 v[254:255], v8 offset:45568\n"
    "v_mfma_f32_16x16x32_bf16 v[68:71], v[232:235], a[104:107], v[68:71]\n"
    "s_waitcnt lgkmcnt(8)\n"
    "v_rcp_f32 v28, v28\n"
    "v_mfma_f32_16x16x32_bf16 v[72:75], v[232:235], a[168:171], v[72:75]\n"
    "v_mul_f32 v184, v184, v25\n"
    "v_mul_f32 v185, v185, v26\n"
    "v_mfma_f32_16x16x32_bf16 v[76:79], v[232:235], a[232:235], v[76:79]\n"
    "v_mul_f32 v186, v186, v27\n"
    "v_mul_f32 v187, v187, v28\n"
    "v_mfma_f32_16x16x32_bf16 v[80:83], v[236:239], a[40:43], v[80:83]\n"
    "ds_read_b64_tr_b16 v[224:225], v4 offset:49152\n"
    "ds_read_b64_tr_b16 v[226:227], v4 offset:49664\n"
    "v_mfma_f32_16x16x32_bf16 v[84:87], v[236:239], a[104:107], v[84:87]\n"
    "s_waitcnt lgkmcnt(8)\n"
    "v_cvt_pk_bf16_f32 v18, v184, v185\n"
    "v_cvt_pk_bf16_f32 v19, v186, v187\n"
    "v_mfma_f32_16x16x32_bf16 v[88:91], v[236:239], a[168:171], v[88:91]\n"
    "s_add_u32 s58, s68, 0x0\n"
    "buffer_store_dwordx4 v[16:19], v1, s[44:47], s58 offen sc1\n"
    "v_mul_f32 v25, 0xbfb8aa3b, v172\n"
    "v_mfma_f32_16x16x32_bf16 v[92:95], v[236:239], a[232:235], v[92:95]\n"
    "v_mul_f32 v26, 0xbfb8aa3b, v173\n"
    "v_mul_f32 v27, 0xbfb8aa3b, v174\n"
    "v_mfma_f32_16x16x32_bf16 v[32:35], v[240:243], a[44:47], v[32:35]\n"
    "ds_read_b64_tr_b16 v[228:229], v5 offset:49152\n"
    "ds_read_b64_tr_b16 v[230:231], v5 offset:49664\n"
    "v_mfma_f32_16x16x32_bf16 v[36:39], v[240:243], a[108:111], v[36:39]\n"
    "s_waitcnt lgkmcnt(8)\n"
    "v_mul_f32 v28, 0xbfb8aa3b, v175\n"
    "v_mfma_f32_16x16x32_bf16 v[40:43], v[240:243], a[172:175], v[40:43]\n"
    "v_exp_f32 v25, v25\n"
    "v_mfma_f32_16x16x32_bf16 v[44:47], v[240:243], a[236:239], v[44:47]\n"
    "v_exp_f32 v26, v26\n"
    "v_mfma_f32_16x16x32_bf16 v[48:51], v[244:247], a[44:47], v[48:51]\n"
    "ds_read_b64_tr_b16 v[232:233], v6 offset:49152\n"
    "ds_read_b64_tr_b16 v[234:235], v6 offset:49664\n"
    "v_mfma_f32_16x16x32_bf16 v[52:55], v[244:247], a[108:111], v[52:55]\n"
    "s_waitcnt lgkmcnt(8)\n"
    "v_exp_f32 v27, v27\n"
    "v_mfma_f32_16x16x32_bf16 v[56:59], v[244:247], a[172:175], v[56:59]\n"
    "v_exp_f32 v28, v28\n"
    "v_mfma_f32_16x16x32_bf16 v[60:63], v[244:247], a[236:239], v[60:63]\n"
    "v_mul_f32 v172, v172, v204\n"
    "v_mul_f32 v173, v173, v205\n"
    "v_mfma_f32_16x16x32_bf16 v[64:67], v[248:251], a[44:47], v[64:67]\n"
    "ds_read_b64_tr_b16 v[236:237], v8 offset:49152\n"
    "ds_read_b64_tr_b16 v[238:239], v8 offset:49664\n"
    "v_mfma_f32_16x16x32_bf16 v[68:71], v[248:251], a[108:111], v[68:71]\n"
    "s_waitcnt lgkmcnt(8)\n"
    "v_mul_f32 v174, v174, v206\n"
    "v_mul_f32 v175, v175, v207\n"
    "v_mfma_f32_16x16x32_bf16 v[72:75], v[248:251], a[172:175], v[72:75]\n"
    "v_add_f32 v25, 1.0, v25\n"
    "v_add_f32 v26, 1.0, v26\n"
    "v_mfma_f32_16x16x32_bf16 v[76:79], v[248:251], a[236:239], v[76:79]\n"
    "v_add_f32 v27, 1.0, v27\n"
    "v_mfma_f32_16x16x32_bf16 v[80:83], v[252:255], a[44:47], v[80:83]\n"
    "ds_read_b64_tr_b16 v[240:241], v4 offset:53248\n"
    "ds_read_b64_tr_b16 v[242:243], v4 offset:53760\n"
    "v_mfma_f32_16x16x32_bf16 v[84:87], v[252:255], a[108:111], v[84:87]\n"
    "s_waitcnt lgkmcnt(8)\n"
    "v_add_f32 v28, 1.0, v28\n"
    "v_mfma_f32_16x16x32_bf16 v[88:91], v[252:255], a[172:175], v[88:91]\n"
    "v_rcp_f32 v25, v25\n"
    "v_mfma_f32_16x16x32_bf16 v[92:95], v[252:255], a[236:239], v[92:95]\n"
    "v_rcp_f32 v26, v26\n"
    "v_mfma_f32_16x16x32_bf16 v[32:35], v[224:227], a[48:51], v[32:35]\n"
    "ds_read_b64_tr_b16 v[244:245], v5 offset:53248\n"
    "ds_read_b64_tr_b16 v[246:247], v5 offset:53760\n"
    "v_mfma_f32_16x16x32_bf16 v[36:39], v[224:227], a[112:115], v[36:39]\n"
    "s_waitcnt lgkmcnt(8)\n"
    "v_rcp_f32 v27, v27\n"
    "v_mfma_f32_16x16x32_bf16 v[40:43], v[224:227], a[176:179], v[40:43]\n"
    "v_rcp_f32 v28, v28\n"
    "v_mfma_f32_16x16x32_bf16 v[44:47], v[224:227], a[240:243], v[44:47]\n"
    "v_mul_f32 v172, v172, v25\n"
    "v_mul_f32 v173, v173, v26\n"
    "v_mfma_f32_16x16x32_bf16 v[48:51], v[228:231], a[48:51], v[48:51]\n"
    "ds_read_b64_tr_b16 v[248:249], v6 offset:53248\n"
    "ds_read_b64_tr_b16 v[250:251], v6 offset:53760\n"
    "v_mfma_f32_16x16x32_bf16 v[52:55], v[228:231], a[112:115], v[52:55]\n"
    "s_waitcnt lgkmcnt(8)\n"
    "v_mul_f32 v174, v174, v27\n"
    "v_mul_f32 v175, v175, v28\n"
    "v_mfma_f32_16x16x32_bf16 v[56:59], v[228:231], a[176:179], v[56:59]\n"
    "v_cvt_pk_bf16_f32 v20, v172, v173\n"
    "v_cvt_pk_bf16_f32 v21, v174, v175\n"
    "v_mfma_f32_16x16x32_bf16 v[60:63], v[228:231], a[240:243], v[60:63]\n"
    "v_mul_f32 v25, 0xbfb8aa3b, v188\n"
    "v_mfma_f32_16x16x32_bf16 v[64:67], v[232:235], a[48:51], v[64:67]\n"
    "ds_read_b64_tr_b16 v[252:253], v8 offset:53248\n"
    "ds_read_b64_tr_b16 v[254:255], v8 offset:53760\n"
    "v_mfma_f32_16x16x32_bf16 v[68:71], v[232:235], a[112:115], v[68:71]\n"
    "s_waitcnt lgkmcnt(8)\n"
    "v_mul_f32 v26, 0xbfb8aa3b, v189\n"
    "v_mul_f32 v27, 0xbfb8aa3b, v190\n"
    "v_mfma_f32_16x16x32_bf16 v[72:75], v[232:235], a[176:179], v[72:75]\n"
    "v_mul_f32 v28, 0xbfb8aa3b, v191\n"
    "v_mfma_f32_16x16x32_bf16 v[76:79], v[232:235], a[240:243], v[76:79]\n"
    "v_exp_f32 v25, v25\n"
    "v_mfma_f32_16x16x32_bf16 v[80:83], v[236:239], a[48:51], v[80:83]\n"
    "ds_read_b64_tr_b16 v[224:225], v4 offset:57344\n"
    "ds_read_b64_tr_b16 v[226:227], v4 offset:57856\n"
    "v_mfma_f32_16x16x32_bf16 v[84:87], v[236:239], a[112:115], v[84:87]\n"
    "s_waitcnt lgkmcnt(8)\n"
    "v_exp_f32 v26, v26\n"
    "v_mfma_f32_16x16x32_bf16 v[88:91], v[236:239], a[176:179], v[88:91]\n"
    "v_exp_f32 v27, v27\n"
    "v_mfma_f32_16x16x32_bf16 v[92:95], v[236:239], a[240:243], v[92:95]\n"
    "v_exp_f32 v28, v28\n"
    "v_mfma_f32_16x16x32_bf16 v[32:35], v[240:243], a[52:55], v[32:35]\n"
    "ds_read_b64_tr_b16 v[228:229], v5 offset:57344\n"
    "ds_read_b64_tr_b16 v[230:231], v5 offset:57856\n"
    "v_mfma_f32_16x16x32_bf16 v[36:39], v[240:243], a[116:119], v[36:39]\n"
    "s_waitcnt lgkmcnt(8)\n"
    "v_mul_f32 v188, v188, v220\n"
    "v_mul_f32 v189, v189, v221\n"
    "v_mfma_f32_16x16x32_bf16 v[40:43], v[240:243], a[180:183], v[40:43]\n"
    "v_mul_f32 v190, v190, v222\n"
    "v_mul_f32 v191, v191, v223\n"
    "v_mfma_f32_16x16x32_bf16 v[44:47], v[240:243], a[244:247], v[44:47]\n"
    "v_add_f32 v25, 1.0, v25\n"
    "v_mfma_f32_16x16x32_bf16 v[48:51], v[244:247], a[52:55], v[48:51]\n"
    "ds_read_b64_tr_b16 v[232:233], v6 offset:57344\n"
    "ds_read_b64_tr_b16 v[234:235], v6 offset:57856\n"
    "v_mfma_f32_16x16x32_bf16 v[52:55], v[244:247], a[116:119], v[52:55]\n"
    "s_waitcnt lgkmcnt(8)\n"
    "v_add_f32 v26, 1.0, v26\n"
    "v_add_f32 v27, 1.0, v27\n"
    "v_mfma_f32_16x16x32_bf16 v[56:59], v[244:247], a[180:183], v[56:59]\n"
    "v_add_f32 v28, 1.0, v28\n"
    "v_mfma_f32_16x16x32_bf16 v[60:63], v[244:247], a[244:247], v[60:63]\n"
    "v_rcp_f32 v25, v25\n"
    "v_mfma_f32_16x16x32_bf16 v[64:67], v[248:251], a[52:55], v[64:67]\n"
    "ds_read_b64_tr_b16 v[236:237], v8 offset:57344\n"
    "ds_read_b64_tr_b16 v[238:239], v8 offset:57856\n"
    "v_mfma_f32_16x16x32_bf16 v[68:71], v[248:251], a[116:119], v[68:71]\n"
    "s_waitcnt lgkmcnt(8)\n"
    "v_rcp_f32 v26, v26\n"
    "v_mfma_f32_16x16x32_bf16 v[72:75], v[248:251], a[180:183], v[72:75]\n"
    "v_rcp_f32 v27, v27\n"
    "v_mfma_f32_16x16x32_bf16 v[76:79], v[248:251], a[244:247], v[76:79]\n"
    "v_rcp_f32 v28, v28\n"
    "v_mfma_f32_16x16x32_bf16 v[80:83], v[252:255], a[52:55], v[80:83]\n"
    "ds_read_b64_tr_b16 v[240:241], v4 offset:61440\n"
    "ds_read_b64_tr_b16 v[242:243], v4 offset:61952\n"
    "v_mfma_f32_16x16x32_bf16 v[84:87], v[252:255], a[116:119], v[84:87]\n"
    "s_waitcnt lgkmcnt(8)\n"
    "v_mul_f32 v188, v188, v25\n"
    "v_mul_f32 v189, v189, v26\n"
    "v_mfma_f32_16x16x32_bf16 v[88:91], v[252:255], a[180:183], v[88:91]\n"
    "v_mul_f32 v190, v190, v27\n"
    "v_mul_f32 v191, v191, v28\n"
    "v_mfma_f32_16x16x32_bf16 v[92:95], v[252:255], a[244:247], v[92:95]\n"
    "v_cvt_pk_bf16_f32 v22, v188, v189\n"
    "v_mfma_f32_16x16x32_bf16 v[32:35], v[224:227], a[56:59], v[32:35]\n"
    "ds_read_b64_tr_b16 v[244:245], v5 offset:61440\n"
    "ds_read_b64_tr_b16 v[246:247], v5 offset:61952\n"
    "v_mfma_f32_16x16x32_bf16 v[36:39], v[224:227], a[120:123], v[36:39]\n"
    "s_waitcnt lgkmcnt(8)\n"
    "v_cvt_pk_bf16_f32 v23, v190, v191\n"
    "v_mfma_f32_16x16x32_bf16 v[40:43], v[224:227], a[184:187], v[40:43]\n"
    "s_add_u32 s58, s68, 0xe000\n"
    "buffer_store_dwordx4 v[20:23], v1, s[44:47], s58 offen sc1\n"
    "v_mfma_f32_16x16x32_bf16 v[44:47], v[224:227], a[248:251], v[44:47]\n"
    "s_waitcnt vmcnt(5)\n"
    "s_lshl_b32 s96, s54, 2\n"
    "s_add_u32 s96, s96, s52\n"
    "s_lshl_b32 s96, s96, 2\n"
    "s_lshl_b32 s95, s53, 7\n"
    "s_add_u32 s96, s96, s95\n"
    "s_add_u32 s96, s96, 0x2000\n"
    "v_mov_b32 v24, s97\n"
    "buffer_store_dword v24, v7, s[44:47], s96 offen sc1\n"
    "v_mfma_f32_16x16x32_bf16 v[48:51], v[228:231], a[56:59], v[48:51]\n"
    "ds_read_b64_tr_b16 v[248:249], v6 offset:61440\n"
    "ds_read_b64_tr_b16 v[250:251], v6 offset:61952\n"
    "v_mfma_f32_16x16x32_bf16 v[52:55], v[228:231], a[120:123], v[52:55]\n"
    "s_waitcnt lgkmcnt(8)\n"
    "v_mfma_f32_16x16x32_bf16 v[56:59], v[228:231], a[184:187], v[56:59]\n"
    "v_mfma_f32_16x16x32_bf16 v[60:63], v[228:231], a[248:251], v[60:63]\n"
    "v_mfma_f32_16x16x32_bf16 v[64:67], v[232:235], a[56:59], v[64:67]\n"
    "ds_read_b64_tr_b16 v[252:253], v8 offset:61440\n"
    "ds_read_b64_tr_b16 v[254:255], v8 offset:61952\n"
    "v_mfma_f32_16x16x32_bf16 v[68:71], v[232:235], a[120:123], v[68:71]\n"
    "s_waitcnt lgkmcnt(8)\n"
    "v_mfma_f32_16x16x32_bf16 v[72:75], v[232:235], a[184:187], v[72:75]\n"
    "v_mfma_f32_16x16x32_bf16 v[76:79], v[232:235], a[248:251], v[76:79]\n"
    "v_xor_b32 v4, 0x10000, v4\n"
    "v_xor_b32 v5, 0x10000, v5\n"
    "v_xor_b32 v6, 0x10000, v6\n"
    "v_xor_b32 v8, 0x10000, v8\n"
    "s_waitcnt vmcnt(3)\n"
    "s_barrier\n"
    "v_mfma_f32_16x16x32_bf16 v[80:83], v[236:239], a[56:59], v[80:83]\n"
    "ds_read_b64_tr_b16 v[224:225], v4 offset:0\n"
    "ds_read_b64_tr_b16 v[226:227], v4 offset:512\n"
    "v_mfma_f32_16x16x32_bf16 v[84:87], v[236:239], a[120:123], v[84:87]\n"
    "s_waitcnt lgkmcnt(8)\n"
    "v_mfma_f32_16x16x32_bf16 v[88:91], v[236:239], a[184:187], v[88:91]\n"
    "v_mfma_f32_16x16x32_bf16 v[92:95], v[236:239], a[248:251], v[92:95]\n"
    "v_mfma_f32_16x16x32_bf16 v[32:35], v[240:243], a[60:63], v[32:35]\n"
    "ds_read_b64_tr_b16 v[228:229], v5 offset:0\n"
    "ds_read_b64_tr_b16 v[230:231], v5 offset:512\n"
    "v_mfma_f32_16x16x32_bf16 v[36:39], v[240:243], a[124:127], v[36:39]\n"
    "s_waitcnt lgkmcnt(8)\n"
    "v_mfma_f32_16x16x32_bf16 v[40:43], v[240:243], a[188:191], v[40:43]\n"
    "v_mfma_f32_16x16x32_bf16 v[44:47], v[240:243], a[252:255], v[44:47]\n"
    "v_mfma_f32_16x16x32_bf16 v[48:51], v[244:247], a[60:63], v[48:51]\n"
    "ds_read_b64_tr_b16 v[232:233], v6 offset:0\n"
    "ds_read_b64_tr_b16 v[234:235], v6 offset:512\n"
    "v_mfma_f32_16x16x32_bf16 v[52:55], v[244:247], a[124:127], v[52:55]\n"
    "s_waitcnt lgkmcnt(8)\n"
    "v_mfma_f32_16x16x32_bf16 v[56:59], v[244:247], a[188:191], v[56:59]\n"
    "v_mfma_f32_16x16x32_bf16 v[60:63], v[244:247], a[252:255], v[60:63]\n"
    "v_mfma_f32_16x16x32_bf16 v[64:67], v[248:251], a[60:63], v[64:67]\n"
    "ds_read_b64_tr_b16 v[236:237], v8 offset:0\n"
    "ds_read_b64_tr_b16 v[238:239], v8 offset:512\n"
    "v_mfma_f32_16x16x32_bf16 v[68:71], v[248:251], a[124:127], v[68:71]\n"
    "s_waitcnt lgkmcnt(8)\n"
    "v_mfma_f32_16x16x32_bf16 v[72:75], v[248:251], a[188:191], v[72:75]\n"
    "v_mfma_f32_16x16x32_bf16 v[76:79], v[248:251], a[252:255], v[76:79]\n"
    "v_mfma_f32_16x16x32_bf16 v[80:83], v[252:255], a[60:63], v[80:83]\n"
    "ds_read_b64_tr_b16 v[240:241], v4 offset:4096\n"
    "ds_read_b64_tr_b16 v[242:243], v4 offset:4608\n"
    "v_mfma_f32_16x16x32_bf16 v[84:87], v[252:255], a[124:127], v[84:87]\n"
    "s_waitcnt lgkmcnt(8)\n"
    "v_mfma_f32_16x16x32_bf16 v[88:91], v[252:255], a[188:191], v[88:91]\n"
    "v_mfma_f32_16x16x32_bf16 v[92:95], v[252:255], a[252:255], v[92:95]\n"
    "v_mfma_f32_16x16x32_bf16 v[160:163], v[224:227], a[0:3], 0\n"
    "ds_read_b64_tr_b16 v[244:245], v5 offset:4096\n"
    "ds_read_b64_tr_b16 v[246:247], v5 offset:4608\n"
    "v_mfma_f32_16x16x32_bf16 v[164:167], v[224:227], a[64:67], 0\n"
    "s_waitcnt lgkmcnt(8)\n"
    "v_mfma_f32_16x16x32_bf16 v[168:171], v[224:227], a[128:131], 0\n"
    "v_lshrrev_b32 v29, 2, v1\n"
    "v_and_b32 v29, 0x7c, v29\n"
    "s_lshl_b32 s96, s53, 7\n"
    "s_add_u32 s96, s96, 0x2000\n"
    "buffer_load_dword v24, v29, s[44:47], s96 offen sc1\n"
    "v_mfma_f32_16x16x32_bf16 v[172:175], v[224:227], a[192:195], 0\n"
    "v_mul_f32 v32, s80, v32\n"
    "v_mul_f32 v33, s80, v33\n"
    "v_mfma_f32_16x16x32_bf16 v[176:179], v[228:231], a[0:3], 0\n"
    "ds_read_b64_tr_b16 v[248:249], v6 offset:4096\n"
    "ds_read_b64_tr_b16 v[250:251], v6 offset:4608\n"
    "v_mfma_f32_16x16x32_bf16 v[180:183], v[228:231], a[64:67], 0\n"
    "s_waitcnt lgkmcnt(8)\n"
    "v_mul_f32 v34, s80, v34\n"
    "v_mul_f32 v35, s80, v35\n"
    "v_mfma_f32_16x16x32_bf16 v[184:187], v[228:231], a[128:131], 0\n"
    "v_mul_f32 v48, s80, v48\n"
    "v_mul_f32 v49, s80, v49\n"
    "v_mfma_f32_16x16x32_bf16 v[188:191], v[228:231], a[192:195], 0\n"
    "v_mul_f32 v50, s80, v50\n"
    "v_mfma_f32_16x16x32_bf16 v[192:195], v[232:235], a[0:3], 0\n"
    "ds_read_b64_tr_b16 v[252:253], v8 offset:4096\n"
    "ds_read_b64_tr_b16 v[254:255], v8 offset:4608\n"
    "v_mfma_f32_16x16x32_bf16 v[196:199], v[232:235], a[64:67], 0\n"
    "s_waitcnt lgkmcnt(8)\n"
    "v_mul_f32 v51, s80, v51\n"
    "v_cvt_pk_bf16_f32 v96, v32, v33\n"
    "v_mfma_f32_16x16x32_bf16 v[200:203], v[232:235], a[128:131], 0\n"
    "v_cvt_pk_bf16_f32 v97, v34, v35\n"
    "v_cvt_pk_bf16_f32 v98, v48, v49\n"
    "v_mfma_f32_16x16x32_bf16 v[204:207], v[232:235], a[192:195], 0\n"
    "v_cvt_pk_bf16_f32 v99, v50, v51\n"
    "v_mfma_f32_16x16x32_bf16 v[208:211], v[236:239], a[0:3], 0\n"
    "ds_read_b64_tr_b16 v[224:225], v4 offset:8192\n"
    "ds_read_b64_tr_b16 v[226:227], v4 offset:8704\n"
    "v_mfma_f32_16x16x32_bf16 v[212:215], v[236:239], a[64:67], 0\n"
    "s_waitcnt lgkmcnt(8)\n"
    "v_mul_f32 v64, s80, v64\n"
    "v_mul_f32 v65, s80, v65\n"
    "v_mfma_f32_16x16x32_bf16 v[216:219], v[236:239], a[128:131], 0\n"
    "v_mul_f32 v66, s80, v66\n"
    "v_mul_f32 v67, s80, v67\n"
    "v_mfma_f32_16x16x32_bf16 v[220:223], v[236:239], a[192:195], 0\n"
    "v_mul_f32 v80, s80, v80\n"
    "v_mfma_f32_16x16x32_bf16 v[160:163], v[240:243], a[4:7], v[160:163]\n"
    "ds_read_b64_tr_b16 v[228:229], v5 offset:8192\n"
    "ds_read_b64_tr_b16 v[230:231], v5 offset:8704\n"
    "v_mfma_f32_16x16x32_bf16 v[164:167], v[240:243], a[68:71], v[164:167]\n"
    "s_waitcnt lgkmcnt(8)\n"
    "v_mul_f32 v81, s80, v81\n"
    "v_mul_f32 v82, s80, v82\n"
    "v_mfma_f32_16x16x32_bf16 v[168:171], v[240:243], a[132:135], v[168:171]\n"
    "v_mul_f32 v83, s80, v83\n"
    "v_cvt_pk_bf16_f32 v100, v64, v65\n"
    "v_mfma_f32_16x16x32_bf16 v[172:175], v[240:243], a[196:199], v[172:175]\n"
    "v_cvt_pk_bf16_f32 v101, v66, v67\n"
    "v_mfma_f32_16x16x32_bf16 v[176:179], v[244:247], a[4:7], v[176:179]\n"
    "ds_read_b64_tr_b16 v[232:233], v6 offset:8192\n"
    "ds_read_b64_tr_b16 v[234:235], v6 offset:8704\n"
    "v_mfma_f32_16x16x32_bf16 v[180:183], v[244:247], a[68:71], v[180:183]\n"
    "s_waitcnt lgkmcnt(8)\n"
    "v_cvt_pk_bf16_f32 v102, v80, v81\n"
    "v_cvt_pk_bf16_f32 v103, v82, v83\n"
    "v_mfma_f32_16x16x32_bf16 v[184:187], v[244:247], a[132:135], v[184:187]\n"
    "v_mul_f32 v36, s80, v36\n"
    "v_mul_f32 v37, s80, v37\n"
    "v_mfma_f32_16x16x32_bf16 v[188:191], v[244:247], a[196:199], v[188:191]\n"
    "v_mul_f32 v38, s80, v38\n"
    "v_mfma_f32_16x16x32_bf16 v[192:195], v[248:251], a[4:7], v[192:195]\n"
    "ds_read_b64_tr_b16 v[236:237], v8 offset:8192\n"
    "ds_read_b64_tr_b16 v[238:239], v8 offset:8704\n"
    "v_mfma_f32_16x16x32_bf16 v[196:199], v[248:251], a[68:71], v[196:199]\n"
    "s_waitcnt lgkmcnt(8)\n"
    "v_mul_f32 v39, s80, v39\n"
    "v_mul_f32 v52, s80, v52\n"
    "v_mfma_f32_16x16x32_bf16 v[200:203], v[248:251], a[132:135], v[200:203]\n"
    "v_mul_f32 v53, s80, v53\n"
    "v_mul_f32 v54, s80, v54\n"
    "v_mfma_f32_16x16x32_bf16 v[204:207], v[248:251], a[196:199], v[204:207]\n"
    "v_mul_f32 v55, s80, v55\n"
    "v_mfma_f32_16x16x32_bf16 v[208:211], v[252:255], a[4:7], v[208:211]\n"
    "ds_read_b64_tr_b16 v[240:241], v4 offset:12288\n"
    "ds_read_b64_tr_b16 v[242:243], v4 offset:12800\n"
    "v_mfma_f32_16x16x32_bf16 v[212:215], v[252:255], a[68:71], v[212:215]\n"
    "s_waitcnt lgkmcnt(8)\n"
    "v_cvt_pk_bf16_f32 v112, v36, v37\n"
    "v_cvt_pk_bf16_f32 v113, v38, v39\n"
    "v_mfma_f32_16x16x32_bf16 v[216:219], v[252:255], a[132:135], v[216:219]\n"
    "v_cvt_pk_bf16_f32 v114, v52, v53\n"
    "v_cvt_pk_bf16_f32 v115, v54, v55\n"
    "v_mfma_f32_16x16x32_bf16 v[220:223], v[252:255], a[196:199], v[220:223]\n"
    "v_mul_f32 v68, s80, v68\n"
    "v_mfma_f32_16x16x32_bf16 v[160:163], v[224:227], a[8:11], v[160:163]\n"
    "ds_read_b64_tr_b16 v[244:245], v5 offset:12288\n"
    "ds_read_b64_tr_b16 v[246:247], v5 offset:12800\n"
    "v_mfma_f32_16x16x32_bf16 v[164:167], v[224:227], a[72:75], v[164:167]\n"
    "s_waitcnt lgkmcnt(8)\n"
    "v_mul_f32 v69, s80, v69\n"
    "v_mul_f32 v70, s80, v70\n"
    "v_mfma_f32_16x16x32_bf16 v[168:171], v[224:227], a[136:139], v[168:171]\n"
    "v_mul_f32 v71, s80, v71\n"
    "v_mul_f32 v84, s80, v84\n"
    "v_mfma_f32_16x16x32_bf16 v[172:175], v[224:227], a[200:203], v[172:175]\n"
    "v_mul_f32 v85, s80, v85\n"
    "v_mfma_f32_16x16x32_bf16 v[176:179], v[228:231], a[8:11], v[176:179]\n"
    "ds_read_b64_tr_b16 v[248:249], v6 offset:12288\n"
    "ds_read_b64_tr_b16 v[250:251], v6 offset:12800\n"
    "v_mfma_f32_16x16x32_bf16 v[180:183], v[228:231], a[72:75], v[180:183]\n"
    "s_waitcnt lgkmcnt(8)\n"
    "v_mul_f32 v86, s80, v86\n"
    "v_mul_f32 v87, s80, v87\n"
    "v_mfma_f32_16x16x32_bf16 v[184:187], v[228:231], a[136:139], v[184:187]\n"
    "v_cvt_pk_bf16_f32 v116, v68, v69\n"
    "v_cvt_pk_bf16_f32 v117, v70, v71\n"
    "v_mfma_f32_16x16x32_bf16 v[188:191], v[228:231], a[200:203], v[188:191]\n"
    "v_cvt_pk_bf16_f32 v118, v84, v85\n"
    "v_mfma_f32_16x16x32_bf16 v[192:195], v[232:235], a[8:11], v[192:195]\n"
    "ds_read_b64_tr_b16 v[252:253], v8 offset:12288\n"
    "ds_read_b64_tr_b16 v[254:255], v8 offset:12800\n"
    "v_mfma_f32_16x16x32_bf16 v[196:199], v[232:235], a[72:75], v[196:199]\n"
    "s_waitcnt lgkmcnt(8)\n"
    "v_cvt_pk_bf16_f32 v119, v86, v87\n"
    "v_mul_f32 v40, s80, v40\n"
    "v_mfma_f32_16x16x32_bf16 v[200:203], v[232:235], a[136:139], v[200:203]\n"
    "v_mul_f32 v41, s80, v41\n"
    "v_mul_f32 v42, s80, v42\n"
    "v_mfma_f32_16x16x32_bf16 v[204:207], v[232:235], a[200:203], v[204:207]\n"
    "v_mul_f32 v43, s80, v43\n"
    "v_mfma_f32_16x16x32_bf16 v[208:211], v[236:239], a[8:11], v[208:211]\n"
    "ds_read_b64_tr_b16 v[224:225], v4 offset:16384\n"
    "ds_read_b64_tr_b16 v[226:227], v4 offset:16896\n"
    "v_mfma_f32_16x16x32_bf16 v[212:215], v[236:239], a[72:75], v[212:215]\n"
    "s_waitcnt lgkmcnt(8)\n"
    "v_mul_f32 v56, s80, v56\n"
    "v_mul_f32 v57, s80, v57\n"
    "v_mfma_f32_16x16x32_bf16 v[216:219], v[236:239], a[136:139], v[216:219]\n"
    "v_mul_f32 v58, s80, v58\n"
    "v_mul_f32 v59, s80, v59\n"
    "v_mfma_f32_16x16x32_bf16 v[220:223], v[236:239], a[200:203], v[220:223]\n"
    "v_cvt_pk_bf16_f32 v128, v40, v41\n"
    "v_mfma_f32_16x16x32_bf16 v[160:163], v[240:243], a[12:15], v[160:163]\n"
    "ds_read_b64_tr_b16 v[228:229], v5 offset:16384\n"
    "ds_read_b64_tr_b16 v[230:231], v5 offset:16896\n"
    "v_mfma_f32_16x16x32_bf16 v[164:167], v[240:243], a[76:79], v[164:167]\n"
    "s_waitcnt lgkmcnt(8)\n"
    "v_cvt_pk_bf16_f32 v129, v42, v43\n"
    "v_cvt_pk_bf16_f32 v130, v56, v57\n"
    "v_mfma_f32_16x16x32_bf16 v[168:171], v[240:243], a[140:143], v[168:171]\n"
    "v_cvt_pk_bf16_f32 v131, v58, v59\n"
    "v_mul_f32 v72, s80, v72\n"
    "v_mfma_f32_16x16x32_bf16 v[172:175], v[240:243], a[204:207], v[172:175]\n"
    "v_mul_f32 v73, s80, v73\n"
    "v_mfma_f32_16x16x32_bf16 v[176:179], v[244:247], a[12:15], v[176:179]\n"
    "ds_read_b64_tr_b16 v[232:233], v6 offset:16384\n"
    "ds_read_b64_tr_b16 v[234:235], v6 offset:16896\n"
    "v_mfma_f32_16x16x32_bf16 v[180:183], v[244:247], a[76:79], v[180:183]\n"
    "s_waitcnt lgkmcnt(8)\n"
    "v_mul_f32 v74, s80, v74\n"
    "v_mul_f32 v75, s80, v75\n"
    "v_mfma_f32_16x16x32_bf16 v[184:187], v[244:247], a[140:143], v[184:187]\n"
    "v_mul_f32 v88, s80, v88\n"
    "v_mul_f32 v89, s80, v89\n"
    "v_mfma_f32_16x16x32_bf16 v[188:191], v[244:247], a[204:207], v[188:191]\n"
    "v_mul_f32 v90, s80, v90\n"
    "v_mfma_f32_16x16x32_bf16 v[192:195], v[248:251], a[12:15], v[192:195]\n"
    "ds_read_b64_tr_b16 v[236:237], v8 offset:16384\n"
    "ds_read_b64_tr_b16 v[238:239], v8 offset:16896\n"
    "v_mfma_f32_16x16x32_bf16 v[196:199], v[248:251], a[76:79], v[196:199]\n"
    "s_waitcnt lgkmcnt(8)\n"
    "v_mul_f32 v91, s80, v91\n"
    "v_cvt_pk_bf16_f32 v132, v72, v73\n"
    "v_mfma_f32_16x16x32_bf16 v[200:203], v[248:251], a[140:143], v[200:203]\n"
    "v_cvt_pk_bf16_f32 v133, v74, v75\n"
    "v_cvt_pk_bf16_f32 v134, v88, v89\n"
    "v_mfma_f32_16x16x32_bf16 v[204:207], v[248:251], a[204:207], v[204:207]\n"
    "v_cvt_pk_bf16_f32 v135, v90, v91\n"
    "v_mfma_f32_16x16x32_bf16 v[208:211], v[252:255], a[12:15], v[208:211]\n"
    "ds_read_b64_tr_b16 v[240:241], v4 offset:20480\n"
    "ds_read_b64_tr_b16 v[242:243], v4 offset:20992\n"
    "v_mfma_f32_16x16x32_bf16 v[212:215], v[252:255], a[76:79], v[212:215]\n"
    "s_waitcnt lgkmcnt(8)\n"
    "v_mul_f32 v44, s80, v44\n"
    "v_mul_f32 v45, s80, v45\n"
    "v_mfma_f32_16x16x32_bf16 v[216:219], v[252:255], a[140:143], v[216:219]\n"
    "s_waitcnt vmcnt(0)\n"
    "v_cmp_eq_u32 vcc, s97, v24\n"
    "s_nop 0\n"
    "s_cmp_eq_u64 vcc, -1\n"
    "s_cbranch_scc1 L_pok_7_%=\n"
    "s_lshl_b32 s96, s53, 7\n"
    "s_add_u32 s96, s96, 0x2000\n"
    "s_mov_b32 s94, 2000000\n"
    "L_psp_8_%=:\n"
    "s_sleep 2\n"
    "buffer_load_dword v24, v29, s[44:47], s96 offen sc1\n"
    "s_waitcnt vmcnt(0)\n"
    "v_cmp_eq_u32 vcc, s97, v24\n"
    "s_nop 4\n"
    "s_cmp_eq_u64 vcc, -1\n"
    "s_cbranch_scc1 L_pok_7_%=\n"
    "s_sub_u32 s94, s94, 1\n"
    "s_cmp_lg_u32 s94, 0\n"
    "s_cbranch_scc1 L_psp_8_%=\n"
    "L_pok_7_%=:\n"
    "v_mfma_f32_16x16x32_bf16 v[220:223], v[252:255], a[204:207], v[220:223]\n"
    "v_mul_f32 v46, s80, v46\n"
    "v_mul_f32 v47, s80, v47\n"
    "v_mfma_f32_16x16x32_bf16 v[160:163], v[224:227], a[16:19], v[160:163]\n"
    "ds_read_b64_tr_b16 v[244:245], v5 offset:20480\n"
    "ds_read_b64_tr_b16 v[246:247], v5 offset:20992\n"
    "v_mfma_f32_16x16x32_bf16 v[164:167], v[224:227], a[80:83], v[164:167]\n"
    "s_waitcnt lgkmcnt(8)\n"
    "v_mul_f32 v60, s80, v60\n"
    "v_mul_f32 v61, s80, v61\n"
    "v_mfma_f32_16x16x32_bf16 v[168:171], v[224:227], a[144:147], v[168:171]\n"
    "v_mul_f32 v62, s80, v62\n"
    "v_mul_f32 v63, s80, v63\n"
    "v_mfma_f32_16x16x32_bf16 v[172:175], v[224:227], a[208:211], v[172:175]\n"
    "v_cvt_pk_bf16_f32 v144, v44, v45\n"
    "v_mfma_f32_16x16x32_bf16 v[176:179], v[228:231], a[16:19], v[176:179]\n"
    "ds_read_b64_tr_b16 v[248:249], v6 offset:20480\n"
    "ds_read_b64_tr_b16 v[250:251], v6 offset:20992\n"
    "v_mfma_f32_16x16x32_bf16 v[180:183], v[228:231], a[80:83], v[180:183]\n"
    "s_waitcnt lgkmcnt(8)\n"
    "s_add_u32 s56, s71, 0\n"
    "v_mfma_f32_16x16x32_bf16 v[184:187], v[228:231], a[144:147], v[184:187]\n"
    "s_add_u32 m0, s75, 0\n"
    "s_nop 0\n"
    "buffer_load_dwordx4 v1, s[44:47], s56 offen sc1 lds\n"
    "v_cvt_pk_bf16_f32 v145, v46, v47\n"
    "v_mfma_f32_16x16x32_bf16 v[188:191], v[228:231], a[208:211], v[188:191]\n"
    "v_cvt_pk_bf16_f32 v146, v60, v61\n"
    "v_cvt_pk_bf16_f32 v147, v62, v63\n"
    "v_mfma_f32_16x16x32_bf16 v[192:195], v[232:235], a[16:19], v[192:195]\n"
    "ds_read_b64_tr_b16 v[252:253], v8 offset:20480\n"
    "ds_read_b64_tr_b16 v[254:255], v8 offset:20992\n"
    "v_mfma_f32_16x16x32_bf16 v[196:199], v[232:235], a[80:83], v[196:199]\n"
    "s_waitcnt lgkmcnt(8)\n"
    "v_mul_f32 v76, s80, v76\n"
    "v_mul_f32 v77, s80, v77\n"
    "v_mfma_f32_16x16x32_bf16 v[200:203], v[232:235], a[144:147], v[200:203]\n"
    "v_mul_f32 v78, s80, v78\n"
    "v_mul_f32 v79, s80, v79\n"
    "v_mfma_f32_16x16x32_bf16 v[204:207], v[232:235], a[208:211], v[204:207]\n"
    "v_mul_f32 v92, s80, v92\n"
    "v_mfma_f32_16x16x32_bf16 v[208:211], v[236:239], a[16:19], v[208:211]\n"
    "ds_read_b64_tr_b16 v[224:225], v4 offset:24576\n"
    "ds_read_b64_tr_b16 v[226:227], v4 offset:25088\n"
    "v_mfma_f32_16x16x32_bf16 v[212:215], v[236:239], a[80:83], v[212:215]\n"
    "s_waitcnt lgkmcnt(8)\n"
    "buffer_load_dwordx4 v1, s[44:47], s56 offen offset:1024 sc1 lds\n"
    "v_mul_f32 v93, s80, v93\n"
    "v_mfma_f32_16x16x32_bf16 v[216:219], v[236:239], a[144:147], v[216:219]\n"
    "v_mul_f32 v94, s80, v94\n"
    "v_mul_f32 v95, s80, v95\n"
    "v_mfma_f32_16x16x32_bf16 v[220:223], v[236:239], a[208:211], v[220:223]\n"
    "v_cvt_pk_bf16_f32 v148, v76, v77\n"
    "v_cvt_pk_bf16_f32 v149, v78, v79\n"
    "v_mfma_f32_16x16x32_bf16 v[160:163], v[240:243], a[20:23], v[160:163]\n"
    "ds_read_b64_tr_b16 v[228:229], v5 offset:24576\n"
    "ds_read_b64_tr_b16 v[230:231], v5 offset:25088\n"
    "v_mfma_f32_16x16x32_bf16 v[164:167], v[240:243], a[84:87], v[164:167]\n"
    "s_waitcnt lgkmcnt(8)\n"
    "v_cvt_pk_bf16_f32 v150, v92, v93\n"
    "v_cvt_pk_bf16_f32 v151, v94, v95\n"
    "v_mfma_f32_16x16x32_bf16 v[168:171], v[240:243], a[148:151], v[168:171]\n"
    "v_mfma_f32_16x16x32_bf16 v[172:175], v[240:243], a[212:215], v[172:175]\n"
    "buffer_load_dwordx4 v1, s[44:47], s56 offen offset:2048 sc1 lds\n"
    "v_mfma_f32_16x16x32_bf16 v[176:179], v[244:247], a[20:23], v[176:179]\n"
    "ds_read_b64_tr_b16 v[232:233], v6 offset:24576\n"
    "ds_read_b64_tr_b16 v[234:235], v6 offset:25088\n"
    "v_mfma_f32_16x16x32_bf16 v[180:183], v[244:247], a[84:87], v[180:183]\n"
    "s_waitcnt lgkmcnt(8)\n"
    "v_mfma_f32_16x16x32_bf16 v[184:187], v[244:247], a[148:151], v[184:187]\n"
    "v_mfma_f32_16x16x32_bf16 v[188:191], v[244:247], a[212:215], v[188:191]\n"
    "v_mfma_f32_16x16x32_bf16 v[192:195], v[248:251], a[20:23], v[192:195]\n"
    "ds_read_b64_tr_b16 v[236:237], v8 offset:24576\n"
    "ds_read_b64_tr_b16 v[238:239], v8 offset:25088\n"
    "v_mfma_f32_16x16x32_bf16 v[196:199], v[248:251], a[84:87], v[196:199]\n"
    "s_waitcnt lgkmcnt(8)\n"
    "v_mfma_f32_16x16x32_bf16 v[200:203], v[248:251], a[148:151], v[200:203]\n"
    "v_mfma_f32_16x16x32_bf16 v[204:207], v[248:251], a[212:215], v[204:207]\n"
    "buffer_load_dwordx4 v1, s[44:47], s56 offen offset:3072 sc1 lds\n"
    "v_mfma_f32_16x16x32_bf16 v[208:211], v[252:255], a[20:23], v[208:211]\n"
    "ds_read_b64_tr_b16 v[240:241], v4 offset:28672\n"
    "ds_read_b64_tr_b16 v[242:243], v4 offset:29184\n"
    "v_mfma_f32_16x16x32_bf16 v[212:215], v[252:255], a[84:87], v[212:215]\n"
    "s_waitcnt lgkmcnt(8)\n"
    "v_mfma_f32_16x16x32_bf16 v[216:219], v[252:255], a[148:151], v[216:219]\n"
    "v_mfma_f32_16x16x32_bf16 v[220:223], v[252:255], a[212:215], v[220:223]\n"
    "v_mfma_f32_16x16x32_bf16 v[160:163], v[224:227], a[24:27], v[160:163]\n"
    "ds_read_b64_tr_b16 v[244:245], v5 offset:28672\n"
    "ds_read_b64_tr_b16 v[246:247], v5 offset:29184\n"
    "v_mfma_f32_16x16x32_bf16 v[164:167], v[224:227], a[88:91], v[164:167]\n"
    "s_waitcnt lgkmcnt(8)\n"
    "v_mfma_f32_16x16x32_bf16 v[168:171], v[224:227], a[152:155], v[168:171]\n"
    "s_add_u32 s71, s71, s73\n"
    "s_xor_b32 s75, s75, 0x10000\n"
    "v_mfma_f32_16x16x32_bf16 v[172:175], v[224:227], a[216:219], v[172:175]\n"
    "s_add_u32 s57, s72, 0\n"
    "v_mfma_f32_16x16x32_bf16 v[176:179], v[228:231], a[24:27], v[176:179]\n"
    "ds_read_b64_tr_b16 v[248:249], v6 offset:28672\n"
    "ds_read_b64_tr_b16 v[250:251], v6 offset:29184\n"
    "v_mfma_f32_16x16x32_bf16 v[180:183], v[228:231], a[88:91], v[180:183]\n"
    "s_waitcnt lgkmcnt(8)\n"
    "v_mfma_f32_16x16x32_bf16 v[184:187], v[228:231], a[152:155], v[184:187]\n"
    "s_add_u32 m0, s76, 0\n"
    "s_nop 0\n"
    "buffer_load_dwordx4 v1, s[44:47], s57 offen sc1 lds\n"
    "v_mfma_f32_16x16x32_bf16 v[188:191], v[228:231], a[216:219], v[188:191]\n"
    "s_lshl_b32 s96, s54, 2\n"
    "s_lshr_b32 s95, s53, 3\n"
    "s_add_u32 s96, s96, s95\n"
    "s_lshl_b32 s96, s96, 7\n"
    "s_add_u32 s96, s96, 0x3000\n"
    "s_and_b32 s95, s53, 7\n"
    "s_lshl_b32 s95, s95, 2\n"
    "s_add_u32 s95, s95, s52\n"
    "s_lshl_b32 s95, s95, 2\n"
    "s_add_u32 s96, s96, s95\n"
    "v_mov_b32 v24, s97\n"
    "buffer_store_dword v24, v7, s[44:47], s96 offen sc1\n"
    "v_mfma_f32_16x16x32_bf16 v[192:195], v[232:235], a[24:27], v[192:195]\n"
    "ds_read_b64_tr_b16 v[252:253], v8 offset:28672\n"
    "ds_read_b64_tr_b16 v[254:255], v8 offset:29184\n"
    "v_mfma_f32_16x16x32_bf16 v[196:199], v[232:235], a[88:91], v[196:199]\n"
    "s_waitcnt lgkmcnt(8)\n"
    "v_mfma_f32_16x16x32_bf16 v[200:203], v[232:235], a[152:155], v[200:203]\n"
    "v_mfma_f32_16x16x32_bf16 v[204:207], v[232:235], a[216:219], v[204:207]\n"
    "buffer_load_dwordx4 v1, s[44:47], s57 offen offset:1024 sc1 lds\n"
    "v_mfma_f32_16x16x32_bf16 v[208:211], v[236:239], a[24:27], v[208:211]\n"
    "ds_read_b64_tr_b16 v[224:225], v4 offset:32768\n"
    "ds_read_b64_tr_b16 v[226:227], v4 offset:33280\n"
    "v_mfma_f32_16x16x32_bf16 v[212:215], v[236:239], a[88:91], v[212:215]\n"
    "s_waitcnt lgkmcnt(8)\n"
    "v_mfma_f32_16x16x32_bf16 v[216:219], v[236:239], a[152:155], v[216:219]\n"
    "v_mfma_f32_16x16x32_bf16 v[220:223], v[236:239], a[216:219], v[220:223]\n"
    "v_mfma_f32_16x16x32_bf16 v[160:163], v[240:243], a[28:31], v[160:163]\n"
    "ds_read_b64_tr_b16 v[228:229], v5 offset:32768\n"
    "ds_read_b64_tr_b16 v[230:231], v5 offset:33280\n"
    "v_mfma_f32_16x16x32_bf16 v[164:167], v[240:243], a[92:95], v[164:167]\n"
    "s_waitcnt lgkmcnt(8)\n"
    "v_mfma_f32_16x16x32_bf16 v[168:171], v[240:243], a[156:159], v[168:171]\n"
    "v_mfma_f32_16x16x32_bf16 v[172:175], v[240:243], a[220:223], v[172:175]\n"
    "buffer_load_dwordx4 v1, s[44:47], s57 offen offset:2048 sc1 lds\n"
    "v_mfma_f32_16x16x32_bf16 v[176:179], v[244:247], a[28:31], v[176:179]\n"
    "ds_read_b64_tr_b16 v[232:233], v6 offset:32768\n"
    "ds_read_b64_tr_b16 v[234:235], v6 offset:33280\n"
    "v_mfma_f32_16x16x32_bf16 v[180:183], v[244:247], a[92:95], v[180:183]\n"
    "s_waitcnt lgkmcnt(8)\n"
    "v_mfma_f32_16x16x32_bf16 v[184:187], v[244:247], a[156:159], v[184:187]\n"
    "v_mfma_f32_16x16x32_bf16 v[188:191], v[244:247], a[220:223], v[188:191]\n"
    "v_mfma_f32_16x16x32_bf16 v[192:195], v[248:251], a[28:31], v[192:195]\n"
    "ds_read_b64_tr_b16 v[236:237], v8 offset:32768\n"
    "ds_read_b64_tr_b16 v[238:239], v8 offset:33280\n"
    "v_mfma_f32_16x16x32_bf16 v[196:199], v[248:251], a[92:95], v[196:199]\n"
    "s_waitcnt lgkmcnt(8)\n"
    "v_mfma_f32_16x16x32_bf16 v[200:203], v[248:251], a[156:159], v[200:203]\n"
    "buffer_load_dwordx4 v1, s[44:47], s57 offen offset:3072 sc1 lds\n"
    "v_mfma_f32_16x16x32_bf16 v[204:207], v[248:251], a[220:223], v[204:207]\n"
    "v_mfma_f32_16x16x32_bf16 v[208:211], v[252:255], a[28:31], v[208:211]\n"
    "ds_read_b64_tr_b16 v[240:241], v4 offset:36864\n"
    "ds_read_b64_tr_b16 v[242:243], v4 offset:37376\n"
    "v_mfma_f32_16x16x32_bf16 v[212:215], v[252:255], a[92:95], v[212:215]\n"
    "s_waitcnt lgkmcnt(8)\n"
    "v_mfma_f32_16x16x32_bf16 v[216:219], v[252:255], a[156:159], v[216:219]\n"
    "v_mfma_f32_16x16x32_bf16 v[220:223], v[252:255], a[220:223], v[220:223]\n"
    "v_mfma_f32_16x16x32_bf16 v[160:163], v[224:227], a[32:35], v[160:163]\n"
    "ds_read_b64_tr_b16 v[244:245], v5 offset:36864\n"
    "ds_read_b64_tr_b16 v[246:247], v5 offset:37376\n"
    "v_mfma_f32_16x16x32_bf16 v[164:167], v[224:227], a[96:99], v[164:167]\n"
    "s_waitcnt lgkmcnt(8)\n"
    "v_mfma_f32_16x16x32_bf16 v[168:171], v[224:227], a[160:163], v[168:171]\n"
    "s_add_u32 s72, s72, s74\n"
    "s_xor_b32 s76, s76, 0x10000\n"
    "s_lshl_b32 s59, s53, 19\n"
    "v_mfma_f32_16x16x32_bf16 v[172:175], v[224:227], a[224:227], v[172:175]\n"
    "s_add_u32 s59, s59, 0x400000\n"
    "v_mfma_f32_16x16x32_bf16 v[176:179], v[228:231], a[32:35], v[176:179]\n"
    "ds_read_b64_tr_b16 v[248:249], v6 offset:36864\n"
    "ds_read_b64_tr_b16 v[250:251], v6 offset:37376\n"
    "v_mfma_f32_16x16x32_bf16 v[180:183], v[228:231], a[96:99], v[180:183]\n"
    "s_waitcnt lgkmcnt(8)\n"
    "buffer_load_dwordx4 v[32:35], v1, s[44:47], s59 offen offset:0 sc1\n"
    "v_mfma_f32_16x16x32_bf16 v[184:187], v[228:231], a[160:163], v[184:187]\n"
    "v_mfma_f32_16x16x32_bf16 v[188:191], v[228:231], a[224:227], v[188:191]\n"
    "v_mfma_f32_16x16x32_bf16 v[192:195], v[232:235], a[32:35], v[192:195]\n"
    "ds_read_b64_tr_b16 v[252:253], v8 offset:36864\n"
    "ds_read_b64_tr_b16 v[254:255], v8 offset:37376\n"
    "v_mfma_f32_16x16x32_bf16 v[196:199], v[232:235], a[96:99], v[196:199]\n"
    "s_waitcnt lgkmcnt(8)\n"
    "v_mfma_f32_16x16x32_bf16 v[200:203], v[232:235], a[160:163], v[200:203]\n"
    "v_mfma_f32_16x16x32_bf16 v[204:207], v[232:235], a[224:227], v[204:207]\n"
    "v_mfma_f32_16x16x32_bf16 v[208:211], v[236:239], a[32:35], v[208:211]\n"
    "ds_read_b64_tr_b16 v[224:225], v4 offset:40960\n"
    "ds_read_b64_tr_b16 v[226:227], v4 offset:41472\n"
    "v_mfma_f32_16x16x32_bf16 v[212:215], v[236:239], a[96:99], v[212:215]\n"
    "s_waitcnt lgkmcnt(8)\n"
    "v_mfma_f32_16x16x32_bf16 v[216:219], v[236:239], a[160:163], v[216:219]\n"
    "v_mfma_f32_16x16x32_bf16 v[220:223], v[236:239], a[224:227], v[220:223]\n"
    "v_mfma_f32_16x16x32_bf16 v[160:163], v[240:243], a[36:39], v[160:163]\n"
    "ds_read_b64_tr_b16 v[228:229], v5 offset:40960\n"
    "ds_read_b64_tr_b16 v[230:231], v5 offset:41472\n"
    "v_mfma_f32_16x16x32_bf16 v[164:167], v[240:243], a[100:103], v[164:167]\n"
    "s_waitcnt lgkmcnt(8)\n"
    "buffer_load_dwordx4 v[36:39], v1, s[44:47], s59 offen offset:1024 sc1\n"
    "v_mfma_f32_16x16x32_bf16 v[168:171], v[240:243], a[164:167], v[168:171]\n"
    "v_mfma_f32_16x16x32_bf16 v[172:175], v[240:243], a[228:231], v[172:175]\n"
    "v_mfma_f32_16x16x32_bf16 v[176:179], v[244:247], a[36:39], v[176:179]\n"
    "ds_read_b64_tr_b16 v[232:233], v6 offset:40960\n"
    "ds_read_b64_tr_b16 v[234:235], v6 offset:41472\n"
    "v_mfma_f32_16x16x32_bf16 v[180:183], v[244:247], a[100:103], v[180:183]\n"
    "s_waitcnt lgkmcnt(8)\n"
    "v_mfma_f32_16x16x32_bf16 v[184:187], v[244:247], a[164:167], v[184:187]\n"
    "v_mfma_f32_16x16x32_bf16 v[188:191], v[244:247], a[228:231], v[188:191]\n"
    "v_mfma_f32_16x16x32_bf16 v[192:195], v[248:251], a[36:39], v[192:195]\n"
    "ds_read_b64_tr_b16 v[236:237], v8 offset:40960\n"
    "ds_read_b64_tr_b16 v[238:239], v8 offset:41472\n"
    "v_mfma_f32_16x16x32_bf16 v[196:199], v[248:251], a[100:103], v[196:199]\n"
    "s_waitcnt lgkmcnt(8)\n"
    "v_mfma_f32_16x16x32_bf16 v[200:203], v[248:251], a[164:167], v[200:203]\n"
    "v_mfma_f32_16x16x32_bf16 v[204:207], v[248:251], a[228:231], v[204:207]\n"
    "v_mfma_f32_16x16x32_bf16 v[208:211], v[252:255], a[36:39], v[208:211]\n"
    "ds_read_b64_tr_b16 v[240:241], v4 offset:45056\n"
    "ds_read_b64_tr_b16 v[242:243], v4 offset:45568\n"
    "v_mfma_f32_16x16x32_bf16 v[212:215], v[252:255], a[100:103], v[212:215]\n"
    "s_waitcnt lgkmcnt(8)\n"
    "buffer_load_dwordx4 v[40:43], v1, s[44:47], s59 offen offset:2048 sc1\n"
    "v_mfma_f32_16x16x32_bf16 v[216:219], v[252:255], a[164:167], v[216:219]\n"
    "v_mfma_f32_16x16x32_bf16 v[220:223], v[252:255], a[228:231], v[220:223]\n"
    "v_mfma_f32_16x16x32_bf16 v[160:163], v[224:227], a[40:43], v[160:163]\n"
    "ds_read_b64_tr_b16 v[244:245], v5 offset:45056\n"
    "ds_read_b64_tr_b16 v[246:247], v5 offset:45568\n"
    "v_mfma_f32_16x16x32_bf16 v[164:167], v[224:227], a[104:107], v[164:167]\n"
    "s_waitcnt lgkmcnt(8)\n"
    "v_mfma_f32_16x16x32_bf16 v[168:171], v[224:227], a[168:171], v[168:171]\n"
    "v_mfma_f32_16x16x32_bf16 v[172:175], v[224:227], a[232:235], v[172:175]\n"
    "v_mfma_f32_16x16x32_bf16 v[176:179], v[228:231], a[40:43], v[176:179]\n"
    "ds_read_b64_tr_b16 v[248:249], v6 offset:45056\n"
    "ds_read_b64_tr_b16 v[250:251], v6 offset:45568\n"
    "v_mfma_f32_16x16x32_bf16 v[180:183], v[228:231], a[104:107], v[180:183]\n"
    "s_waitcnt lgkmcnt(8)\n"
    "v_mfma_f32_16x16x32_bf16 v[184:187], v[228:231], a[168:171], v[184:187]\n"
    "v_mfma_f32_16x16x32_bf16 v[188:191], v[228:231], a[232:235], v[188:191]\n"
    "v_mfma_f32_16x16x32_bf16 v[192:195], v[232:235], a[40:43], v[192:195]\n"
    "ds_read_b64_tr_b16 v[252:253], v8 offset:45056\n"
    "ds_read_b64_tr_b16 v[254:255], v8 offset:45568\n"
    "v_mfma_f32_16x16x32_bf16 v[196:199], v[232:235], a[104:107], v[196:199]\n"
    "s_waitcnt lgkmcnt(8)\n"
    "buffer_load_dwordx4 v[44:47], v1, s[44:47], s59 offen offset:3072 sc1\n"
    "v_mfma_f32_16x16x32_bf16 v[200:203], v[232:235], a[168:171], v[200:203]\n"
    "v_mfma_f32_16x16x32_bf16 v[204:207], v[232:235], a[232:235], v[204:207]\n"
    "v_mfma_f32_16x16x32_bf16 v[208:211], v[236:239], a[40:43], v[208:211]\n"
    "ds_read_b64_tr_b16 v[224:225], v4 offset:49152\n"
    "ds_read_b64_tr_b16 v[226:227], v4 offset:49664\n"
    "v_mfma_f32_16x16x32_bf16 v[212:215], v[236:239], a[104:107], v[212:215]\n"
    "s_waitcnt lgkmcnt(8)\n"
    "v_mfma_f32_16x16x32_bf16 v[216:219], v[236:239], a[168:171], v[216:219]\n"
    "v_mfma_f32_16x16x32_bf16 v[220:223], v[236:239], a[232:235], v[220:223]\n"
    "v_mfma_f32_16x16x32_bf16 v[160:163], v[240:243], a[44:47], v[160:163]\n"
    "ds_read_b64_tr_b16 v[228:229], v5 offset:49152\n"
    "ds_read_b64_tr_b16 v[230:231], v5 offset:49664\n"
    "v_mfma_f32_16x16x32_bf16 v[164:167], v[240:243], a[108:111], v[164:167]\n"
    "s_waitcnt lgkmcnt(8)\n"
    "v_mfma_f32_16x16x32_bf16 v[168:171], v[240:243], a[172:175], v[168:171]\n"
    "v_mfma_f32_16x16x32_bf16 v[172:175], v[240:243], a[236:239], v[172:175]\n"
    "v_mfma_f32_16x16x32_bf16 v[176:179], v[244:247], a[44:47], v[176:179]\n"
    "ds_read_b64_tr_b16 v[232:233], v6 offset:49152\n"
    "ds_read_b64_tr_b16 v[234:235], v6 offset:49664\n"
    "v_mfma_f32_16x16x32_bf16 v[180:183], v[244:247], a[108:111], v[180:183]\n"
    "s_waitcnt lgkmcnt(8)\n"
    "v_mfma_f32_16x16x32_bf16 v[184:187], v[244:247], a[172:175], v[184:187]\n"
    "v_mfma_f32_16x16x32_bf16 v[188:191], v[244:247], a[236:239], v[188:191]\n"
    "v_mfma_f32_16x16x32_bf16 v[192:195], v[248:251], a[44:47], v[192:195]\n"
    "ds_read_b64_tr_b16 v[236:237], v8 offset:49152\n"
    "ds_read_b64_tr_b16 v[238:239], v8 offset:49664\n"
    "v_mfma_f32_16x16x32_bf16 v[196:199], v[248:251], a[108:111], v[196:199]\n"
    "s_waitcnt lgkmcnt(8)\n"
    "v_mfma_f32_16x16x32_bf16 v[200:203], v[248:251], a[172:175], v[200:203]\n"
    "v_mfma_f32_16x16x32_bf16 v[204:207], v[248:251], a[236:239], v[204:207]\n"
    "v_mfma_f32_16x16x32_bf16 v[208:211], v[252:255], a[44:47], v[208:211]\n"
    "ds_read_b64_tr_b16 v[240:241], v4 offset:53248\n"
    "ds_read_b64_tr_b16 v[242:243], v4 offset:53760\n"
    "v_mfma_f32_16x16x32_bf16 v[212:215], v[252:255], a[108:111], v[212:215]\n"
    "s_waitcnt lgkmcnt(8)\n"
    "v_mfma_f32_16x16x32_bf16 v[216:219], v[252:255], a[172:175], v[216:219]\n"
    "v_mfma_f32_16x16x32_bf16 v[220:223], v[252:255], a[236:239], v[220:223]\n"
    "v_mfma_f32_16x16x32_bf16 v[160:163], v[224:227], a[48:51], v[160:163]\n"
    "ds_read_b64_tr_b16 v[244:245], v5 offset:53248\n"
    "ds_read_b64_tr_b16 v[246:247], v5 offset:53760\n"
    "v_mfma_f32_16x16x32_bf16 v[164:167], v[224:227], a[112:115], v[164:167]\n"
    "s_waitcnt lgkmcnt(8)\n"
    "v_mfma_f32_16x16x32_bf16 v[168:171], v[224:227], a[176:179], v[168:171]\n"
    "v_mfma_f32_16x16x32_bf16 v[172:175], v[224:227], a[240:243], v[172:175]\n"
    "v_mfma_f32_16x16x32_bf16 v[176:179], v[228:231], a[48:51], v[176:179]\n"
    "ds_read_b64_tr_b16 v[248:249], v6 offset:53248\n"
    "ds_read_b64_tr_b16 v[250:251], v6 offset:53760\n"
    "v_mfma_f32_16x16x32_bf16 v[180:183], v[228:231], a[112:115], v[180:183]\n"
    "s_waitcnt lgkmcnt(8)\n"
    "v_mfma_f32_16x16x32_bf16 v[184:187], v[228:231], a[176:179], v[184:187]\n"
    "v_mfma_f32_16x16x32_bf16 v[188:191], v[228:231], a[240:243], v[188:191]\n"
    "v_mfma_f32_16x16x32_bf16 v[192:195], v[232:235], a[48:51], v[192:195]\n"
    "ds_read_b64_tr_b16 v[252:253], v8 offset:53248\n"
    "ds_read_b64_tr_b16 v[254:255], v8 offset:53760\n"
    "v_mfma_f32_16x16x32_bf16 v[196:199], v[232:235], a[112:115], v[196:199]\n"
    "s_waitcnt lgkmcnt(8)\n"
    "v_mfma_f32_16x16x32_bf16 v[200:203], v[232:235], a[176:179], v[200:203]\n"
    "v_mfma_f32_16x16x32_bf16 v[204:207], v[232:235], a[240:243], v[204:207]\n"
    "v_mfma_f32_16x16x32_bf16 v[208:211], v[236:239], a[48:51], v[208:211]\n"
    "ds_read_b64_tr_b16 v[224:225], v4 offset:57344\n"
    "ds_read_b64_tr_b16 v[226:227], v4 offset:57856\n"
    "v_mfma_f32_16x16x32_bf16 v[212:215], v[236:239], a[112:115], v[212:215]\n"
    "s_waitcnt lgkmcnt(8)\n"
    "v_mfma_f32_16x16x32_bf16 v[216:219], v[236:239], a[176:179], v[216:219]\n"
    "v_mfma_f32_16x16x32_bf16 v[220:223], v[236:239], a[240:243], v[220:223]\n"
    "v_mfma_f32_16x16x32_bf16 v[160:163], v[240:243], a[52:55], v[160:163]\n"
    "ds_read_b64_tr_b16 v[228:229], v5 offset:57344\n"
    "ds_read_b64_tr_b16 v[230:231], v5 offset:57856\n"
    "v_mfma_f32_16x16x32_bf16 v[164:167], v[240:243], a[116:119], v[164:167]\n"
    "s_waitcnt lgkmcnt(8)\n"
    "v_mfma_f32_16x16x32_bf16 v[168:171], v[240:243], a[180:183], v[168:171]\n"
    "v_mfma_f32_16x16x32_bf16 v[172:175], v[240:243], a[244:247], v[172:175]\n"
    "v_mfma_f32_16x16x32_bf16 v[176:179], v[244:247], a[52:55], v[176:179]\n"
    "ds_read_b64_tr_b16 v[232:233], v6 offset:57344\n"
    "ds_read_b64_tr_b16 v[234:235], v6 offset:57856\n"
    "v_mfma_f32_16x16x32_bf16 v[180:183], v[244:247], a[116:119], v[180:183]\n"
    "s_waitcnt lgkmcnt(8)\n"
    "v_mfma_f32_16x16x32_bf16 v[184:187], v[244:247], a[180:183], v[184:187]\n"
    "v_mfma_f32_16x16x32_bf16 v[188:191], v[244:247], a[244:247], v[188:191]\n"
    "v_mfma_f32_16x16x32_bf16 v[192:195], v[248:251], a[52:55], v[192:195]\n"
    "ds_read_b64_tr_b16 v[236:237], v8 offset:57344\n"
    "ds_read_b64_tr_b16 v[238:239], v8 offset:57856\n"
    "v_mfma_f32_16x16x32_bf16 v[196:199], v[248:251], a[116:119], v[196:199]\n"
    "s_waitcnt lgkmcnt(8)\n"
    "v_mfma_f32_16x16x32_bf16 v[200:203], v[248:251], a[180:183], v[200:203]\n"
    "v_mfma_f32_16x16x32_bf16 v[204:207], v[248:251], a[244:247], v[204:207]\n"
    "v_mfma_f32_16x16x32_bf16 v[208:211], v[252:255], a[52:55], v[208:211]\n"
    "ds_read_b64_tr_b16 v[240:241], v4 offset:61440\n"
    "ds_read_b64_tr_b16 v[242:243], v4 offset:61952\n"
    "v_mfma_f32_16x16x32_bf16 v[212:215], v[252:255], a[116:119], v[212:215]\n"
    "s_waitcnt lgkmcnt(8)\n"
    "v_mfma_f32_16x16x32_bf16 v[216:219], v[252:255], a[180:183], v[216:219]\n"
    "v_mfma_f32_16x16x32_bf16 v[220:223], v[252:255], a[244:247], v[220:223]\n"
    "v_mfma_f32_16x16x32_bf16 v[160:163], v[224:227], a[56:59], v[160:163]\n"
    "ds_read_b64_tr_b16 v[244:245], v5 offset:61440\n"
    "ds_read_b64_tr_b16 v[246:247], v5 offset:61952\n"
    "v_mfma_f32_16x16x32_bf16 v[164:167], v[224:227], a[120:123], v[164:167]\n"
    "s_waitcnt lgkmcnt(8)\n"
    "v_mfma_f32_16x16x32_bf16 v[168:171], v[224:227], a[184:187], v[168:171]\n"
    "v_mfma_f32_16x16x32_bf16 v[172:175], v[224:227], a[248:251], v[172:175]\n"
    "v_mfma_f32_16x16x32_bf16 v[176:179], v[228:231], a[56:59], v[176:179]\n"
    "ds_read_b64_tr_b16 v[248:249], v6 offset:61440\n"
    "ds_read_b64_tr_b16 v[250:251], v6 offset:61952\n"
    "v_mfma_f32_16x16x32_bf16 v[180:183], v[228:231], a[120:123], v[180:183]\n"
    "s_waitcnt lgkmcnt(8)\n"
    "v_mfma_f32_16x16x32_bf16 v[184:187], v[228:231], a[184:187], v[184:187]\n"
    "v_mfma_f32_16x16x32_bf16 v[188:191], v[228:231], a[248:251], v[188:191]\n"
    "v_mfma_f32_16x16x32_bf16 v[192:195], v[232:235], a[56:59], v[192:195]\n"
    "ds_read_b64_tr_b16 v[252:253], v8 offset:61440\n"
    "ds_read_b64_tr_b16 v[254:255], v8 offset:61952\n"
    "v_mfma_f32_16x16x32_bf16 v[196:199], v[232:235], a[120:123], v[196:199]\n"
    "s_waitcnt lgkmcnt(8)\n"
    "v_mfma_f32_16x16x32_bf16 v[200:203], v[232:235], a[184:187], v[200:203]\n"
    "v_mfma_f32_16x16x32_bf16 v[204:207], v[232:235], a[248:251], v[204:207]\n"
    "v_mfma_f32_16x16x32_bf16 v[208:211], v[236:239], a[56:59], v[208:211]\n"
    "v_mfma_f32_16x16x32_bf16 v[212:215], v[236:239], a[120:123], v[212:215]\n"
    "s_waitcnt lgkmcnt(6)\n"
    "v_mfma_f32_16x16x32_bf16 v[216:219], v[236:239], a[184:187], v[216:219]\n"
    "v_mfma_f32_16x16x32_bf16 v[220:223], v[236:239], a[248:251], v[220:223]\n"
    "v_mfma_f32_16x16x32_bf16 v[160:163], v[240:243], a[60:63], v[160:163]\n"
    "v_mfma_f32_16x16x32_bf16 v[164:167], v[240:243], a[124:127], v[164:167]\n"
    "s_waitcnt lgkmcnt(4)\n"
    "v_mfma_f32_16x16x32_bf16 v[168:171], v[240:243], a[188:191], v[168:171]\n"
    "v_mfma_f32_16x16x32_bf16 v[172:175], v[240:243], a[252:255], v[172:175]\n"
    "v_mfma_f32_16x16x32_bf16 v[176:179], v[244:247], a[60:63], v[176:179]\n"
    "v_mfma_f32_16x16x32_bf16 v[180:183], v[244:247], a[124:127], v[180:183]\n"
    "s_waitcnt lgkmcnt(2)\n"
    "v_mfma_f32_16x16x32_bf16 v[184:187], v[244:247], a[188:191], v[184:187]\n"
    "v_mfma_f32_16x16x32_bf16 v[188:191], v[244:247], a[252:255], v[188:191]\n"
    "v_mfma_f32_16x16x32_bf16 v[192:195], v[248:251], a[60:63], v[192:195]\n"
    "v_mfma_f32_16x16x32_bf16 v[196:199], v[248:251], a[124:127], v[196:199]\n"
    "s_waitcnt lgkmcnt(0)\n"
    "v_mfma_f32_16x16x32_bf16 v[200:203], v[248:251], a[188:191], v[200:203]\n"
    "v_mfma_f32_16x16x32_bf16 v[204:207], v[248:251], a[252:255], v[204:207]\n"
    "v_mfma_f32_16x16x32_bf16 v[208:211], v[252:255], a[60:63], v[208:211]\n"
    "v_mfma_f32_16x16x32_bf16 v[212:215], v[252:255], a[124:127], v[212:215]\n"
    "v_mfma_f32_16x16x32_bf16 v[216:219], v[252:255], a[188:191], v[216:219]\n"
    "v_mfma_f32_16x16x32_bf16 v[220:223], v[252:255], a[252:255], v[220:223]\n"
    "s_barrier\n"
    "s_load_dwordx2 s[60:61], %11, 0x28\n"
    "s_load_dwordx2 s[62:63], %11, 0x40\n"
    "s_add_u32 s56, s71, 0\n"
    "s_add_u32 m0, s75, 0\n"
    "s_nop 0\n"
    "buffer_load_dwordx4 v1, s[44:47], s56 offen sc1 lds\n"
    "buffer_load_dwordx4 v1, s[44:47], s56 offen offset:1024 sc1 lds\n"
    "buffer_load_dwordx4 v1, s[44:47], s56 offen offset:2048 sc1 lds\n"
    "buffer_load_dwordx4 v1, s[44:47], s56 offen offset:3072 sc1 lds\n"
    "s_add_u32 s71, s71, s73\n"
    "s_xor_b32 s75, s75, 0x10000\n"
    "v_mov_b32 v16, 0\n"
    "v_mov_b32 v17, 0\n"
    "v_mov_b32 v18, 0\n"
    "v_mov_b32 v19, 0\n"
    "v_mul_f32 v160, s80, v160\n"
    "v_mul_f32 v161, s80, v161\n"
    "v_mul_f32 v162, s80, v162\n"
    "v_mul_f32 v163, s80, v163\n"
    "v_mul_f32 v176, s80, v176\n"
    "v_mul_f32 v177, s80, v177\n"
    "v_mul_f32 v178, s80, v178\n"
    "v_mul_f32 v179, s80, v179\n"
    "v_cvt_pk_bf16_f32 v104, v160, v161\n"
    "v_cvt_pk_bf16_f32 v105, v162, v163\n"
    "v_cvt_pk_bf16_f32 v106, v176, v177\n"
    "v_cvt_pk_bf16_f32 v107, v178, v179\n"
    "v_mfma_f32_32x32x16_bf16 a[0:15], v[16:19], v[16:19], 0\n"
    "v_mul_f32 v192, s80, v192\n"
    "v_mul_f32 v193, s80, v193\n"
    "v_mul_f32 v194, s80, v194\n"
    "v_mul_f32 v195, s80, v195\n"
    "v_mul_f32 v208, s80, v208\n"
    "v_mul_f32 v209, s80, v209\n"
    "v_mul_f32 v210, s80, v210\n"
    "v_mul_f32 v211, s80, v211\n"
    "v_cvt_pk_bf16_f32 v108, v192, v193\n"
    "v_cvt_pk_bf16_f32 v109, v194, v195\n"
    "v_cvt_pk_bf16_f32 v110, v208, v209\n"
    "v_cvt_pk_bf16_f32 v111, v210, v211\n"
    "v_mfma_f32_32x32x16_bf16 a[16:31], v[16:19], v[16:19], 0\n"
    "v_mul_f32 v164, s80, v164\n"
    "v_mul_f32 v165, s80, v165\n"
    "v_mul_f32 v166, s80, v166\n"
    "v_mul_f32 v167, s80, v167\n"
    "v_mul_f32 v180, s80, v180\n"
    "v_mul_f32 v181, s80, v181\n"
    "v_mul_f32 v182, s80, v182\n"
    "v_mul_f32 v183, s80, v183\n"
    "v_cvt_pk_bf16_f32 v120, v164, v165\n"
    "v_cvt_pk_bf16_f32 v121, v166, v167\n"
    "v_cvt_pk_bf16_f32 v122, v180, v181\n"
    "v_cvt_pk_bf16_f32 v123, v182, v183\n"
    "v_mfma_f32_32x32x16_bf16 a[32:47], v[16:19], v[16:19], 0\n"
    "v_mul_f32 v196, s80, v196\n"
    "v_mul_f32 v197, s80, v197\n"
    "v_mul_f32 v198, s80, v198\n"
    "v_mul_f32 v199, s80, v199\n"
    "v_mul_f32 v212, s80, v212\n"
    "v_mul_f32 v213, s80, v213\n"
    "v_mul_f32 v214, s80, v214\n"
    "v_mul_f32 v215, s80, v215\n"
    "v_cvt_pk_bf16_f32 v124, v196, v197\n"
    "v_cvt_pk_bf16_f32 v125, v198, v199\n"
    "v_cvt_pk_bf16_f32 v126, v212, v213\n"
    "v_cvt_pk_bf16_f32 v127, v214, v215\n"
    "v_mfma_f32_32x32x16_bf16 a[48:63], v[16:19], v[16:19], 0\n"
    "v_mul_f32 v168, s80, v168\n"
    "v_mul_f32 v169, s80, v169\n"
    "v_mul_f32 v170, s80, v170\n"
    "v_mul_f32 v171, s80, v171\n"
    "v_mul_f32 v184, s80, v184\n"
    "v_mul_f32 v185, s80, v185\n"
    "v_mul_f32 v186, s80, v186\n"
    "v_mul_f32 v187, s80, v187\n"
    "v_cvt_pk_bf16_f32 v136, v168, v169\n"
    "v_cvt_pk_bf16_f32 v137, v170, v171\n"
    "v_cvt_pk_bf16_f32 v138, v184, v185\n"
    "v_cvt_pk_bf16_f32 v139, v186, v187\n"
    "v_mfma_f32_32x32x16_bf16 a[64:79], v[16:19], v[16:19], 0\n"
    "v_mul_f32 v200, s80, v200\n"
    "v_mul_f32 v201, s80, v201\n"
    "v_mul_f32 v202, s80, v202\n"
    "v_mul_f32 v203, s80, v203\n"
    "v_mul_f32 v216, s80, v216\n"
    "v_mul_f32 v217, s80, v217\n"
    "v_mul_f32 v218, s80, v218\n"
    "v_mul_f32 v219, s80, v219\n"
    "v_cvt_pk_bf16_f32 v140, v200, v201\n"
    "v_cvt_pk_bf16_f32 v141, v202, v203\n"
    "v_cvt_pk_bf16_f32 v142, v216, v217\n"
    "v_cvt_pk_bf16_f32 v143, v218, v219\n"
    "v_mfma_f32_32x32x16_bf16 a[80:95], v[16:19], v[16:19], 0\n"
    "v_mul_f32 v172, s80, v172\n"
    "v_mul_f32 v173, s80, v173\n"
    "v_mul_f32 v174, s80, v174\n"
    "v_mul_f32 v175, s80, v175\n"
    "v_mul_f32 v188, s80, v188\n"
    "v_mul_f32 v189, s80, v189\n"
    "v_mul_f32 v190, s80, v190\n"
    "v_mul_f32 v191, s80, v191\n"
    "v_cvt_pk_bf16_f32 v152, v172, v173\n"
    "v_cvt_pk_bf16_f32 v153, v174, v175\n"
    "v_cvt_pk_bf16_f32 v154, v188, v189\n"
    "v_cvt_pk_bf16_f32 v155, v190, v191\n"
    "v_mfma_f32_32x32x16_bf16 a[96:111], v[16:19], v[16:19], 0\n"
    "v_mul_f32 v204, s80, v204\n"
    "v_mul_f32 v205, s80, v205\n"
    "v_mul_f32 v206, s80, v206\n"
    "v_mul_f32 v207, s80, v207\n"
    "v_mul_f32 v220, s80, v220\n"
    "v_mul_f32 v221, s80, v221\n"
    "v_mul_f32 v222, s80, v222\n"
    "v_mul_f32 v223, s80, v223\n"
    "v_cvt_pk_bf16_f32 v156, v204, v205\n"
    "v_cvt_pk_bf16_f32 v157, v206, v207\n"
    "v_cvt_pk_bf16_f32 v158, v220, v221\n"
    "v_cvt_pk_bf16_f32 v159, v222, v223\n"
    "v_mfma_f32_32x32x16_bf16 a[112:127], v[16:19], v[16:19], 0\n"
    "s_waitcnt vmcnt(7)\n"
    "v_mfma_f32_16x16x32_bf16 v[160:163], v[32:35], v[96:99], 0\n"
    "s_waitcnt vmcnt(6)\n"
    "v_mfma_f32_16x16x32_bf16 v[160:163], v[36:39], v[100:103], v[160:163]\n"
    "s_waitcnt vmcnt(5)\n"
    "v_mfma_f32_16x16x32_bf16 v[160:163], v[40:43], v[104:107], v[160:163]\n"
    "s_waitcnt vmcnt(4)\n"
    "v_mfma_f32_16x16x32_bf16 v[160:163], v[44:47], v[108:111], v[160:163]\n"
    "v_mfma_f32_16x16x32_bf16 v[164:167], v[32:35], v[112:115], 0\n"
    "v_mfma_f32_16x16x32_bf16 v[164:167], v[36:39], v[116:119], v[164:167]\n"
    "v_mfma_f32_16x16x32_bf16 v[164:167], v[40:43], v[120:123], v[164:167]\n"
    "v_mfma_f32_16x16x32_bf16 v[164:167], v[44:47], v[124:127], v[164:167]\n"
    "v_mfma_f32_16x16x32_bf16 v[168:171], v[32:35], v[128:131], 0\n"
    "v_mfma_f32_16x16x32_bf16 v[168:171], v[36:39], v[132:135], v[168:171]\n"
    "v_mfma_f32_16x16x32_bf16 v[168:171], v[40:43], v[136:139], v[168:171]\n"
    "v_mfma_f32_16x16x32_bf16 v[168:171], v[44:47], v[140:143], v[168:171]\n"
    "v_mfma_f32_16x16x32_bf16 v[172:175], v[32:35], v[144:147], 0\n"
    "v_mfma_f32_16x16x32_bf16 v[172:175], v[36:39], v[148:151], v[172:175]\n"
    "v_mfma_f32_16x16x32_bf16 v[172:175], v[40:43], v[152:155], v[172:175]\n"
    "v_mfma_f32_16x16x32_bf16 v[172:175], v[44:47], v[156:159], v[172:175]\n"
    "v_and_b32 v21, 63, v0\n"
    "v_and_b32 v22, 15, v21\n"
    "v_lshlrev_b32 v20, 2, v22\n"
    "ds_bpermute_b32 v12, v20, v160\n"
    "ds_bpermute_b32 v13, v20, v164\n"
    "ds_bpermute_b32 v14, v20, v168\n"
    "s_nop 1\n"
    "ds_bpermute_b32 v15, v20, v172\n"
    "v_lshrrev_b32 v23, 4, v21\n"
    "v_lshlrev_b32 v23, 2, v23\n"
    "v_sub_u32 v5, v22, v23\n"
    "v_mov_b32 v4, 0xff800000\n"
    "v_add_u32 v2, 0x0, v1\n"
    "v_add_u32 v3, 0x8000, v1\n"
    "s_waitcnt lgkmcnt(0)\n"
    "v_sub_f32 v12, 0, v12\n"
    "v_sub_f32 v13, 0, v13\n"
    "v_sub_f32 v14, 0, v14\n"
    "v_sub_f32 v15, 0, v15\n"
    "s_lshl_b32 s56, s52, 13\n"
    "s_add_u32 s56, s56, 131072\n"
    "v_add_u32 v22, s56, v1\n"
    "ds_write_b128 v22, v[96:99] offset:0\n"
    "ds_write_b128 v22, v[100:103] offset:1024\n"
    "ds_write_b128 v22, v[104:107] offset:2048\n"
    "ds_write_b128 v22, v[108:111] offset:3072\n"
    "ds_write_b128 v22, v[112:115] offset:4096\n"
    "ds_write_b128 v22, v[116:119] offset:5120\n"
    "ds_write_b128 v22, v[120:123] offset:6144\n"
    "ds_write_b128 v22, v[124:127] offset:7168\n"
    "s_lshl_b32 s56, s52, 9\n"
    "s_add_u32 s56, s56, 0x4000\n"
    "v_lshrrev_b32 v23, 2, v1\n"
    "v_add_u32 v23, s56, v23\n"
    "ds_write_b32 v23, v12\n"
    "ds_write_b32 v23, v13 offset:256\n"
    "v_mov_b32 v32, v12\n"
    "v_mov_b32 v33, v12\n"
    "v_mov_b32 v34, v12\n"
    "v_mov_b32 v35, v12\n"
    "v_mov_b32 v36, v12\n"
    "v_mov_b32 v37, v12\n"
    "v_mov_b32 v38, v12\n"
    "v_mov_b32 v39, v12\n"
    "v_mov_b32 v40, v12\n"
    "v_mov_b32 v41, v12\n"
    "v_mov_b32 v42, v12\n"
    "v_mov_b32 v43, v12\n"
    "v_mov_b32 v44, v12\n"
    "v_mov_b32 v45, v12\n"
    "v_mov_b32 v46, v12\n"
    "v_mov_b32 v47, v12\n"
    "v_mov_b32 v48, v13\n"
    "v_mov_b32 v49, v13\n"
    "v_mov_b32 v50, v13\n"
    "v_mov_b32 v51, v13\n"
    "v_mov_b32 v52, v13\n"
    "v_mov_b32 v53, v13\n"
    "v_mov_b32 v54, v13\n"
    "v_mov_b32 v55, v13\n"
    "v_mov_b32 v56, v13\n"
    "v_mov_b32 v57, v13\n"
    "v_mov_b32 v58, v13\n"
    "v_mov_b32 v59, v13\n"
    "v_mov_b32 v60, v13\n"
    "v_mov_b32 v61, v13\n"
    "v_mov_b32 v62, v13\n"
    "v_mov_b32 v63, v13\n"
    "v_mov_b32 v64, v14\n"
    "v_mov_b32 v65, v14\n"
    "v_mov_b32 v66, v14\n"
    "v_mov_b32 v67, v14\n"
    "v_mov_b32 v68, v14\n"
    "v_mov_b32 v69, v14\n"
    "v_mov_b32 v70, v14\n"
    "v_mov_b32 v71, v14\n"
    "v_mov_b32 v72, v14\n"
    "v_mov_b32 v73, v14\n"
    "v_mov_b32 v74, v14\n"
    "v_mov_b32 v75, v14\n"
    "v_mov_b32 v76, v14\n"
    "v_mov_b32 v77, v14\n"
    "v_mov_b32 v78, v14\n"
    "v_mov_b32 v79, v14\n"
    "v_mov_b32 v80, v15\n"
    "v_mov_b32 v81, v15\n"
    "v_mov_b32 v82, v15\n"
    "v_mov_b32 v83, v15\n"
    "v_mov_b32 v84, v15\n"
    "v_mov_b32 v85, v15\n"
    "v_mov_b32 v86, v15\n"
    "v_mov_b32 v87, v15\n"
    "v_mov_b32 v88, v15\n"
    "v_mov_b32 v89, v15\n"
    "v_mov_b32 v90, v15\n"
    "v_mov_b32 v91, v15\n"
    "v_mov_b32 v92, v15\n"
    "v_mov_b32 v93, v15\n"
    "v_mov_b32 v94, v15\n"
    "v_mov_b32 v95, v15\n"
    "v_mov_b32 v8, 0\n"
    "v_mov_b32 v9, 0\n"
    "v_mov_b32 v10, 0\n"
    "v_mov_b32 v11, 0\n"
    "v_mov_b32 v200, 0\n"
    "v_mov_b32 v201, 0\n"
    "v_mov_b32 v202, 0\n"
    "v_mov_b32 v203, 0\n"
    "v_mov_b32 v204, 0\n"
    "v_mov_b32 v205, 0\n"
    "v_mov_b32 v206, 0\n"
    "v_mov_b32 v207, 0\n"
    "v_mov_b32 v216, 0\n"
    "v_mov_b32 v217, 0\n"
    "v_mov_b32 v218, 0\n"
    "v_mov_b32 v219, 0\n"
    "v_mov_b32 v220, 0\n"
    "v_mov_b32 v221, 0\n"
    "v_mov_b32 v222, 0\n"
    "v_mov_b32 v223, 0\n"
    "s_waitcnt lgkmcnt(0)\n"
    "s_barrier\n"
    "v_xor_b32 v23, 512, v23\n"
    "ds_read_b32 v26, v23\n"
    "ds_read_b32 v27, v23 offset:256\n"
    "ds_read_b128 a[128:131], v2 offset:0\n"
    "ds_read_b128 a[132:135], v2 offset:1024\n"
    "ds_read_b128 a[136:139], v2 offset:2048\n"
    "ds_read_b128 a[140:143], v2 offset:3072\n"
    "ds_read_b128 a[144:147], v2 offset:4096\n"
    "ds_read_b128 a[148:151], v2 offset:5120\n"
    "ds_read_b128 a[152:155], v2 offset:6144\n"
    "ds_read_b128 a[156:159], v2 offset:7168\n"
    "ds_read_b128 a[160:163], v2 offset:8192\n"
    "ds_read_b128 a[164:167], v2 offset:9216\n"
    "ds_read_b128 a[168:171], v2 offset:10240\n"
    "ds_read_b128 a[172:175], v2 offset:11264\n"
    "ds_read_b128 a[176:179], v2 offset:12288\n"
    "s_waitcnt lgkmcnt(8)\n"
    "ds_read_b128 a[180:183], v2 offset:13312\n"
    "ds_read_b128 a[184:187], v2 offset:14336\n"
    "ds_read_b128 a[188:191], v2 offset:15360\n"
    "s_waitcnt lgkmcnt(0)\n"
    "L_join_4_%=:\n"
    "s_lshl_b32 s77, s54, 1\n"
    "s_cmp_eq_u32 s77, 0\n"
    "s_cbranch_scc1 L_l1e_10_%=\n"
    "L_l1_9_%=:\n"
    "s_waitcnt vmcnt(0)\n"
    "s_waitcnt lgkmcnt(0)\n"
    "s_barrier\n"
    "v_mfma_f32_16x16x32_bf16 v[160:163], a[128:131], v[96:99], v[32:35]\n"
    "s_add_u32 s57, s72, 0\n"
    "ds_read_b128 a[192:195], v3 offset:0\n"
    "v_add_f32 v10, v10, v200\n"
    "v_mfma_f32_16x16x32_bf16 v[160:163], a[132:135], v[100:103], v[160:163]\n"
    "s_add_u32 m0, s76, 0\n"
    "s_nop 0\n"
    "buffer_load_dwordx4 v1, s[44:47], s57 offen sc1 lds\n"
    "v_add_f32 v10, v10, v201\n"
    "v_mfma_f32_16x16x32_bf16 v[160:163], a[136:139], v[104:107], v[160:163]\n"
    "ds_read_b128 a[200:203], v3 offset:2048\n"
    "v_add_f32 v10, v10, v202\n"
    "v_mfma_f32_16x16x32_bf16 v[160:163], a[140:143], v[108:111], v[160:163]\n"
    "ds_read_b128 a[208:211], v3 offset:4096\n"
    "v_add_f32 v10, v10, v203\n"
    "v_mfma_f32_16x16x32_bf16 v[164:167], a[144:147], v[96:99], v[36:39]\n"
    "ds_read_b128 a[216:219], v3 offset:6144\n"
    "v_add_f32 v10, v10, v204\n"
    "v_mfma_f32_16x16x32_bf16 v[164:167], a[148:151], v[100:103], v[164:167]\n"
    "buffer_load_dwordx4 v1, s[44:47], s57 offen offset:1024 sc1 lds\n"
    "v_add_f32 v10, v10, v205\n"
    "v_mfma_f32_16x16x32_bf16 v[164:167], a[152:155], v[104:107], v[164:167]\n"
    "ds_read_b128 a[224:227], v3 offset:8192\n"
    "v_add_f32 v10, v10, v206\n"
    "v_mfma_f32_16x16x32_bf16 v[164:167], a[156:159], v[108:111], v[164:167]\n"
    "ds_read_b128 a[232:235], v3 offset:10240\n"
    "v_add_f32 v10, v10, v207\n"
    "v_mfma_f32_16x16x32_bf16 v[176:179], a[128:131], v[112:115], v[48:51]\n"
    "ds_read_b128 a[240:243], v3 offset:12288\n"
    "v_add_f32 v11, v11, v216\n"
    "v_mfma_f32_16x16x32_bf16 v[176:179], a[132:135], v[116:119], v[176:179]\n"
    "v_exp_f32 v160, v160\n"
    "v_add_f32 v11, v11, v220\n"
    "v_mfma_f32_16x16x32_bf16 v[176:179], a[136:139], v[120:123], v[176:179]\n"
    "v_exp_f32 v161, v161\n"
    "v_add_f32 v11, v11, v221\n"
    "v_mfma_f32_16x16x32_bf16 v[176:179], a[140:143], v[124:127], v[176:179]\n"
    "buffer_load_dwordx4 v1, s[44:47], s57 offen offset:2048 sc1 lds\n"
    "v_cvt_pk_bf16_f32 v224, v160, v161\n"
    "v_mfma_f32_16x16x32_bf16 v[180:183], a[144:147], v[112:115], v[52:55]\n"
    "v_exp_f32 v162, v162\n"
    "v_add_f32 v11, v11, v222\n"
    "v_mfma_f32_16x16x32_bf16 v[180:183], a[148:151], v[116:119], v[180:183]\n"
    "v_exp_f32 v163, v163\n"
    "v_add_f32 v11, v11, v223\n"
    "v_mfma_f32_16x16x32_bf16 v[180:183], a[152:155], v[120:123], v[180:183]\n"
    "ds_read_b128 a[248:251], v3 offset:14336\n"
    "v_cvt_pk_bf16_f32 v225, v162, v163\n"
    "v_mfma_f32_16x16x32_bf16 v[180:183], a[156:159], v[124:127], v[180:183]\n"
    "v_exp_f32 v164, v164\n"
    "v_mfma_f32_16x16x32_bf16 v[192:195], a[128:131], v[128:131], v[64:67]\n"
    "buffer_load_dwordx4 v1, s[44:47], s57 offen offset:3072 sc1 lds\n"
    "v_add_f32 v11, v11, v217\n"
    "v_mfma_f32_16x16x32_bf16 v[192:195], a[132:135], v[132:135], v[192:195]\n"
    "v_exp_f32 v165, v165\n"
    "v_mfma_f32_16x16x32_bf16 v[192:195], a[136:139], v[136:139], v[192:195]\n"
    "v_exp_f32 v166, v166\n"
    "v_mfma_f32_16x16x32_bf16 v[192:195], a[140:143], v[140:143], v[192:195]\n"
    "ds_read_b128 a[196:199], v3 offset:1024\n"
    "v_cvt_pk_bf16_f32 v226, v164, v165\n"
    "v_mfma_f32_16x16x32_bf16 v[196:199], a[144:147], v[128:131], v[68:71]\n"
    "v_exp_f32 v167, v167\n"
    "v_mfma_f32_16x16x32_bf16 v[196:199], a[148:151], v[132:135], v[196:199]\n"
    "s_add_u32 s56, s71, 0\n"
    "s_add_u32 s72, s72, s74\n"
    "s_xor_b32 s76, s76, 0x10000\n"
    "v_mfma_f32_16x16x32_bf16 v[196:199], a[152:155], v[136:139], v[196:199]\n"
    "s_add_u32 m0, s75, 0\n"
    "s_nop 0\n"
    "buffer_load_dwordx4 v1, s[44:47], s56 offen sc1 lds\n"
    "v_cvt_pk_bf16_f32 v227, v166, v167\n"
    "v_mfma_f32_16x16x32_bf16 v[196:199], a[156:159], v[140:143], v[196:199]\n"
    "v_exp_f32 v176, v176\n"
    "v_mfma_f32_16x16x32_bf16 v[208:211], a[128:131], v[144:147], v[80:83]\n"
    "ds_read_b128 a[204:207], v3 offset:3072\n"
    "v_add_f32 v11, v11, v218\n"
    "v_mfma_f32_16x16x32_bf16 v[208:211], a[132:135], v[148:151], v[208:211]\n"
    "v_exp_f32 v177, v177\n"
    "v_mfma_f32_16x16x32_bf16 v[208:211], a[136:139], v[152:155], v[208:211]\n"
    "v_exp_f32 v178, v178\n"
    "v_mfma_f32_16x16x32_bf16 v[208:211], a[140:143], v[156:159], v[208:211]\n"
    "buffer_load_dwordx4 v1, s[44:47], s56 offen offset:1024 sc1 lds\n"
    "v_cvt_pk_bf16_f32 v232, v176, v177\n"
    "v_mfma_f32_16x16x32_bf16 v[212:215], a[144:147], v[144:147], v[84:87]\n"
    "v_exp_f32 v179, v179\n"
    "v_mfma_f32_16x16x32_bf16 v[212:215], a[148:151], v[148:151], v[212:215]\n"
    "v_exp_f32 v180, v180\n"
    "v_mfma_f32_16x16x32_bf16 v[212:215], a[152:155], v[152:155], v[212:215]\n"
    "ds_read_b128 a[212:215], v3 offset:5120\n"
    "v_cvt_pk_bf16_f32 v233, v178, v179\n"
    "v_mfma_f32_16x16x32_bf16 v[212:215], a[156:159], v[156:159], v[212:215]\n"
    "v_exp_f32 v181, v181\n"
    "v_mfma_f32_16x16x32_bf16 v[168:171], a[160:163], v[96:99], v[40:43]\n"
    "buffer_load_dwordx4 v1, s[44:47], s56 offen offset:2048 sc1 lds\n"
    "v_cvt_pk_bf16_f32 v234, v180, v181\n"
    "v_mfma_f32_16x16x32_bf16 v[168:171], a[164:167], v[100:103], v[168:171]\n"
    "v_exp_f32 v182, v182\n"
    "v_mfma_f32_16x16x32_bf16 v[168:171], a[168:171], v[104:107], v[168:171]\n"
    "v_exp_f32 v183, v183\n"
    "v_mfma_f32_16x16x32_bf16 v[168:171], a[172:175], v[108:111], v[168:171]\n"
    "ds_read_b128 a[220:223], v3 offset:7168\n"
    "v_cvt_pk_bf16_f32 v235, v182, v183\n"
    "v_mfma_f32_16x16x32_bf16 v[172:175], a[176:179], v[96:99], v[44:47]\n"
    "v_exp_f32 v192, v192\n"
    "v_mfma_f32_16x16x32_bf16 v[172:175], a[180:183], v[100:103], v[172:175]\n"
    "v_exp_f32 v193, v193\n"
    "v_mfma_f32_16x16x32_bf16 v[172:175], a[184:187], v[104:107], v[172:175]\n"
    "buffer_load_dwordx4 v1, s[44:47], s56 offen offset:3072 sc1 lds\n"
    "v_cvt_pk_bf16_f32 v240, v192, v193\n"
    "v_mfma_f32_16x16x32_bf16 v[172:175], a[188:191], v[108:111], v[172:175]\n"
    "s_add_u32 s71, s71, s73\n"
    "s_xor_b32 s75, s75, 0x10000\n"
    "v_mfma_f32_16x16x32_bf16 v[184:187], a[160:163], v[112:115], v[56:59]\n"
    "ds_read_b128 a[228:231], v3 offset:9216\n"
    "v_add_f32 v11, v11, v219\n"
    "v_mfma_f32_16x16x32_bf16 v[184:187], a[164:167], v[116:119], v[184:187]\n"
    "v_exp_f32 v194, v194\n"
    "v_mfma_f32_16x16x32_bf16 v[184:187], a[168:171], v[120:123], v[184:187]\n"
    "v_exp_f32 v195, v195\n"
    "v_mfma_f32_16x16x32_bf16 v[184:187], a[172:175], v[124:127], v[184:187]\n"
    "v_exp_f32 v196, v196\n"
    "v_mfma_f32_16x16x32_bf16 v[188:191], a[176:179], v[112:115], v[60:63]\n"
    "v_exp_f32 v197, v197\n"
    "v_mfma_f32_16x16x32_bf16 v[188:191], a[180:183], v[116:119], v[188:191]\n"
    "v_exp_f32 v198, v198\n"
    "v_mfma_f32_16x16x32_bf16 v[188:191], a[184:187], v[120:123], v[188:191]\n"
    "ds_read_b128 a[236:239], v3 offset:11264\n"
    "v_cvt_pk_bf16_f32 v241, v194, v195\n"
    "v_mfma_f32_16x16x32_bf16 v[188:191], a[188:191], v[124:127], v[188:191]\n"
    "v_exp_f32 v199, v199\n"
    "v_mfma_f32_16x16x32_bf16 v[200:203], a[160:163], v[128:131], v[72:75]\n"
    "v_exp_f32 v208, v208\n"
    "v_mfma_f32_16x16x32_bf16 v[200:203], a[164:167], v[132:135], v[200:203]\n"
    "v_exp_f32 v209, v209\n"
    "v_mfma_f32_16x16x32_bf16 v[200:203], a[168:171], v[136:139], v[200:203]\n"
    "v_exp_f32 v210, v210\n"
    "v_mfma_f32_16x16x32_bf16 v[200:203], a[172:175], v[140:143], v[200:203]\n"
    "ds_read_b128 a[244:247], v3 offset:13312\n"
    "v_cvt_pk_bf16_f32 v242, v196, v197\n"
    "v_mfma_f32_16x16x32_bf16 v[204:207], a[176:179], v[128:131], v[76:79]\n"
    "v_exp_f32 v211, v211\n"
    "v_mfma_f32_16x16x32_bf16 v[204:207], a[180:183], v[132:135], v[204:207]\n"
    "v_exp_f32 v212, v212\n"
    "v_mfma_f32_16x16x32_bf16 v[204:207], a[184:187], v[136:139], v[204:207]\n"
    "s_waitcnt lgkmcnt(8)\n"
    "ds_read_b128 a[252:255], v3 offset:15360\n"
    "v_cvt_pk_bf16_f32 v243, v198, v199\n"
    "v_mfma_f32_16x16x32_bf16 v[204:207], a[188:191], v[140:143], v[204:207]\n"
    "v_xor_b32 v3, 0x10000, v3\n"
    "v_cvt_pk_bf16_f32 v248, v208, v209\n"
    "v_mfma_f32_16x16x32_bf16 v[216:219], a[160:163], v[144:147], v[88:91]\n"
    "v_exp_f32 v213, v213\n"
    "v_mfma_f32_16x16x32_bf16 v[216:219], a[164:167], v[148:151], v[216:219]\n"
    "v_exp_f32 v214, v214\n"
    "v_mfma_f32_16x16x32_bf16 v[216:219], a[168:171], v[152:155], v[216:219]\n"
    "v_exp_f32 v215, v215\n"
    "v_mfma_f32_16x16x32_bf16 v[216:219], a[172:175], v[156:159], v[216:219]\n"
    "v_exp_f32 v168, v168\n"
    "v_mfma_f32_16x16x32_bf16 v[220:223], a[176:179], v[144:147], v[92:95]\n"
    "v_exp_f32 v169, v169\n"
    "v_add_f32 v8, v8, v168\n"
    "v_mfma_f32_16x16x32_bf16 v[220:223], a[180:183], v[148:151], v[220:223]\n"
    "s_waitcnt lgkmcnt(8)\n"
    "v_xor_b32 v2, 0x10000, v2\n"
    "v_cvt_pk_bf16_f32 v249, v210, v211\n"
    "v_mfma_f32_16x16x32_bf16 v[220:223], a[184:187], v[152:155], v[220:223]\n"
    "ds_read_b128 a[128:131], v2 offset:0\n"
    "v_cvt_pk_bf16_f32 v250, v212, v213\n"
    "v_mfma_f32_16x16x32_bf16 v[220:223], a[188:191], v[156:159], v[220:223]\n"
    "v_exp_f32 v170, v170\n"
    "v_add_f32 v8, v8, v169\n"
    "v_mfma_f32_16x16x32_bf16 a[0:3], a[192:195], v[224:227], a[0:3]\n"
    "ds_read_b128 a[132:135], v2 offset:1024\n"
    "v_cvt_pk_bf16_f32 v251, v214, v215\n"
    "v_mfma_f32_16x16x32_bf16 a[4:7], a[200:203], v[224:227], a[4:7]\n"
    "v_exp_f32 v171, v171\n"
    "v_add_f32 v8, v8, v170\n"
    "v_mfma_f32_16x16x32_bf16 a[8:11], a[208:211], v[224:227], a[8:11]\n"
    "v_exp_f32 v172, v172\n"
    "v_add_f32 v8, v8, v171\n"
    "v_mfma_f32_16x16x32_bf16 a[12:15], a[216:219], v[224:227], a[12:15]\n"
    "ds_read_b128 a[136:139], v2 offset:2048\n"
    "v_cvt_pk_bf16_f32 v228, v168, v169\n"
    "v_mfma_f32_16x16x32_bf16 a[16:19], a[224:227], v[224:227], a[16:19]\n"
    "v_exp_f32 v173, v173\n"
    "v_add_f32 v8, v8, v172\n"
    "v_mfma_f32_16x16x32_bf16 a[20:23], a[232:235], v[224:227], a[20:23]\n"
    "v_exp_f32 v174, v174\n"
    "v_add_f32 v8, v8, v173\n"
    "v_mfma_f32_16x16x32_bf16 a[24:27], a[240:243], v[224:227], a[24:27]\n"
    "ds_read_b128 a[140:143], v2 offset:3072\n"
    "v_cvt_pk_bf16_f32 v229, v170, v171\n"
    "v_mfma_f32_16x16x32_bf16 a[28:31], a[248:251], v[224:227], a[28:31]\n"
    "v_exp_f32 v175, v175\n"
    "v_add_f32 v8, v8, v174\n"
    "v_mfma_f32_16x16x32_bf16 a[32:35], a[192:195], v[232:235], a[32:35]\n"
    "ds_read_b128 a[144:147], v2 offset:4096\n"
    "v_cvt_pk_bf16_f32 v230, v172, v173\n"
    "v_mfma_f32_16x16x32_bf16 a[36:39], a[200:203], v[232:235], a[36:39]\n"
    "v_exp_f32 v184, v184\n"
    "v_add_f32 v8, v8, v175\n"
    "v_mfma_f32_16x16x32_bf16 a[40:43], a[208:211], v[232:235], a[40:43]\n"
    "v_exp_f32 v185, v185\n"
    "v_add_f32 v9, v9, v184\n"
    "v_mfma_f32_16x16x32_bf16 a[44:47], a[216:219], v[232:235], a[44:47]\n"
    "ds_read_b128 a[148:151], v2 offset:5120\n"
    "v_cvt_pk_bf16_f32 v231, v174, v175\n"
    "v_mfma_f32_16x16x32_bf16 a[48:51], a[224:227], v[232:235], a[48:51]\n"
    "v_exp_f32 v186, v186\n"
    "v_add_f32 v9, v9, v185\n"
    "v_mfma_f32_16x16x32_bf16 a[52:55], a[232:235], v[232:235], a[52:55]\n"
    "v_exp_f32 v187, v187\n"
    "v_add_f32 v9, v9, v186\n"
    "v_mfma_f32_16x16x32_bf16 a[56:59], a[240:243], v[232:235], a[56:59]\n"
    "ds_read_b128 a[152:155], v2 offset:6144\n"
    "v_cvt_pk_bf16_f32 v236, v184, v185\n"
    "v_mfma_f32_16x16x32_bf16 a[60:63], a[248:251], v[232:235], a[60:63]\n"
    "v_exp_f32 v188, v188\n"
    "v_add_f32 v9, v9, v187\n"
    "v_mfma_f32_16x16x32_bf16 a[64:67], a[192:195], v[240:243], a[64:67]\n"
    "s_waitcnt lgkmcnt(8)\n"
    "ds_read_b128 a[156:159], v2 offset:7168\n"
    "v_cvt_pk_bf16_f32 v237, v186, v187\n"
    "v_mfma_f32_16x16x32_bf16 a[68:71], a[200:203], v[240:243], a[68:71]\n"
    "v_exp_f32 v189, v189\n"
    "v_add_f32 v9, v9, v188\n"
    "v_mfma_f32_16x16x32_bf16 a[72:75], a[208:211], v[240:243], a[72:75]\n"
    "v_exp_f32 v190, v190\n"
    "v_add_f32 v9, v9, v189\n"
    "v_mfma_f32_16x16x32_bf16 a[76:79], a[216:219], v[240:243], a[76:79]\n"
    "ds_read_b128 a[160:163], v2 offset:8192\n"
    "v_cvt_pk_bf16_f32 v238, v188, v189\n"
    "v_mfma_f32_16x16x32_bf16 a[80:83], a[224:227], v[240:243], a[80:83]\n"
    "v_exp_f32 v191, v191\n"
    "v_add_f32 v9, v9, v190\n"
    "v_mfma_f32_16x16x32_bf16 a[84:87], a[232:235], v[240:243], a[84:87]\n"
    "v_exp_f32 v200, v200\n"
    "v_add_f32 v9, v9, v191\n"
    "v_mfma_f32_16x16x32_bf16 a[88:91], a[240:243], v[240:243], a[88:91]\n"
    "ds_read_b128 a[164:167], v2 offset:9216\n"
    "v_cvt_pk_bf16_f32 v239, v190, v191\n"
    "v_mfma_f32_16x16x32_bf16 a[92:95], a[248:251], v[240:243], a[92:95]\n"
    "v_exp_f32 v201, v201\n"
    "v_mfma_f32_16x16x32_bf16 a[96:99], a[192:195], v[248:251], a[96:99]\n"
    "ds_read_b128 a[168:171], v2 offset:10240\n"
    "v_cvt_pk_bf16_f32 v244, v200, v201\n"
    "v_mfma_f32_16x16x32_bf16 a[100:103], a[200:203], v[248:251], a[100:103]\n"
    "v_exp_f32 v202, v202\n"
    "v_mfma_f32_16x16x32_bf16 a[104:107], a[208:211], v[248:251], a[104:107]\n"
    "v_exp_f32 v203, v203\n"
    "v_mfma_f32_16x16x32_bf16 a[108:111], a[216:219], v[248:251], a[108:111]\n"
    "ds_read_b128 a[172:175], v2 offset:11264\n"
    "v_cvt_pk_bf16_f32 v245, v202, v203\n"
    "v_mfma_f32_16x16x32_bf16 a[112:115], a[224:227], v[248:251], a[112:115]\n"
    "v_exp_f32 v204, v204\n"
    "v_mfma_f32_16x16x32_bf16 a[116:119], a[232:235], v[248:251], a[116:119]\n"
    "s_waitcnt lgkmcnt(12)\n"
    "v_exp_f32 v205, v205\n"
    "v_mfma_f32_16x16x32_bf16 a[120:123], a[240:243], v[248:251], a[120:123]\n"
    "ds_read_b128 a[176:179], v2 offset:12288\n"
    "v_cvt_pk_bf16_f32 v246, v204, v205\n"
    "v_mfma_f32_16x16x32_bf16 a[124:127], a[248:251], v[248:251], a[124:127]\n"
    "v_exp_f32 v206, v206\n"
    "v_mfma_f32_16x16x32_bf16 a[0:3], a[196:199], v[228:231], a[0:3]\n"
    "ds_read_b128 a[180:183], v2 offset:13312\n"
    "v_add_f32 v8, v8, v160\n"
    "v_mfma_f32_16x16x32_bf16 a[4:7], a[204:207], v[228:231], a[4:7]\n"
    "v_exp_f32 v207, v207\n"
    "v_mfma_f32_16x16x32_bf16 a[8:11], a[212:215], v[228:231], a[8:11]\n"
    "v_exp_f32 v216, v216\n"
    "v_mfma_f32_16x16x32_bf16 a[12:15], a[220:223], v[228:231], a[12:15]\n"
    "ds_read_b128 a[184:187], v2 offset:14336\n"
    "v_cvt_pk_bf16_f32 v247, v206, v207\n"
    "v_mfma_f32_16x16x32_bf16 a[16:19], a[228:231], v[228:231], a[16:19]\n"
    "v_exp_f32 v217, v217\n"
    "v_mfma_f32_16x16x32_bf16 a[20:23], a[236:239], v[228:231], a[20:23]\n"
    "v_exp_f32 v218, v218\n"
    "v_mfma_f32_16x16x32_bf16 a[24:27], a[244:247], v[228:231], a[24:27]\n"
    "s_waitcnt lgkmcnt(8)\n"
    "ds_read_b128 a[188:191], v2 offset:15360\n"
    "v_cvt_pk_bf16_f32 v252, v216, v217\n"
    "v_mfma_f32_16x16x32_bf16 a[28:31], a[252:255], v[228:231], a[28:31]\n"
    "v_exp_f32 v219, v219\n"
    "v_mfma_f32_16x16x32_bf16 a[32:35], a[196:199], v[236:239], a[32:35]\n"
    "v_exp_f32 v220, v220\n"
    "v_mfma_f32_16x16x32_bf16 a[36:39], a[204:207], v[236:239], a[36:39]\n"
    "v_exp_f32 v221, v221\n"
    "v_mfma_f32_16x16x32_bf16 a[40:43], a[212:215], v[236:239], a[40:43]\n"
    "v_exp_f32 v222, v222\n"
    "v_mfma_f32_16x16x32_bf16 a[44:47], a[220:223], v[236:239], a[44:47]\n"
    "v_exp_f32 v223, v223\n"
    "v_mfma_f32_16x16x32_bf16 a[48:51], a[228:231], v[236:239], a[48:51]\n"
    "v_cvt_pk_bf16_f32 v253, v218, v219\n"
    "v_cvt_pk_bf16_f32 v254, v220, v221\n"
    "v_mfma_f32_16x16x32_bf16 a[52:55], a[236:239], v[236:239], a[52:55]\n"
    "v_cvt_pk_bf16_f32 v255, v222, v223\n"
    "v_add_f32 v8, v8, v161\n"
    "v_mfma_f32_16x16x32_bf16 a[56:59], a[244:247], v[236:239], a[56:59]\n"
    "v_add_f32 v8, v8, v162\n"
    "v_mfma_f32_16x16x32_bf16 a[60:63], a[252:255], v[236:239], a[60:63]\n"
    "v_add_f32 v8, v8, v163\n"
    "v_add_f32 v8, v8, v164\n"
    "v_mfma_f32_16x16x32_bf16 a[64:67], a[196:199], v[244:247], a[64:67]\n"
    "v_add_f32 v8, v8, v165\n"
    "v_add_f32 v8, v8, v166\n"
    "v_mfma_f32_16x16x32_bf16 a[68:71], a[204:207], v[244:247], a[68:71]\n"
    "v_add_f32 v8, v8, v167\n"
    "v_mfma_f32_16x16x32_bf16 a[72:75], a[212:215], v[244:247], a[72:75]\n"
    "v_add_f32 v9, v9, v176\n"
    "v_add_f32 v9, v9, v177\n"
    "v_mfma_f32_16x16x32_bf16 a[76:79], a[220:223], v[244:247], a[76:79]\n"
    "v_add_f32 v9, v9, v178\n"
    "v_add_f32 v9, v9, v179\n"
    "v_mfma_f32_16x16x32_bf16 a[80:83], a[228:231], v[244:247], a[80:83]\n"
    "v_add_f32 v9, v9, v180\n"
    "v_mfma_f32_16x16x32_bf16 a[84:87], a[236:239], v[244:247], a[84:87]\n"
    "v_add_f32 v9, v9, v181\n"
    "v_add_f32 v9, v9, v182\n"
    "v_mfma_f32_16x16x32_bf16 a[88:91], a[244:247], v[244:247], a[88:91]\n"
    "v_add_f32 v9, v9, v183\n"
    "v_add_f32 v10, v10, v192\n"
    "v_mfma_f32_16x16x32_bf16 a[92:95], a[252:255], v[244:247], a[92:95]\n"
    "v_add_f32 v10, v10, v193\n"
    "v_mfma_f32_16x16x32_bf16 a[96:99], a[196:199], v[252:255], a[96:99]\n"
    "v_add_f32 v10, v10, v194\n"
    "v_add_f32 v10, v10, v195\n"
    "v_mfma_f32_16x16x32_bf16 a[100:103], a[204:207], v[252:255], a[100:103]\n"
    "v_add_f32 v10, v10, v196\n"
    "v_add_f32 v10, v10, v197\n"
    "v_mfma_f32_16x16x32_bf16 a[104:107], a[212:215], v[252:255], a[104:107]\n"
    "v_add_f32 v10, v10, v198\n"
    "v_mfma_f32_16x16x32_bf16 a[108:111], a[220:223], v[252:255], a[108:111]\n"
    "v_add_f32 v10, v10, v199\n"
    "v_add_f32 v11, v11, v208\n"
    "v_mfma_f32_16x16x32_bf16 a[112:115], a[228:231], v[252:255], a[112:115]\n"
    "v_add_f32 v11, v11, v209\n"
    "v_add_f32 v11, v11, v210\n"
    "v_mfma_f32_16x16x32_bf16 a[116:119], a[236:239], v[252:255], a[116:119]\n"
    "v_add_f32 v11, v11, v211\n"
    "v_mfma_f32_16x16x32_bf16 a[120:123], a[244:247], v[252:255], a[120:123]\n"
    "v_add_f32 v11, v11, v212\n"
    "v_add_f32 v11, v11, v213\n"
    "v_mfma_f32_16x16x32_bf16 a[124:127], a[252:255], v[252:255], a[124:127]\n"
    "v_add_f32 v11, v11, v214\n"
    "v_add_f32 v11, v11, v215\n"
    "s_sub_u32 s77, s77, 1\n"
    "s_cmp_lg_u32 s77, 0\n"
    "s_cbranch_scc1 L_l1_9_%=\n"
    "L_l1e_10_%=:\n"
    "s_lshl_b32 s80, s54, 7\n"
    "s_mov_b32 s73, 0x8000\n"
    "s_sub_i32 s58, s79, s80\n"
    "v_add_u32 v16, s58, v5\n"
    "v_cmp_le_i32 s[84:85], 0, v16\n"
    "v_cmp_le_i32 s[86:87], 1, v16\n"
    "v_cmp_le_i32 s[88:89], 2, v16\n"
    "v_cndmask_b32 v64, v4, v14, s[84:85]\n"
    "v_cmp_le_i32 s[90:91], 3, v16\n"
    "v_cndmask_b32 v65, v4, v14, s[86:87]\n"
    "v_cmp_le_i32 s[84:85], 16, v16\n"
    "v_cndmask_b32 v66, v4, v14, s[88:89]\n"
    "v_cmp_le_i32 s[86:87], 17, v16\n"
    "v_cndmask_b32 v67, v4, v14, s[90:91]\n"
    "v_cmp_le_i32 s[88:89], 18, v16\n"
    "v_cndmask_b32 v68, v4, v14, s[84:85]\n"
    "v_cmp_le_i32 s[90:91], 19, v16\n"
    "v_cndmask_b32 v69, v4, v14, s[86:87]\n"
    "v_cmp_le_i32 s[84:85], 32, v16\n"
    "v_cndmask_b32 v70, v4, v14, s[88:89]\n"
    "v_cmp_le_i32 s[86:87], 33, v16\n"
    "v_cndmask_b32 v71, v4, v14, s[90:91]\n"
    "v_cmp_le_i32 s[88:89], 34, v16\n"
    "v_cndmask_b32 v72, v4, v14, s[84:85]\n"
    "v_cmp_le_i32 s[90:91], 35, v16\n"
    "v_cndmask_b32 v73, v4, v14, s[86:87]\n"
    "v_cmp_le_i32 s[84:85], 48, v16\n"
    "v_cndmask_b32 v74, v4, v14, s[88:89]\n"
    "v_cmp_le_i32 s[86:87], 49, v16\n"
    "v_cndmask_b32 v75, v4, v14, s[90:91]\n"
    "v_cmp_le_i32 s[88:89], 50, v16\n"
    "v_cndmask_b32 v76, v4, v14, s[84:85]\n"
    "v_cmp_le_i32 s[90:91], 51, v16\n"
    "v_cndmask_b32 v77, v4, v14, s[86:87]\n"
    "v_cmp_le_i32 s[84:85], -16, v16\n"
    "v_cndmask_b32 v78, v4, v14, s[88:89]\n"
    "v_cmp_le_i32 s[86:87], -15, v16\n"
    "v_cndmask_b32 v79, v4, v14, s[90:91]\n"
    "v_cmp_le_i32 s[88:89], -14, v16\n"
    "v_cndmask_b32 v80, v4, v15, s[84:85]\n"
    "v_cmp_le_i32 s[90:91], -13, v16\n"
    "v_cndmask_b32 v81, v4, v15, s[86:87]\n"
    "v_cmp_le_i32 s[84:85], 0, v16\n"
    "v_cndmask_b32 v82, v4, v15, s[88:89]\n"
    "v_cmp_le_i32 s[86:87], 1, v16\n"
    "v_cndmask_b32 v83, v4, v15, s[90:91]\n"
    "v_cmp_le_i32 s[88:89], 2, v16\n"
    "v_cndmask_b32 v84, v4, v15, s[84:85]\n"
    "v_cmp_le_i32 s[90:91], 3, v16\n"
    "v_cndmask_b32 v85, v4, v15, s[86:87]\n"
    "v_cmp_le_i32 s[84:85], 16, v16\n"
    "v_cndmask_b32 v86, v4, v15, s[88:89]\n"
    "v_cmp_le_i32 s[86:87], 17, v16\n"
    "v_cndmask_b32 v87, v4, v15, s[90:91]\n"
    "v_cmp_le_i32 s[88:89], 18, v16\n"
    "v_cndmask_b32 v88, v4, v15, s[84:85]\n"
    "v_cmp_le_i32 s[90:91], 19, v16\n"
    "v_cndmask_b32 v89, v4, v15, s[86:87]\n"
    "v_cmp_le_i32 s[84:85], 32, v16\n"
    "v_cndmask_b32 v90, v4, v15, s[88:89]\n"
    "v_cmp_le_i32 s[86:87], 33, v16\n"
    "v_cndmask_b32 v91, v4, v15, s[90:91]\n"
    "v_cmp_le_i32 s[88:89], 34, v16\n"
    "v_cndmask_b32 v92, v4, v15, s[84:85]\n"
    "v_cmp_le_i32 s[90:91], 35, v16\n"
    "v_cndmask_b32 v93, v4, v15, s[86:87]\n"
    "v_cndmask_b32 v94, v4, v15, s[88:89]\n"
    "v_cndmask_b32 v95, v4, v15, s[90:91]\n"
    "s_waitcnt vmcnt(0)\n"
    "s_waitcnt lgkmcnt(0)\n"
    "s_barrier\n"
    "v_mfma_f32_16x16x32_bf16 v[160:163], a[128:131], v[96:99], v[32:35]\n"
    "s_add_u32 s57, s72, 0\n"
    "ds_read_b128 a[192:195], v3 offset:0\n"
    "v_add_f32 v10, v10, v200\n"
    "v_mfma_f32_16x16x32_bf16 v[160:163], a[132:135], v[100:103], v[160:163]\n"
    "s_add_u32 m0, s76, 0\n"
    "s_nop 0\n"
    "buffer_load_dwordx4 v1, s[44:47], s57 offen sc1 lds\n"
    "v_add_f32 v10, v10, v201\n"
    "v_mfma_f32_16x16x32_bf16 v[160:163], a[136:139], v[104:107], v[160:163]\n"
    "ds_read_b128 a[200:203], v3 offset:2048\n"
    "v_add_f32 v10, v10, v202\n"
    "v_mfma_f32_16x16x32_bf16 v[160:163], a[140:143], v[108:111], v[160:163]\n"
    "ds_read_b128 a[208:211], v3 offset:4096\n"
    "v_add_f32 v10, v10, v203\n"
    "v_mfma_f32_16x16x32_bf16 v[164:167], a[144:147], v[96:99], v[36:39]\n"
    "ds_read_b128 a[216:219], v3 offset:6144\n"
    "v_add_f32 v10, v10, v204\n"
    "v_mfma_f32_16x16x32_bf16 v[164:167], a[148:151], v[100:103], v[164:167]\n"
    "buffer_load_dwordx4 v1, s[44:47], s57 offen offset:1024 sc1 lds\n"
    "v_add_f32 v10, v10, v205\n"
    "v_mfma_f32_16x16x32_bf16 v[164:167], a[152:155], v[104:107], v[164:167]\n"
    "ds_read_b128 a[224:227], v3 offset:8192\n"
    "v_add_f32 v10, v10, v206\n"
    "v_mfma_f32_16x16x32_bf16 v[164:167], a[156:159], v[108:111], v[164:167]\n"
    "ds_read_b128 a[232:235], v3 offset:10240\n"
    "v_add_f32 v10, v10, v207\n"
    "v_mfma_f32_16x16x32_bf16 v[176:179], a[128:131], v[112:115], v[48:51]\n"
    "ds_read_b128 a[240:243], v3 offset:12288\n"
    "v_add_f32 v11, v11, v216\n"
    "v_mfma_f32_16x16x32_bf16 v[176:179], a[132:135], v[116:119], v[176:179]\n"
    "v_exp_f32 v160, v160\n"
    "v_add_f32 v11, v11, v222\n"
    "v_mfma_f32_16x16x32_bf16 v[176:179], a[136:139], v[120:123], v[176:179]\n"
    "v_exp_f32 v161, v161\n"
    "v_add_f32 v11, v11, v223\n"
    "v_mfma_f32_16x16x32_bf16 v[176:179], a[140:143], v[124:127], v[176:179]\n"
    "buffer_load_dwordx4 v1, s[44:47], s57 offen offset:2048 sc1 lds\n"
    "v_cvt_pk_bf16_f32 v224, v160, v161\n"
    "v_mfma_f32_16x16x32_bf16 v[180:183], a[144:147], v[112:115], v[52:55]\n"
    "v_exp_f32 v162, v162\n"
    "v_mfma_f32_16x16x32_bf16 v[180:183], a[148:151], v[116:119], v[180:183]\n"
    "v_exp_f32 v163, v163\n"
    "v_mfma_f32_16x16x32_bf16 v[180:183], a[152:155], v[120:123], v[180:183]\n"
    "ds_read_b128 a[248:251], v3 offset:14336\n"
    "v_cvt_pk_bf16_f32 v225, v162, v163\n"
    "v_mfma_f32_16x16x32_bf16 v[180:183], a[156:159], v[124:127], v[180:183]\n"
    "v_exp_f32 v164, v164\n"
    "v_mfma_f32_16x16x32_bf16 v[192:195], a[128:131], v[128:131], v[64:67]\n"
    "buffer_load_dwordx4 v1, s[44:47], s57 offen offset:3072 sc1 lds\n"
    "v_add_f32 v11, v11, v217\n"
    "v_mfma_f32_16x16x32_bf16 v[192:195], a[132:135], v[132:135], v[192:195]\n"
    "v_exp_f32 v165, v165\n"
    "v_mfma_f32_16x16x32_bf16 v[192:195], a[136:139], v[136:139], v[192:195]\n"
    "v_exp_f32 v166, v166\n"
    "v_mfma_f32_16x16x32_bf16 v[192:195], a[140:143], v[140:143], v[192:195]\n"
    "ds_read_b128 a[196:199], v3 offset:1024\n"
    "v_cvt_pk_bf16_f32 v226, v164, v165\n"
    "v_mfma_f32_16x16x32_bf16 v[196:199], a[144:147], v[128:131], v[68:71]\n"
    "v_exp_f32 v167, v167\n"
    "v_mfma_f32_16x16x32_bf16 v[196:199], a[148:151], v[132:135], v[196:199]\n"
    "s_add_u32 s57, s72, 16384\n"
    "v_exp_f32 v176, v176\n"
    "v_mfma_f32_16x16x32_bf16 v[196:199], a[152:155], v[136:139], v[196:199]\n"
    "s_add_u32 m0, s76, 16384\n"
    "s_nop 0\n"
    "buffer_load_dwordx4 v1, s[44:47], s57 offen sc1 lds\n"
    "v_cvt_pk_bf16_f32 v227, v166, v167\n"
    "v_mfma_f32_16x16x32_bf16 v[196:199], a[156:159], v[140:143], v[196:199]\n"
    "v_exp_f32 v177, v177\n"
    "v_mfma_f32_16x16x32_bf16 v[208:211], a[128:131], v[144:147], v[80:83]\n"
    "ds_read_b128 a[204:207], v3 offset:3072\n"
    "v_cvt_pk_bf16_f32 v232, v176, v177\n"
    "v_mfma_f32_16x16x32_bf16 v[208:211], a[132:135], v[148:151], v[208:211]\n"
    "v_exp_f32 v178, v178\n"
    "v_mfma_f32_16x16x32_bf16 v[208:211], a[136:139], v[152:155], v[208:211]\n"
    "v_exp_f32 v179, v179\n"
    "v_mfma_f32_16x16x32_bf16 v[208:211], a[140:143], v[156:159], v[208:211]\n"
    "buffer_load_dwordx4 v1, s[44:47], s57 offen offset:1024 sc1 lds\n"
    "v_cvt_pk_bf16_f32 v233, v178, v179\n"
    "v_mfma_f32_16x16x32_bf16 v[212:215], a[144:147], v[144:147], v[84:87]\n"
    "v_exp_f32 v180, v180\n"
    "v_mfma_f32_16x16x32_bf16 v[212:215], a[148:151], v[148:151], v[212:215]\n"
    "v_exp_f32 v181, v181\n"
    "v_mfma_f32_16x16x32_bf16 v[212:215], a[152:155], v[152:155], v[212:215]\n"
    "ds_read_b128 a[212:215], v3 offset:5120\n"
    "v_cvt_pk_bf16_f32 v234, v180, v181\n"
    "v_mfma_f32_16x16x32_bf16 v[212:215], a[156:159], v[156:159], v[212:215]\n"
    "v_exp_f32 v182, v182\n"
    "v_mfma_f32_16x16x32_bf16 v[168:171], a[160:163], v[96:99], v[40:43]\n"
    "buffer_load_dwordx4 v1, s[44:47], s57 offen offset:2048 sc1 lds\n"
    "v_add_f32 v11, v11, v218\n"
    "v_mfma_f32_16x16x32_bf16 v[168:171], a[164:167], v[100:103], v[168:171]\n"
    "v_exp_f32 v183, v183\n"
    "v_mfma_f32_16x16x32_bf16 v[168:171], a[168:171], v[104:107], v[168:171]\n"
    "v_exp_f32 v192, v192\n"
    "v_mfma_f32_16x16x32_bf16 v[168:171], a[172:175], v[108:111], v[168:171]\n"
    "ds_read_b128 a[220:223], v3 offset:7168\n"
    "v_cvt_pk_bf16_f32 v235, v182, v183\n"
    "v_mfma_f32_16x16x32_bf16 v[172:175], a[176:179], v[96:99], v[44:47]\n"
    "v_exp_f32 v193, v193\n"
    "v_mfma_f32_16x16x32_bf16 v[172:175], a[180:183], v[100:103], v[172:175]\n"
    "v_exp_f32 v194, v194\n"
    "v_mfma_f32_16x16x32_bf16 v[172:175], a[184:187], v[104:107], v[172:175]\n"
    "buffer_load_dwordx4 v1, s[44:47], s57 offen offset:3072 sc1 lds\n"
    "v_cvt_pk_bf16_f32 v240, v192, v193\n"
    "v_mfma_f32_16x16x32_bf16 v[172:175], a[188:191], v[108:111], v[172:175]\n"
    "v_exp_f32 v195, v195\n"
    "v_mfma_f32_16x16x32_bf16 v[184:187], a[160:163], v[112:115], v[56:59]\n"
    "ds_read_b128 a[228:231], v3 offset:9216\n"
    "v_cvt_pk_bf16_f32 v241, v194, v195\n"
    "v_mfma_f32_16x16x32_bf16 v[184:187], a[164:167], v[116:119], v[184:187]\n"
    "v_exp_f32 v196, v196\n"
    "v_mfma_f32_16x16x32_bf16 v[184:187], a[168:171], v[120:123], v[184:187]\n"
    "s_add_u32 s56, s71, 0\n"
    "s_add_u32 s72, s72, s74\n"
    "s_xor_b32 s76, s76, 0x10000\n"
    "v_mfma_f32_16x16x32_bf16 v[184:187], a[172:175], v[124:127], v[184:187]\n"
    "s_add_u32 m0, s75, 0\n"
    "s_nop 0\n"
    "buffer_load_dwordx4 v1, s[44:47], s56 offen sc1 lds\n"
    "v_add_f32 v11, v11, v219\n"
    "v_mfma_f32_16x16x32_bf16 v[188:191], a[176:179], v[112:115], v[60:63]\n"
    "v_exp_f32 v197, v197\n"
    "v_mfma_f32_16x16x32_bf16 v[188:191], a[180:183], v[116:119], v[188:191]\n"
    "v_exp_f32 v198, v198\n"
    "v_mfma_f32_16x16x32_bf16 v[188:191], a[184:187], v[120:123], v[188:191]\n"
    "ds_read_b128 a[236:239], v3 offset:11264\n"
    "v_cvt_pk_bf16_f32 v242, v196, v197\n"
    "v_mfma_f32_16x16x32_bf16 v[188:191], a[188:191], v[124:127], v[188:191]\n"
    "v_exp_f32 v199, v199\n"
    "v_mfma_f32_16x16x32_bf16 v[200:203], a[160:163], v[128:131], v[72:75]\n"
    "buffer_load_dwordx4 v1, s[44:47], s56 offen offset:1024 sc1 lds\n"
    "v_cvt_pk_bf16_f32 v243, v198, v199\n"
    "v_mfma_f32_16x16x32_bf16 v[200:203], a[164:167], v[132:135], v[200:203]\n"
    "v_exp_f32 v208, v208\n"
    "v_mfma_f32_16x16x32_bf16 v[200:203], a[168:171], v[136:139], v[200:203]\n"
    "v_exp_f32 v209, v209\n"
    "v_mfma_f32_16x16x32_bf16 v[200:203], a[172:175], v[140:143], v[200:203]\n"
    "ds_read_b128 a[244:247], v3 offset:13312\n"
    "v_cvt_pk_bf16_f32 v248, v208, v209\n"
    "v_mfma_f32_16x16x32_bf16 v[204:207], a[176:179], v[128:131], v[76:79]\n"
    "v_exp_f32 v210, v210\n"
    "v_add_f32 v11, v11, v209\n"
    "v_mfma_f32_16x16x32_bf16 v[204:207], a[180:183], v[132:135], v[204:207]\n"
    "v_exp_f32 v211, v211\n"
    "v_add_f32 v11, v11, v210\n"
    "v_mfma_f32_16x16x32_bf16 v[204:207], a[184:187], v[136:139], v[204:207]\n"
    "buffer_load_dwordx4 v1, s[44:47], s56 offen offset:2048 sc1 lds\n"
    "v_cvt_pk_bf16_f32 v249, v210, v211\n"
    "v_mfma_f32_16x16x32_bf16 v[204:207], a[188:191], v[140:143], v[204:207]\n"
    "v_exp_f32 v212, v212\n"
    "v_add_f32 v11, v11, v211\n"
    "v_mfma_f32_16x16x32_bf16 v[216:219], a[160:163], v[144:147], v[88:91]\n"
    "s_waitcnt lgkmcnt(8)\n"
    "ds_read_b128 a[252:255], v3 offset:15360\n"
    "v_add_f32 v11, v11, v220\n"
    "v_mfma_f32_16x16x32_bf16 v[216:219], a[164:167], v[148:151], v[216:219]\n"
    "v_xor_b32 v3, 0x10000, v3\n"
    "v_add_f32 v11, v11, v221\n"
    "v_mfma_f32_16x16x32_bf16 v[216:219], a[168:171], v[152:155], v[216:219]\n"
    "v_exp_f32 v213, v213\n"
    "v_add_f32 v11, v11, v212\n"
    "v_mfma_f32_16x16x32_bf16 v[216:219], a[172:175], v[156:159], v[216:219]\n"
    "buffer_load_dwordx4 v1, s[44:47], s56 offen offset:3072 sc1 lds\n"
    "v_cvt_pk_bf16_f32 v250, v212, v213\n"
    "v_mfma_f32_16x16x32_bf16 v[220:223], a[176:179], v[144:147], v[92:95]\n"
    "v_exp_f32 v214, v214\n"
    "v_add_f32 v11, v11, v213\n"
    "v_mfma_f32_16x16x32_bf16 v[220:223], a[180:183], v[148:151], v[220:223]\n"
    "s_waitcnt lgkmcnt(8)\n"
    "v_xor_b32 v2, 0x10000, v2\n"
    "v_add_f32 v8, v8, v160\n"
    "v_mfma_f32_16x16x32_bf16 v[220:223], a[184:187], v[152:155], v[220:223]\n"
    "ds_read_b128 a[128:131], v2 offset:0\n"
    "v_add_f32 v8, v8, v161\n"
    "v_mfma_f32_16x16x32_bf16 v[220:223], a[188:191], v[156:159], v[220:223]\n"
    "s_add_u32 s56, s71, 16384\n"
    "v_exp_f32 v215, v215\n"
    "v_add_f32 v11, v11, v214\n"
    "v_mfma_f32_16x16x32_bf16 a[0:3], a[192:195], v[224:227], a[0:3]\n"
    "s_add_u32 m0, s75, 16384\n"
    "s_nop 0\n"
    "buffer_load_dwordx4 v1, s[44:47], s56 offen sc1 lds\n"
    "v_cvt_pk_bf16_f32 v251, v214, v215\n"
    "v_mfma_f32_16x16x32_bf16 a[4:7], a[200:203], v[224:227], a[4:7]\n"
    "v_exp_f32 v168, v168\n"
    "v_add_f32 v11, v11, v215\n"
    "v_mfma_f32_16x16x32_bf16 a[8:11], a[208:211], v[224:227], a[8:11]\n"
    "v_exp_f32 v169, v169\n"
    "v_add_f32 v8, v8, v168\n"
    "v_mfma_f32_16x16x32_bf16 a[12:15], a[216:219], v[224:227], a[12:15]\n"
    "ds_read_b128 a[132:135], v2 offset:1024\n"
    "v_cvt_pk_bf16_f32 v228, v168, v169\n"
    "v_mfma_f32_16x16x32_bf16 a[16:19], a[224:227], v[224:227], a[16:19]\n"
    "v_exp_f32 v170, v170\n"
    "v_add_f32 v8, v8, v169\n"
    "v_mfma_f32_16x16x32_bf16 a[20:23], a[232:235], v[224:227], a[20:23]\n"
    "v_exp_f32 v171, v171\n"
    "v_add_f32 v8, v8, v170\n"
    "v_mfma_f32_16x16x32_bf16 a[24:27], a[240:243], v[224:227], a[24:27]\n"
    "buffer_load_dwordx4 v1, s[44:47], s56 offen offset:1024 sc1 lds\n"
    "v_cvt_pk_bf16_f32 v229, v170, v171\n"
    "v_mfma_f32_16x16x32_bf16 a[28:31], a[248:251], v[224:227], a[28:31]\n"
    "v_exp_f32 v172, v172\n"
    "v_add_f32 v8, v8, v171\n"
    "v_mfma_f32_16x16x32_bf16 a[32:35], a[192:195], v[232:235], a[32:35]\n"
    "ds_read_b128 a[136:139], v2 offset:2048\n"
    "v_add_f32 v8, v8, v162\n"
    "v_mfma_f32_16x16x32_bf16 a[36:39], a[200:203], v[232:235], a[36:39]\n"
    "v_exp_f32 v173, v173\n"
    "v_add_f32 v8, v8, v172\n"
    "v_mfma_f32_16x16x32_bf16 a[40:43], a[208:211], v[232:235], a[40:43]\n"
    "v_exp_f32 v174, v174\n"
    "v_add_f32 v8, v8, v173\n"
    "v_mfma_f32_16x16x32_bf16 a[44:47], a[216:219], v[232:235], a[44:47]\n"
    "buffer_load_dwordx4 v1, s[44:47], s56 offen offset:2048 sc1 lds\n"
    "v_cvt_pk_bf16_f32 v230, v172, v173\n"
    "v_mfma_f32_16x16x32_bf16 a[48:51], a[224:227], v[232:235], a[48:51]\n"
    "v_exp_f32 v175, v175\n"
    "v_add_f32 v8, v8, v174\n"
    "v_mfma_f32_16x16x32_bf16 a[52:55], a[232:235], v[232:235], a[52:55]\n"
    "v_exp_f32 v184, v184\n"
    "v_add_f32 v8, v8, v175\n"
    "v_mfma_f32_16x16x32_bf16 a[56:59], a[240:243], v[232:235], a[56:59]\n"
    "ds_read_b128 a[140:143], v2 offset:3072\n"
    "v_cvt_pk_bf16_f32 v231, v174, v175\n"
    "v_mfma_f32_16x16x32_bf16 a[60:63], a[248:251], v[232:235], a[60:63]\n"
    "v_exp_f32 v185, v185\n"
    "v_add_f32 v9, v9, v184\n"
    "v_mfma_f32_16x16x32_bf16 a[64:67], a[192:195], v[240:243], a[64:67]\n"
    "buffer_load_dwordx4 v1, s[44:47], s56 offen offset:3072 sc1 lds\n"
    "v_cvt_pk_bf16_f32 v236, v184, v185\n"
    "v_mfma_f32_16x16x32_bf16 a[68:71], a[200:203], v[240:243], a[68:71]\n"
    "s_add_u32 s71, s71, s73\n"
    "s_xor_b32 s75, s75, 0x10000\n"
    "v_mfma_f32_16x16x32_bf16 a[72:75], a[208:211], v[240:243], a[72:75]\n"
    "v_exp_f32 v186, v186\n"
    "v_add_f32 v9, v9, v185\n"
    "v_mfma_f32_16x16x32_bf16 a[76:79], a[216:219], v[240:243], a[76:79]\n"
    "ds_read_b128 a[144:147], v2 offset:4096\n"
    "v_add_f32 v8, v8, v163\n"
    "v_mfma_f32_16x16x32_bf16 a[80:83], a[224:227], v[240:243], a[80:83]\n"
    "v_exp_f32 v187, v187\n"
    "v_add_f32 v9, v9, v186\n"
    "v_mfma_f32_16x16x32_bf16 a[84:87], a[232:235], v[240:243], a[84:87]\n"
    "v_exp_f32 v188, v188\n"
    "v_add_f32 v9, v9, v187\n"
    "v_mfma_f32_16x16x32_bf16 a[88:91], a[240:243], v[240:243], a[88:91]\n"
    "v_exp_f32 v189, v189\n"
    "v_add_f32 v9, v9, v188\n"
    "v_mfma_f32_16x16x32_bf16 a[92:95], a[248:251], v[240:243], a[92:95]\n"
    "v_exp_f32 v190, v190\n"
    "v_add_f32 v9, v9, v189\n"
    "v_mfma_f32_16x16x32_bf16 a[96:99], a[192:195], v[248:251], a[96:99]\n"
    "ds_read_b128 a[148:151], v2 offset:5120\n"
    "v_cvt_pk_bf16_f32 v237, v186, v187\n"
    "v_mfma_f32_16x16x32_bf16 a[100:103], a[200:203], v[248:251], a[100:103]\n"
    "v_exp_f32 v191, v191\n"
    "v_add_f32 v9, v9, v190\n"
    "v_mfma_f32_16x16x32_bf16 a[104:107], a[208:211], v[248:251], a[104:107]\n"
    "v_exp_f32 v200, v200\n"
    "v_add_f32 v9, v9, v191\n"
    "v_mfma_f32_16x16x32_bf16 a[108:111], a[216:219], v[248:251], a[108:111]\n"
    "v_exp_f32 v201, v201\n"
    "v_mfma_f32_16x16x32_bf16 a[112:115], a[224:227], v[248:251], a[112:115]\n"
    "v_exp_f32 v202, v202\n"
    "v_mfma_f32_16x16x32_bf16 a[116:119], a[232:235], v[248:251], a[116:119]\n"
    "s_waitcnt lgkmcnt(6)\n"
    "v_exp_f32 v203, v203\n"
    "v_mfma_f32_16x16x32_bf16 a[120:123], a[240:243], v[248:251], a[120:123]\n"
    "ds_read_b128 a[152:155], v2 offset:6144\n"
    "v_cvt_pk_bf16_f32 v238, v188, v189\n"
    "v_mfma_f32_16x16x32_bf16 a[124:127], a[248:251], v[248:251], a[124:127]\n"
    "v_exp_f32 v204, v204\n"
    "v_mfma_f32_16x16x32_bf16 a[0:3], a[196:199], v[228:231], a[0:3]\n"
    "ds_read_b128 a[156:159], v2 offset:7168\n"
    "v_cvt_pk_bf16_f32 v239, v190, v191\n"
    "v_mfma_f32_16x16x32_bf16 a[4:7], a[204:207], v[228:231], a[4:7]\n"
    "v_exp_f32 v205, v205\n"
    "v_mfma_f32_16x16x32_bf16 a[8:11], a[212:215], v[228:231], a[8:11]\n"
    "v_exp_f32 v206, v206\n"
    "v_mfma_f32_16x16x32_bf16 a[12:15], a[220:223], v[228:231], a[12:15]\n"
    "ds_read_b128 a[160:163], v2 offset:8192\n"
    "v_cvt_pk_bf16_f32 v244, v200, v201\n"
    "v_mfma_f32_16x16x32_bf16 a[16:19], a[228:231], v[228:231], a[16:19]\n"
    "v_exp_f32 v207, v207\n"
    "v_mfma_f32_16x16x32_bf16 a[20:23], a[236:239], v[228:231], a[20:23]\n"
    "v_exp_f32 v216, v216\n"
    "v_mfma_f32_16x16x32_bf16 a[24:27], a[244:247], v[228:231], a[24:27]\n"
    "ds_read_b128 a[164:167], v2 offset:9216\n"
    "v_cvt_pk_bf16_f32 v245, v202, v203\n"
    "v_mfma_f32_16x16x32_bf16 a[28:31], a[252:255], v[228:231], a[28:31]\n"
    "v_exp_f32 v217, v217\n"
    "v_mfma_f32_16x16x32_bf16 a[32:35], a[196:199], v[236:239], a[32:35]\n"
    "ds_read_b128 a[168:171], v2 offset:10240\n"
    "v_cvt_pk_bf16_f32 v246, v204, v205\n"
    "v_mfma_f32_16x16x32_bf16 a[36:39], a[204:207], v[236:239], a[36:39]\n"
    "v_exp_f32 v218, v218\n"
    "v_mfma_f32_16x16x32_bf16 a[40:43], a[212:215], v[236:239], a[40:43]\n"
    "v_exp_f32 v219, v219\n"
    "v_mfma_f32_16x16x32_bf16 a[44:47], a[220:223], v[236:239], a[44:47]\n"
    "ds_read_b128 a[172:175], v2 offset:11264\n"
    "v_cvt_pk_bf16_f32 v247, v206, v207\n"
    "v_mfma_f32_16x16x32_bf16 a[48:51], a[228:231], v[236:239], a[48:51]\n"
    "v_exp_f32 v220, v220\n"
    "v_mfma_f32_16x16x32_bf16 a[52:55], a[236:239], v[236:239], a[52:55]\n"
    "v_exp_f32 v221, v221\n"
    "v_mfma_f32_16x16x32_bf16 a[56:59], a[244:247], v[236:239], a[56:59]\n"
    "ds_read_b128 a[176:179], v2 offset:12288\n"
    "v_cvt_pk_bf16_f32 v252, v216, v217\n"
    "v_mfma_f32_16x16x32_bf16 a[60:63], a[252:255], v[236:239], a[60:63]\n"
    "v_exp_f32 v222, v222\n"
    "v_mfma_f32_16x16x32_bf16 a[64:67], a[196:199], v[244:247], a[64:67]\n"
    "ds_read_b128 a[180:183], v2 offset:13312\n"
    "v_cvt_pk_bf16_f32 v253, v218, v219\n"
    "v_mfma_f32_16x16x32_bf16 a[68:71], a[204:207], v[244:247], a[68:71]\n"
    "v_exp_f32 v223, v223\n"
    "v_mfma_f32_16x16x32_bf16 a[72:75], a[212:215], v[244:247], a[72:75]\n"
    "v_cvt_pk_bf16_f32 v254, v220, v221\n"
    "v_cvt_pk_bf16_f32 v255, v222, v223\n"
    "v_mfma_f32_16x16x32_bf16 a[76:79], a[220:223], v[244:247], a[76:79]\n"
    "ds_read_b128 a[184:187], v2 offset:14336\n"
    "v_add_f32 v8, v8, v164\n"
    "v_mfma_f32_16x16x32_bf16 a[80:83], a[228:231], v[244:247], a[80:83]\n"
    "v_add_f32 v8, v8, v165\n"
    "v_add_f32 v8, v8, v166\n"
    "v_mfma_f32_16x16x32_bf16 a[84:87], a[236:239], v[244:247], a[84:87]\n"
    "v_add_f32 v8, v8, v167\n"
    "v_add_f32 v9, v9, v176\n"
    "v_mfma_f32_16x16x32_bf16 a[88:91], a[244:247], v[244:247], a[88:91]\n"
    "s_waitcnt lgkmcnt(8)\n"
    "ds_read_b128 a[188:191], v2 offset:15360\n"
    "v_add_f32 v9, v9, v177\n"
    "v_mfma_f32_16x16x32_bf16 a[92:95], a[252:255], v[244:247], a[92:95]\n"
    "v_add_f32 v9, v9, v178\n"
    "v_add_f32 v9, v9, v179\n"
    "v_mfma_f32_16x16x32_bf16 a[96:99], a[196:199], v[252:255], a[96:99]\n"
    "v_add_f32 v9, v9, v180\n"
    "v_add_f32 v9, v9, v181\n"
    "v_mfma_f32_16x16x32_bf16 a[100:103], a[204:207], v[252:255], a[100:103]\n"
    "v_add_f32 v9, v9, v182\n"
    "v_mfma_f32_16x16x32_bf16 a[104:107], a[212:215], v[252:255], a[104:107]\n"
    "v_add_f32 v9, v9, v183\n"
    "v_add_f32 v10, v10, v192\n"
    "v_mfma_f32_16x16x32_bf16 a[108:111], a[220:223], v[252:255], a[108:111]\n"
    "v_add_f32 v10, v10, v193\n"
    "v_add_f32 v10, v10, v194\n"
    "v_mfma_f32_16x16x32_bf16 a[112:115], a[228:231], v[252:255], a[112:115]\n"
    "v_add_f32 v10, v10, v195\n"
    "v_mfma_f32_16x16x32_bf16 a[116:119], a[236:239], v[252:255], a[116:119]\n"
    "v_add_f32 v10, v10, v196\n"
    "v_add_f32 v10, v10, v197\n"
    "v_mfma_f32_16x16x32_bf16 a[120:123], a[244:247], v[252:255], a[120:123]\n"
    "v_add_f32 v10, v10, v198\n"
    "v_add_f32 v10, v10, v199\n"
    "v_mfma_f32_16x16x32_bf16 a[124:127], a[252:255], v[252:255], a[124:127]\n"
    "v_add_f32 v11, v11, v208\n"
    "s_add_u32 s80, s80, 64\n"
    "s_mov_b32 s74, 0x8000\n"
    "s_sub_i32 s58, s79, s80\n"
    "v_add_u32 v16, s58, v5\n"
    "v_cmp_le_i32 s[84:85], 0, v16\n"
    "v_cmp_le_i32 s[86:87], 1, v16\n"
    "v_cmp_le_i32 s[88:89], 2, v16\n"
    "v_cndmask_b32 v64, v4, v14, s[84:85]\n"
    "v_cmp_le_i32 s[90:91], 3, v16\n"
    "v_cndmask_b32 v65, v4, v14, s[86:87]\n"
    "v_cmp_le_i32 s[84:85], 16, v16\n"
    "v_cndmask_b32 v66, v4, v14, s[88:89]\n"
    "v_cmp_le_i32 s[86:87], 17, v16\n"
    "v_cndmask_b32 v67, v4, v14, s[90:91]\n"
    "v_cmp_le_i32 s[88:89], 18, v16\n"
    "v_cndmask_b32 v68, v4, v14, s[84:85]\n"
    "v_cmp_le_i32 s[90:91], 19, v16\n"
    "v_cndmask_b32 v69, v4, v14, s[86:87]\n"
    "v_cmp_le_i32 s[84:85], 32, v16\n"
    "v_cndmask_b32 v70, v4, v14, s[88:89]\n"
    "v_cmp_le_i32 s[86:87], 33, v16\n"
    "v_cndmask_b32 v71, v4, v14, s[90:91]\n"
    "v_cmp_le_i32 s[88:89], 34, v16\n"
    "v_cndmask_b32 v72, v4, v14, s[84:85]\n"
    "v_cmp_le_i32 s[90:91], 35, v16\n"
    "v_cndmask_b32 v73, v4, v14, s[86:87]\n"
    "v_cmp_le_i32 s[84:85], 48, v16\n"
    "v_cndmask_b32 v74, v4, v14, s[88:89]\n"
    "v_cmp_le_i32 s[86:87], 49, v16\n"
    "v_cndmask_b32 v75, v4, v14, s[90:91]\n"
    "v_cmp_le_i32 s[88:89], 50, v16\n"
    "v_cndmask_b32 v76, v4, v14, s[84:85]\n"
    "v_cmp_le_i32 s[90:91], 51, v16\n"
    "v_cndmask_b32 v77, v4, v14, s[86:87]\n"
    "v_cmp_le_i32 s[84:85], -16, v16\n"
    "v_cndmask_b32 v78, v4, v14, s[88:89]\n"
    "v_cmp_le_i32 s[86:87], -15, v16\n"
    "v_cndmask_b32 v79, v4, v14, s[90:91]\n"
    "v_cmp_le_i32 s[88:89], -14, v16\n"
    "v_cndmask_b32 v80, v4, v15, s[84:85]\n"
    "v_cmp_le_i32 s[90:91], -13, v16\n"
    "v_cndmask_b32 v81, v4, v15, s[86:87]\n"
    "v_cmp_le_i32 s[84:85], 0, v16\n"
    "v_cndmask_b32 v82, v4, v15, s[88:89]\n"
    "v_cmp_le_i32 s[86:87], 1, v16\n"
    "v_cndmask_b32 v83, v4, v15, s[90:91]\n"
    "v_cmp_le_i32 s[88:89], 2, v16\n"
    "v_cndmask_b32 v84, v4, v15, s[84:85]\n"
    "v_cmp_le_i32 s[90:91], 3, v16\n"
    "v_cndmask_b32 v85, v4, v15, s[86:87]\n"
    "v_cmp_le_i32 s[84:85], 16, v16\n"
    "v_cndmask_b32 v86, v4, v15, s[88:89]\n"
    "v_cmp_le_i32 s[86:87], 17, v16\n"
    "v_cndmask_b32 v87, v4, v15, s[90:91]\n"
    "v_cmp_le_i32 s[88:89], 18, v16\n"
    "v_cndmask_b32 v88, v4, v15, s[84:85]\n"
    "v_cmp_le_i32 s[90:91], 19, v16\n"
    "v_cndmask_b32 v89, v4, v15, s[86:87]\n"
    "v_cmp_le_i32 s[84:85], 32, v16\n"
    "v_cndmask_b32 v90, v4, v15, s[88:89]\n"
    "v_cmp_le_i32 s[86:87], 33, v16\n"
    "v_cndmask_b32 v91, v4, v15, s[90:91]\n"
    "v_cmp_le_i32 s[88:89], 34, v16\n"
    "v_cndmask_b32 v92, v4, v15, s[84:85]\n"
    "v_cmp_le_i32 s[90:91], 35, v16\n"
    "v_cndmask_b32 v93, v4, v15, s[86:87]\n"
    "v_cndmask_b32 v94, v4, v15, s[88:89]\n"
    "v_cndmask_b32 v95, v4, v15, s[90:91]\n"
    "v_add_u32 v2, s83, v2\n"
    "s_waitcnt vmcnt(0)\n"
    "s_waitcnt lgkmcnt(0)\n"
    "s_barrier\n"
    "v_mfma_f32_16x16x32_bf16 v[160:163], a[128:131], v[96:99], v[32:35]\n"
    "s_add_u32 s57, s72, 0\n"
    "ds_read_b128 a[192:195], v3 offset:0\n"
    "v_add_f32 v10, v10, v200\n"
    "v_mfma_f32_16x16x32_bf16 v[160:163], a[132:135], v[100:103], v[160:163]\n"
    "s_add_u32 m0, s76, 0\n"
    "s_nop 0\n"
    "buffer_load_dwordx4 v1, s[44:47], s57 offen sc1 lds\n"
    "v_add_f32 v10, v10, v201\n"
    "v_mfma_f32_16x16x32_bf16 v[160:163], a[136:139], v[104:107], v[160:163]\n"
    "ds_read_b128 a[200:203], v3 offset:2048\n"
    "v_add_f32 v10, v10, v202\n"
    "v_mfma_f32_16x16x32_bf16 v[160:163], a[140:143], v[108:111], v[160:163]\n"
    "ds_read_b128 a[208:211], v3 offset:4096\n"
    "v_add_f32 v10, v10, v203\n"
    "v_mfma_f32_16x16x32_bf16 v[164:167], a[144:147], v[96:99], v[36:39]\n"
    "ds_read_b128 a[216:219], v3 offset:6144\n"
    "v_add_f32 v10, v10, v204\n"
    "v_mfma_f32_16x16x32_bf16 v[164:167], a[148:151], v[100:103], v[164:167]\n"
    "buffer_load_dwordx4 v1, s[44:47], s57 offen offset:1024 sc1 lds\n"
    "v_add_f32 v10, v10, v205\n"
    "v_mfma_f32_16x16x32_bf16 v[164:167], a[152:155], v[104:107], v[164:167]\n"
    "ds_read_b128 a[224:227], v3 offset:8192\n"
    "v_add_f32 v10, v10, v206\n"
    "v_mfma_f32_16x16x32_bf16 v[164:167], a[156:159], v[108:111], v[164:167]\n"
    "ds_read_b128 a[232:235], v3 offset:10240\n"
    "v_add_f32 v10, v10, v207\n"
    "v_mfma_f32_16x16x32_bf16 v[176:179], a[128:131], v[112:115], v[48:51]\n"
    "ds_read_b128 a[240:243], v3 offset:12288\n"
    "v_add_f32 v11, v11, v216\n"
    "v_mfma_f32_16x16x32_bf16 v[176:179], a[132:135], v[116:119], v[176:179]\n"
    "v_exp_f32 v160, v160\n"
    "v_add_f32 v11, v11, v222\n"
    "v_mfma_f32_16x16x32_bf16 v[176:179], a[136:139], v[120:123], v[176:179]\n"
    "v_exp_f32 v161, v161\n"
    "v_add_f32 v11, v11, v223\n"
    "v_mfma_f32_16x16x32_bf16 v[176:179], a[140:143], v[124:127], v[176:179]\n"
    "buffer_load_dwordx4 v1, s[44:47], s57 offen offset:2048 sc1 lds\n"
    "v_cvt_pk_bf16_f32 v224, v160, v161\n"
    "v_mfma_f32_16x16x32_bf16 v[180:183], a[144:147], v[112:115], v[52:55]\n"
    "v_exp_f32 v162, v162\n"
    "v_mfma_f32_16x16x32_bf16 v[180:183], a[148:151], v[116:119], v[180:183]\n"
    "v_exp_f32 v163, v163\n"
    "v_mfma_f32_16x16x32_bf16 v[180:183], a[152:155], v[120:123], v[180:183]\n"
    "ds_read_b128 a[248:251], v3 offset:14336\n"
    "v_cvt_pk_bf16_f32 v225, v162, v163\n"
    "v_mfma_f32_16x16x32_bf16 v[180:183], a[156:159], v[124:127], v[180:183]\n"
    "v_exp_f32 v164, v164\n"
    "v_mfma_f32_16x16x32_bf16 v[192:195], a[128:131], v[128:131], v[64:67]\n"
    "buffer_load_dwordx4 v1, s[44:47], s57 offen offset:3072 sc1 lds\n"
    "v_add_f32 v11, v11, v217\n"
    "v_mfma_f32_16x16x32_bf16 v[192:195], a[132:135], v[132:135], v[192:195]\n"
    "v_exp_f32 v165, v165\n"
    "v_mfma_f32_16x16x32_bf16 v[192:195], a[136:139], v[136:139], v[192:195]\n"
    "v_exp_f32 v166, v166\n"
    "v_mfma_f32_16x16x32_bf16 v[192:195], a[140:143], v[140:143], v[192:195]\n"
    "ds_read_b128 a[196:199], v3 offset:1024\n"
    "v_cvt_pk_bf16_f32 v226, v164, v165\n"
    "v_mfma_f32_16x16x32_bf16 v[196:199], a[144:147], v[128:131], v[68:71]\n"
    "v_exp_f32 v167, v167\n"
    "v_mfma_f32_16x16x32_bf16 v[196:199], a[148:151], v[132:135], v[196:199]\n"
    "s_add_u32 s57, s72, 16384\n"
    "v_exp_f32 v176, v176\n"
    "v_mfma_f32_16x16x32_bf16 v[196:199], a[152:155], v[136:139], v[196:199]\n"
    "s_add_u32 m0, s76, 16384\n"
    "s_nop 0\n"
    "buffer_load_dwordx4 v1, s[44:47], s57 offen sc1 lds\n"
    "v_cvt_pk_bf16_f32 v227, v166, v167\n"
    "v_mfma_f32_16x16x32_bf16 v[196:199], a[156:159], v[140:143], v[196:199]\n"
    "v_exp_f32 v177, v177\n"
    "v_mfma_f32_16x16x32_bf16 v[208:211], a[128:131], v[144:147], v[80:83]\n"
    "ds_read_b128 a[204:207], v3 offset:3072\n"
    "v_cvt_pk_bf16_f32 v232, v176, v177\n"
    "v_mfma_f32_16x16x32_bf16 v[208:211], a[132:135], v[148:151], v[208:211]\n"
    "v_exp_f32 v178, v178\n"
    "v_mfma_f32_16x16x32_bf16 v[208:211], a[136:139], v[152:155], v[208:211]\n"
    "v_exp_f32 v179, v179\n"
    "v_mfma_f32_16x16x32_bf16 v[208:211], a[140:143], v[156:159], v[208:211]\n"
    "buffer_load_dwordx4 v1, s[44:47], s57 offen offset:1024 sc1 lds\n"
    "v_cvt_pk_bf16_f32 v233, v178, v179\n"
    "v_mfma_f32_16x16x32_bf16 v[212:215], a[144:147], v[144:147], v[84:87]\n"
    "v_exp_f32 v180, v180\n"
    "v_mfma_f32_16x16x32_bf16 v[212:215], a[148:151], v[148:151], v[212:215]\n"
    "v_exp_f32 v181, v181\n"
    "v_mfma_f32_16x16x32_bf16 v[212:215], a[152:155], v[152:155], v[212:215]\n"
    "ds_read_b128 a[212:215], v3 offset:5120\n"
    "v_cvt_pk_bf16_f32 v234, v180, v181\n"
    "v_mfma_f32_16x16x32_bf16 v[212:215], a[156:159], v[156:159], v[212:215]\n"
    "v_exp_f32 v182, v182\n"
    "v_mfma_f32_16x16x32_bf16 v[168:171], a[160:163], v[96:99], v[40:43]\n"
    "buffer_load_dwordx4 v1, s[44:47], s57 offen offset:2048 sc1 lds\n"
    "v_add_f32 v11, v11, v218\n"
    "v_mfma_f32_16x16x32_bf16 v[168:171], a[164:167], v[100:103], v[168:171]\n"
    "v_exp_f32 v183, v183\n"
    "v_mfma_f32_16x16x32_bf16 v[168:171], a[168:171], v[104:107], v[168:171]\n"
    "v_exp_f32 v192, v192\n"
    "v_mfma_f32_16x16x32_bf16 v[168:171], a[172:175], v[108:111], v[168:171]\n"
    "ds_read_b128 a[220:223], v3 offset:7168\n"
    "v_cvt_pk_bf16_f32 v235, v182, v183\n"
    "v_mfma_f32_16x16x32_bf16 v[172:175], a[176:179], v[96:99], v[44:47]\n"
    "v_exp_f32 v193, v193\n"
    "v_mfma_f32_16x16x32_bf16 v[172:175], a[180:183], v[100:103], v[172:175]\n"
    "v_exp_f32 v194, v194\n"
    "v_mfma_f32_16x16x32_bf16 v[172:175], a[184:187], v[104:107], v[172:175]\n"
    "buffer_load_dwordx4 v1, s[44:47], s57 offen offset:3072 sc1 lds\n"
    "v_cvt_pk_bf16_f32 v240, v192, v193\n"
    "v_mfma_f32_16x16x32_bf16 v[172:175], a[188:191], v[108:111], v[172:175]\n"
    "v_exp_f32 v195, v195\n"
    "v_mfma_f32_16x16x32_bf16 v[184:187], a[160:163], v[112:115], v[56:59]\n"
    "ds_read_b128 a[228:231], v3 offset:9216\n"
    "v_cvt_pk_bf16_f32 v241, v194, v195\n"
    "v_mfma_f32_16x16x32_bf16 v[184:187], a[164:167], v[116:119], v[184:187]\n"
    "v_exp_f32 v196, v196\n"
    "v_mfma_f32_16x16x32_bf16 v[184:187], a[168:171], v[120:123], v[184:187]\n"
    "s_add_u32 s56, s71, 0\n"
    "s_add_u32 s72, s72, s74\n"
    "s_xor_b32 s76, s76, 0x10000\n"
    "v_mfma_f32_16x16x32_bf16 v[184:187], a[172:175], v[124:127], v[184:187]\n"
    "s_add_u32 m0, s75, 0\n"
    "s_nop 0\n"
    "buffer_load_dwordx4 v1, s[44:47], s56 offen sc1 lds\n"
    "v_add_f32 v11, v11, v219\n"
    "v_mfma_f32_16x16x32_bf16 v[188:191], a[176:179], v[112:115], v[60:63]\n"
    "v_exp_f32 v197, v197\n"
    "v_mfma_f32_16x16x32_bf16 v[188:191], a[180:183], v[116:119], v[188:191]\n"
    "v_exp_f32 v198, v198\n"
    "v_mfma_f32_16x16x32_bf16 v[188:191], a[184:187], v[120:123], v[188:191]\n"
    "ds_read_b128 a[236:239], v3 offset:11264\n"
    "v_cvt_pk_bf16_f32 v242, v196, v197\n"
    "v_mfma_f32_16x16x32_bf16 v[188:191], a[188:191], v[124:127], v[188:191]\n"
    "v_exp_f32 v199, v199\n"
    "v_mfma_f32_16x16x32_bf16 v[200:203], a[160:163], v[128:131], v[72:75]\n"
    "buffer_load_dwordx4 v1, s[44:47], s56 offen offset:1024 sc1 lds\n"
    "v_cvt_pk_bf16_f32 v243, v198, v199\n"
    "v_mfma_f32_16x16x32_bf16 v[200:203], a[164:167], v[132:135], v[200:203]\n"
    "v_exp_f32 v208, v208\n"
    "v_mfma_f32_16x16x32_bf16 v[200:203], a[168:171], v[136:139], v[200:203]\n"
    "v_exp_f32 v209, v209\n"
    "v_mfma_f32_16x16x32_bf16 v[200:203], a[172:175], v[140:143], v[200:203]\n"
    "ds_read_b128 a[244:247], v3 offset:13312\n"
    "v_cvt_pk_bf16_f32 v248, v208, v209\n"
    "v_mfma_f32_16x16x32_bf16 v[204:207], a[176:179], v[128:131], v[76:79]\n"
    "v_exp_f32 v210, v210\n"
    "v_add_f32 v11, v11, v209\n"
    "v_mfma_f32_16x16x32_bf16 v[204:207], a[180:183], v[132:135], v[204:207]\n"
    "v_exp_f32 v211, v211\n"
    "v_add_f32 v11, v11, v210\n"
    "v_mfma_f32_16x16x32_bf16 v[204:207], a[184:187], v[136:139], v[204:207]\n"
    "buffer_load_dwordx4 v1, s[44:47], s56 offen offset:2048 sc1 lds\n"
    "v_cvt_pk_bf16_f32 v249, v210, v211\n"
    "v_mfma_f32_16x16x32_bf16 v[204:207], a[188:191], v[140:143], v[204:207]\n"
    "v_exp_f32 v212, v212\n"
    "v_add_f32 v11, v11, v211\n"
    "v_mfma_f32_16x16x32_bf16 v[216:219], a[160:163], v[144:147], v[88:91]\n"
    "s_waitcnt lgkmcnt(8)\n"
    "ds_read_b128 a[252:255], v3 offset:15360\n"
    "v_add_f32 v11, v11, v220\n"
    "v_mfma_f32_16x16x32_bf16 v[216:219], a[164:167], v[148:151], v[216:219]\n"
    "v_xor_b32 v3, 0x10000, v3\n"
    "v_add_f32 v11, v11, v221\n"
    "v_mfma_f32_16x16x32_bf16 v[216:219], a[168:171], v[152:155], v[216:219]\n"
    "v_exp_f32 v213, v213\n"
    "v_add_f32 v11, v11, v212\n"
    "v_mfma_f32_16x16x32_bf16 v[216:219], a[172:175], v[156:159], v[216:219]\n"
    "buffer_load_dwordx4 v1, s[44:47], s56 offen offset:3072 sc1 lds\n"
    "v_cvt_pk_bf16_f32 v250, v212, v213\n"
    "v_mfma_f32_16x16x32_bf16 v[220:223], a[176:179], v[144:147], v[92:95]\n"
    "v_exp_f32 v214, v214\n"
    "v_add_f32 v11, v11, v213\n"
    "v_mfma_f32_16x16x32_bf16 v[220:223], a[180:183], v[148:151], v[220:223]\n"
    "s_waitcnt lgkmcnt(8)\n"
    "v_xor_b32 v2, 0x10000, v2\n"
    "v_add_f32 v8, v8, v160\n"
    "v_mfma_f32_16x16x32_bf16 v[220:223], a[184:187], v[152:155], v[220:223]\n"
    "ds_read_b128 a[128:131], v2 offset:0\n"
    "v_add_f32 v8, v8, v161\n"
    "v_mfma_f32_16x16x32_bf16 v[220:223], a[188:191], v[156:159], v[220:223]\n"
    "s_add_u32 s56, s71, 16384\n"
    "v_exp_f32 v215, v215\n"
    "v_add_f32 v11, v11, v214\n"
    "v_mfma_f32_16x16x32_bf16 a[0:3], a[192:195], v[224:227], a[0:3]\n"
    "s_add_u32 m0, s75, 16384\n"
    "s_nop 0\n"
    "buffer_load_dwordx4 v1, s[44:47], s56 offen sc1 lds\n"
    "v_cvt_pk_bf16_f32 v251, v214, v215\n"
    "v_mfma_f32_16x16x32_bf16 a[4:7], a[200:203], v[224:227], a[4:7]\n"
    "v_exp_f32 v168, v168\n"
    "v_add_f32 v11, v11, v215\n"
    "v_mfma_f32_16x16x32_bf16 a[8:11], a[208:211], v[224:227], a[8:11]\n"
    "v_exp_f32 v169, v169\n"
    "v_add_f32 v8, v8, v168\n"
    "v_mfma_f32_16x16x32_bf16 a[12:15], a[216:219], v[224:227], a[12:15]\n"
    "ds_read_b128 a[132:135], v2 offset:1024\n"
    "v_cvt_pk_bf16_f32 v228, v168, v169\n"
    "v_mfma_f32_16x16x32_bf16 a[16:19], a[224:227], v[224:227], a[16:19]\n"
    "v_exp_f32 v170, v170\n"
    "v_add_f32 v8, v8, v169\n"
    "v_mfma_f32_16x16x32_bf16 a[20:23], a[232:235], v[224:227], a[20:23]\n"
    "v_exp_f32 v171, v171\n"
    "v_add_f32 v8, v8, v170\n"
    "v_mfma_f32_16x16x32_bf16 a[24:27], a[240:243], v[224:227], a[24:27]\n"
    "buffer_load_dwordx4 v1, s[44:47], s56 offen offset:1024 sc1 lds\n"
    "v_cvt_pk_bf16_f32 v229, v170, v171\n"
    "v_mfma_f32_16x16x32_bf16 a[28:31], a[248:251], v[224:227], a[28:31]\n"
    "v_exp_f32 v172, v172\n"
    "v_add_f32 v8, v8, v171\n"
    "v_mfma_f32_16x16x32_bf16 a[32:35], a[192:195], v[232:235], a[32:35]\n"
    "ds_read_b128 a[136:139], v2 offset:2048\n"
    "v_add_f32 v8, v8, v162\n"
    "v_mfma_f32_16x16x32_bf16 a[36:39], a[200:203], v[232:235], a[36:39]\n"
    "v_exp_f32 v173, v173\n"
    "v_add_f32 v8, v8, v172\n"
    "v_mfma_f32_16x16x32_bf16 a[40:43], a[208:211], v[232:235], a[40:43]\n"
    "v_exp_f32 v174, v174\n"
    "v_add_f32 v8, v8, v173\n"
    "v_mfma_f32_16x16x32_bf16 a[44:47], a[216:219], v[232:235], a[44:47]\n"
    "buffer_load_dwordx4 v1, s[44:47], s56 offen offset:2048 sc1 lds\n"
    "v_cvt_pk_bf16_f32 v230, v172, v173\n"
    "v_mfma_f32_16x16x32_bf16 a[48:51], a[224:227], v[232:235], a[48:51]\n"
    "v_exp_f32 v175, v175\n"
    "v_add_f32 v8, v8, v174\n"
    "v_mfma_f32_16x16x32_bf16 a[52:55], a[232:235], v[232:235], a[52:55]\n"
    "v_exp_f32 v184, v184\n"
    "v_add_f32 v8, v8, v175\n"
    "v_mfma_f32_16x16x32_bf16 a[56:59], a[240:243], v[232:235], a[56:59]\n"
    "ds_read_b128 a[140:143], v2 offset:3072\n"
    "v_cvt_pk_bf16_f32 v231, v174, v175\n"
    "v_mfma_f32_16x16x32_bf16 a[60:63], a[248:251], v[232:235], a[60:63]\n"
    "v_exp_f32 v185, v185\n"
    "v_add_f32 v9, v9, v184\n"
    "v_mfma_f32_16x16x32_bf16 a[64:67], a[192:195], v[240:243], a[64:67]\n"
    "buffer_load_dwordx4 v1, s[44:47], s56 offen offset:3072 sc1 lds\n"
    "v_cvt_pk_bf16_f32 v236, v184, v185\n"
    "v_mfma_f32_16x16x32_bf16 a[68:71], a[200:203], v[240:243], a[68:71]\n"
    "s_add_u32 s71, s71, s73\n"
    "s_xor_b32 s75, s75, 0x10000\n"
    "v_mfma_f32_16x16x32_bf16 a[72:75], a[208:211], v[240:243], a[72:75]\n"
    "v_exp_f32 v186, v186\n"
    "v_add_f32 v9, v9, v185\n"
    "v_mfma_f32_16x16x32_bf16 a[76:79], a[216:219], v[240:243], a[76:79]\n"
    "ds_read_b128 a[144:147], v2 offset:4096\n"
    "v_add_f32 v8, v8, v163\n"
    "v_mfma_f32_16x16x32_bf16 a[80:83], a[224:227], v[240:243], a[80:83]\n"
    "v_exp_f32 v187, v187\n"
    "v_add_f32 v9, v9, v186\n"
    "v_mfma_f32_16x16x32_bf16 a[84:87], a[232:235], v[240:243], a[84:87]\n"
    "v_exp_f32 v188, v188\n"
    "v_add_f32 v9, v9, v187\n"
    "v_mfma_f32_16x16x32_bf16 a[88:91], a[240:243], v[240:243], a[88:91]\n"
    "v_exp_f32 v189, v189\n"
    "v_add_f32 v9, v9, v188\n"
    "v_mfma_f32_16x16x32_bf16 a[92:95], a[248:251], v[240:243], a[92:95]\n"
    "v_exp_f32 v190, v190\n"
    "v_add_f32 v9, v9, v189\n"
    "v_mfma_f32_16x16x32_bf16 a[96:99], a[192:195], v[248:251], a[96:99]\n"
    "ds_read_b128 a[148:151], v2 offset:5120\n"
    "v_cvt_pk_bf16_f32 v237, v186, v187\n"
    "v_mfma_f32_16x16x32_bf16 a[100:103], a[200:203], v[248:251], a[100:103]\n"
    "v_exp_f32 v191, v191\n"
    "v_add_f32 v9, v9, v190\n"
    "v_mfma_f32_16x16x32_bf16 a[104:107], a[208:211], v[248:251], a[104:107]\n"
    "v_exp_f32 v200, v200\n"
    "v_add_f32 v9, v9, v191\n"
    "v_mfma_f32_16x16x32_bf16 a[108:111], a[216:219], v[248:251], a[108:111]\n"
    "v_exp_f32 v201, v201\n"
    "v_mfma_f32_16x16x32_bf16 a[112:115], a[224:227], v[248:251], a[112:115]\n"
    "v_exp_f32 v202, v202\n"
    "v_mfma_f32_16x16x32_bf16 a[116:119], a[232:235], v[248:251], a[116:119]\n"
    "s_waitcnt lgkmcnt(6)\n"
    "v_exp_f32 v203, v203\n"
    "v_mfma_f32_16x16x32_bf16 a[120:123], a[240:243], v[248:251], a[120:123]\n"
    "ds_read_b128 a[152:155], v2 offset:6144\n"
    "v_cvt_pk_bf16_f32 v238, v188, v189\n"
    "v_mfma_f32_16x16x32_bf16 a[124:127], a[248:251], v[248:251], a[124:127]\n"
    "v_exp_f32 v204, v204\n"
    "v_mfma_f32_16x16x32_bf16 a[0:3], a[196:199], v[228:231], a[0:3]\n"
    "ds_read_b128 a[156:159], v2 offset:7168\n"
    "v_cvt_pk_bf16_f32 v239, v190, v191\n"
    "v_mfma_f32_16x16x32_bf16 a[4:7], a[204:207], v[228:231], a[4:7]\n"
    "v_exp_f32 v205, v205\n"
    "v_mfma_f32_16x16x32_bf16 a[8:11], a[212:215], v[228:231], a[8:11]\n"
    "v_exp_f32 v206, v206\n"
    "v_mfma_f32_16x16x32_bf16 a[12:15], a[220:223], v[228:231], a[12:15]\n"
    "ds_read_b128 a[160:163], v2 offset:8192\n"
    "v_cvt_pk_bf16_f32 v244, v200, v201\n"
    "v_mfma_f32_16x16x32_bf16 a[16:19], a[228:231], v[228:231], a[16:19]\n"
    "v_exp_f32 v207, v207\n"
    "v_mfma_f32_16x16x32_bf16 a[20:23], a[236:239], v[228:231], a[20:23]\n"
    "v_exp_f32 v216, v216\n"
    "v_mfma_f32_16x16x32_bf16 a[24:27], a[244:247], v[228:231], a[24:27]\n"
    "ds_read_b128 a[164:167], v2 offset:9216\n"
    "v_cvt_pk_bf16_f32 v245, v202, v203\n"
    "v_mfma_f32_16x16x32_bf16 a[28:31], a[252:255], v[228:231], a[28:31]\n"
    "v_exp_f32 v217, v217\n"
    "v_mfma_f32_16x16x32_bf16 a[32:35], a[196:199], v[236:239], a[32:35]\n"
    "ds_read_b128 a[168:171], v2 offset:10240\n"
    "v_cvt_pk_bf16_f32 v246, v204, v205\n"
    "v_mfma_f32_16x16x32_bf16 a[36:39], a[204:207], v[236:239], a[36:39]\n"
    "v_exp_f32 v218, v218\n"
    "v_mfma_f32_16x16x32_bf16 a[40:43], a[212:215], v[236:239], a[40:43]\n"
    "v_exp_f32 v219, v219\n"
    "v_mfma_f32_16x16x32_bf16 a[44:47], a[220:223], v[236:239], a[44:47]\n"
    "ds_read_b128 a[172:175], v2 offset:11264\n"
    "v_cvt_pk_bf16_f32 v247, v206, v207\n"
    "v_mfma_f32_16x16x32_bf16 a[48:51], a[228:231], v[236:239], a[48:51]\n"
    "v_exp_f32 v220, v220\n"
    "v_mfma_f32_16x16x32_bf16 a[52:55], a[236:239], v[236:239], a[52:55]\n"
    "v_exp_f32 v221, v221\n"
    "v_mfma_f32_16x16x32_bf16 a[56:59], a[244:247], v[236:239], a[56:59]\n"
    "ds_read_b128 a[176:179], v2 offset:12288\n"
    "v_cvt_pk_bf16_f32 v252, v216, v217\n"
    "v_mfma_f32_16x16x32_bf16 a[60:63], a[252:255], v[236:239], a[60:63]\n"
    "v_exp_f32 v222, v222\n"
    "v_mfma_f32_16x16x32_bf16 a[64:67], a[196:199], v[244:247], a[64:67]\n"
    "ds_read_b128 a[180:183], v2 offset:13312\n"
    "v_cvt_pk_bf16_f32 v253, v218, v219\n"
    "v_mfma_f32_16x16x32_bf16 a[68:71], a[204:207], v[244:247], a[68:71]\n"
    "v_exp_f32 v223, v223\n"
    "v_mfma_f32_16x16x32_bf16 a[72:75], a[212:215], v[244:247], a[72:75]\n"
    "v_cvt_pk_bf16_f32 v254, v220, v221\n"
    "v_cvt_pk_bf16_f32 v255, v222, v223\n"
    "v_mfma_f32_16x16x32_bf16 a[76:79], a[220:223], v[244:247], a[76:79]\n"
    "ds_read_b128 a[184:187], v2 offset:14336\n"
    "v_add_f32 v8, v8, v164\n"
    "v_mfma_f32_16x16x32_bf16 a[80:83], a[228:231], v[244:247], a[80:83]\n"
    "v_add_f32 v8, v8, v165\n"
    "v_add_f32 v8, v8, v166\n"
    "v_mfma_f32_16x16x32_bf16 a[84:87], a[236:239], v[244:247], a[84:87]\n"
    "v_add_f32 v8, v8, v167\n"
    "v_add_f32 v9, v9, v176\n"
    "v_mfma_f32_16x16x32_bf16 a[88:91], a[244:247], v[244:247], a[88:91]\n"
    "s_waitcnt lgkmcnt(8)\n"
    "ds_read_b128 a[188:191], v2 offset:15360\n"
    "v_add_f32 v9, v9, v177\n"
    "v_mfma_f32_16x16x32_bf16 a[92:95], a[252:255], v[244:247], a[92:95]\n"
    "v_add_f32 v9, v9, v178\n"
    "v_add_f32 v9, v9, v179\n"
    "v_mfma_f32_16x16x32_bf16 a[96:99], a[196:199], v[252:255], a[96:99]\n"
    "v_add_f32 v9, v9, v180\n"
    "v_add_f32 v9, v9, v181\n"
    "v_mfma_f32_16x16x32_bf16 a[100:103], a[204:207], v[252:255], a[100:103]\n"
    "v_add_f32 v9, v9, v182\n"
    "v_mfma_f32_16x16x32_bf16 a[104:107], a[212:215], v[252:255], a[104:107]\n"
    "v_add_f32 v9, v9, v183\n"
    "v_add_f32 v10, v10, v192\n"
    "v_mfma_f32_16x16x32_bf16 a[108:111], a[220:223], v[252:255], a[108:111]\n"
    "v_add_f32 v10, v10, v193\n"
    "v_add_f32 v10, v10, v194\n"
    "v_mfma_f32_16x16x32_bf16 a[112:115], a[228:231], v[252:255], a[112:115]\n"
    "v_add_f32 v10, v10, v195\n"
    "v_mfma_f32_16x16x32_bf16 a[116:119], a[236:239], v[252:255], a[116:119]\n"
    "v_add_f32 v10, v10, v196\n"
    "v_add_f32 v10, v10, v197\n"
    "v_mfma_f32_16x16x32_bf16 a[120:123], a[244:247], v[252:255], a[120:123]\n"
    "v_add_f32 v10, v10, v198\n"
    "v_add_f32 v10, v10, v199\n"
    "v_mfma_f32_16x16x32_bf16 a[124:127], a[252:255], v[252:255], a[124:127]\n"
    "v_add_f32 v11, v11, v208\n"
    "s_waitcnt lgkmcnt(0)\n"
    "v_add_f32 v10, v10, v200\n"
    "v_add_f32 v10, v10, v201\n"
    "v_add_f32 v10, v10, v202\n"
    "v_add_f32 v10, v10, v203\n"
    "v_add_f32 v10, v10, v204\n"
    "v_add_f32 v10, v10, v205\n"
    "v_add_f32 v10, v10, v206\n"
    "v_add_f32 v10, v10, v207\n"
    "v_add_f32 v11, v11, v216\n"
    "v_add_f32 v11, v11, v217\n"
    "v_add_f32 v11, v11, v218\n"
    "v_add_f32 v11, v11, v219\n"
    "v_add_f32 v11, v11, v220\n"
    "v_add_f32 v11, v11, v221\n"
    "v_add_f32 v11, v11, v222\n"
    "v_add_f32 v11, v11, v223\n"
    "v_mov_b32 v16, v10\n"
    "v_mov_b32 v20, v16\n"
    "s_nop 1\n"
    "v_permlane16_swap_b32 v16, v20\n"
    "v_add_f32 v16, v16, v20\n"
    "v_mov_b32 v20, v16\n"
    "s_nop 1\n"
    "v_permlane32_swap_b32 v16, v20\n"
    "v_add_f32 v16, v16, v20\n"
    "v_mov_b32 v17, v11\n"
    "v_mov_b32 v20, v17\n"
    "s_nop 1\n"
    "v_permlane16_swap_b32 v17, v20\n"
    "v_add_f32 v17, v17, v20\n"
    "v_mov_b32 v20, v17\n"
    "s_nop 1\n"
    "v_permlane32_swap_b32 v17, v20\n"
    "v_add_f32 v17, v17, v20\n"
    "v_rcp_f32 v18, v16\n"
    "v_rcp_f32 v19, v17\n"
    "v_mov_b32 v20, 0x53800000\n"
    "v_cmp_nlt_f32 vcc, v16, v20\n"
    "s_nop 0\n"
    "s_mov_b64 s[92:93], vcc\n"
    "v_cmp_nlt_f32 vcc, v17, v20\n"
    "s_nop 0\n"
    "s_or_b64 s[92:93], s[92:93], vcc\n"
    "v_lshrrev_b32 v20, 2, v1\n"
    "v_and_b32 v20, 0x7c, v20\n"
    "s_lshl_b32 s96, s54, 2\n"
    "s_lshr_b32 s95, s53, 3\n"
    "s_add_u32 s96, s96, s95\n"
    "s_lshl_b32 s96, s96, 7\n"
    "s_add_u32 s96, s96, 0x3000\n"
    "buffer_load_dword v24, v20, s[44:47], s96 offen sc1\n"
    "v_accvgpr_read_b32 v160, a64\n"
    "v_accvgpr_read_b32 v161, a65\n"
    "v_accvgpr_read_b32 v162, a66\n"
    "v_accvgpr_read_b32 v163, a67\n"
    "v_accvgpr_read_b32 v164, a68\n"
    "v_accvgpr_read_b32 v165, a69\n"
    "v_accvgpr_read_b32 v166, a70\n"
    "v_accvgpr_read_b32 v167, a71\n"
    "v_pk_mul_f32 v[160:161], v[160:161], v[18:19] op_sel:[0,0] op_sel_hi:[1,0]\n"
    "v_pk_mul_f32 v[162:163], v[162:163], v[18:19] op_sel:[0,0] op_sel_hi:[1,0]\n"
    "v_pk_mul_f32 v[164:165], v[164:165], v[18:19] op_sel:[0,0] op_sel_hi:[1,0]\n"
    "v_pk_mul_f32 v[166:167], v[166:167], v[18:19] op_sel:[0,0] op_sel_hi:[1,0]\n"
    "v_cvt_pk_bf16_f32 v224, v160, v161\n"
    "v_cvt_pk_bf16_f32 v225, v162, v163\n"
    "v_cvt_pk_bf16_f32 v226, v164, v165\n"
    "v_cvt_pk_bf16_f32 v227, v166, v167\n"
    "s_add_u32 s58, s82, 0\n"
    "buffer_store_dwordx4 v[224:227], v1, s[44:47], s58 offen sc1\n"
    "v_accvgpr_read_b32 v168, a72\n"
    "v_accvgpr_read_b32 v169, a73\n"
    "v_accvgpr_read_b32 v170, a74\n"
    "v_accvgpr_read_b32 v171, a75\n"
    "v_accvgpr_read_b32 v172, a76\n"
    "v_accvgpr_read_b32 v173, a77\n"
    "v_accvgpr_read_b32 v174, a78\n"
    "v_accvgpr_read_b32 v175, a79\n"
    "v_pk_mul_f32 v[168:169], v[168:169], v[18:19] op_sel:[0,0] op_sel_hi:[1,0]\n"
    "v_pk_mul_f32 v[170:171], v[170:171], v[18:19] op_sel:[0,0] op_sel_hi:[1,0]\n"
    "v_pk_mul_f32 v[172:173], v[172:173], v[18:19] op_sel:[0,0] op_sel_hi:[1,0]\n"
    "v_pk_mul_f32 v[174:175], v[174:175], v[18:19] op_sel:[0,0] op_sel_hi:[1,0]\n"
    "v_cvt_pk_bf16_f32 v228, v168, v169\n"
    "v_cvt_pk_bf16_f32 v229, v170, v171\n"
    "v_cvt_pk_bf16_f32 v230, v172, v173\n"
    "v_cvt_pk_bf16_f32 v231, v174, v175\n"
    "s_add_u32 s58, s82, 1024\n"
    "buffer_store_dwordx4 v[228:231], v1, s[44:47], s58 offen sc1\n"
    "v_accvgpr_read_b32 v176, a80\n"
    "v_accvgpr_read_b32 v177, a81\n"
    "v_accvgpr_read_b32 v178, a82\n"
    "v_accvgpr_read_b32 v179, a83\n"
    "v_accvgpr_read_b32 v180, a84\n"
    "v_accvgpr_read_b32 v181, a85\n"
    "v_accvgpr_read_b32 v182, a86\n"
    "v_accvgpr_read_b32 v183, a87\n"
    "v_pk_mul_f32 v[176:177], v[176:177], v[18:19] op_sel:[0,0] op_sel_hi:[1,0]\n"
    "v_pk_mul_f32 v[178:179], v[178:179], v[18:19] op_sel:[0,0] op_sel_hi:[1,0]\n"
    "v_pk_mul_f32 v[180:181], v[180:181], v[18:19] op_sel:[0,0] op_sel_hi:[1,0]\n"
    "v_pk_mul_f32 v[182:183], v[182:183], v[18:19] op_sel:[0,0] op_sel_hi:[1,0]\n"
    "v_cvt_pk_bf16_f32 v232, v176, v177\n"
    "v_cvt_pk_bf16_f32 v233, v178, v179\n"
    "v_cvt_pk_bf16_f32 v234, v180, v181\n"
    "v_cvt_pk_bf16_f32 v235, v182, v183\n"
    "s_add_u32 s58, s82, 2048\n"
    "buffer_store_dwordx4 v[232:235], v1, s[44:47], s58 offen sc1\n"
    "v_accvgpr_read_b32 v184, a88\n"
    "v_accvgpr_read_b32 v185, a89\n"
    "v_accvgpr_read_b32 v186, a90\n"
    "v_accvgpr_read_b32 v187, a91\n"
    "v_accvgpr_read_b32 v188, a92\n"
    "v_accvgpr_read_b32 v189, a93\n"
    "v_accvgpr_read_b32 v190, a94\n"
    "v_accvgpr_read_b32 v191, a95\n"
    "v_pk_mul_f32 v[184:185], v[184:185], v[18:19] op_sel:[0,0] op_sel_hi:[1,0]\n"
    "v_pk_mul_f32 v[186:187], v[186:187], v[18:19] op_sel:[0,0] op_sel_hi:[1,0]\n"
    "v_pk_mul_f32 v[188:189], v[188:189], v[18:19] op_sel:[0,0] op_sel_hi:[1,0]\n"
    "v_pk_mul_f32 v[190:191], v[190:191], v[18:19] op_sel:[0,0] op_sel_hi:[1,0]\n"
    "v_cvt_pk_bf16_f32 v236, v184, v185\n"
    "v_cvt_pk_bf16_f32 v237, v186, v187\n"
    "v_cvt_pk_bf16_f32 v238, v188, v189\n"
    "v_cvt_pk_bf16_f32 v239, v190, v191\n"
    "s_add_u32 s58, s82, 3072\n"
    "buffer_store_dwordx4 v[236:239], v1, s[44:47], s58 offen sc1\n"
    "v_accvgpr_read_b32 v192, a96\n"
    "v_accvgpr_read_b32 v193, a97\n"
    "v_accvgpr_read_b32 v194, a98\n"
    "v_accvgpr_read_b32 v195, a99\n"
    "v_accvgpr_read_b32 v196, a100\n"
    "v_accvgpr_read_b32 v197, a101\n"
    "v_accvgpr_read_b32 v198, a102\n"
    "v_accvgpr_read_b32 v199, a103\n"
    "v_pk_mul_f32 v[192:193], v[192:193], v[18:19] op_sel:[0,1] op_sel_hi:[1,1]\n"
    "v_pk_mul_f32 v[194:195], v[194:195], v[18:19] op_sel:[0,1] op_sel_hi:[1,1]\n"
    "v_pk_mul_f32 v[196:197], v[196:197], v[18:19] op_sel:[0,1] op_sel_hi:[1,1]\n"
    "v_pk_mul_f32 v[198:199], v[198:199], v[18:19] op_sel:[0,1] op_sel_hi:[1,1]\n"
    "v_cvt_pk_bf16_f32 v240, v192, v193\n"
    "v_cvt_pk_bf16_f32 v241, v194, v195\n"
    "v_cvt_pk_bf16_f32 v242, v196, v197\n"
    "v_cvt_pk_bf16_f32 v243, v198, v199\n"
    "s_add_u32 s58, s82, 131072\n"
    "buffer_store_dwordx4 v[240:243], v1, s[44:47], s58 offen sc1\n"
    "v_accvgpr_read_b32 v200, a104\n"
    "v_accvgpr_read_b32 v201, a105\n"
    "v_accvgpr_read_b32 v202, a106\n"
    "v_accvgpr_read_b32 v203, a107\n"
    "v_accvgpr_read_b32 v204, a108\n"
    "v_accvgpr_read_b32 v205, a109\n"
    "v_accvgpr_read_b32 v206, a110\n"
    "v_accvgpr_read_b32 v207, a111\n"
    "v_pk_mul_f32 v[200:201], v[200:201], v[18:19] op_sel:[0,1] op_sel_hi:[1,1]\n"
    "v_pk_mul_f32 v[202:203], v[202:203], v[18:19] op_sel:[0,1] op_sel_hi:[1,1]\n"
    "v_pk_mul_f32 v[204:205], v[204:205], v[18:19] op_sel:[0,1] op_sel_hi:[1,1]\n"
    "v_pk_mul_f32 v[206:207], v[206:207], v[18:19] op_sel:[0,1] op_sel_hi:[1,1]\n"
    "v_cvt_pk_bf16_f32 v244, v200, v201\n"
    "v_cvt_pk_bf16_f32 v245, v202, v203\n"
    "v_cvt_pk_bf16_f32 v246, v204, v205\n"
    "v_cvt_pk_bf16_f32 v247, v206, v207\n"
    "s_add_u32 s58, s82, 132096\n"
    "buffer_store_dwordx4 v[244:247], v1, s[44:47], s58 offen sc1\n"
    "v_accvgpr_read_b32 v208, a112\n"
    "v_accvgpr_read_b32 v209, a113\n"
    "v_accvgpr_read_b32 v210, a114\n"
    "v_accvgpr_read_b32 v211, a115\n"
    "v_accvgpr_read_b32 v212, a116\n"
    "v_accvgpr_read_b32 v213, a117\n"
    "v_accvgpr_read_b32 v214, a118\n"
    "v_accvgpr_read_b32 v215, a119\n"
    "v_pk_mul_f32 v[208:209], v[208:209], v[18:19] op_sel:[0,1] op_sel_hi:[1,1]\n"
    "v_pk_mul_f32 v[210:211], v[210:211], v[18:19] op_sel:[0,1] op_sel_hi:[1,1]\n"
    "v_pk_mul_f32 v[212:213], v[212:213], v[18:19] op_sel:[0,1] op_sel_hi:[1,1]\n"
    "v_pk_mul_f32 v[214:215], v[214:215], v[18:19] op_sel:[0,1] op_sel_hi:[1,1]\n"
    "v_cvt_pk_bf16_f32 v248, v208, v209\n"
    "v_cvt_pk_bf16_f32 v249, v210, v211\n"
    "v_cvt_pk_bf16_f32 v250, v212, v213\n"
    "v_cvt_pk_bf16_f32 v251, v214, v215\n"
    "s_add_u32 s58, s82, 133120\n"
    "buffer_store_dwordx4 v[248:251], v1, s[44:47], s58 offen sc1\n"
    "v_accvgpr_read_b32 v216, a120\n"
    "v_accvgpr_read_b32 v217, a121\n"
    "v_accvgpr_read_b32 v218, a122\n"
    "v_accvgpr_read_b32 v219, a123\n"
    "v_accvgpr_read_b32 v220, a124\n"
    "v_accvgpr_read_b32 v221, a125\n"
    "v_accvgpr_read_b32 v222, a126\n"
    "v_accvgpr_read_b32 v223, a127\n"
    "v_pk_mul_f32 v[216:217], v[216:217], v[18:19] op_sel:[0,1] op_sel_hi:[1,1]\n"
    "v_pk_mul_f32 v[218:219], v[218:219], v[18:19] op_sel:[0,1] op_sel_hi:[1,1]\n"
    "v_pk_mul_f32 v[220:221], v[220:221], v[18:19] op_sel:[0,1] op_sel_hi:[1,1]\n"
    "v_pk_mul_f32 v[222:223], v[222:223], v[18:19] op_sel:[0,1] op_sel_hi:[1,1]\n"
    "v_cvt_pk_bf16_f32 v252, v216, v217\n"
    "v_cvt_pk_bf16_f32 v253, v218, v219\n"
    "v_cvt_pk_bf16_f32 v254, v220, v221\n"
    "v_cvt_pk_bf16_f32 v255, v222, v223\n"
    "s_add_u32 s58, s82, 134144\n"
    "buffer_store_dwordx4 v[252:255], v1, s[44:47], s58 offen sc1\n"
    "s_and_b32 s59, s53, 7\n"
    "s_lshl_b32 s59, s59, 3\n"
    "s_add_u32 s59, s59, s54\n"
    "s_lshl_b32 s59, s59, 2\n"
    "s_add_u32 s59, s59, s52\n"
    "s_lshl_b32 s59, s59, 2\n"
    "s_lshr_b32 s58, s53, 3\n"
    "s_add_u32 s59, s59, s58\n"
    "s_lshl_b32 s59, s59, 13\n"
    "s_add_u32 s59, s59, 0x4000800\n"
    "s_add_u32 s58, s59, 4096\n"
    "v_mov_b32 v224, -1\n"
    "v_mov_b32 v225, -1\n"
    "v_mov_b32 v226, -1\n"
    "v_mov_b32 v227, -1\n"
    "buffer_store_dwordx4 v[224:227], v1, s[44:47], s59 offen offset:0 sc1\n"
    "buffer_store_dwordx4 v[224:227], v1, s[44:47], s59 offen offset:1024 sc1\n"
    "buffer_store_dwordx4 v[224:227], v1, s[44:47], s59 offen offset:2048 sc1\n"
    "buffer_store_dwordx4 v[224:227], v1, s[44:47], s59 offen offset:3072 sc1\n"
    "buffer_store_dwordx4 v[224:227], v1, s[44:47], s58 offen offset:0 sc1\n"
    "buffer_store_dwordx4 v[224:227], v1, s[44:47], s58 offen offset:1024 sc1\n"
    "s_xor_b32 s56, s52, 1\n"
    "s_lshl_b32 s56, s56, 13\n"
    "s_add_u32 s56, s56, 131072\n"
    "v_add_u32 v16, s56, v1\n"
    "ds_read_b128 v[128:131], v16 offset:0\n"
    "ds_read_b128 v[132:135], v16 offset:1024\n"
    "ds_read_b128 v[136:139], v16 offset:2048\n"
    "ds_read_b128 v[140:143], v16 offset:3072\n"
    "ds_read_b128 v[144:147], v16 offset:4096\n"
    "ds_read_b128 v[148:151], v16 offset:5120\n"
    "ds_read_b128 v[152:155], v16 offset:6144\n"
    "ds_read_b128 v[156:159], v16 offset:7168\n"
    "v_mov_b32 v14, v26\n"
    "v_mov_b32 v15, v27\n"
    "v_mov_b32 v64, v14\n"
    "v_mov_b32 v65, v14\n"
    "v_mov_b32 v66, v14\n"
    "v_mov_b32 v67, v14\n"
    "v_mov_b32 v68, v14\n"
    "v_mov_b32 v69, v14\n"
    "v_mov_b32 v70, v14\n"
    "v_mov_b32 v71, v14\n"
    "v_mov_b32 v72, v14\n"
    "v_mov_b32 v73, v14\n"
    "v_mov_b32 v74, v14\n"
    "v_mov_b32 v75, v14\n"
    "v_mov_b32 v76, v14\n"
    "v_mov_b32 v77, v14\n"
    "v_mov_b32 v78, v14\n"
    "v_mov_b32 v79, v14\n"
    "v_mov_b32 v80, v15\n"
    "v_mov_b32 v81, v15\n"
    "v_mov_b32 v82, v15\n"
    "v_mov_b32 v83, v15\n"
    "v_mov_b32 v84, v15\n"
    "v_mov_b32 v85, v15\n"
    "v_mov_b32 v86, v15\n"
    "v_mov_b32 v87, v15\n"
    "v_mov_b32 v88, v15\n"
    "v_mov_b32 v89, v15\n"
    "v_mov_b32 v90, v15\n"
    "v_mov_b32 v91, v15\n"
    "v_mov_b32 v92, v15\n"
    "v_mov_b32 v93, v15\n"
    "v_mov_b32 v94, v15\n"
    "v_mov_b32 v95, v15\n"
    "v_mov_b32 v10, 0\n"
    "v_mov_b32 v11, 0\n"
    "v_mov_b32 v20, 0\n"
    "v_mov_b32 v21, 0\n"
    "v_mov_b32 v22, 0\n"
    "v_mov_b32 v23, 0\n"
    "s_nop 1\n"
    "v_mfma_f32_32x32x16_bf16 a[64:79], v[20:23], v[20:23], 0\n"
    "v_mfma_f32_32x32x16_bf16 a[80:95], v[20:23], v[20:23], 0\n"
    "v_mfma_f32_32x32x16_bf16 a[96:111], v[20:23], v[20:23], 0\n"
    "v_mfma_f32_32x32x16_bf16 a[112:127], v[20:23], v[20:23], 0\n"
    "v_mov_b32 v200, 0\n"
    "v_mov_b32 v201, 0\n"
    "v_mov_b32 v202, 0\n"
    "v_mov_b32 v203, 0\n"
    "v_mov_b32 v204, 0\n"
    "v_mov_b32 v205, 0\n"
    "v_mov_b32 v206, 0\n"
    "v_mov_b32 v207, 0\n"
    "v_mov_b32 v216, 0\n"
    "v_mov_b32 v217, 0\n"
    "v_mov_b32 v218, 0\n"
    "v_mov_b32 v219, 0\n"
    "v_mov_b32 v220, 0\n"
    "v_mov_b32 v221, 0\n"
    "v_mov_b32 v222, 0\n"
    "v_mov_b32 v223, 0\n"
    "v_add_u32 v3, s83, v3\n"
    "s_xor_b32 s79, s78, 32\n"
    "s_lshl_b32 s77, s54, 1\n"
    "s_sub_u32 s77, 14, s77\n"
    "s_cmp_eq_u32 s77, 0\n"
    "s_cbranch_scc1 L_l2e_12_%=\n"
    "L_l2_11_%=:\n"
    "s_waitcnt vmcnt(0)\n"
    "s_waitcnt lgkmcnt(0)\n"
    "s_barrier\n"
    "v_mfma_f32_16x16x32_bf16 v[160:163], a[128:131], v[96:99], v[32:35]\n"
    "s_add_u32 s57, s72, 0\n"
    "ds_read_b128 a[192:195], v3 offset:0\n"
    "v_add_f32 v10, v10, v200\n"
    "v_mfma_f32_16x16x32_bf16 v[160:163], a[132:135], v[100:103], v[160:163]\n"
    "s_add_u32 m0, s76, 0\n"
    "s_nop 0\n"
    "buffer_load_dwordx4 v1, s[44:47], s57 offen sc1 lds\n"
    "v_add_f32 v10, v10, v201\n"
    "v_mfma_f32_16x16x32_bf16 v[160:163], a[136:139], v[104:107], v[160:163]\n"
    "ds_read_b128 a[200:203], v3 offset:2048\n"
    "v_add_f32 v10, v10, v202\n"
    "v_mfma_f32_16x16x32_bf16 v[160:163], a[140:143], v[108:111], v[160:163]\n"
    "ds_read_b128 a[208:211], v3 offset:4096\n"
    "v_add_f32 v10, v10, v203\n"
    "v_mfma_f32_16x16x32_bf16 v[164:167], a[144:147], v[96:99], v[36:39]\n"
    "ds_read_b128 a[216:219], v3 offset:6144\n"
    "v_add_f32 v10, v10, v204\n"
    "v_mfma_f32_16x16x32_bf16 v[164:167], a[148:151], v[100:103], v[164:167]\n"
    "buffer_load_dwordx4 v1, s[44:47], s57 offen offset:1024 sc1 lds\n"
    "v_add_f32 v10, v10, v205\n"
    "v_mfma_f32_16x16x32_bf16 v[164:167], a[152:155], v[104:107], v[164:167]\n"
    "ds_read_b128 a[224:227], v3 offset:8192\n"
    "v_add_f32 v10, v10, v206\n"
    "v_mfma_f32_16x16x32_bf16 v[164:167], a[156:159], v[108:111], v[164:167]\n"
    "ds_read_b128 a[232:235], v3 offset:10240\n"
    "v_add_f32 v10, v10, v207\n"
    "v_mfma_f32_16x16x32_bf16 v[176:179], a[128:131], v[112:115], v[48:51]\n"
    "ds_read_b128 a[240:243], v3 offset:12288\n"
    "v_add_f32 v11, v11, v216\n"
    "v_mfma_f32_16x16x32_bf16 v[176:179], a[132:135], v[116:119], v[176:179]\n"
    "v_exp_f32 v160, v160\n"
    "v_add_f32 v11, v11, v222\n"
    "v_mfma_f32_16x16x32_bf16 v[176:179], a[136:139], v[120:123], v[176:179]\n"
    "v_exp_f32 v161, v161\n"
    "v_add_f32 v11, v11, v223\n"
    "v_mfma_f32_16x16x32_bf16 v[176:179], a[140:143], v[124:127], v[176:179]\n"
    "buffer_load_dwordx4 v1, s[44:47], s57 offen offset:2048 sc1 lds\n"
    "v_cvt_pk_bf16_f32 v224, v160, v161\n"
    "v_mfma_f32_16x16x32_bf16 v[180:183], a[144:147], v[112:115], v[52:55]\n"
    "v_exp_f32 v162, v162\n"
    "v_mfma_f32_16x16x32_bf16 v[180:183], a[148:151], v[116:119], v[180:183]\n"
    "v_exp_f32 v163, v163\n"
    "v_mfma_f32_16x16x32_bf16 v[180:183], a[152:155], v[120:123], v[180:183]\n"
    "ds_read_b128 a[248:251], v3 offset:14336\n"
    "v_cvt_pk_bf16_f32 v225, v162, v163\n"
    "v_mfma_f32_16x16x32_bf16 v[180:183], a[156:159], v[124:127], v[180:183]\n"
    "v_exp_f32 v164, v164\n"
    "v_mfma_f32_16x16x32_bf16 v[192:195], a[128:131], v[128:131], v[64:67]\n"
    "buffer_load_dwordx4 v1, s[44:47], s57 offen offset:3072 sc1 lds\n"
    "v_add_f32 v11, v11, v217\n"
    "v_mfma_f32_16x16x32_bf16 v[192:195], a[132:135], v[132:135], v[192:195]\n"
    "v_exp_f32 v165, v165\n"
    "v_mfma_f32_16x16x32_bf16 v[192:195], a[136:139], v[136:139], v[192:195]\n"
    "v_exp_f32 v166, v166\n"
    "v_mfma_f32_16x16x32_bf16 v[192:195], a[140:143], v[140:143], v[192:195]\n"
    "ds_read_b128 a[196:199], v3 offset:1024\n"
    "v_cvt_pk_bf16_f32 v226, v164, v165\n"
    "v_mfma_f32_16x16x32_bf16 v[196:199], a[144:147], v[128:131], v[68:71]\n"
    "v_exp_f32 v167, v167\n"
    "v_mfma_f32_16x16x32_bf16 v[196:199], a[148:151], v[132:135], v[196:199]\n"
    "s_add_u32 s57, s72, 16384\n"
    "v_exp_f32 v176, v176\n"
    "v_mfma_f32_16x16x32_bf16 v[196:199], a[152:155], v[136:139], v[196:199]\n"
    "s_add_u32 m0, s76, 16384\n"
    "s_nop 0\n"
    "buffer_load_dwordx4 v1, s[44:47], s57 offen sc1 lds\n"
    "v_cvt_pk_bf16_f32 v227, v166, v167\n"
    "v_mfma_f32_16x16x32_bf16 v[196:199], a[156:159], v[140:143], v[196:199]\n"
    "v_exp_f32 v177, v177\n"
    "v_mfma_f32_16x16x32_bf16 v[208:211], a[128:131], v[144:147], v[80:83]\n"
    "ds_read_b128 a[204:207], v3 offset:3072\n"
    "v_cvt_pk_bf16_f32 v232, v176, v177\n"
    "v_mfma_f32_16x16x32_bf16 v[208:211], a[132:135], v[148:151], v[208:211]\n"
    "v_exp_f32 v178, v178\n"
    "v_mfma_f32_16x16x32_bf16 v[208:211], a[136:139], v[152:155], v[208:211]\n"
    "v_exp_f32 v179, v179\n"
    "v_mfma_f32_16x16x32_bf16 v[208:211], a[140:143], v[156:159], v[208:211]\n"
    "buffer_load_dwordx4 v1, s[44:47], s57 offen offset:1024 sc1 lds\n"
    "v_cvt_pk_bf16_f32 v233, v178, v179\n"
    "v_mfma_f32_16x16x32_bf16 v[212:215], a[144:147], v[144:147], v[84:87]\n"
    "v_exp_f32 v180, v180\n"
    "v_mfma_f32_16x16x32_bf16 v[212:215], a[148:151], v[148:151], v[212:215]\n"
    "v_exp_f32 v181, v181\n"
    "v_mfma_f32_16x16x32_bf16 v[212:215], a[152:155], v[152:155], v[212:215]\n"
    "ds_read_b128 a[212:215], v3 offset:5120\n"
    "v_cvt_pk_bf16_f32 v234, v180, v181\n"
    "v_mfma_f32_16x16x32_bf16 v[212:215], a[156:159], v[156:159], v[212:215]\n"
    "v_exp_f32 v182, v182\n"
    "v_mfma_f32_16x16x32_bf16 v[168:171], a[160:163], v[96:99], v[40:43]\n"
    "buffer_load_dwordx4 v1, s[44:47], s57 offen offset:2048 sc1 lds\n"
    "v_add_f32 v11, v11, v218\n"
    "v_mfma_f32_16x16x32_bf16 v[168:171], a[164:167], v[100:103], v[168:171]\n"
    "v_exp_f32 v183, v183\n"
    "v_mfma_f32_16x16x32_bf16 v[168:171], a[168:171], v[104:107], v[168:171]\n"
    "v_exp_f32 v192, v192\n"
    "v_mfma_f32_16x16x32_bf16 v[168:171], a[172:175], v[108:111], v[168:171]\n"
    "ds_read_b128 a[220:223], v3 offset:7168\n"
    "v_cvt_pk_bf16_f32 v235, v182, v183\n"
    "v_mfma_f32_16x16x32_bf16 v[172:175], a[176:179], v[96:99], v[44:47]\n"
    "v_exp_f32 v193, v193\n"
    "v_mfma_f32_16x16x32_bf16 v[172:175], a[180:183], v[100:103], v[172:175]\n"
    "v_exp_f32 v194, v194\n"
    "v_mfma_f32_16x16x32_bf16 v[172:175], a[184:187], v[104:107], v[172:175]\n"
    "buffer_load_dwordx4 v1, s[44:47], s57 offen offset:3072 sc1 lds\n"
    "v_cvt_pk_bf16_f32 v240, v192, v193\n"
    "v_mfma_f32_16x16x32_bf16 v[172:175], a[188:191], v[108:111], v[172:175]\n"
    "v_exp_f32 v195, v195\n"
    "v_mfma_f32_16x16x32_bf16 v[184:187], a[160:163], v[112:115], v[56:59]\n"
    "ds_read_b128 a[228:231], v3 offset:9216\n"
    "v_cvt_pk_bf16_f32 v241, v194, v195\n"
    "v_mfma_f32_16x16x32_bf16 v[184:187], a[164:167], v[116:119], v[184:187]\n"
    "v_exp_f32 v196, v196\n"
    "v_mfma_f32_16x16x32_bf16 v[184:187], a[168:171], v[120:123], v[184:187]\n"
    "s_add_u32 s56, s71, 0\n"
    "s_add_u32 s72, s72, s74\n"
    "s_xor_b32 s76, s76, 0x10000\n"
    "v_mfma_f32_16x16x32_bf16 v[184:187], a[172:175], v[124:127], v[184:187]\n"
    "s_add_u32 m0, s75, 0\n"
    "s_nop 0\n"
    "buffer_load_dwordx4 v1, s[44:47], s56 offen sc1 lds\n"
    "v_add_f32 v11, v11, v219\n"
    "v_mfma_f32_16x16x32_bf16 v[188:191], a[176:179], v[112:115], v[60:63]\n"
    "v_exp_f32 v197, v197\n"
    "v_mfma_f32_16x16x32_bf16 v[188:191], a[180:183], v[116:119], v[188:191]\n"
    "v_exp_f32 v198, v198\n"
    "v_mfma_f32_16x16x32_bf16 v[188:191], a[184:187], v[120:123], v[188:191]\n"
    "ds_read_b128 a[236:239], v3 offset:11264\n"
    "v_cvt_pk_bf16_f32 v242, v196, v197\n"
    "v_mfma_f32_16x16x32_bf16 v[188:191], a[188:191], v[124:127], v[188:191]\n"
    "v_exp_f32 v199, v199\n"
    "v_mfma_f32_16x16x32_bf16 v[200:203], a[160:163], v[128:131], v[72:75]\n"
    "buffer_load_dwordx4 v1, s[44:47], s56 offen offset:1024 sc1 lds\n"
    "v_cvt_pk_bf16_f32 v243, v198, v199\n"
    "v_mfma_f32_16x16x32_bf16 v[200:203], a[164:167], v[132:135], v[200:203]\n"
    "v_exp_f32 v208, v208\n"
    "v_mfma_f32_16x16x32_bf16 v[200:203], a[168:171], v[136:139], v[200:203]\n"
    "v_exp_f32 v209, v209\n"
    "v_mfma_f32_16x16x32_bf16 v[200:203], a[172:175], v[140:143], v[200:203]\n"
    "ds_read_b128 a[244:247], v3 offset:13312\n"
    "v_cvt_pk_bf16_f32 v248, v208, v209\n"
    "v_mfma_f32_16x16x32_bf16 v[204:207], a[176:179], v[128:131], v[76:79]\n"
    "v_exp_f32 v210, v210\n"
    "v_add_f32 v11, v11, v209\n"
    "v_mfma_f32_16x16x32_bf16 v[204:207], a[180:183], v[132:135], v[204:207]\n"
    "v_exp_f32 v211, v211\n"
    "v_add_f32 v11, v11, v210\n"
    "v_mfma_f32_16x16x32_bf16 v[204:207], a[184:187], v[136:139], v[204:207]\n"
    "buffer_load_dwordx4 v1, s[44:47], s56 offen offset:2048 sc1 lds\n"
    "v_cvt_pk_bf16_f32 v249, v210, v211\n"
    "v_mfma_f32_16x16x32_bf16 v[204:207], a[188:191], v[140:143], v[204:207]\n"
    "v_exp_f32 v212, v212\n"
    "v_add_f32 v11, v11, v211\n"
    "v_mfma_f32_16x16x32_bf16 v[216:219], a[160:163], v[144:147], v[88:91]\n"
    "s_waitcnt lgkmcnt(8)\n"
    "ds_read_b128 a[252:255], v3 offset:15360\n"
    "v_add_f32 v11, v11, v220\n"
    "v_mfma_f32_16x16x32_bf16 v[216:219], a[164:167], v[148:151], v[216:219]\n"
    "v_xor_b32 v3, 0x10000, v3\n"
    "v_add_f32 v11, v11, v221\n"
    "v_mfma_f32_16x16x32_bf16 v[216:219], a[168:171], v[152:155], v[216:219]\n"
    "v_exp_f32 v213, v213\n"
    "v_add_f32 v11, v11, v212\n"
    "v_mfma_f32_16x16x32_bf16 v[216:219], a[172:175], v[156:159], v[216:219]\n"
    "buffer_load_dwordx4 v1, s[44:47], s56 offen offset:3072 sc1 lds\n"
    "v_cvt_pk_bf16_f32 v250, v212, v213\n"
    "v_mfma_f32_16x16x32_bf16 v[220:223], a[176:179], v[144:147], v[92:95]\n"
    "v_exp_f32 v214, v214\n"
    "v_add_f32 v11, v11, v213\n"
    "v_mfma_f32_16x16x32_bf16 v[220:223], a[180:183], v[148:151], v[220:223]\n"
    "s_waitcnt lgkmcnt(8)\n"
    "v_xor_b32 v2, 0x10000, v2\n"
    "v_add_f32 v8, v8, v160\n"
    "v_mfma_f32_16x16x32_bf16 v[220:223], a[184:187], v[152:155], v[220:223]\n"
    "ds_read_b128 a[128:131], v2 offset:0\n"
    "v_add_f32 v8, v8, v161\n"
    "v_mfma_f32_16x16x32_bf16 v[220:223], a[188:191], v[156:159], v[220:223]\n"
    "s_add_u32 s56, s71, 16384\n"
    "v_exp_f32 v215, v215\n"
    "v_add_f32 v11, v11, v214\n"
    "v_mfma_f32_16x16x32_bf16 a[0:3], a[192:195], v[224:227], a[0:3]\n"
    "s_add_u32 m0, s75, 16384\n"
    "s_nop 0\n"
    "buffer_load_dwordx4 v1, s[44:47], s56 offen sc1 lds\n"
    "v_cvt_pk_bf16_f32 v251, v214, v215\n"
    "v_mfma_f32_16x16x32_bf16 a[4:7], a[200:203], v[224:227], a[4:7]\n"
    "v_exp_f32 v168, v168\n"
    "v_add_f32 v11, v11, v215\n"
    "v_mfma_f32_16x16x32_bf16 a[8:11], a[208:211], v[224:227], a[8:11]\n"
    "v_exp_f32 v169, v169\n"
    "v_add_f32 v8, v8, v168\n"
    "v_mfma_f32_16x16x32_bf16 a[12:15], a[216:219], v[224:227], a[12:15]\n"
    "ds_read_b128 a[132:135], v2 offset:1024\n"
    "v_cvt_pk_bf16_f32 v228, v168, v169\n"
    "v_mfma_f32_16x16x32_bf16 a[16:19], a[224:227], v[224:227], a[16:19]\n"
    "v_exp_f32 v170, v170\n"
    "v_add_f32 v8, v8, v169\n"
    "v_mfma_f32_16x16x32_bf16 a[20:23], a[232:235], v[224:227], a[20:23]\n"
    "v_exp_f32 v171, v171\n"
    "v_add_f32 v8, v8, v170\n"
    "v_mfma_f32_16x16x32_bf16 a[24:27], a[240:243], v[224:227], a[24:27]\n"
    "buffer_load_dwordx4 v1, s[44:47], s56 offen offset:1024 sc1 lds\n"
    "v_cvt_pk_bf16_f32 v229, v170, v171\n"
    "v_mfma_f32_16x16x32_bf16 a[28:31], a[248:251], v[224:227], a[28:31]\n"
    "v_exp_f32 v172, v172\n"
    "v_add_f32 v8, v8, v171\n"
    "v_mfma_f32_16x16x32_bf16 a[32:35], a[192:195], v[232:235], a[32:35]\n"
    "ds_read_b128 a[136:139], v2 offset:2048\n"
    "v_add_f32 v8, v8, v162\n"
    "v_mfma_f32_16x16x32_bf16 a[36:39], a[200:203], v[232:235], a[36:39]\n"
    "v_exp_f32 v173, v173\n"
    "v_add_f32 v8, v8, v172\n"
    "v_mfma_f32_16x16x32_bf16 a[40:43], a[208:211], v[232:235], a[40:43]\n"
    "v_exp_f32 v174, v174\n"
    "v_add_f32 v8, v8, v173\n"
    "v_mfma_f32_16x16x32_bf16 a[44:47], a[216:219], v[232:235], a[44:47]\n"
    "buffer_load_dwordx4 v1, s[44:47], s56 offen offset:2048 sc1 lds\n"
    "v_cvt_pk_bf16_f32 v230, v172, v173\n"
    "v_mfma_f32_16x16x32_bf16 a[48:51], a[224:227], v[232:235], a[48:51]\n"
    "v_exp_f32 v175, v175\n"
    "v_add_f32 v8, v8, v174\n"
    "v_mfma_f32_16x16x32_bf16 a[52:55], a[232:235], v[232:235], a[52:55]\n"
    "v_exp_f32 v184, v184\n"
    "v_add_f32 v8, v8, v175\n"
    "v_mfma_f32_16x16x32_bf16 a[56:59], a[240:243], v[232:235], a[56:59]\n"
    "ds_read_b128 a[140:143], v2 offset:3072\n"
    "v_cvt_pk_bf16_f32 v231, v174, v175\n"
    "v_mfma_f32_16x16x32_bf16 a[60:63], a[248:251], v[232:235], a[60:63]\n"
    "v_exp_f32 v185, v185\n"
    "v_add_f32 v9, v9, v184\n"
    "v_mfma_f32_16x16x32_bf16 a[64:67], a[192:195], v[240:243], a[64:67]\n"
    "buffer_load_dwordx4 v1, s[44:47], s56 offen offset:3072 sc1 lds\n"
    "v_cvt_pk_bf16_f32 v236, v184, v185\n"
    "v_mfma_f32_16x16x32_bf16 a[68:71], a[200:203], v[240:243], a[68:71]\n"
    "s_add_u32 s71, s71, s73\n"
    "s_xor_b32 s75, s75, 0x10000\n"
    "v_mfma_f32_16x16x32_bf16 a[72:75], a[208:211], v[240:243], a[72:75]\n"
    "v_exp_f32 v186, v186\n"
    "v_add_f32 v9, v9, v185\n"
    "v_mfma_f32_16x16x32_bf16 a[76:79], a[216:219], v[240:243], a[76:79]\n"
    "ds_read_b128 a[144:147], v2 offset:4096\n"
    "v_add_f32 v8, v8, v163\n"
    "v_mfma_f32_16x16x32_bf16 a[80:83], a[224:227], v[240:243], a[80:83]\n"
    "v_exp_f32 v187, v187\n"
    "v_add_f32 v9, v9, v186\n"
    "v_mfma_f32_16x16x32_bf16 a[84:87], a[232:235], v[240:243], a[84:87]\n"
    "v_exp_f32 v188, v188\n"
    "v_add_f32 v9, v9, v187\n"
    "v_mfma_f32_16x16x32_bf16 a[88:91], a[240:243], v[240:243], a[88:91]\n"
    "v_exp_f32 v189, v189\n"
    "v_add_f32 v9, v9, v188\n"
    "v_mfma_f32_16x16x32_bf16 a[92:95], a[248:251], v[240:243], a[92:95]\n"
    "v_exp_f32 v190, v190\n"
    "v_add_f32 v9, v9, v189\n"
    "v_mfma_f32_16x16x32_bf16 a[96:99], a[192:195], v[248:251], a[96:99]\n"
    "ds_read_b128 a[148:151], v2 offset:5120\n"
    "v_cvt_pk_bf16_f32 v237, v186, v187\n"
    "v_mfma_f32_16x16x32_bf16 a[100:103], a[200:203], v[248:251], a[100:103]\n"
    "v_exp_f32 v191, v191\n"
    "v_add_f32 v9, v9, v190\n"
    "v_mfma_f32_16x16x32_bf16 a[104:107], a[208:211], v[248:251], a[104:107]\n"
    "v_exp_f32 v200, v200\n"
    "v_add_f32 v9, v9, v191\n"
    "v_mfma_f32_16x16x32_bf16 a[108:111], a[216:219], v[248:251], a[108:111]\n"
    "v_exp_f32 v201, v201\n"
    "v_mfma_f32_16x16x32_bf16 a[112:115], a[224:227], v[248:251], a[112:115]\n"
    "v_exp_f32 v202, v202\n"
    "v_mfma_f32_16x16x32_bf16 a[116:119], a[232:235], v[248:251], a[116:119]\n"
    "s_waitcnt lgkmcnt(6)\n"
    "v_exp_f32 v203, v203\n"
    "v_mfma_f32_16x16x32_bf16 a[120:123], a[240:243], v[248:251], a[120:123]\n"
    "ds_read_b128 a[152:155], v2 offset:6144\n"
    "v_cvt_pk_bf16_f32 v238, v188, v189\n"
    "v_mfma_f32_16x16x32_bf16 a[124:127], a[248:251], v[248:251], a[124:127]\n"
    "v_exp_f32 v204, v204\n"
    "v_mfma_f32_16x16x32_bf16 a[0:3], a[196:199], v[228:231], a[0:3]\n"
    "ds_read_b128 a[156:159], v2 offset:7168\n"
    "v_cvt_pk_bf16_f32 v239, v190, v191\n"
    "v_mfma_f32_16x16x32_bf16 a[4:7], a[204:207], v[228:231], a[4:7]\n"
    "v_exp_f32 v205, v205\n"
    "v_mfma_f32_16x16x32_bf16 a[8:11], a[212:215], v[228:231], a[8:11]\n"
    "v_exp_f32 v206, v206\n"
    "v_mfma_f32_16x16x32_bf16 a[12:15], a[220:223], v[228:231], a[12:15]\n"
    "ds_read_b128 a[160:163], v2 offset:8192\n"
    "v_cvt_pk_bf16_f32 v244, v200, v201\n"
    "v_mfma_f32_16x16x32_bf16 a[16:19], a[228:231], v[228:231], a[16:19]\n"
    "v_exp_f32 v207, v207\n"
    "v_mfma_f32_16x16x32_bf16 a[20:23], a[236:239], v[228:231], a[20:23]\n"
    "v_exp_f32 v216, v216\n"
    "v_mfma_f32_16x16x32_bf16 a[24:27], a[244:247], v[228:231], a[24:27]\n"
    "ds_read_b128 a[164:167], v2 offset:9216\n"
    "v_cvt_pk_bf16_f32 v245, v202, v203\n"
    "v_mfma_f32_16x16x32_bf16 a[28:31], a[252:255], v[228:231], a[28:31]\n"
    "v_exp_f32 v217, v217\n"
    "v_mfma_f32_16x16x32_bf16 a[32:35], a[196:199], v[236:239], a[32:35]\n"
    "ds_read_b128 a[168:171], v2 offset:10240\n"
    "v_cvt_pk_bf16_f32 v246, v204, v205\n"
    "v_mfma_f32_16x16x32_bf16 a[36:39], a[204:207], v[236:239], a[36:39]\n"
    "v_exp_f32 v218, v218\n"
    "v_mfma_f32_16x16x32_bf16 a[40:43], a[212:215], v[236:239], a[40:43]\n"
    "v_exp_f32 v219, v219\n"
    "v_mfma_f32_16x16x32_bf16 a[44:47], a[220:223], v[236:239], a[44:47]\n"
    "ds_read_b128 a[172:175], v2 offset:11264\n"
    "v_cvt_pk_bf16_f32 v247, v206, v207\n"
    "v_mfma_f32_16x16x32_bf16 a[48:51], a[228:231], v[236:239], a[48:51]\n"
    "v_exp_f32 v220, v220\n"
    "v_mfma_f32_16x16x32_bf16 a[52:55], a[236:239], v[236:239], a[52:55]\n"
    "v_exp_f32 v221, v221\n"
    "v_mfma_f32_16x16x32_bf16 a[56:59], a[244:247], v[236:239], a[56:59]\n"
    "ds_read_b128 a[176:179], v2 offset:12288\n"
    "v_cvt_pk_bf16_f32 v252, v216, v217\n"
    "v_mfma_f32_16x16x32_bf16 a[60:63], a[252:255], v[236:239], a[60:63]\n"
    "v_exp_f32 v222, v222\n"
    "v_mfma_f32_16x16x32_bf16 a[64:67], a[196:199], v[244:247], a[64:67]\n"
    "ds_read_b128 a[180:183], v2 offset:13312\n"
    "v_cvt_pk_bf16_f32 v253, v218, v219\n"
    "v_mfma_f32_16x16x32_bf16 a[68:71], a[204:207], v[244:247], a[68:71]\n"
    "v_exp_f32 v223, v223\n"
    "v_mfma_f32_16x16x32_bf16 a[72:75], a[212:215], v[244:247], a[72:75]\n"
    "v_cvt_pk_bf16_f32 v254, v220, v221\n"
    "v_cvt_pk_bf16_f32 v255, v222, v223\n"
    "v_mfma_f32_16x16x32_bf16 a[76:79], a[220:223], v[244:247], a[76:79]\n"
    "ds_read_b128 a[184:187], v2 offset:14336\n"
    "v_add_f32 v8, v8, v164\n"
    "v_mfma_f32_16x16x32_bf16 a[80:83], a[228:231], v[244:247], a[80:83]\n"
    "v_add_f32 v8, v8, v165\n"
    "v_add_f32 v8, v8, v166\n"
    "v_mfma_f32_16x16x32_bf16 a[84:87], a[236:239], v[244:247], a[84:87]\n"
    "v_add_f32 v8, v8, v167\n"
    "v_add_f32 v9, v9, v176\n"
    "v_mfma_f32_16x16x32_bf16 a[88:91], a[244:247], v[244:247], a[88:91]\n"
    "s_waitcnt lgkmcnt(8)\n"
    "ds_read_b128 a[188:191], v2 offset:15360\n"
    "v_add_f32 v9, v9, v177\n"
    "v_mfma_f32_16x16x32_bf16 a[92:95], a[252:255], v[244:247], a[92:95]\n"
    "v_add_f32 v9, v9, v178\n"
    "v_add_f32 v9, v9, v179\n"
    "v_mfma_f32_16x16x32_bf16 a[96:99], a[196:199], v[252:255], a[96:99]\n"
    "v_add_f32 v9, v9, v180\n"
    "v_add_f32 v9, v9, v181\n"
    "v_mfma_f32_16x16x32_bf16 a[100:103], a[204:207], v[252:255], a[100:103]\n"
    "v_add_f32 v9, v9, v182\n"
    "v_mfma_f32_16x16x32_bf16 a[104:107], a[212:215], v[252:255], a[104:107]\n"
    "v_add_f32 v9, v9, v183\n"
    "v_add_f32 v10, v10, v192\n"
    "v_mfma_f32_16x16x32_bf16 a[108:111], a[220:223], v[252:255], a[108:111]\n"
    "v_add_f32 v10, v10, v193\n"
    "v_add_f32 v10, v10, v194\n"
    "v_mfma_f32_16x16x32_bf16 a[112:115], a[228:231], v[252:255], a[112:115]\n"
    "v_add_f32 v10, v10, v195\n"
    "v_mfma_f32_16x16x32_bf16 a[116:119], a[236:239], v[252:255], a[116:119]\n"
    "v_add_f32 v10, v10, v196\n"
    "v_add_f32 v10, v10, v197\n"
    "v_mfma_f32_16x16x32_bf16 a[120:123], a[244:247], v[252:255], a[120:123]\n"
    "v_add_f32 v10, v10, v198\n"
    "v_add_f32 v10, v10, v199\n"
    "v_mfma_f32_16x16x32_bf16 a[124:127], a[252:255], v[252:255], a[124:127]\n"
    "v_add_f32 v11, v11, v208\n"
    "s_sub_u32 s77, s77, 1\n"
    "s_cmp_lg_u32 s77, 0\n"
    "s_cbranch_scc1 L_l2_11_%=\n"
    "L_l2e_12_%=:\n"
    "v_lshrrev_b32 v25, 2, v1\n"
    "v_and_b32 v25, 0x7c, v25\n"
    "s_mov_b64 s[40:41], s[60:61]\n"
    "s_mov_b64 s[36:37], s[62:63]\n"
    "s_lshr_b32 s58, s53, 3\n"
    "s_mul_i32 s59, s58, 1048576\n"
    "s_add_u32 s40, s40, s59\n"
    "s_addc_u32 s41, s41, 0\n"
    "s_and_b32 s41, s41, 0xffff\n"
    "s_mov_b32 s42, 0x100000\n"
    "s_mov_b32 s43, 0x20000\n"
    "s_mul_i32 s59, s58, 458752\n"
    "s_add_u32 s36, s36, s59\n"
    "s_addc_u32 s37, s37, 0\n"
    "s_and_b32 s37, s37, 0xffff\n"
    "s_mov_b32 s38, 0x70000\n"
    "s_mov_b32 s39, 0x20000\n"
    "s_sub_u32 s56, 15, s54\n"
    "s_bitcmp1_b32 s53, 2\n"
    "s_cselect_b32 s56, s54, s56\n"
    "s_lshl_b32 s56, s56, 2\n"
    "s_add_u32 s56, s56, s52\n"
    "s_lshr_b32 s57, s53, 3\n"
    "s_mul_i32 s60, s56, 114688\n"
    "s_mul_i32 s58, s57, 14336\n"
    "s_add_u32 s60, s60, s58\n"
    "s_add_u32 s60, s60, 0x2400000\n"
    "s_lshl_b32 s61, s56, 18\n"
    "s_lshl_b32 s58, s57, 15\n"
    "s_add_u32 s61, s61, s58\n"
    "s_add_u32 s61, s61, 0x2c00000\n"
    "s_xor_b32 s58, s53, 2\n"
    "s_and_b32 s58, s58, 3\n"
    "s_lshl_b32 s58, s58, 8\n"
    "s_lshl_b32 s59, s52, 13\n"
    "s_add_u32 s58, s58, s59\n"
    "s_mov_b32 s62, s58\n"
    "s_mov_b32 s63, s58\n"
    "s_lshl_b32 s59, s52, 11\n"
    "s_and_b32 s58, s53, 3\n"
    "s_cmp_eq_u32 s58, 3\n"
    "s_mov_b32 s64, 0x0\n"
    "s_cselect_b32 s64, 0x10000, s64\n"
    "s_add_u32 s64, s64, s59\n"
    "s_cmp_eq_u32 s58, 3\n"
    "s_mov_b32 s65, 0xc000\n"
    "s_cselect_b32 s65, 0x1c000, s65\n"
    "s_add_u32 s65, s65, s59\n"
    "s_cmp_eq_u32 s58, 3\n"
    "s_mov_b32 s66, 0x18000\n"
    "s_cselect_b32 s66, 0x0, s66\n"
    "s_add_u32 s66, s66, s59\n"
    "s_cmp_eq_u32 s58, 3\n"
    "s_cselect_b32 s67, 0, 0x18000\n"
    "s_mov_b32 s68, 0x16000\n"
    "s_cselect_b32 s68, 0x26000, s68\n"
    "v_lshrrev_b32 v16, 4, v1\n"
    "v_lshrrev_b32 v17, 4, v16\n"
    "v_and_b32 v18, 15, v16\n"
    "v_lshlrev_b32 v19, 1, v17\n"
    "v_xor_b32 v18, v18, v19\n"
    "v_lshlrev_b32 v18, 4, v18\n"
    "v_lshlrev_b32 v17, 10, v17\n"
    "v_add_u32 v28, v17, v18\n"
    "v_lshrrev_b32 v17, 4, v16\n"
    "v_add_u32 v17, 4, v17\n"
    "v_and_b32 v18, 15, v16\n"
    "v_lshlrev_b32 v19, 1, v17\n"
    "v_xor_b32 v18, v18, v19\n"
    "v_lshlrev_b32 v18, 4, v18\n"
    "v_lshlrev_b32 v17, 10, v17\n"
    "v_add_u32 v29, v17, v18\n"
    "s_and_b32 s58, s53, 7\n"
    "s_lshl_b32 s69, s58, 12\n"
    "s_add_u32 s69, s69, s61\n"
    "s_lshl_b32 s70, s58, 17\n"
    "s_add_u32 s70, s70, s63\n"
    "s_lshl_b32 s56, s54, 2\n"
    "s_add_u32 s56, s56, s52\n"
    "v_lshrrev_b32 v16, 4, v1\n"
    "v_lshrrev_b32 v17, 1, v16\n"
    "v_lshlrev_b32 v17, 10, v17\n"
    "v_and_b32 v18, 1, v16\n"
    "v_lshlrev_b32 v18, 7, v18\n"
    "v_add_u32 v17, v17, v18\n"
    "s_lshl_b32 s57, s52, 13\n"
    "s_sub_u32 s58, s63, s57\n"
    "s_lshl_b32 s59, s56, 15\n"
    "s_add_u32 s58, s58, s59\n"
    "buffer_load_dword v27, v17, s[40:43], s58 offen\n"
    "v_cmp_gt_u32 vcc, 28, v16\n"
    "v_mov_b32 v18, 0x7ffffff0\n"
    "s_nop 0\n"
    "v_cndmask_b32 v17, v18, v17, vcc\n"
    "s_sub_u32 s58, s62, s57\n"
    "s_mul_i32 s59, s56, 14336\n"
    "s_add_u32 s58, s58, s59\n"
    "buffer_load_dword v26, v17, s[36:39], s58 offen\n"
    "s_sub_u32 s80, 15, s54\n"
    "s_lshl_b32 s80, s80, 7\n"
    "s_lshr_b32 s56, s83, 8\n"
    "s_add_u32 s80, s80, s56\n"
    "s_sub_i32 s58, s78, s80\n"
    "v_add_u32 v16, s58, v5\n"
    "v_cmp_le_i32 s[84:85], 0, v16\n"
    "v_cmp_le_i32 s[86:87], 1, v16\n"
    "v_cmp_le_i32 s[88:89], 2, v16\n"
    "v_cndmask_b32 v32, v4, v12, s[84:85]\n"
    "v_cmp_le_i32 s[90:91], 3, v16\n"
    "v_cndmask_b32 v33, v4, v12, s[86:87]\n"
    "v_cmp_le_i32 s[84:85], 16, v16\n"
    "v_cndmask_b32 v34, v4, v12, s[88:89]\n"
    "v_cmp_le_i32 s[86:87], 17, v16\n"
    "v_cndmask_b32 v35, v4, v12, s[90:91]\n"
    "v_cmp_le_i32 s[88:89], 18, v16\n"
    "v_cndmask_b32 v36, v4, v12, s[84:85]\n"
    "v_cmp_le_i32 s[90:91], 19, v16\n"
    "v_cndmask_b32 v37, v4, v12, s[86:87]\n"
    "v_cmp_le_i32 s[84:85], 32, v16\n"
    "v_cndmask_b32 v38, v4, v12, s[88:89]\n"
    "v_cmp_le_i32 s[86:87], 33, v16\n"
    "v_cndmask_b32 v39, v4, v12, s[90:91]\n"
    "v_cmp_le_i32 s[88:89], 34, v16\n"
    "v_cndmask_b32 v40, v4, v12, s[84:85]\n"
    "v_cmp_le_i32 s[90:91], 35, v16\n"
    "v_cndmask_b32 v41, v4, v12, s[86:87]\n"
    "v_cmp_le_i32 s[84:85], 48, v16\n"
    "v_cndmask_b32 v42, v4, v12, s[88:89]\n"
    "v_cmp_le_i32 s[86:87], 49, v16\n"
    "v_cndmask_b32 v43, v4, v12, s[90:91]\n"
    "v_cmp_le_i32 s[88:89], 50, v16\n"
    "v_cndmask_b32 v44, v4, v12, s[84:85]\n"
    "v_cmp_le_i32 s[90:91], 51, v16\n"
    "v_cndmask_b32 v45, v4, v12, s[86:87]\n"
    "v_cmp_le_i32 s[84:85], -16, v16\n"
    "v_cndmask_b32 v46, v4, v12, s[88:89]\n"
    "v_cmp_le_i32 s[86:87], -15, v16\n"
    "v_cndmask_b32 v47, v4, v12, s[90:91]\n"
    "v_cmp_le_i32 s[88:89], -14, v16\n"
    "v_cndmask_b32 v48, v4, v13, s[84:85]\n"
    "v_cmp_le_i32 s[90:91], -13, v16\n"
    "v_cndmask_b32 v49, v4, v13, s[86:87]\n"
    "v_cmp_le_i32 s[84:85], 0, v16\n"
    "v_cndmask_b32 v50, v4, v13, s[88:89]\n"
    "v_cmp_le_i32 s[86:87], 1, v16\n"
    "v_cndmask_b32 v51, v4, v13, s[90:91]\n"
    "v_cmp_le_i32 s[88:89], 2, v16\n"
    "v_cndmask_b32 v52, v4, v13, s[84:85]\n"
    "v_cmp_le_i32 s[90:91], 3, v16\n"
    "v_cndmask_b32 v53, v4, v13, s[86:87]\n"
    "v_cmp_le_i32 s[84:85], 16, v16\n"
    "v_cndmask_b32 v54, v4, v13, s[88:89]\n"
    "v_cmp_le_i32 s[86:87], 17, v16\n"
    "v_cndmask_b32 v55, v4, v13, s[90:91]\n"
    "v_cmp_le_i32 s[88:89], 18, v16\n"
    "v_cndmask_b32 v56, v4, v13, s[84:85]\n"
    "v_cmp_le_i32 s[90:91], 19, v16\n"
    "v_cndmask_b32 v57, v4, v13, s[86:87]\n"
    "v_cmp_le_i32 s[84:85], 32, v16\n"
    "v_cndmask_b32 v58, v4, v13, s[88:89]\n"
    "v_cmp_le_i32 s[86:87], 33, v16\n"
    "v_cndmask_b32 v59, v4, v13, s[90:91]\n"
    "v_cmp_le_i32 s[88:89], 34, v16\n"
    "v_cndmask_b32 v60, v4, v13, s[84:85]\n"
    "v_cmp_le_i32 s[90:91], 35, v16\n"
    "v_cndmask_b32 v61, v4, v13, s[86:87]\n"
    "v_cndmask_b32 v62, v4, v13, s[88:89]\n"
    "v_cndmask_b32 v63, v4, v13, s[90:91]\n"
    "s_sub_i32 s58, s79, s80\n"
    "v_add_u32 v16, s58, v5\n"
    "v_cmp_le_i32 s[84:85], 0, v16\n"
    "v_cmp_le_i32 s[86:87], 1, v16\n"
    "v_cmp_le_i32 s[88:89], 2, v16\n"
    "v_cndmask_b32 v64, v4, v14, s[84:85]\n"
    "v_cmp_le_i32 s[90:91], 3, v16\n"
    "v_cndmask_b32 v65, v4, v14, s[86:87]\n"
    "v_cmp_le_i32 s[84:85], 16, v16\n"
    "v_cndmask_b32 v66, v4, v14, s[88:89]\n"
    "v_cmp_le_i32 s[86:87], 17, v16\n"
    "v_cndmask_b32 v67, v4, v14, s[90:91]\n"
    "v_cmp_le_i32 s[88:89], 18, v16\n"
    "v_cndmask_b32 v68, v4, v14, s[84:85]\n"
    "v_cmp_le_i32 s[90:91], 19, v16\n"
    "v_cndmask_b32 v69, v4, v14, s[86:87]\n"
    "v_cmp_le_i32 s[84:85], 32, v16\n"
    "v_cndmask_b32 v70, v4, v14, s[88:89]\n"
    "v_cmp_le_i32 s[86:87], 33, v16\n"
    "v_cndmask_b32 v71, v4, v14, s[90:91]\n"
    "v_cmp_le_i32 s[88:89], 34, v16\n"
    "v_cndmask_b32 v72, v4, v14, s[84:85]\n"
    "v_cmp_le_i32 s[90:91], 35, v16\n"
    "v_cndmask_b32 v73, v4, v14, s[86:87]\n"
    "v_cmp_le_i32 s[84:85], 48, v16\n"
    "v_cndmask_b32 v74, v4, v14, s[88:89]\n"
    "v_cmp_le_i32 s[86:87], 49, v16\n"
    "v_cndmask_b32 v75, v4, v14, s[90:91]\n"
    "v_cmp_le_i32 s[88:89], 50, v16\n"
    "v_cndmask_b32 v76, v4, v14, s[84:85]\n"
    "v_cmp_le_i32 s[90:91], 51, v16\n"
    "v_cndmask_b32 v77, v4, v14, s[86:87]\n"
    "v_cmp_le_i32 s[84:85], -16, v16\n"
    "v_cndmask_b32 v78, v4, v14, s[88:89]\n"
    "v_cmp_le_i32 s[86:87], -15, v16\n"
    "v_cndmask_b32 v79, v4, v14, s[90:91]\n"
    "v_cmp_le_i32 s[88:89], -14, v16\n"
    "v_cndmask_b32 v80, v4, v15, s[84:85]\n"
    "v_cmp_le_i32 s[90:91], -13, v16\n"
    "v_cndmask_b32 v81, v4, v15, s[86:87]\n"
    "v_cmp_le_i32 s[84:85], 0, v16\n"
    "v_cndmask_b32 v82, v4, v15, s[88:89]\n"
    "v_cmp_le_i32 s[86:87], 1, v16\n"
    "v_cndmask_b32 v83, v4, v15, s[90:91]\n"
    "v_cmp_le_i32 s[88:89], 2, v16\n"
    "v_cndmask_b32 v84, v4, v15, s[84:85]\n"
    "v_cmp_le_i32 s[90:91], 3, v16\n"
    "v_cndmask_b32 v85, v4, v15, s[86:87]\n"
    "v_cmp_le_i32 s[84:85], 16, v16\n"
    "v_cndmask_b32 v86, v4, v15, s[88:89]\n"
    "v_cmp_le_i32 s[86:87], 17, v16\n"
    "v_cndmask_b32 v87, v4, v15, s[90:91]\n"
    "v_cmp_le_i32 s[88:89], 18, v16\n"
    "v_cndmask_b32 v88, v4, v15, s[84:85]\n"
    "v_cmp_le_i32 s[90:91], 19, v16\n"
    "v_cndmask_b32 v89, v4, v15, s[86:87]\n"
    "v_cmp_le_i32 s[84:85], 32, v16\n"
    "v_cndmask_b32 v90, v4, v15, s[88:89]\n"
    "v_cmp_le_i32 s[86:87], 33, v16\n"
    "v_cndmask_b32 v91, v4, v15, s[90:91]\n"
    "v_cmp_le_i32 s[88:89], 34, v16\n"
    "v_cndmask_b32 v92, v4, v15, s[84:85]\n"
    "v_cmp_le_i32 s[90:91], 35, v16\n"
    "v_cndmask_b32 v93, v4, v15, s[86:87]\n"
    "v_cndmask_b32 v94, v4, v15, s[88:89]\n"
    "v_cndmask_b32 v95, v4, v15, s[90:91]\n"
    "s_waitcnt vmcnt(16)\n"
    "v_cmp_eq_u32 vcc, s97, v24\n"
    "s_nop 0\n"
    "s_cmp_eq_u64 vcc, -1\n"
    "s_cbranch_scc1 L_pok_13_%=\n"
    "s_lshl_b32 s96, s54, 2\n"
    "s_lshr_b32 s95, s53, 3\n"
    "s_add_u32 s96, s96, s95\n"
    "s_lshl_b32 s96, s96, 7\n"
    "s_add_u32 s96, s96, 0x3000\n"
    "s_mov_b32 s94, 2000000\n"
    "L_psp_14_%=:\n"
    "s_sleep 2\n"
    "buffer_load_dword v24, v25, s[44:47], s96 offen sc1\n"
    "s_waitcnt vmcnt(0)\n"
    "v_cmp_eq_u32 vcc, s97, v24\n"
    "s_nop 4\n"
    "s_cmp_eq_u64 vcc, -1\n"
    "s_cbranch_scc1 L_pok_13_%=\n"
    "s_sub_u32 s94, s94, 1\n"
    "s_cmp_lg_u32 s94, 0\n"
    "s_cbranch_scc1 L_psp_14_%=\n"
    "L_pok_13_%=:\n"
    "s_waitcnt vmcnt(2)\n"
    "s_waitcnt lgkmcnt(0)\n"
    "s_barrier\n"
    "v_mfma_f32_16x16x32_bf16 v[160:163], a[128:131], v[96:99], v[32:35]\n"
    "s_add_u32 s57, s62, 0x0\n"
    "ds_read_b128 a[192:195], v3 offset:0\n"
    "v_add_f32 v10, v10, v200\n"
    "v_mfma_f32_16x16x32_bf16 v[160:163], a[132:135], v[100:103], v[160:163]\n"
    "s_add_u32 m0, s64, 0\n"
    "s_nop 0\n"
    "buffer_load_dwordx4 v28, s[36:39], s57 offen lds\n"
    "v_add_f32 v10, v10, v201\n"
    "v_mfma_f32_16x16x32_bf16 v[160:163], a[136:139], v[104:107], v[160:163]\n"
    "ds_read_b128 a[200:203], v3 offset:2048\n"
    "v_add_f32 v10, v10, v202\n"
    "v_mfma_f32_16x16x32_bf16 v[160:163], a[140:143], v[108:111], v[160:163]\n"
    "ds_read_b128 a[208:211], v3 offset:4096\n"
    "v_add_f32 v10, v10, v203\n"
    "v_mfma_f32_16x16x32_bf16 v[164:167], a[144:147], v[96:99], v[36:39]\n"
    "s_add_u32 m0, s64, 1024\n"
    "s_nop 0\n"
    "buffer_load_dwordx4 v29, s[36:39], s57 offen lds\n"
    "v_add_f32 v10, v10, v204\n"
    "v_mfma_f32_16x16x32_bf16 v[164:167], a[148:151], v[100:103], v[164:167]\n"
    "ds_read_b128 a[216:219], v3 offset:6144\n"
    "v_add_f32 v10, v10, v205\n"
    "v_mfma_f32_16x16x32_bf16 v[164:167], a[152:155], v[104:107], v[164:167]\n"
    "s_add_u32 s57, s62, 0x8000\n"
    "ds_read_b128 a[224:227], v3 offset:8192\n"
    "v_add_f32 v10, v10, v206\n"
    "v_mfma_f32_16x16x32_bf16 v[164:167], a[156:159], v[108:111], v[164:167]\n"
    "s_add_u32 m0, s64, 8192\n"
    "s_nop 0\n"
    "buffer_load_dwordx4 v28, s[36:39], s57 offen lds\n"
    "v_add_f32 v10, v10, v207\n"
    "v_mfma_f32_16x16x32_bf16 v[176:179], a[128:131], v[112:115], v[48:51]\n"
    "ds_read_b128 a[232:235], v3 offset:10240\n"
    "v_add_f32 v11, v11, v216\n"
    "v_mfma_f32_16x16x32_bf16 v[176:179], a[132:135], v[116:119], v[176:179]\n"
    "v_exp_f32 v160, v160\n"
    "v_add_f32 v11, v11, v220\n"
    "v_mfma_f32_16x16x32_bf16 v[176:179], a[136:139], v[120:123], v[176:179]\n"
    "v_exp_f32 v161, v161\n"
    "v_add_f32 v11, v11, v221\n"
    "v_mfma_f32_16x16x32_bf16 v[176:179], a[140:143], v[124:127], v[176:179]\n"
    "s_add_u32 m0, s64, 9216\n"
    "s_nop 0\n"
    "buffer_load_dwordx4 v29, s[36:39], s57 offen lds\n"
    "v_cvt_pk_bf16_f32 v224, v160, v161\n"
    "v_mfma_f32_16x16x32_bf16 v[180:183], a[144:147], v[112:115], v[52:55]\n"
    "v_exp_f32 v162, v162\n"
    "v_add_f32 v11, v11, v222\n"
    "v_mfma_f32_16x16x32_bf16 v[180:183], a[148:151], v[116:119], v[180:183]\n"
    "s_add_u32 s57, s62, 0x10000\n"
    "v_exp_f32 v163, v163\n"
    "v_add_f32 v11, v11, v223\n"
    "v_mfma_f32_16x16x32_bf16 v[180:183], a[152:155], v[120:123], v[180:183]\n"
    "s_add_u32 m0, s64, 16384\n"
    "s_nop 0\n"
    "buffer_load_dwordx4 v28, s[36:39], s57 offen lds\n"
    "v_cvt_pk_bf16_f32 v225, v162, v163\n"
    "v_mfma_f32_16x16x32_bf16 v[180:183], a[156:159], v[124:127], v[180:183]\n"
    "v_exp_f32 v164, v164\n"
    "v_mfma_f32_16x16x32_bf16 v[192:195], a[128:131], v[128:131], v[64:67]\n"
    "ds_read_b128 a[240:243], v3 offset:12288\n"
    "v_add_f32 v11, v11, v217\n"
    "v_mfma_f32_16x16x32_bf16 v[192:195], a[132:135], v[132:135], v[192:195]\n"
    "v_exp_f32 v165, v165\n"
    "v_mfma_f32_16x16x32_bf16 v[192:195], a[136:139], v[136:139], v[192:195]\n"
    "v_exp_f32 v166, v166\n"
    "v_mfma_f32_16x16x32_bf16 v[192:195], a[140:143], v[140:143], v[192:195]\n"
    "s_add_u32 m0, s64, 17408\n"
    "s_nop 0\n"
    "buffer_load_dwordx4 v29, s[36:39], s57 offen lds\n"
    "v_cvt_pk_bf16_f32 v226, v164, v165\n"
    "v_mfma_f32_16x16x32_bf16 v[196:199], a[144:147], v[128:131], v[68:71]\n"
    "v_exp_f32 v167, v167\n"
    "v_mfma_f32_16x16x32_bf16 v[196:199], a[148:151], v[132:135], v[196:199]\n"
    "s_add_u32 s57, s62, 0x18000\n"
    "v_exp_f32 v176, v176\n"
    "v_mfma_f32_16x16x32_bf16 v[196:199], a[152:155], v[136:139], v[196:199]\n"
    "s_add_u32 m0, s64, 24576\n"
    "s_nop 0\n"
    "buffer_load_dwordx4 v28, s[36:39], s57 offen lds\n"
    "v_cvt_pk_bf16_f32 v227, v166, v167\n"
    "v_mfma_f32_16x16x32_bf16 v[196:199], a[156:159], v[140:143], v[196:199]\n"
    "v_exp_f32 v177, v177\n"
    "v_mfma_f32_16x16x32_bf16 v[208:211], a[128:131], v[144:147], v[80:83]\n"
    "ds_read_b128 a[248:251], v3 offset:14336\n"
    "v_cvt_pk_bf16_f32 v232, v176, v177\n"
    "v_mfma_f32_16x16x32_bf16 v[208:211], a[132:135], v[148:151], v[208:211]\n"
    "v_exp_f32 v178, v178\n"
    "v_mfma_f32_16x16x32_bf16 v[208:211], a[136:139], v[152:155], v[208:211]\n"
    "v_exp_f32 v179, v179\n"
    "v_mfma_f32_16x16x32_bf16 v[208:211], a[140:143], v[156:159], v[208:211]\n"
    "s_add_u32 m0, s64, 25600\n"
    "s_nop 0\n"
    "buffer_load_dwordx4 v29, s[36:39], s57 offen lds\n"
    "v_cvt_pk_bf16_f32 v233, v178, v179\n"
    "v_mfma_f32_16x16x32_bf16 v[212:215], a[144:147], v[144:147], v[84:87]\n"
    "v_exp_f32 v180, v180\n"
    "v_mfma_f32_16x16x32_bf16 v[212:215], a[148:151], v[148:151], v[212:215]\n"
    "s_add_u32 s57, s62, 0x20000\n"
    "v_exp_f32 v181, v181\n"
    "v_mfma_f32_16x16x32_bf16 v[212:215], a[152:155], v[152:155], v[212:215]\n"
    "s_add_u32 m0, s65, 0\n"
    "s_nop 0\n"
    "buffer_load_dwordx4 v28, s[36:39], s57 offen lds\n"
    "v_cvt_pk_bf16_f32 v234, v180, v181\n"
    "v_mfma_f32_16x16x32_bf16 v[212:215], a[156:159], v[156:159], v[212:215]\n"
    "v_exp_f32 v182, v182\n"
    "v_mfma_f32_16x16x32_bf16 v[168:171], a[160:163], v[96:99], v[40:43]\n"
    "ds_read_b128 a[196:199], v3 offset:1024\n"
    "v_add_f32 v11, v11, v218\n"
    "v_mfma_f32_16x16x32_bf16 v[168:171], a[164:167], v[100:103], v[168:171]\n"
    "v_exp_f32 v183, v183\n"
    "v_mfma_f32_16x16x32_bf16 v[168:171], a[168:171], v[104:107], v[168:171]\n"
    "v_exp_f32 v192, v192\n"
    "v_mfma_f32_16x16x32_bf16 v[168:171], a[172:175], v[108:111], v[168:171]\n"
    "s_add_u32 m0, s65, 1024\n"
    "s_nop 0\n"
    "buffer_load_dwordx4 v29, s[36:39], s57 offen lds\n"
    "v_cvt_pk_bf16_f32 v235, v182, v183\n"
    "v_mfma_f32_16x16x32_bf16 v[172:175], a[176:179], v[96:99], v[44:47]\n"
    "v_exp_f32 v193, v193\n"
    "v_mfma_f32_16x16x32_bf16 v[172:175], a[180:183], v[100:103], v[172:175]\n"
    "s_add_u32 s57, s62, 0x28000\n"
    "v_exp_f32 v194, v194\n"
    "v_mfma_f32_16x16x32_bf16 v[172:175], a[184:187], v[104:107], v[172:175]\n"
    "s_add_u32 m0, s65, 8192\n"
    "s_nop 0\n"
    "buffer_load_dwordx4 v28, s[36:39], s57 offen lds\n"
    "v_cvt_pk_bf16_f32 v240, v192, v193\n"
    "v_mfma_f32_16x16x32_bf16 v[172:175], a[188:191], v[108:111], v[172:175]\n"
    "v_exp_f32 v195, v195\n"
    "v_mfma_f32_16x16x32_bf16 v[184:187], a[160:163], v[112:115], v[56:59]\n"
    "ds_read_b128 a[204:207], v3 offset:3072\n"
    "v_cvt_pk_bf16_f32 v241, v194, v195\n"
    "v_mfma_f32_16x16x32_bf16 v[184:187], a[164:167], v[116:119], v[184:187]\n"
    "v_exp_f32 v196, v196\n"
    "v_mfma_f32_16x16x32_bf16 v[184:187], a[168:171], v[120:123], v[184:187]\n"
    "v_exp_f32 v197, v197\n"
    "v_mfma_f32_16x16x32_bf16 v[184:187], a[172:175], v[124:127], v[184:187]\n"
    "s_add_u32 m0, s65, 9216\n"
    "s_nop 0\n"
    "buffer_load_dwordx4 v29, s[36:39], s57 offen lds\n"
    "v_cvt_pk_bf16_f32 v242, v196, v197\n"
    "v_mfma_f32_16x16x32_bf16 v[188:191], a[176:179], v[112:115], v[60:63]\n"
    "v_exp_f32 v198, v198\n"
    "v_mfma_f32_16x16x32_bf16 v[188:191], a[180:183], v[116:119], v[188:191]\n"
    "s_add_u32 s57, s62, 0x30000\n"
    "v_exp_f32 v199, v199\n"
    "v_mfma_f32_16x16x32_bf16 v[188:191], a[184:187], v[120:123], v[188:191]\n"
    "s_add_u32 m0, s65, 16384\n"
    "s_nop 0\n"
    "buffer_load_dwordx4 v28, s[36:39], s57 offen lds\n"
    "v_cvt_pk_bf16_f32 v243, v198, v199\n"
    "v_mfma_f32_16x16x32_bf16 v[188:191], a[188:191], v[124:127], v[188:191]\n"
    "v_exp_f32 v208, v208\n"
    "v_mfma_f32_16x16x32_bf16 v[200:203], a[160:163], v[128:131], v[72:75]\n"
    "ds_read_b128 a[212:215], v3 offset:5120\n"
    "v_add_f32 v11, v11, v219\n"
    "v_mfma_f32_16x16x32_bf16 v[200:203], a[164:167], v[132:135], v[200:203]\n"
    "v_exp_f32 v209, v209\n"
    "v_mfma_f32_16x16x32_bf16 v[200:203], a[168:171], v[136:139], v[200:203]\n"
    "v_exp_f32 v210, v210\n"
    "v_mfma_f32_16x16x32_bf16 v[200:203], a[172:175], v[140:143], v[200:203]\n"
    "s_add_u32 m0, s65, 17408\n"
    "s_nop 0\n"
    "buffer_load_dwordx4 v29, s[36:39], s57 offen lds\n"
    "v_cvt_pk_bf16_f32 v248, v208, v209\n"
    "v_mfma_f32_16x16x32_bf16 v[204:207], a[176:179], v[128:131], v[76:79]\n"
    "v_exp_f32 v211, v211\n"
    "v_mfma_f32_16x16x32_bf16 v[204:207], a[180:183], v[132:135], v[204:207]\n"
    "s_add_u32 s57, s62, 0x38000\n"
    "v_exp_f32 v212, v212\n"
    "v_mfma_f32_16x16x32_bf16 v[204:207], a[184:187], v[136:139], v[204:207]\n"
    "s_add_u32 m0, s65, 24576\n"
    "s_nop 0\n"
    "buffer_load_dwordx4 v28, s[36:39], s57 offen lds\n"
    "v_cvt_pk_bf16_f32 v249, v210, v211\n"
    "v_mfma_f32_16x16x32_bf16 v[204:207], a[188:191], v[140:143], v[204:207]\n"
    "v_exp_f32 v213, v213\n"
    "v_mfma_f32_16x16x32_bf16 v[216:219], a[160:163], v[144:147], v[88:91]\n"
    "ds_read_b128 a[220:223], v3 offset:7168\n"
    "v_cvt_pk_bf16_f32 v250, v212, v213\n"
    "v_mfma_f32_16x16x32_bf16 v[216:219], a[164:167], v[148:151], v[216:219]\n"
    "v_exp_f32 v214, v214\n"
    "v_mfma_f32_16x16x32_bf16 v[216:219], a[168:171], v[152:155], v[216:219]\n"
    "v_exp_f32 v215, v215\n"
    "v_add_f32 v11, v11, v214\n"
    "v_mfma_f32_16x16x32_bf16 v[216:219], a[172:175], v[156:159], v[216:219]\n"
    "s_add_u32 m0, s65, 25600\n"
    "s_nop 0\n"
    "buffer_load_dwordx4 v29, s[36:39], s57 offen lds\n"
    "v_cvt_pk_bf16_f32 v251, v214, v215\n"
    "v_mfma_f32_16x16x32_bf16 v[220:223], a[176:179], v[144:147], v[92:95]\n"
    "v_exp_f32 v168, v168\n"
    "v_add_f32 v11, v11, v215\n"
    "v_mfma_f32_16x16x32_bf16 v[220:223], a[180:183], v[148:151], v[220:223]\n"
    "s_waitcnt lgkmcnt(4)\n"
    "v_exp_f32 v169, v169\n"
    "v_add_f32 v8, v8, v168\n"
    "v_mfma_f32_16x16x32_bf16 v[220:223], a[184:187], v[152:155], v[220:223]\n"
    "ds_read_b128 a[228:231], v3 offset:9216\n"
    "v_cvt_pk_bf16_f32 v228, v168, v169\n"
    "v_mfma_f32_16x16x32_bf16 v[220:223], a[188:191], v[156:159], v[220:223]\n"
    "v_exp_f32 v170, v170\n"
    "v_add_f32 v8, v8, v169\n"
    "v_mfma_f32_16x16x32_bf16 a[0:3], a[192:195], v[224:227], a[0:3]\n"
    "ds_read_b128 a[236:239], v3 offset:11264\n"
    "v_add_f32 v8, v8, v160\n"
    "v_mfma_f32_16x16x32_bf16 a[4:7], a[200:203], v[224:227], a[4:7]\n"
    "v_exp_f32 v171, v171\n"
    "v_add_f32 v8, v8, v170\n"
    "v_mfma_f32_16x16x32_bf16 a[8:11], a[208:211], v[224:227], a[8:11]\n"
    "s_add_u32 s56, s60, 0x0\n"
    "v_exp_f32 v172, v172\n"
    "v_add_f32 v8, v8, v171\n"
    "v_mfma_f32_16x16x32_bf16 a[12:15], a[216:219], v[224:227], a[12:15]\n"
    "buffer_load_dwordx4 v[96:99], v1, s[44:47], s56 offen sc1\n"
    "v_cvt_pk_bf16_f32 v229, v170, v171\n"
    "v_mfma_f32_16x16x32_bf16 a[16:19], a[224:227], v[224:227], a[16:19]\n"
    "v_exp_f32 v173, v173\n"
    "v_add_f32 v8, v8, v172\n"
    "v_mfma_f32_16x16x32_bf16 a[20:23], a[232:235], v[224:227], a[20:23]\n"
    "s_add_u32 s56, s60, 0xe000\n"
    "v_exp_f32 v174, v174\n"
    "v_add_f32 v8, v8, v173\n"
    "v_mfma_f32_16x16x32_bf16 a[24:27], a[240:243], v[224:227], a[24:27]\n"
    "buffer_load_dwordx4 v[100:103], v1, s[44:47], s56 offen sc1\n"
    "v_cvt_pk_bf16_f32 v230, v172, v173\n"
    "v_mfma_f32_16x16x32_bf16 a[28:31], a[248:251], v[224:227], a[28:31]\n"
    "v_exp_f32 v175, v175\n"
    "v_add_f32 v8, v8, v174\n"
    "v_mfma_f32_16x16x32_bf16 a[32:35], a[192:195], v[232:235], a[32:35]\n"
    "ds_read_b128 a[244:247], v3 offset:13312\n"
    "v_cvt_pk_bf16_f32 v231, v174, v175\n"
    "v_mfma_f32_16x16x32_bf16 a[36:39], a[200:203], v[232:235], a[36:39]\n"
    "v_exp_f32 v184, v184\n"
    "v_add_f32 v8, v8, v175\n"
    "v_mfma_f32_16x16x32_bf16 a[40:43], a[208:211], v[232:235], a[40:43]\n"
    "s_add_u32 s56, s60, 0x400\n"
    "v_exp_f32 v185, v185\n"
    "v_add_f32 v9, v9, v184\n"
    "v_mfma_f32_16x16x32_bf16 a[44:47], a[216:219], v[232:235], a[44:47]\n"
    "buffer_load_dwordx4 v[104:107], v1, s[44:47], s56 offen sc1\n"
    "v_cvt_pk_bf16_f32 v236, v184, v185\n"
    "v_mfma_f32_16x16x32_bf16 a[48:51], a[224:227], v[232:235], a[48:51]\n"
    "v_exp_f32 v186, v186\n"
    "v_add_f32 v9, v9, v185\n"
    "v_mfma_f32_16x16x32_bf16 a[52:55], a[232:235], v[232:235], a[52:55]\n"
    "s_add_u32 s56, s60, 0xe400\n"
    "v_exp_f32 v187, v187\n"
    "v_add_f32 v9, v9, v186\n"
    "v_mfma_f32_16x16x32_bf16 a[56:59], a[240:243], v[232:235], a[56:59]\n"
    "buffer_load_dwordx4 v[108:111], v1, s[44:47], s56 offen sc1\n"
    "v_cvt_pk_bf16_f32 v237, v186, v187\n"
    "v_mfma_f32_16x16x32_bf16 a[60:63], a[248:251], v[232:235], a[60:63]\n"
    "v_exp_f32 v188, v188\n"
    "v_add_f32 v9, v9, v187\n"
    "v_mfma_f32_16x16x32_bf16 a[64:67], a[192:195], v[240:243], a[64:67]\n"
    "ds_read_b128 a[252:255], v3 offset:15360\n"
    "v_add_f32 v8, v8, v161\n"
    "v_mfma_f32_16x16x32_bf16 a[68:71], a[200:203], v[240:243], a[68:71]\n"
    "v_xor_b32 v3, 0x10000, v3\n"
    "v_add_f32 v8, v8, v162\n"
    "v_mfma_f32_16x16x32_bf16 a[72:75], a[208:211], v[240:243], a[72:75]\n"
    "s_add_u32 s56, s60, 0x800\n"
    "v_exp_f32 v189, v189\n"
    "v_add_f32 v9, v9, v188\n"
    "v_mfma_f32_16x16x32_bf16 a[76:79], a[216:219], v[240:243], a[76:79]\n"
    "buffer_load_dwordx4 v[112:115], v1, s[44:47], s56 offen sc1\n"
    "v_cvt_pk_bf16_f32 v238, v188, v189\n"
    "v_mfma_f32_16x16x32_bf16 a[80:83], a[224:227], v[240:243], a[80:83]\n"
    "v_exp_f32 v190, v190\n"
    "v_add_f32 v9, v9, v189\n"
    "v_mfma_f32_16x16x32_bf16 a[84:87], a[232:235], v[240:243], a[84:87]\n"
    "s_add_u32 s56, s60, 0xe800\n"
    "v_exp_f32 v191, v191\n"
    "v_add_f32 v9, v9, v190\n"
    "v_mfma_f32_16x16x32_bf16 a[88:91], a[240:243], v[240:243], a[88:91]\n"
    "buffer_load_dwordx4 v[116:119], v1, s[44:47], s56 offen sc1\n"
    "v_cvt_pk_bf16_f32 v239, v190, v191\n"
    "v_mfma_f32_16x16x32_bf16 a[92:95], a[248:251], v[240:243], a[92:95]\n"
    "v_exp_f32 v200, v200\n"
    "v_add_f32 v9, v9, v191\n"
    "v_mfma_f32_16x16x32_bf16 a[96:99], a[192:195], v[248:251], a[96:99]\n"
    "v_exp_f32 v201, v201\n"
    "v_mfma_f32_16x16x32_bf16 a[100:103], a[200:203], v[248:251], a[100:103]\n"
    "v_exp_f32 v202, v202\n"
    "v_mfma_f32_16x16x32_bf16 a[104:107], a[208:211], v[248:251], a[104:107]\n"
    "s_add_u32 s56, s60, 0xc00\n"
    "v_exp_f32 v203, v203\n"
    "v_mfma_f32_16x16x32_bf16 a[108:111], a[216:219], v[248:251], a[108:111]\n"
    "buffer_load_dwordx4 v[120:123], v1, s[44:47], s56 offen sc1\n"
    "v_cvt_pk_bf16_f32 v244, v200, v201\n"
    "v_mfma_f32_16x16x32_bf16 a[112:115], a[224:227], v[248:251], a[112:115]\n"
    "v_exp_f32 v204, v204\n"
    "v_mfma_f32_16x16x32_bf16 a[116:119], a[232:235], v[248:251], a[116:119]\n"
    "s_add_u32 s56, s60, 0xec00\n"
    "s_waitcnt lgkmcnt(0)\n"
    "v_mfma_f32_16x16x32_bf16 a[120:123], a[240:243], v[248:251], a[120:123]\n"
    "buffer_load_dwordx4 v[124:127], v1, s[44:47], s56 offen sc1\n"
    "v_cvt_pk_bf16_f32 v245, v202, v203\n"
    "v_mfma_f32_16x16x32_bf16 a[124:127], a[248:251], v[248:251], a[124:127]\n"
    "v_exp_f32 v205, v205\n"
    "v_mfma_f32_16x16x32_bf16 a[0:3], a[196:199], v[228:231], a[0:3]\n"
    "v_exp_f32 v206, v206\n"
    "v_mfma_f32_16x16x32_bf16 a[4:7], a[204:207], v[228:231], a[4:7]\n"
    "v_exp_f32 v207, v207\n"
    "v_mfma_f32_16x16x32_bf16 a[8:11], a[212:215], v[228:231], a[8:11]\n"
    "s_add_u32 s56, s60, 0x1000\n"
    "s_waitcnt vmcnt(32)\n"
    "s_waitcnt vmcnt(26)\n"
    "s_and_b32 s96, s53, 7\n"
    "s_lshl_b32 s96, s96, 3\n"
    "s_add_u32 s96, s96, s54\n"
    "s_lshl_b32 s96, s96, 2\n"
    "s_add_u32 s96, s96, s52\n"
    "s_lshl_b32 s96, s96, 4\n"
    "s_add_u32 s96, s96, 0x6000\n"
    "s_lshr_b32 s95, s53, 3\n"
    "s_lshl_b32 s95, s95, 2\n"
    "s_add_u32 s96, s96, s95\n"
    "v_mov_b32 v24, s97\n"
    "buffer_store_dword v24, v7, s[44:47], s96 offen sc1\n"
    "s_nop 1\n"
    "s_cmp_lg_u64 s[92:93], 0\n"
    "s_cbranch_scc1 L_noe1_15_%=\n"
    "s_lshl_b32 s96, s54, 2\n"
    "s_lshr_b32 s95, s53, 3\n"
    "s_add_u32 s96, s96, s95\n"
    "s_lshl_b32 s96, s96, 7\n"
    "s_add_u32 s96, s96, 0x5000\n"
    "s_and_b32 s95, s53, 7\n"
    "s_lshl_b32 s95, s95, 2\n"
    "s_add_u32 s95, s95, s52\n"
    "s_lshl_b32 s95, s95, 2\n"
    "s_add_u32 s96, s96, s95\n"
    "v_mov_b32 v24, s97\n"
    "s_nop 0\n"
    "buffer_store_dword v24, v7, s[44:47], s96 offen sc1\n"
    "L_noe1_15_%=:\n"
    "v_mfma_f32_16x16x32_bf16 a[12:15], a[220:223], v[228:231], a[12:15]\n"
    "buffer_load_dwordx4 v[128:131], v1, s[44:47], s56 offen sc1\n"
    "v_cvt_pk_bf16_f32 v246, v204, v205\n"
    "v_mfma_f32_16x16x32_bf16 a[16:19], a[228:231], v[228:231], a[16:19]\n"
    "v_exp_f32 v216, v216\n"
    "v_mfma_f32_16x16x32_bf16 a[20:23], a[236:239], v[228:231], a[20:23]\n"
    "s_add_u32 s56, s60, 0xf000\n"
    "v_exp_f32 v217, v217\n"
    "v_mfma_f32_16x16x32_bf16 a[24:27], a[244:247], v[228:231], a[24:27]\n"
    "buffer_load_dwordx4 v[132:135], v1, s[44:47], s56 offen sc1\n"
    "v_cvt_pk_bf16_f32 v247, v206, v207\n"
    "v_mfma_f32_16x16x32_bf16 a[28:31], a[252:255], v[228:231], a[28:31]\n"
    "v_exp_f32 v218, v218\n"
    "v_mfma_f32_16x16x32_bf16 a[32:35], a[196:199], v[236:239], a[32:35]\n"
    "v_exp_f32 v219, v219\n"
    "v_mfma_f32_16x16x32_bf16 a[36:39], a[204:207], v[236:239], a[36:39]\n"
    "v_exp_f32 v220, v220\n"
    "v_mfma_f32_16x16x32_bf16 a[40:43], a[212:215], v[236:239], a[40:43]\n"
    "s_add_u32 s56, s60, 0x1400\n"
    "v_exp_f32 v221, v221\n"
    "v_mfma_f32_16x16x32_bf16 a[44:47], a[220:223], v[236:239], a[44:47]\n"
    "buffer_load_dwordx4 v[136:139], v1, s[44:47], s56 offen sc1\n"
    "v_cvt_pk_bf16_f32 v252, v216, v217\n"
    "v_mfma_f32_16x16x32_bf16 a[48:51], a[228:231], v[236:239], a[48:51]\n"
    "v_exp_f32 v222, v222\n"
    "v_mfma_f32_16x16x32_bf16 a[52:55], a[236:239], v[236:239], a[52:55]\n"
    "s_add_u32 s56, s60, 0xf400\n"
    "v_exp_f32 v223, v223\n"
    "v_mfma_f32_16x16x32_bf16 a[56:59], a[244:247], v[236:239], a[56:59]\n"
    "buffer_load_dwordx4 v[140:143], v1, s[44:47], s56 offen sc1\n"
    "v_cvt_pk_bf16_f32 v253, v218, v219\n"
    "v_mfma_f32_16x16x32_bf16 a[60:63], a[252:255], v[236:239], a[60:63]\n"
    "v_cvt_pk_bf16_f32 v254, v220, v221\n"
    "v_cvt_pk_bf16_f32 v255, v222, v223\n"
    "v_mfma_f32_16x16x32_bf16 a[64:67], a[196:199], v[244:247], a[64:67]\n"
    "v_add_f32 v8, v8, v163\n"
    "v_add_f32 v8, v8, v164\n"
    "v_mfma_f32_16x16x32_bf16 a[68:71], a[204:207], v[244:247], a[68:71]\n"
    "v_add_f32 v8, v8, v165\n"
    "v_mfma_f32_16x16x32_bf16 a[72:75], a[212:215], v[244:247], a[72:75]\n"
    "v_add_f32 v8, v8, v166\n"
    "v_add_f32 v8, v8, v167\n"
    "v_mfma_f32_16x16x32_bf16 a[76:79], a[220:223], v[244:247], a[76:79]\n"
    "v_add_f32 v9, v9, v176\n"
    "v_add_f32 v9, v9, v177\n"
    "v_mfma_f32_16x16x32_bf16 a[80:83], a[228:231], v[244:247], a[80:83]\n"
    "v_add_f32 v9, v9, v178\n"
    "v_mfma_f32_16x16x32_bf16 a[84:87], a[236:239], v[244:247], a[84:87]\n"
    "v_add_f32 v9, v9, v179\n"
    "v_add_f32 v9, v9, v180\n"
    "v_mfma_f32_16x16x32_bf16 a[88:91], a[244:247], v[244:247], a[88:91]\n"
    "v_add_f32 v9, v9, v181\n"
    "v_add_f32 v9, v9, v182\n"
    "v_mfma_f32_16x16x32_bf16 a[92:95], a[252:255], v[244:247], a[92:95]\n"
    "v_add_f32 v9, v9, v183\n"
    "v_mfma_f32_16x16x32_bf16 a[96:99], a[196:199], v[252:255], a[96:99]\n"
    "v_add_f32 v10, v10, v192\n"
    "v_add_f32 v10, v10, v193\n"
    "v_mfma_f32_16x16x32_bf16 a[100:103], a[204:207], v[252:255], a[100:103]\n"
    "v_add_f32 v10, v10, v194\n"
    "v_add_f32 v10, v10, v195\n"
    "v_mfma_f32_16x16x32_bf16 a[104:107], a[212:215], v[252:255], a[104:107]\n"
    "v_add_f32 v10, v10, v196\n"
    "v_mfma_f32_16x16x32_bf16 a[108:111], a[220:223], v[252:255], a[108:111]\n"
    "v_add_f32 v10, v10, v197\n"
    "v_add_f32 v10, v10, v198\n"
    "v_mfma_f32_16x16x32_bf16 a[112:115], a[228:231], v[252:255], a[112:115]\n"
    "v_add_f32 v10, v10, v199\n"
    "v_add_f32 v11, v11, v208\n"
    "v_mfma_f32_16x16x32_bf16 a[116:119], a[236:239], v[252:255], a[116:119]\n"
    "v_add_f32 v11, v11, v209\n"
    "v_mfma_f32_16x16x32_bf16 a[120:123], a[244:247], v[252:255], a[120:123]\n"
    "v_add_f32 v11, v11, v210\n"
    "v_add_f32 v11, v11, v211\n"
    "v_mfma_f32_16x16x32_bf16 a[124:127], a[252:255], v[252:255], a[124:127]\n"
    "v_add_f32 v11, v11, v212\n"
    "v_add_f32 v11, v11, v213\n"
    "v_add_f32 v10, v10, v200\n"
    "v_add_f32 v10, v10, v201\n"
    "v_add_f32 v10, v10, v202\n"
    "v_add_f32 v10, v10, v203\n"
    "v_add_f32 v10, v10, v204\n"
    "v_add_f32 v10, v10, v205\n"
    "v_add_f32 v10, v10, v206\n"
    "v_add_f32 v10, v10, v207\n"
    "v_add_f32 v11, v11, v216\n"
    "v_add_f32 v11, v11, v217\n"
    "v_add_f32 v11, v11, v218\n"
    "v_add_f32 v11, v11, v219\n"
    "v_add_f32 v11, v11, v220\n"
    "v_add_f32 v11, v11, v221\n"
    "v_add_f32 v11, v11, v222\n"
    "v_add_f32 v11, v11, v223\n"
    "s_lshl_b32 s56, s52, 14\n"
    "s_add_u32 s56, s56, s67\n"
    "v_add_u32 v21, s56, v1\n"
    "s_lshl_b32 s56, s52, 9\n"
    "s_add_u32 s56, s56, s68\n"
    "v_lshrrev_b32 v22, 2, v1\n"
    "v_add_u32 v22, s56, v22\n"
    "s_waitcnt lgkmcnt(0)\n"
    "s_barrier\n"
    "ds_write_b128 v21, a[64:67] offset:0\n"
    "v_mov_b32 v16, v10\n"
    "v_mov_b32 v23, v16\n"
    "s_nop 1\n"
    "v_permlane16_swap_b32 v16, v23\n"
    "v_add_f32 v16, v16, v23\n"
    "v_mov_b32 v23, v16\n"
    "ds_write_b128 v21, a[68:71] offset:1024\n"
    "s_nop 0\n"
    "v_permlane32_swap_b32 v16, v23\n"
    "v_add_f32 v16, v16, v23\n"
    "v_mov_b32 v17, v11\n"
    "v_mov_b32 v23, v17\n"
    "s_nop 1\n"
    "v_permlane16_swap_b32 v17, v23\n"
    "v_add_f32 v17, v17, v23\n"
    "ds_write_b128 v21, a[72:75] offset:2048\n"
    "v_mov_b32 v23, v17\n"
    "s_nop 1\n"
    "v_permlane32_swap_b32 v17, v23\n"
    "v_add_f32 v17, v17, v23\n"
    "ds_write_b32 v22, v16\n"
    "ds_write_b32 v22, v17 offset:256\n"
    "v_mov_b32 v18, v8\n"
    "ds_write_b128 v21, a[76:79] offset:3072\n"
    "v_mov_b32 v23, v18\n"
    "s_nop 1\n"
    "v_permlane16_swap_b32 v18, v23\n"
    "v_add_f32 v18, v18, v23\n"
    "v_mov_b32 v23, v18\n"
    "s_nop 1\n"
    "v_permlane32_swap_b32 v18, v23\n"
    "v_add_f32 v18, v18, v23\n"
    "ds_write_b128 v21, a[80:83] offset:4096\n"
    "v_mov_b32 v19, v9\n"
    "v_mov_b32 v23, v19\n"
    "s_nop 1\n"
    "v_permlane16_swap_b32 v19, v23\n"
    "v_add_f32 v19, v19, v23\n"
    "v_mov_b32 v23, v19\n"
    "s_nop 1\n"
    "v_permlane32_swap_b32 v19, v23\n"
    "ds_write_b128 v21, a[84:87] offset:5120\n"
    "v_add_f32 v19, v19, v23\n"
    "v_accvgpr_read_b32 v160, a0\n"
    "v_accvgpr_read_b32 v161, a1\n"
    "v_accvgpr_read_b32 v162, a2\n"
    "v_accvgpr_read_b32 v163, a3\n"
    "v_accvgpr_read_b32 v164, a4\n"
    "ds_write_b128 v21, a[88:91] offset:6144\n"
    "v_accvgpr_read_b32 v165, a5\n"
    "v_accvgpr_read_b32 v166, a6\n"
    "v_accvgpr_read_b32 v167, a7\n"
    "v_accvgpr_read_b32 v168, a8\n"
    "v_accvgpr_read_b32 v169, a9\n"
    "v_accvgpr_read_b32 v170, a10\n"
    "ds_write_b128 v21, a[92:95] offset:7168\n"
    "v_accvgpr_read_b32 v171, a11\n"
    "v_accvgpr_read_b32 v172, a12\n"
    "v_accvgpr_read_b32 v173, a13\n"
    "v_accvgpr_read_b32 v174, a14\n"
    "v_accvgpr_read_b32 v175, a15\n"
    "v_accvgpr_read_b32 v176, a16\n"
    "ds_write_b128 v21, a[96:99] offset:8192\n"
    "v_accvgpr_read_b32 v177, a17\n"
    "v_accvgpr_read_b32 v178, a18\n"
    "v_accvgpr_read_b32 v179, a19\n"
    "v_accvgpr_read_b32 v180, a20\n"
    "v_accvgpr_read_b32 v181, a21\n"
    "ds_write_b128 v21, a[100:103] offset:9216\n"
    "v_accvgpr_read_b32 v182, a22\n"
    "v_accvgpr_read_b32 v183, a23\n"
    "v_accvgpr_read_b32 v184, a24\n"
    "v_accvgpr_read_b32 v185, a25\n"
    "v_accvgpr_read_b32 v186, a26\n"
    "v_accvgpr_read_b32 v187, a27\n"
    "ds_write_b128 v21, a[104:107] offset:10240\n"
    "v_accvgpr_read_b32 v188, a28\n"
    "v_accvgpr_read_b32 v189, a29\n"
    "v_accvgpr_read_b32 v190, a30\n"
    "v_accvgpr_read_b32 v191, a31\n"
    "v_accvgpr_read_b32 v192, a32\n"
    "v_accvgpr_read_b32 v193, a33\n"
    "ds_write_b128 v21, a[108:111] offset:11264\n"
    "v_accvgpr_read_b32 v194, a34\n"
    "v_accvgpr_read_b32 v195, a35\n"
    "v_accvgpr_read_b32 v196, a36\n"
    "v_accvgpr_read_b32 v197, a37\n"
    "v_accvgpr_read_b32 v198, a38\n"
    "v_accvgpr_read_b32 v199, a39\n"
    "ds_write_b128 v21, a[112:115] offset:12288\n"
    "v_accvgpr_read_b32 v200, a40\n"
    "v_accvgpr_read_b32 v201, a41\n"
    "v_accvgpr_read_b32 v202, a42\n"
    "v_accvgpr_read_b32 v203, a43\n"
    "v_accvgpr_read_b32 v204, a44\n"
    "v_accvgpr_read_b32 v205, a45\n"
    "s_waitcnt lgkmcnt(8)\n"
    "ds_write_b128 v21, a[116:119] offset:13312\n"
    "v_accvgpr_read_b32 v206, a46\n"
    "v_accvgpr_read_b32 v207, a47\n"
    "v_accvgpr_read_b32 v208, a48\n"
    "v_accvgpr_read_b32 v209, a49\n"
    "v_accvgpr_read_b32 v210, a50\n"
    "v_accvgpr_read_b32 v211, a51\n"
    "ds_write_b128 v21, a[120:123] offset:14336\n"
    "v_accvgpr_read_b32 v212, a52\n"
    "v_accvgpr_read_b32 v213, a53\n"
    "v_accvgpr_read_b32 v214, a54\n"
    "v_accvgpr_read_b32 v215, a55\n"
    "v_accvgpr_read_b32 v216, a56\n"
    "v_accvgpr_read_b32 v217, a57\n"
    "ds_write_b128 v21, a[124:127] offset:15360\n"
    "v_accvgpr_read_b32 v218, a58\n"
    "v_accvgpr_read_b32 v219, a59\n"
    "v_accvgpr_read_b32 v220, a60\n"
    "v_accvgpr_read_b32 v221, a61\n"
    "v_accvgpr_read_b32 v222, a62\n"
    "v_accvgpr_read_b32 v223, a63\n"
    "s_waitcnt lgkmcnt(0)\n"
    "s_barrier\n"
    "v_xor_b32 v21, 0x4000, v21\n"
    "v_xor_b32 v22, 512, v22\n"
    "ds_read_b32 v16, v22\n"
    "ds_read_b32 v17, v22 offset:256\n"
    "ds_read_b128 v[32:35], v21 offset:0\n"
    "ds_read_b128 v[36:39], v21 offset:1024\n"
    "ds_read_b128 v[40:43], v21 offset:2048\n"
    "ds_read_b128 v[44:47], v21 offset:3072\n"
    "ds_read_b128 v[48:51], v21 offset:4096\n"
    "ds_read_b128 v[52:55], v21 offset:5120\n"
    "ds_read_b128 v[56:59], v21 offset:6144\n"
    "ds_read_b128 v[60:63], v21 offset:7168\n"
    "ds_read_b128 v[64:67], v21 offset:8192\n"
    "ds_read_b128 v[68:71], v21 offset:9216\n"
    "ds_read_b128 v[72:75], v21 offset:10240\n"
    "ds_read_b128 v[76:79], v21 offset:11264\n"
    "s_waitcnt lgkmcnt(13)\n"
    "v_add_f32 v16, v16, v18\n"
    "s_waitcnt lgkmcnt(12)\n"
    "v_add_f32 v17, v17, v19\n"
    "ds_read_b128 v[80:83], v21 offset:12288\n"
    "ds_read_b128 v[84:87], v21 offset:13312\n"
    "ds_read_b128 v[88:91], v21 offset:14336\n"
    "s_waitcnt lgkmcnt(8)\n"
    "ds_read_b128 v[92:95], v21 offset:15360\n"
    "v_rcp_f32 v18, v16\n"
    "v_rcp_f32 v19, v17\n"
    "v_mov_b32 v20, 0x53800000\n"
    "v_cmp_nlt_f32 vcc, v16, v20\n"
    "s_nop 0\n"
    "s_or_b64 s[92:93], s[92:93], vcc\n"
    "v_cmp_nlt_f32 vcc, v17, v20\n"
    "s_nop 0\n"
    "s_or_b64 s[92:93], s[92:93], vcc\n"
    "s_cmp_lg_u64 s[92:93], 0\n"
    "s_cselect_b32 s56, 1, 0\n"
    "v_mov_b32 v16, s56\n"
    "s_lshl_b32 s56, s52, 2\n"
    "s_add_u32 s56, s56, s68\n"
    "s_add_u32 s56, s56, 0x800\n"
    "v_mov_b32 v17, s56\n"
    "ds_write_b32 v17, v16\n"
    "v_pk_add_f32 v[160:161], v[160:161], v[32:33]\n"
    "v_pk_add_f32 v[162:163], v[162:163], v[34:35]\n"
    "v_pk_add_f32 v[164:165], v[164:165], v[36:37]\n"
    "v_pk_add_f32 v[166:167], v[166:167], v[38:39]\n"
    "v_pk_mul_f32 v[160:161], v[160:161], v[18:19] op_sel:[0,0] op_sel_hi:[1,0]\n"
    "v_pk_mul_f32 v[162:163], v[162:163], v[18:19] op_sel:[0,0] op_sel_hi:[1,0]\n"
    "v_pk_mul_f32 v[164:165], v[164:165], v[18:19] op_sel:[0,0] op_sel_hi:[1,0]\n"
    "v_pk_mul_f32 v[166:167], v[166:167], v[18:19] op_sel:[0,0] op_sel_hi:[1,0]\n"
    "v_cvt_pk_bf16_f32 v224, v160, v161\n"
    "v_cvt_pk_bf16_f32 v225, v162, v163\n"
    "v_cvt_pk_bf16_f32 v226, v164, v165\n"
    "v_cvt_pk_bf16_f32 v227, v166, v167\n"
    "s_add_u32 s58, s81, 0\n"
    "buffer_store_dwordx4 v[224:227], v1, s[44:47], s58 offen sc1\n"
    "v_pk_add_f32 v[168:169], v[168:169], v[40:41]\n"
    "v_pk_add_f32 v[170:171], v[170:171], v[42:43]\n"
    "v_pk_add_f32 v[172:173], v[172:173], v[44:45]\n"
    "v_pk_add_f32 v[174:175], v[174:175], v[46:47]\n"
    "v_pk_mul_f32 v[168:169], v[168:169], v[18:19] op_sel:[0,0] op_sel_hi:[1,0]\n"
    "v_pk_mul_f32 v[170:171], v[170:171], v[18:19] op_sel:[0,0] op_sel_hi:[1,0]\n"
    "v_pk_mul_f32 v[172:173], v[172:173], v[18:19] op_sel:[0,0] op_sel_hi:[1,0]\n"
    "v_pk_mul_f32 v[174:175], v[174:175], v[18:19] op_sel:[0,0] op_sel_hi:[1,0]\n"
    "v_cvt_pk_bf16_f32 v228, v168, v169\n"
    "v_cvt_pk_bf16_f32 v229, v170, v171\n"
    "v_cvt_pk_bf16_f32 v230, v172, v173\n"
    "v_cvt_pk_bf16_f32 v231, v174, v175\n"
    "s_add_u32 s58, s81, 1024\n"
    "buffer_store_dwordx4 v[228:231], v1, s[44:47], s58 offen sc1\n"
    "v_pk_add_f32 v[176:177], v[176:177], v[48:49]\n"
    "v_pk_add_f32 v[178:179], v[178:179], v[50:51]\n"
    "v_pk_add_f32 v[180:181], v[180:181], v[52:53]\n"
    "v_pk_add_f32 v[182:183], v[182:183], v[54:55]\n"
    "v_pk_mul_f32 v[176:177], v[176:177], v[18:19] op_sel:[0,0] op_sel_hi:[1,0]\n"
    "v_pk_mul_f32 v[178:179], v[178:179], v[18:19] op_sel:[0,0] op_sel_hi:[1,0]\n"
    "v_pk_mul_f32 v[180:181], v[180:181], v[18:19] op_sel:[0,0] op_sel_hi:[1,0]\n"
    "v_pk_mul_f32 v[182:183], v[182:183], v[18:19] op_sel:[0,0] op_sel_hi:[1,0]\n"
    "v_cvt_pk_bf16_f32 v232, v176, v177\n"
    "v_cvt_pk_bf16_f32 v233, v178, v179\n"
    "v_cvt_pk_bf16_f32 v234, v180, v181\n"
    "v_cvt_pk_bf16_f32 v235, v182, v183\n"
    "s_add_u32 s58, s81, 2048\n"
    "buffer_store_dwordx4 v[232:235], v1, s[44:47], s58 offen sc1\n"
    "v_pk_add_f32 v[184:185], v[184:185], v[56:57]\n"
    "v_pk_add_f32 v[186:187], v[186:187], v[58:59]\n"
    "s_waitcnt lgkmcnt(9)\n"
    "v_pk_add_f32 v[188:189], v[188:189], v[60:61]\n"
    "v_pk_add_f32 v[190:191], v[190:191], v[62:63]\n"
    "v_pk_mul_f32 v[184:185], v[184:185], v[18:19] op_sel:[0,0] op_sel_hi:[1,0]\n"
    "v_pk_mul_f32 v[186:187], v[186:187], v[18:19] op_sel:[0,0] op_sel_hi:[1,0]\n"
    "v_pk_mul_f32 v[188:189], v[188:189], v[18:19] op_sel:[0,0] op_sel_hi:[1,0]\n"
    "v_pk_mul_f32 v[190:191], v[190:191], v[18:19] op_sel:[0,0] op_sel_hi:[1,0]\n"
    "v_cvt_pk_bf16_f32 v236, v184, v185\n"
    "v_cvt_pk_bf16_f32 v237, v186, v187\n"
    "v_cvt_pk_bf16_f32 v238, v188, v189\n"
    "v_cvt_pk_bf16_f32 v239, v190, v191\n"
    "s_add_u32 s58, s81, 3072\n"
    "buffer_store_dwordx4 v[236:239], v1, s[44:47], s58 offen sc1\n"
    "s_waitcnt lgkmcnt(8)\n"
    "v_pk_add_f32 v[192:193], v[192:193], v[64:65]\n"
    "v_pk_add_f32 v[194:195], v[194:195], v[66:67]\n"
    "s_waitcnt lgkmcnt(7)\n"
    "v_pk_add_f32 v[196:197], v[196:197], v[68:69]\n"
    "v_pk_add_f32 v[198:199], v[198:199], v[70:71]\n"
    "v_pk_mul_f32 v[192:193], v[192:193], v[18:19] op_sel:[0,1] op_sel_hi:[1,1]\n"
    "v_pk_mul_f32 v[194:195], v[194:195], v[18:19] op_sel:[0,1] op_sel_hi:[1,1]\n"
    "v_pk_mul_f32 v[196:197], v[196:197], v[18:19] op_sel:[0,1] op_sel_hi:[1,1]\n"
    "v_pk_mul_f32 v[198:199], v[198:199], v[18:19] op_sel:[0,1] op_sel_hi:[1,1]\n"
    "v_cvt_pk_bf16_f32 v240, v192, v193\n"
    "v_cvt_pk_bf16_f32 v241, v194, v195\n"
    "v_cvt_pk_bf16_f32 v242, v196, v197\n"
    "v_cvt_pk_bf16_f32 v243, v198, v199\n"
    "s_add_u32 s58, s81, 131072\n"
    "buffer_store_dwordx4 v[240:243], v1, s[44:47], s58 offen sc1\n"
    "s_waitcnt lgkmcnt(6)\n"
    "v_pk_add_f32 v[200:201], v[200:201], v[72:73]\n"
    "v_pk_add_f32 v[202:203], v[202:203], v[74:75]\n"
    "s_waitcnt lgkmcnt(5)\n"
    "v_pk_add_f32 v[204:205], v[204:205], v[76:77]\n"
    "v_pk_add_f32 v[206:207], v[206:207], v[78:79]\n"
    "v_pk_mul_f32 v[200:201], v[200:201], v[18:19] op_sel:[0,1] op_sel_hi:[1,1]\n"
    "v_pk_mul_f32 v[202:203], v[202:203], v[18:19] op_sel:[0,1] op_sel_hi:[1,1]\n"
    "v_pk_mul_f32 v[204:205], v[204:205], v[18:19] op_sel:[0,1] op_sel_hi:[1,1]\n"
    "v_pk_mul_f32 v[206:207], v[206:207], v[18:19] op_sel:[0,1] op_sel_hi:[1,1]\n"
    "v_cvt_pk_bf16_f32 v244, v200, v201\n"
    "v_cvt_pk_bf16_f32 v245, v202, v203\n"
    "v_cvt_pk_bf16_f32 v246, v204, v205\n"
    "v_cvt_pk_bf16_f32 v247, v206, v207\n"
    "s_add_u32 s58, s81, 132096\n"
    "buffer_store_dwordx4 v[244:247], v1, s[44:47], s58 offen sc1\n"
    "s_waitcnt lgkmcnt(4)\n"
    "v_pk_add_f32 v[208:209], v[208:209], v[80:81]\n"
    "v_pk_add_f32 v[210:211], v[210:211], v[82:83]\n"
    "s_waitcnt lgkmcnt(3)\n"
    "v_pk_add_f32 v[212:213], v[212:213], v[84:85]\n"
    "v_pk_add_f32 v[214:215], v[214:215], v[86:87]\n"
    "v_pk_mul_f32 v[208:209], v[208:209], v[18:19] op_sel:[0,1] op_sel_hi:[1,1]\n"
    "v_pk_mul_f32 v[210:211], v[210:211], v[18:19] op_sel:[0,1] op_sel_hi:[1,1]\n"
    "v_pk_mul_f32 v[212:213], v[212:213], v[18:19] op_sel:[0,1] op_sel_hi:[1,1]\n"
    "v_pk_mul_f32 v[214:215], v[214:215], v[18:19] op_sel:[0,1] op_sel_hi:[1,1]\n"
    "v_cvt_pk_bf16_f32 v248, v208, v209\n"
    "v_cvt_pk_bf16_f32 v249, v210, v211\n"
    "v_cvt_pk_bf16_f32 v250, v212, v213\n"
    "v_cvt_pk_bf16_f32 v251, v214, v215\n"
    "s_add_u32 s58, s81, 133120\n"
    "buffer_store_dwordx4 v[248:251], v1, s[44:47], s58 offen sc1\n"
    "s_waitcnt lgkmcnt(2)\n"
    "v_pk_add_f32 v[216:217], v[216:217], v[88:89]\n"
    "v_pk_add_f32 v[218:219], v[218:219], v[90:91]\n"
    "s_waitcnt lgkmcnt(1)\n"
    "v_pk_add_f32 v[220:221], v[220:221], v[92:93]\n"
    "v_pk_add_f32 v[222:223], v[222:223], v[94:95]\n"
    "v_pk_mul_f32 v[216:217], v[216:217], v[18:19] op_sel:[0,1] op_sel_hi:[1,1]\n"
    "v_pk_mul_f32 v[218:219], v[218:219], v[18:19] op_sel:[0,1] op_sel_hi:[1,1]\n"
    "v_pk_mul_f32 v[220:221], v[220:221], v[18:19] op_sel:[0,1] op_sel_hi:[1,1]\n"
    "v_pk_mul_f32 v[222:223], v[222:223], v[18:19] op_sel:[0,1] op_sel_hi:[1,1]\n"
    "v_cvt_pk_bf16_f32 v252, v216, v217\n"
    "v_cvt_pk_bf16_f32 v253, v218, v219\n"
    "v_cvt_pk_bf16_f32 v254, v220, v221\n"
    "v_cvt_pk_bf16_f32 v255, v222, v223\n"
    "s_add_u32 s58, s81, 134144\n"
    "buffer_store_dwordx4 v[252:255], v1, s[44:47], s58 offen sc1\n"
    "s_waitcnt vmcnt(8)\n"
    "s_waitcnt lgkmcnt(0)\n"
    "s_barrier\n"
    "s_add_u32 s56, s68, 0x800\n"
    "v_mov_b32 v17, s56\n"
    "ds_read_b128 v[20:23], v17\n"
    "s_waitcnt lgkmcnt(0)\n"
    "v_or_b32 v20, v20, v21\n"
    "v_or_b32 v22, v22, v23\n"
    "v_or_b32 v20, v20, v22\n"
    "s_nop 0\n"
    "v_readfirstlane_b32 %0, v20\n"
    "s_mov_b32 %1, s99\n"
    "s_cmp_lg_u32 %0, 0\n"
    "s_cbranch_scc1 L_endA_16_%=\n"
    "v_and_b32 v13, 63, v0\n"
    "s_lshr_b32 s75, s53, 3\n"
    "s_lshl_b32 s59, s52, 11\n"
    "s_sub_u32 s59, s64, s59\n"
    "v_lshrrev_b32 v12, 4, v13\n"
    "v_bfe_u32 v14, v13, 2, 2\n"
    "v_lshlrev_b32 v15, 2, v12\n"
    "v_add_u32 v15, v15, v14\n"
    "v_and_b32 v14, 7, v15\n"
    "v_lshlrev_b32 v15, 8, v15\n"
    "v_and_b32 v12, 3, v13\n"
    "v_lshlrev_b32 v12, 3, v12\n"
    "v_add_u32 v15, v15, v12\n"
    "v_add_u32 v15, s59, v15\n"
    "s_lshl_b32 s57, s75, 1\n"
    "s_xor_b32 s58, s57, 0\n"
    "v_xor_b32 v12, s58, v14\n"
    "v_lshlrev_b32 v12, 5, v12\n"
    "v_add_u32 v8, v15, v12\n"
    "s_xor_b32 s58, s57, 1\n"
    "v_xor_b32 v12, s58, v14\n"
    "v_lshlrev_b32 v12, 5, v12\n"
    "v_add_u32 v9, v15, v12\n"
    "s_xor_b32 s58, s57, 2\n"
    "v_xor_b32 v12, s58, v14\n"
    "v_lshlrev_b32 v12, 5, v12\n"
    "v_add_u32 v10, v15, v12\n"
    "s_xor_b32 s58, s57, 3\n"
    "v_xor_b32 v12, s58, v14\n"
    "v_lshlrev_b32 v12, 5, v12\n"
    "v_add_u32 v11, v15, v12\n"
    "s_xor_b32 s58, s57, 4\n"
    "v_xor_b32 v12, s58, v14\n"
    "v_lshlrev_b32 v12, 5, v12\n"
    "v_add_u32 v16, v15, v12\n"
    "s_xor_b32 s58, s57, 5\n"
    "v_xor_b32 v12, s58, v14\n"
    "v_lshlrev_b32 v12, 5, v12\n"
    "v_add_u32 v17, v15, v12\n"
    "s_xor_b32 s58, s57, 6\n"
    "v_xor_b32 v12, s58, v14\n"
    "v_lshlrev_b32 v12, 5, v12\n"
    "v_add_u32 v18, v15, v12\n"
    "s_xor_b32 s58, s57, 7\n"
    "v_xor_b32 v12, s58, v14\n"
    "v_lshlrev_b32 v12, 5, v12\n"
    "v_add_u32 v19, v15, v12\n"
    "ds_read_b64_tr_b16 v[32:33], v8 offset:0\n"
    "ds_read_b64_tr_b16 v[34:35], v8 offset:4096\n"
    "ds_read_b64_tr_b16 v[36:37], v9 offset:0\n"
    "ds_read_b64_tr_b16 v[38:39], v9 offset:4096\n"
    "ds_read_b64_tr_b16 v[40:41], v10 offset:0\n"
    "ds_read_b64_tr_b16 v[42:43], v10 offset:4096\n"
    "ds_read_b64_tr_b16 v[44:45], v11 offset:0\n"
    "ds_read_b64_tr_b16 v[46:47], v11 offset:4096\n"
    "ds_read_b64_tr_b16 v[48:49], v16 offset:0\n"
    "ds_read_b64_tr_b16 v[50:51], v16 offset:4096\n"
    "ds_read_b64_tr_b16 v[52:53], v17 offset:0\n"
    "ds_read_b64_tr_b16 v[54:55], v17 offset:4096\n"
    "ds_read_b64_tr_b16 v[56:57], v18 offset:0\n"
    "ds_read_b64_tr_b16 v[58:59], v18 offset:4096\n"
    "ds_read_b64_tr_b16 v[60:61], v19 offset:0\n"
    "s_waitcnt lgkmcnt(8)\n"
    "ds_read_b64_tr_b16 v[62:63], v19 offset:4096\n"
    "s_sub_u32 s76, s65, s64\n"
    "s_sub_u32 s77, s66, s65\n"
    "s_sub_u32 s78, s64, s66\n"
    "s_sub_u32 s56, 15, s54\n"
    "s_bitcmp1_b32 s53, 2\n"
    "s_cselect_b32 s56, s54, s56\n"
    "s_lshl_b32 s56, s56, 2\n"
    "s_add_u32 s56, s56, s52\n"
    "s_lshl_b32 s73, s56, 15\n"
    "s_xor_b32 s56, s53, 2\n"
    "s_and_b32 s56, s56, 3\n"
    "s_lshl_b32 s56, s56, 8\n"
    "s_add_u32 s73, s73, s56\n"
    "s_lshl_b32 s56, s75, 6\n"
    "s_add_u32 s73, s73, s56\n"
    "s_add_u32 s72, s73, 16384\n"
    "v_and_b32 v12, 15, v13\n"
    "v_lshlrev_b32 v12, 10, v12\n"
    "v_lshrrev_b32 v14, 4, v13\n"
    "v_lshlrev_b32 v14, 3, v14\n"
    "v_add_u32 v20, v12, v14\n"
    "s_mov_b64 s[48:49], %10\n"
    "s_and_b32 s49, s49, 0xffff\n"
    "s_mov_b32 s50, 0x200000\n"
    "s_mov_b32 s51, 0x20000\n"
    "s_waitcnt lgkmcnt(8)\n"
    "s_and_b32 s56, s53, 7\n"
    "s_lshl_b32 s56, s56, 3\n"
    "s_add_u32 s56, s56, s54\n"
    "s_lshl_b32 s56, s56, 2\n"
    "s_add_u32 s56, s56, s52\n"
    "s_lshl_b32 s74, s56, 15\n"
    "s_add_u32 s74, s74, 0x4000000\n"
    "s_xor_b32 s57, s75, 1\n"
    "s_lshl_b32 s57, s57, 13\n"
    "s_add_u32 s57, s57, 2048\n"
    "s_add_u32 s81, s57, s74\n"
    "s_xor_b32 s57, s75, 2\n"
    "s_lshl_b32 s57, s57, 13\n"
    "s_add_u32 s57, s57, 4096\n"
    "s_add_u32 s82, s57, s74\n"
    "s_xor_b32 s57, s75, 3\n"
    "s_lshl_b32 s57, s57, 13\n"
    "s_add_u32 s57, s57, 6144\n"
    "s_add_u32 s83, s57, s74\n"
    "v_lshrrev_b32 v21, 2, v1\n"
    "v_and_b32 v21, 0x7c, v21\n"
    "v_and_b32 v22, 12, v21\n"
    "v_mfma_f32_16x16x32_bf16 v[160:163], v[32:35], v[96:99], 0\n"
    "s_add_u32 s56, s60, 0x1800\n"
    "ds_read_b64_tr_b16 v[64:65], v8 offset:8192\n"
    "v_mfma_f32_16x16x32_bf16 v[164:167], v[32:35], v[100:103], 0\n"
    "ds_read_b64_tr_b16 v[66:67], v8 offset:12288\n"
    "buffer_load_dwordx4 v[144:147], v1, s[44:47], s56 offen sc1\n"
    "v_mfma_f32_16x16x32_bf16 v[168:171], v[36:39], v[96:99], 0\n"
    "s_add_u32 s57, s62, 0x40000\n"
    "ds_read_b64_tr_b16 v[68:69], v9 offset:8192\n"
    "v_mfma_f32_16x16x32_bf16 v[172:175], v[36:39], v[100:103], 0\n"
    "s_add_u32 m0, s66, 0\n"
    "ds_read_b64_tr_b16 v[70:71], v9 offset:12288\n"
    "v_mfma_f32_16x16x32_bf16 v[176:179], v[40:43], v[96:99], 0\n"
    "ds_read_b64_tr_b16 v[72:73], v10 offset:8192\n"
    "buffer_load_dwordx4 v28, s[36:39], s57 offen lds\n"
    "v_mfma_f32_16x16x32_bf16 v[180:183], v[40:43], v[100:103], 0\n"
    "s_add_u32 s56, s60, 0xf800\n"
    "ds_read_b64_tr_b16 v[74:75], v10 offset:12288\n"
    "v_mfma_f32_16x16x32_bf16 v[184:187], v[44:47], v[96:99], 0\n"
    "s_add_u32 m0, s66, 1024\n"
    "ds_read_b64_tr_b16 v[76:77], v11 offset:8192\n"
    "v_mfma_f32_16x16x32_bf16 v[188:191], v[44:47], v[100:103], 0\n"
    "s_waitcnt lgkmcnt(7)\n"
    "ds_read_b64_tr_b16 v[78:79], v11 offset:12288\n"
    "v_mfma_f32_16x16x32_bf16 v[192:195], v[48:51], v[96:99], 0\n"
    "ds_read_b64_tr_b16 v[32:33], v16 offset:8192\n"
    "buffer_load_dwordx4 v29, s[36:39], s57 offen lds\n"
    "v_mfma_f32_16x16x32_bf16 v[196:199], v[48:51], v[100:103], 0\n"
    "ds_read_b64_tr_b16 v[34:35], v16 offset:12288\n"
    "v_mfma_f32_16x16x32_bf16 v[200:203], v[52:55], v[96:99], 0\n"
    "ds_read_b64_tr_b16 v[36:37], v17 offset:8192\n"
    "v_mfma_f32_16x16x32_bf16 v[204:207], v[52:55], v[100:103], 0\n"
    "ds_read_b64_tr_b16 v[38:39], v17 offset:12288\n"
    "buffer_load_dwordx4 v[148:151], v1, s[44:47], s56 offen sc1\n"
    "v_mfma_f32_16x16x32_bf16 v[208:211], v[56:59], v[96:99], 0\n"
    "s_add_u32 s57, s62, 0x48000\n"
    "ds_read_b64_tr_b16 v[40:41], v18 offset:8192\n"
    "v_mfma_f32_16x16x32_bf16 v[212:215], v[56:59], v[100:103], 0\n"
    "s_add_u32 m0, s66, 8192\n"
    "ds_read_b64_tr_b16 v[42:43], v18 offset:12288\n"
    "v_mfma_f32_16x16x32_bf16 v[216:219], v[60:63], v[96:99], 0\n"
    "ds_read_b64_tr_b16 v[44:45], v19 offset:8192\n"
    "buffer_load_dwordx4 v28, s[36:39], s57 offen lds\n"
    "v_mfma_f32_16x16x32_bf16 v[220:223], v[60:63], v[100:103], 0\n"
    "s_waitcnt lgkmcnt(7)\n"
    "ds_read_b64_tr_b16 v[46:47], v19 offset:12288\n"
    "v_mfma_f32_16x16x32_bf16 v[160:163], v[64:67], v[104:107], v[160:163]\n"
    "s_add_u32 m0, s66, 9216\n"
    "ds_read_b64_tr_b16 v[48:49], v8 offset:16384\n"
    "v_mfma_f32_16x16x32_bf16 v[164:167], v[64:67], v[108:111], v[164:167]\n"
    "ds_read_b64_tr_b16 v[50:51], v8 offset:20480\n"
    "buffer_load_dwordx4 v29, s[36:39], s57 offen lds\n"
    "v_mfma_f32_16x16x32_bf16 v[168:171], v[68:71], v[104:107], v[168:171]\n"
    "s_add_u32 s56, s60, 0x1c00\n"
    "ds_read_b64_tr_b16 v[52:53], v9 offset:16384\n"
    "v_mfma_f32_16x16x32_bf16 v[172:175], v[68:71], v[108:111], v[172:175]\n"
    "ds_read_b64_tr_b16 v[54:55], v9 offset:20480\n"
    "v_mfma_f32_16x16x32_bf16 v[176:179], v[72:75], v[104:107], v[176:179]\n"
    "ds_read_b64_tr_b16 v[56:57], v10 offset:16384\n"
    "buffer_load_dwordx4 v[152:155], v1, s[44:47], s56 offen sc1\n"
    "v_mfma_f32_16x16x32_bf16 v[180:183], v[72:75], v[108:111], v[180:183]\n"
    "s_add_u32 s57, s62, 0x50000\n"
    "ds_read_b64_tr_b16 v[58:59], v10 offset:20480\n"
    "v_mfma_f32_16x16x32_bf16 v[184:187], v[76:79], v[104:107], v[184:187]\n"
    "s_add_u32 m0, s66, 16384\n"
    "ds_read_b64_tr_b16 v[60:61], v11 offset:16384\n"
    "v_mfma_f32_16x16x32_bf16 v[188:191], v[76:79], v[108:111], v[188:191]\n"
    "s_waitcnt lgkmcnt(7)\n"
    "ds_read_b64_tr_b16 v[62:63], v11 offset:20480\n"
    "v_mfma_f32_16x16x32_bf16 v[192:195], v[32:35], v[104:107], v[192:195]\n"
    "ds_read_b64_tr_b16 v[64:65], v16 offset:16384\n"
    "buffer_load_dwordx4 v28, s[36:39], s57 offen lds\n"
    "v_mfma_f32_16x16x32_bf16 v[196:199], v[32:35], v[108:111], v[196:199]\n"
    "s_add_u32 s56, s60, 0xfc00\n"
    "ds_read_b64_tr_b16 v[66:67], v16 offset:20480\n"
    "v_mfma_f32_16x16x32_bf16 v[200:203], v[36:39], v[104:107], v[200:203]\n"
    "ds_read_b64_tr_b16 v[68:69], v17 offset:16384\n"
    "v_mfma_f32_16x16x32_bf16 v[204:207], v[36:39], v[108:111], v[204:207]\n"
    "ds_read_b64_tr_b16 v[70:71], v17 offset:20480\n"
    "buffer_load_dwordx4 v[156:159], v1, s[44:47], s56 offen sc1\n"
    "v_mfma_f32_16x16x32_bf16 v[208:211], v[40:43], v[104:107], v[208:211]\n"
    "s_add_u32 m0, s66, 17408\n"
    "ds_read_b64_tr_b16 v[72:73], v18 offset:16384\n"
    "v_mfma_f32_16x16x32_bf16 v[212:215], v[40:43], v[108:111], v[212:215]\n"
    "s_add_u32 s56, s60, 0x2000\n"
    "ds_read_b64_tr_b16 v[74:75], v18 offset:20480\n"
    "v_mfma_f32_16x16x32_bf16 v[216:219], v[44:47], v[104:107], v[216:219]\n"
    "ds_read_b64_tr_b16 v[76:77], v19 offset:16384\n"
    "buffer_load_dwordx4 v29, s[36:39], s57 offen lds\n"
    "v_mfma_f32_16x16x32_bf16 v[220:223], v[44:47], v[108:111], v[220:223]\n"
    "s_waitcnt lgkmcnt(7)\n"
    "ds_read_b64_tr_b16 v[78:79], v19 offset:20480\n"
    "v_mfma_f32_16x16x32_bf16 v[160:163], v[48:51], v[112:115], v[160:163]\n"
    "s_add_u32 s57, s62, 0x58000\n"
    "ds_read_b64_tr_b16 v[32:33], v8 offset:24576\n"
    "v_mfma_f32_16x16x32_bf16 v[164:167], v[48:51], v[116:119], v[164:167]\n"
    "ds_read_b64_tr_b16 v[34:35], v8 offset:28672\n"
    "buffer_load_dwordx4 v[96:99], v1, s[44:47], s56 offen sc1\n"
    "v_mfma_f32_16x16x32_bf16 v[168:171], v[52:55], v[112:115], v[168:171]\n"
    "s_add_u32 m0, s66, 24576\n"
    "ds_read_b64_tr_b16 v[36:37], v9 offset:24576\n"
    "v_mfma_f32_16x16x32_bf16 v[172:175], v[52:55], v[116:119], v[172:175]\n"
    "s_add_u32 s56, s60, 0x10000\n"
    "ds_read_b64_tr_b16 v[38:39], v9 offset:28672\n"
    "v_mfma_f32_16x16x32_bf16 v[176:179], v[56:59], v[112:115], v[176:179]\n"
    "ds_read_b64_tr_b16 v[40:41], v10 offset:24576\n"
    "buffer_load_dwordx4 v28, s[36:39], s57 offen lds\n"
    "v_mfma_f32_16x16x32_bf16 v[180:183], v[56:59], v[116:119], v[180:183]\n"
    "s_add_u32 m0, s66, 25600\n"
    "ds_read_b64_tr_b16 v[42:43], v10 offset:28672\n"
    "v_mfma_f32_16x16x32_bf16 v[184:187], v[60:63], v[112:115], v[184:187]\n"
    "ds_read_b64_tr_b16 v[44:45], v11 offset:24576\n"
    "v_mfma_f32_16x16x32_bf16 v[188:191], v[60:63], v[116:119], v[188:191]\n"
    "s_waitcnt lgkmcnt(7)\n"
    "ds_read_b64_tr_b16 v[46:47], v11 offset:28672\n"
    "v_mfma_f32_16x16x32_bf16 v[192:195], v[64:67], v[112:115], v[192:195]\n"
    "ds_read_b64_tr_b16 v[48:49], v16 offset:24576\n"
    "buffer_load_dwordx4 v29, s[36:39], s57 offen lds\n"
    "v_mfma_f32_16x16x32_bf16 v[196:199], v[64:67], v[116:119], v[196:199]\n"
    "ds_read_b64_tr_b16 v[50:51], v16 offset:28672\n"
    "v_mfma_f32_16x16x32_bf16 v[200:203], v[68:71], v[112:115], v[200:203]\n"
    "s_add_u32 s57, s62, 0x60000\n"
    "ds_read_b64_tr_b16 v[52:53], v17 offset:24576\n"
    "v_mfma_f32_16x16x32_bf16 v[204:207], v[68:71], v[116:119], v[204:207]\n"
    "ds_read_b64_tr_b16 v[54:55], v17 offset:28672\n"
    "buffer_load_dwordx4 v[100:103], v1, s[44:47], s56 offen sc1\n"
    "v_mfma_f32_16x16x32_bf16 v[208:211], v[72:75], v[112:115], v[208:211]\n"
    "s_add_u32 m0, s66, 32768\n"
    "ds_read_b64_tr_b16 v[56:57], v18 offset:24576\n"
    "v_mfma_f32_16x16x32_bf16 v[212:215], v[72:75], v[116:119], v[212:215]\n"
    "s_add_u32 s56, s60, 0x2400\n"
    "ds_read_b64_tr_b16 v[58:59], v18 offset:28672\n"
    "v_mfma_f32_16x16x32_bf16 v[216:219], v[76:79], v[112:115], v[216:219]\n"
    "ds_read_b64_tr_b16 v[60:61], v19 offset:24576\n"
    "buffer_load_dwordx4 v28, s[36:39], s57 offen lds\n"
    "v_mfma_f32_16x16x32_bf16 v[220:223], v[76:79], v[116:119], v[220:223]\n"
    "s_waitcnt lgkmcnt(7)\n"
    "ds_read_b64_tr_b16 v[62:63], v19 offset:28672\n"
    "s_waitcnt vmcnt(15)\n"
    "s_lshl_b32 s96, s54, 2\n"
    "s_lshr_b32 s95, s53, 3\n"
    "s_add_u32 s96, s96, s95\n"
    "s_lshl_b32 s96, s96, 7\n"
    "s_add_u32 s96, s96, 0x4000\n"
    "s_and_b32 s95, s53, 7\n"
    "s_lshl_b32 s95, s95, 2\n"
    "s_add_u32 s95, s95, s52\n"
    "s_lshl_b32 s95, s95, 2\n"
    "s_add_u32 s96, s96, s95\n"
    "v_mov_b32 v24, s97\n"
    "buffer_store_dword v24, v7, s[44:47], s96 offen sc1\n"
    "s_barrier\n"
    "v_add_u32 v8, s76, v8\n"
    "v_mfma_f32_16x16x32_bf16 v[160:163], v[32:35], v[120:123], v[160:163]\n"
    "s_add_u32 m0, s66, 33792\n"
    "ds_read_b64_tr_b16 v[64:65], v8 offset:0\n"
    "v_mfma_f32_16x16x32_bf16 v[164:167], v[32:35], v[124:127], v[164:167]\n"
    "ds_read_b64_tr_b16 v[66:67], v8 offset:4096\n"
    "v_add_u32 v9, s76, v9\n"
    "v_mfma_f32_16x16x32_bf16 v[168:171], v[36:39], v[120:123], v[168:171]\n"
    "ds_read_b64_tr_b16 v[68:69], v9 offset:0\n"
    "buffer_load_dwordx4 v29, s[36:39], s57 offen lds\n"
    "v_mfma_f32_16x16x32_bf16 v[172:175], v[36:39], v[124:127], v[172:175]\n"
    "ds_read_b64_tr_b16 v[70:71], v9 offset:4096\n"
    "v_add_u32 v10, s76, v10\n"
    "v_mfma_f32_16x16x32_bf16 v[176:179], v[40:43], v[120:123], v[176:179]\n"
    "s_add_u32 s57, s62, 0x68000\n"
    "ds_read_b64_tr_b16 v[72:73], v10 offset:0\n"
    "v_mfma_f32_16x16x32_bf16 v[180:183], v[40:43], v[124:127], v[180:183]\n"
    "s_add_u32 m0, s66, 40960\n"
    "ds_read_b64_tr_b16 v[74:75], v10 offset:4096\n"
    "v_add_u32 v11, s76, v11\n"
    "v_mfma_f32_16x16x32_bf16 v[184:187], v[44:47], v[120:123], v[184:187]\n"
    "ds_read_b64_tr_b16 v[76:77], v11 offset:0\n"
    "buffer_load_dwordx4 v28, s[36:39], s57 offen lds\n"
    "v_mfma_f32_16x16x32_bf16 v[188:191], v[44:47], v[124:127], v[188:191]\n"
    "s_waitcnt lgkmcnt(7)\n"
    "ds_read_b64_tr_b16 v[78:79], v11 offset:4096\n"
    "v_add_u32 v16, s76, v16\n"
    "v_mfma_f32_16x16x32_bf16 v[192:195], v[48:51], v[120:123], v[192:195]\n"
    "ds_read_b64_tr_b16 v[32:33], v16 offset:0\n"
    "v_mfma_f32_16x16x32_bf16 v[196:199], v[48:51], v[124:127], v[196:199]\n"
    "ds_read_b64_tr_b16 v[34:35], v16 offset:4096\n"
    "v_add_u32 v17, s76, v17\n"
    "v_mfma_f32_16x16x32_bf16 v[200:203], v[52:55], v[120:123], v[200:203]\n"
    "ds_read_b64_tr_b16 v[36:37], v17 offset:0\n"
    "buffer_load_dwordx4 v[104:107], v1, s[44:47], s56 offen sc1\n"
    "v_mfma_f32_16x16x32_bf16 v[204:207], v[52:55], v[124:127], v[204:207]\n"
    "s_add_u32 m0, s66, 41984\n"
    "ds_read_b64_tr_b16 v[38:39], v17 offset:4096\n"
    "v_add_u32 v18, s76, v18\n"
    "v_mfma_f32_16x16x32_bf16 v[208:211], v[56:59], v[120:123], v[208:211]\n"
    "s_add_u32 s56, s60, 0x10400\n"
    "ds_read_b64_tr_b16 v[40:41], v18 offset:0\n"
    "v_mfma_f32_16x16x32_bf16 v[212:215], v[56:59], v[124:127], v[212:215]\n"
    "ds_read_b64_tr_b16 v[42:43], v18 offset:4096\n"
    "v_add_u32 v19, s76, v19\n"
    "v_mfma_f32_16x16x32_bf16 v[216:219], v[60:63], v[120:123], v[216:219]\n"
    "ds_read_b64_tr_b16 v[44:45], v19 offset:0\n"
    "buffer_load_dwordx4 v29, s[36:39], s57 offen lds\n"
    "v_mfma_f32_16x16x32_bf16 v[220:223], v[60:63], v[124:127], v[220:223]\n"
    "s_waitcnt lgkmcnt(7)\n"
    "ds_read_b64_tr_b16 v[46:47], v19 offset:4096\n"
    "v_mfma_f32_16x16x32_bf16 v[160:163], v[64:67], v[128:131], v[160:163]\n"
    "s_add_u32 s57, s70, 0x0\n"
    "ds_read_b64_tr_b16 v[48:49], v8 offset:8192\n"
    "v_mfma_f32_16x16x32_bf16 v[164:167], v[64:67], v[132:135], v[164:167]\n"
    "ds_read_b64_tr_b16 v[50:51], v8 offset:12288\n"
    "buffer_load_dwordx4 v[108:111], v1, s[44:47], s56 offen sc1\n"
    "v_mfma_f32_16x16x32_bf16 v[168:171], v[68:71], v[128:131], v[168:171]\n"
    "s_add_u32 m0, s64, 0\n"
    "ds_read_b64_tr_b16 v[52:53], v9 offset:8192\n"
    "v_mfma_f32_16x16x32_bf16 v[172:175], v[68:71], v[132:135], v[172:175]\n"
    "s_add_u32 s56, s60, 0x2800\n"
    "ds_read_b64_tr_b16 v[54:55], v9 offset:12288\n"
    "v_mfma_f32_16x16x32_bf16 v[176:179], v[72:75], v[128:131], v[176:179]\n"
    "ds_read_b64_tr_b16 v[56:57], v10 offset:8192\n"
    "buffer_load_dwordx4 v28, s[40:43], s57 offen lds\n"
    "v_mfma_f32_16x16x32_bf16 v[180:183], v[72:75], v[132:135], v[180:183]\n"
    "s_add_u32 m0, s64, 1024\n"
    "ds_read_b64_tr_b16 v[58:59], v10 offset:12288\n"
    "v_mfma_f32_16x16x32_bf16 v[184:187], v[76:79], v[128:131], v[184:187]\n"
    "ds_read_b64_tr_b16 v[60:61], v11 offset:8192\n"
    "v_mfma_f32_16x16x32_bf16 v[188:191], v[76:79], v[132:135], v[188:191]\n"
    "s_waitcnt lgkmcnt(7)\n"
    "ds_read_b64_tr_b16 v[62:63], v11 offset:12288\n"
    "v_mfma_f32_16x16x32_bf16 v[192:195], v[32:35], v[128:131], v[192:195]\n"
    "ds_read_b64_tr_b16 v[64:65], v16 offset:8192\n"
    "buffer_load_dwordx4 v29, s[40:43], s57 offen lds\n"
    "v_mfma_f32_16x16x32_bf16 v[196:199], v[32:35], v[132:135], v[196:199]\n"
    "s_add_u32 s57, s70, 0x8000\n"
    "ds_read_b64_tr_b16 v[66:67], v16 offset:12288\n"
    "v_mfma_f32_16x16x32_bf16 v[200:203], v[36:39], v[128:131], v[200:203]\n"
    "s_add_u32 m0, s64, 8192\n"
    "ds_read_b64_tr_b16 v[68:69], v17 offset:8192\n"
    "v_mfma_f32_16x16x32_bf16 v[204:207], v[36:39], v[132:135], v[204:207]\n"
    "ds_read_b64_tr_b16 v[70:71], v17 offset:12288\n"
    "buffer_load_dwordx4 v28, s[40:43], s57 offen lds\n"
    "v_mfma_f32_16x16x32_bf16 v[208:211], v[40:43], v[128:131], v[208:211]\n"
    "s_add_u32 m0, s64, 9216\n"
    "ds_read_b64_tr_b16 v[72:73], v18 offset:8192\n"
    "v_mfma_f32_16x16x32_bf16 v[212:215], v[40:43], v[132:135], v[212:215]\n"
    "ds_read_b64_tr_b16 v[74:75], v18 offset:12288\n"
    "v_mfma_f32_16x16x32_bf16 v[216:219], v[44:47], v[128:131], v[216:219]\n"
    "ds_read_b64_tr_b16 v[76:77], v19 offset:8192\n"
    "buffer_load_dwordx4 v29, s[40:43], s57 offen lds\n"
    "v_mfma_f32_16x16x32_bf16 v[220:223], v[44:47], v[132:135], v[220:223]\n"
    "s_waitcnt lgkmcnt(7)\n"
    "ds_read_b64_tr_b16 v[78:79], v19 offset:12288\n"
    "v_mfma_f32_16x16x32_bf16 v[160:163], v[48:51], v[136:139], v[160:163]\n"
    "s_add_u32 s57, s70, 0x10000\n"
    "ds_read_b64_tr_b16 v[32:33], v8 offset:16384\n"
    "v_mfma_f32_16x16x32_bf16 v[164:167], v[48:51], v[140:143], v[164:167]\n"
    "ds_read_b64_tr_b16 v[34:35], v8 offset:20480\n"
    "buffer_load_dwordx4 v[112:115], v1, s[44:47], s56 offen sc1\n"
    "v_mfma_f32_16x16x32_bf16 v[168:171], v[52:55], v[136:139], v[168:171]\n"
    "s_add_u32 m0, s64, 16384\n"
    "ds_read_b64_tr_b16 v[36:37], v9 offset:16384\n"
    "v_mfma_f32_16x16x32_bf16 v[172:175], v[52:55], v[140:143], v[172:175]\n"
    "s_add_u32 s56, s60, 0x10800\n"
    "ds_read_b64_tr_b16 v[38:39], v9 offset:20480\n"
    "v_mfma_f32_16x16x32_bf16 v[176:179], v[56:59], v[136:139], v[176:179]\n"
    "ds_read_b64_tr_b16 v[40:41], v10 offset:16384\n"
    "buffer_load_dwordx4 v28, s[40:43], s57 offen lds\n"
    "v_mfma_f32_16x16x32_bf16 v[180:183], v[56:59], v[140:143], v[180:183]\n"
    "s_add_u32 m0, s64, 17408\n"
    "ds_read_b64_tr_b16 v[42:43], v10 offset:20480\n"
    "v_mfma_f32_16x16x32_bf16 v[184:187], v[60:63], v[136:139], v[184:187]\n"
    "ds_read_b64_tr_b16 v[44:45], v11 offset:16384\n"
    "v_mfma_f32_16x16x32_bf16 v[188:191], v[60:63], v[140:143], v[188:191]\n"
    "s_waitcnt lgkmcnt(7)\n"
    "ds_read_b64_tr_b16 v[46:47], v11 offset:20480\n"
    "v_mfma_f32_16x16x32_bf16 v[192:195], v[64:67], v[136:139], v[192:195]\n"
    "ds_read_b64_tr_b16 v[48:49], v16 offset:16384\n"
    "buffer_load_dwordx4 v29, s[40:43], s57 offen lds\n"
    "v_mfma_f32_16x16x32_bf16 v[196:199], v[64:67], v[140:143], v[196:199]\n"
    "s_add_u32 s57, s70, 0x18000\n"
    "ds_read_b64_tr_b16 v[50:51], v16 offset:20480\n"
    "v_mfma_f32_16x16x32_bf16 v[200:203], v[68:71], v[136:139], v[200:203]\n"
    "s_add_u32 m0, s64, 24576\n"
    "ds_read_b64_tr_b16 v[52:53], v17 offset:16384\n"
    "v_mfma_f32_16x16x32_bf16 v[204:207], v[68:71], v[140:143], v[204:207]\n"
    "ds_read_b64_tr_b16 v[54:55], v17 offset:20480\n"
    "buffer_load_dwordx4 v28, s[40:43], s57 offen lds\n"
    "v_mfma_f32_16x16x32_bf16 v[208:211], v[72:75], v[136:139], v[208:211]\n"
    "s_waitcnt vmcnt(25)\n"
    "ds_read_b64_tr_b16 v[56:57], v18 offset:16384\n"
    "v_mfma_f32_16x16x32_bf16 v[212:215], v[72:75], v[140:143], v[212:215]\n"
    "s_add_u32 m0, s64, 25600\n"
    "ds_read_b64_tr_b16 v[58:59], v18 offset:20480\n"
    "v_mfma_f32_16x16x32_bf16 v[216:219], v[76:79], v[136:139], v[216:219]\n"
    "ds_read_b64_tr_b16 v[60:61], v19 offset:16384\n"
    "buffer_load_dwordx4 v29, s[40:43], s57 offen lds\n"
    "v_mfma_f32_16x16x32_bf16 v[220:223], v[76:79], v[140:143], v[220:223]\n"
    "s_waitcnt lgkmcnt(7)\n"
    "ds_read_b64_tr_b16 v[62:63], v19 offset:20480\n"
    "v_mfma_f32_16x16x32_bf16 v[160:163], v[32:35], v[144:147], v[160:163]\n"
    "s_add_u32 s57, s53, 1\n"
    "ds_read_b64_tr_b16 v[64:65], v8 offset:24576\n"
    "v_mfma_f32_16x16x32_bf16 v[164:167], v[32:35], v[148:151], v[164:167]\n"
    "s_and_b32 s57, s57, 7\n"
    "s_lshl_b32 s70, s57, 17\n"
    "s_add_u32 s70, s70, s63\n"
    "ds_read_b64_tr_b16 v[66:67], v8 offset:28672\n"
    "v_mfma_f32_16x16x32_bf16 v[168:171], v[36:39], v[144:147], v[168:171]\n"
    "s_add_u32 s57, s70, 0x0\n"
    "s_add_u32 m0, s64, 32768\n"
    "ds_read_b64_tr_b16 v[68:69], v9 offset:24576\n"
    "v_mfma_f32_16x16x32_bf16 v[172:175], v[36:39], v[148:151], v[172:175]\n"
    "ds_read_b64_tr_b16 v[70:71], v9 offset:28672\n"
    "buffer_load_dwordx4 v28, s[40:43], s57 offen lds\n"
    "v_mfma_f32_16x16x32_bf16 v[176:179], v[40:43], v[144:147], v[176:179]\n"
    "s_add_u32 m0, s64, 33792\n"
    "ds_read_b64_tr_b16 v[72:73], v10 offset:24576\n"
    "v_mfma_f32_16x16x32_bf16 v[180:183], v[40:43], v[148:151], v[180:183]\n"
    "ds_read_b64_tr_b16 v[74:75], v10 offset:28672\n"
    "v_mfma_f32_16x16x32_bf16 v[184:187], v[44:47], v[144:147], v[184:187]\n"
    "ds_read_b64_tr_b16 v[76:77], v11 offset:24576\n"
    "buffer_load_dwordx4 v29, s[40:43], s57 offen lds\n"
    "v_mfma_f32_16x16x32_bf16 v[188:191], v[44:47], v[148:151], v[188:191]\n"
    "s_waitcnt lgkmcnt(7)\n"
    "ds_read_b64_tr_b16 v[78:79], v11 offset:28672\n"
    "v_mfma_f32_16x16x32_bf16 v[192:195], v[48:51], v[144:147], v[192:195]\n"
    "ds_read_b64_tr_b16 v[32:33], v16 offset:24576\n"
    "buffer_load_dwordx4 v[116:119], v1, s[44:47], s56 offen sc1\n"
    "v_mfma_f32_16x16x32_bf16 v[196:199], v[48:51], v[148:151], v[196:199]\n"
    "s_add_u32 s57, s70, 0x8000\n"
    "s_add_u32 m0, s64, 40960\n"
    "ds_read_b64_tr_b16 v[34:35], v16 offset:28672\n"
    "v_mfma_f32_16x16x32_bf16 v[200:203], v[52:55], v[144:147], v[200:203]\n"
    "s_add_u32 s56, s60, 0x2c00\n"
    "ds_read_b64_tr_b16 v[36:37], v17 offset:24576\n"
    "v_mfma_f32_16x16x32_bf16 v[204:207], v[52:55], v[148:151], v[204:207]\n"
    "ds_read_b64_tr_b16 v[38:39], v17 offset:28672\n"
    "buffer_load_dwordx4 v28, s[40:43], s57 offen lds\n"
    "v_mfma_f32_16x16x32_bf16 v[208:211], v[56:59], v[144:147], v[208:211]\n"
    "s_waitcnt vmcnt(25)\n"
    "s_add_u32 m0, s64, 41984\n"
    "ds_read_b64_tr_b16 v[40:41], v18 offset:24576\n"
    "v_mfma_f32_16x16x32_bf16 v[212:215], v[56:59], v[148:151], v[212:215]\n"
    "ds_read_b64_tr_b16 v[42:43], v18 offset:28672\n"
    "v_mfma_f32_16x16x32_bf16 v[216:219], v[60:63], v[144:147], v[216:219]\n"
    "ds_read_b64_tr_b16 v[44:45], v19 offset:24576\n"
    "buffer_load_dwordx4 v29, s[40:43], s57 offen lds\n"
    "v_mfma_f32_16x16x32_bf16 v[220:223], v[60:63], v[148:151], v[220:223]\n"
    "s_waitcnt lgkmcnt(7)\n"
    "ds_read_b64_tr_b16 v[46:47], v19 offset:28672\n"
    "s_waitcnt vmcnt(15)\n"
    "s_barrier\n"
    "v_add_u32 v8, s77, v8\n"
    "v_mfma_f32_16x16x32_bf16 v[160:163], v[64:67], v[152:155], v[160:163]\n"
    "ds_read_b64_tr_b16 v[48:49], v8 offset:0\n"
    "v_mfma_f32_16x16x32_bf16 v[164:167], v[64:67], v[156:159], v[164:167]\n"
    "s_add_u32 s57, s70, 0x10000\n"
    "ds_read_b64_tr_b16 v[50:51], v8 offset:4096\n"
    "v_add_u32 v9, s77, v9\n"
    "v_mfma_f32_16x16x32_bf16 v[168:171], v[68:71], v[152:155], v[168:171]\n"
    "ds_read_b64_tr_b16 v[52:53], v9 offset:0\n"
    "buffer_load_dwordx4 v[120:123], v1, s[44:47], s56 offen sc1\n"
    "v_mfma_f32_16x16x32_bf16 v[172:175], v[68:71], v[156:159], v[172:175]\n"
    "s_add_u32 s56, s60, 0x10c00\n"
    "ds_read_b64_tr_b16 v[54:55], v9 offset:4096\n"
    "v_add_u32 v10, s77, v10\n"
    "v_mfma_f32_16x16x32_bf16 v[176:179], v[72:75], v[152:155], v[176:179]\n"
    "s_add_u32 m0, s65, 0\n"
    "ds_read_b64_tr_b16 v[56:57], v10 offset:0\n"
    "v_mfma_f32_16x16x32_bf16 v[180:183], v[72:75], v[156:159], v[180:183]\n"
    "ds_read_b64_tr_b16 v[58:59], v10 offset:4096\n"
    "v_add_u32 v11, s77, v11\n"
    "v_mfma_f32_16x16x32_bf16 v[184:187], v[76:79], v[152:155], v[184:187]\n"
    "ds_read_b64_tr_b16 v[60:61], v11 offset:0\n"
    "buffer_load_dwordx4 v[124:127], v1, s[44:47], s56 offen sc1\n"
    "v_mfma_f32_16x16x32_bf16 v[188:191], v[76:79], v[156:159], v[188:191]\n"
    "s_waitcnt lgkmcnt(7)\n"
    "ds_read_b64_tr_b16 v[62:63], v11 offset:4096\n"
    "v_add_u32 v16, s77, v16\n"
    "v_mfma_f32_16x16x32_bf16 v[192:195], v[32:35], v[152:155], v[192:195]\n"
    "s_add_u32 s56, s60, 0x3000\n"
    "ds_read_b64_tr_b16 v[64:65], v16 offset:0\n"
    "v_mfma_f32_16x16x32_bf16 v[196:199], v[32:35], v[156:159], v[196:199]\n"
    "ds_read_b64_tr_b16 v[66:67], v16 offset:4096\n"
    "v_add_u32 v17, s77, v17\n"
    "v_mfma_f32_16x16x32_bf16 v[200:203], v[36:39], v[152:155], v[200:203]\n"
    "ds_read_b64_tr_b16 v[68:69], v17 offset:0\n"
    "buffer_load_dwordx4 v[128:131], v1, s[44:47], s56 offen sc1\n"
    "v_mfma_f32_16x16x32_bf16 v[204:207], v[36:39], v[156:159], v[204:207]\n"
    "s_add_u32 s56, s60, 0x11000\n"
    "ds_read_b64_tr_b16 v[70:71], v17 offset:4096\n"
    "v_add_u32 v18, s77, v18\n"
    "v_mfma_f32_16x16x32_bf16 v[208:211], v[40:43], v[152:155], v[208:211]\n"
    "ds_read_b64_tr_b16 v[72:73], v18 offset:0\n"
    "v_mfma_f32_16x16x32_bf16 v[212:215], v[40:43], v[156:159], v[212:215]\n"
    "ds_read_b64_tr_b16 v[74:75], v18 offset:4096\n"
    "v_add_u32 v19, s77, v19\n"
    "v_mfma_f32_16x16x32_bf16 v[216:219], v[44:47], v[152:155], v[216:219]\n"
    "ds_read_b64_tr_b16 v[76:77], v19 offset:0\n"
    "buffer_load_dwordx4 v[132:135], v1, s[44:47], s56 offen sc1\n"
    "v_mfma_f32_16x16x32_bf16 v[220:223], v[44:47], v[156:159], v[220:223]\n"
    "s_waitcnt lgkmcnt(7)\n"
    "ds_read_b64_tr_b16 v[78:79], v19 offset:4096\n"
    "v_mfma_f32_16x16x32_bf16 v[160:163], v[48:51], v[96:99], v[160:163]\n"
    "s_add_u32 s56, s60, 0x3400\n"
    "ds_read_b64_tr_b16 v[32:33], v8 offset:8192\n"
    "v_mfma_f32_16x16x32_bf16 v[164:167], v[48:51], v[100:103], v[164:167]\n"
    "ds_read_b64_tr_b16 v[34:35], v8 offset:12288\n"
    "buffer_load_dwordx4 v[136:139], v1, s[44:47], s56 offen sc1\n"
    "v_mfma_f32_16x16x32_bf16 v[168:171], v[52:55], v[96:99], v[168:171]\n"
    "s_add_u32 s56, s60, 0x11400\n"
    "ds_read_b64_tr_b16 v[36:37], v9 offset:8192\n"
    "v_mfma_f32_16x16x32_bf16 v[172:175], v[52:55], v[100:103], v[172:175]\n"
    "ds_read_b64_tr_b16 v[38:39], v9 offset:12288\n"
    "v_mfma_f32_16x16x32_bf16 v[176:179], v[56:59], v[96:99], v[176:179]\n"
    "ds_read_b64_tr_b16 v[40:41], v10 offset:8192\n"
    "buffer_load_dwordx4 v[140:143], v1, s[44:47], s56 offen sc1\n"
    "v_mfma_f32_16x16x32_bf16 v[180:183], v[56:59], v[100:103], v[180:183]\n"
    "s_add_u32 s56, s69, 0x0\n"
    "ds_read_b64_tr_b16 v[42:43], v10 offset:12288\n"
    "v_mfma_f32_16x16x32_bf16 v[184:187], v[60:63], v[96:99], v[184:187]\n"
    "ds_read_b64_tr_b16 v[44:45], v11 offset:8192\n"
    "v_mfma_f32_16x16x32_bf16 v[188:191], v[60:63], v[100:103], v[188:191]\n"
    "s_waitcnt lgkmcnt(7)\n"
    "ds_read_b64_tr_b16 v[46:47], v11 offset:12288\n"
    "v_mfma_f32_16x16x32_bf16 v[192:195], v[64:67], v[96:99], v[192:195]\n"
    "ds_read_b64_tr_b16 v[48:49], v16 offset:8192\n"
    "buffer_load_dwordx4 v[144:147], v1, s[44:47], s56 offen sc1\n"
    "v_mfma_f32_16x16x32_bf16 v[196:199], v[64:67], v[100:103], v[196:199]\n"
    "s_add_u32 s56, s69, 0x20000\n"
    "ds_read_b64_tr_b16 v[50:51], v16 offset:12288\n"
    "v_mfma_f32_16x16x32_bf16 v[200:203], v[68:71], v[96:99], v[200:203]\n"
    "ds_read_b64_tr_b16 v[52:53], v17 offset:8192\n"
    "v_mfma_f32_16x16x32_bf16 v[204:207], v[68:71], v[100:103], v[204:207]\n"
    "ds_read_b64_tr_b16 v[54:55], v17 offset:12288\n"
    "buffer_load_dwordx4 v28, s[40:43], s57 offen lds\n"
    "v_mfma_f32_16x16x32_bf16 v[208:211], v[72:75], v[96:99], v[208:211]\n"
    "s_waitcnt vmcnt(22)\n"
    "ds_read_b64_tr_b16 v[56:57], v18 offset:8192\n"
    "v_mfma_f32_16x16x32_bf16 v[212:215], v[72:75], v[100:103], v[212:215]\n"
    "s_add_u32 m0, s65, 1024\n"
    "ds_read_b64_tr_b16 v[58:59], v18 offset:12288\n"
    "v_mfma_f32_16x16x32_bf16 v[216:219], v[76:79], v[96:99], v[216:219]\n"
    "ds_read_b64_tr_b16 v[60:61], v19 offset:8192\n"
    "buffer_load_dwordx4 v[148:151], v1, s[44:47], s56 offen sc1\n"
    "v_mfma_f32_16x16x32_bf16 v[220:223], v[76:79], v[100:103], v[220:223]\n"
    "s_waitcnt lgkmcnt(7)\n"
    "ds_read_b64_tr_b16 v[62:63], v19 offset:12288\n"
    "s_lshl_b32 s96, s54, 2\n"
    "s_lshr_b32 s95, s53, 3\n"
    "s_add_u32 s96, s96, s95\n"
    "s_lshl_b32 s96, s96, 7\n"
    "s_add_u32 s96, s96, 0x4000\n"
    "s_and_b32 s95, s53, 4\n"
    "s_lshl_b32 s95, s95, 10\n"
    "s_add_u32 s96, s96, s95\n"
    "buffer_load_dword v24, v21, s[44:47], s96 offen sc1\n"
    "v_mfma_f32_16x16x32_bf16 v[160:163], v[32:35], v[104:107], v[160:163]\n"
    "s_add_u32 s56, s69, 0x400\n"
    "ds_read_b64_tr_b16 v[64:65], v8 offset:16384\n"
    "v_mfma_f32_16x16x32_bf16 v[164:167], v[32:35], v[108:111], v[164:167]\n"
    "ds_read_b64_tr_b16 v[66:67], v8 offset:20480\n"
    "buffer_load_dwordx4 v29, s[40:43], s57 offen lds\n"
    "v_mfma_f32_16x16x32_bf16 v[168:171], v[36:39], v[104:107], v[168:171]\n"
    "s_add_u32 s57, s70, 0x18000\n"
    "ds_read_b64_tr_b16 v[68:69], v9 offset:16384\n"
    "v_mfma_f32_16x16x32_bf16 v[172:175], v[36:39], v[108:111], v[172:175]\n"
    "s_add_u32 m0, s65, 8192\n"
    "ds_read_b64_tr_b16 v[70:71], v9 offset:20480\n"
    "v_mfma_f32_16x16x32_bf16 v[176:179], v[40:43], v[104:107], v[176:179]\n"
    "ds_read_b64_tr_b16 v[72:73], v10 offset:16384\n"
    "buffer_load_dwordx4 v[152:155], v1, s[44:47], s56 offen sc1\n"
    "v_mfma_f32_16x16x32_bf16 v[180:183], v[40:43], v[108:111], v[180:183]\n"
    "s_add_u32 s56, s69, 0x20400\n"
    "ds_read_b64_tr_b16 v[74:75], v10 offset:20480\n"
    "v_mfma_f32_16x16x32_bf16 v[184:187], v[44:47], v[104:107], v[184:187]\n"
    "ds_read_b64_tr_b16 v[76:77], v11 offset:16384\n"
    "v_mfma_f32_16x16x32_bf16 v[188:191], v[44:47], v[108:111], v[188:191]\n"
    "s_waitcnt lgkmcnt(7)\n"
    "ds_read_b64_tr_b16 v[78:79], v11 offset:20480\n"
    "v_mfma_f32_16x16x32_bf16 v[192:195], v[48:51], v[104:107], v[192:195]\n"
    "ds_read_b64_tr_b16 v[32:33], v16 offset:16384\n"
    "buffer_load_dwordx4 v28, s[40:43], s57 offen lds\n"
    "v_mfma_f32_16x16x32_bf16 v[196:199], v[48:51], v[108:111], v[196:199]\n"
    "s_add_u32 m0, s65, 9216\n"
    "ds_read_b64_tr_b16 v[34:35], v16 offset:20480\n"
    "v_mfma_f32_16x16x32_bf16 v[200:203], v[52:55], v[104:107], v[200:203]\n"
    "ds_read_b64_tr_b16 v[36:37], v17 offset:16384\n"
    "v_mfma_f32_16x16x32_bf16 v[204:207], v[52:55], v[108:111], v[204:207]\n"
    "ds_read_b64_tr_b16 v[38:39], v17 offset:20480\n"
    "buffer_load_dwordx4 v[156:159], v1, s[44:47], s56 offen sc1\n"
    "v_mfma_f32_16x16x32_bf16 v[208:211], v[56:59], v[104:107], v[208:211]\n"
    "s_waitcnt vmcnt(16)\n"
    "ds_read_b64_tr_b16 v[40:41], v18 offset:16384\n"
    "v_mfma_f32_16x16x32_bf16 v[212:215], v[56:59], v[108:111], v[212:215]\n"
    "s_add_u32 s56, s69, 0x800\n"
    "ds_read_b64_tr_b16 v[42:43], v18 offset:20480\n"
    "v_mfma_f32_16x16x32_bf16 v[216:219], v[60:63], v[104:107], v[216:219]\n"
    "ds_read_b64_tr_b16 v[44:45], v19 offset:16384\n"
    "buffer_load_dwordx4 v[96:99], v1, s[44:47], s56 offen sc1\n"
    "v_mfma_f32_16x16x32_bf16 v[220:223], v[60:63], v[108:111], v[220:223]\n"
    "s_waitcnt lgkmcnt(7)\n"
    "ds_read_b64_tr_b16 v[46:47], v19 offset:20480\n"
    "v_mfma_f32_16x16x32_bf16 v[160:163], v[64:67], v[112:115], v[160:163]\n"
    "s_add_u32 s56, s69, 0x20800\n"
    "ds_read_b64_tr_b16 v[48:49], v8 offset:24576\n"
    "v_mfma_f32_16x16x32_bf16 v[164:167], v[64:67], v[116:119], v[164:167]\n"
    "ds_read_b64_tr_b16 v[50:51], v8 offset:28672\n"
    "buffer_load_dwordx4 v29, s[40:43], s57 offen lds\n"
    "v_mfma_f32_16x16x32_bf16 v[168:171], v[68:71], v[112:115], v[168:171]\n"
    "s_add_u32 s57, s53, 2\n"
    "ds_read_b64_tr_b16 v[52:53], v9 offset:24576\n"
    "v_mfma_f32_16x16x32_bf16 v[172:175], v[68:71], v[116:119], v[172:175]\n"
    "s_and_b32 s57, s57, 7\n"
    "ds_read_b64_tr_b16 v[54:55], v9 offset:28672\n"
    "v_mfma_f32_16x16x32_bf16 v[176:179], v[72:75], v[112:115], v[176:179]\n"
    "ds_read_b64_tr_b16 v[56:57], v10 offset:24576\n"
    "buffer_load_dwordx4 v[100:103], v1, s[44:47], s56 offen sc1\n"
    "v_mfma_f32_16x16x32_bf16 v[180:183], v[72:75], v[116:119], v[180:183]\n"
    "s_lshl_b32 s70, s57, 17\n"
    "ds_read_b64_tr_b16 v[58:59], v10 offset:28672\n"
    "v_mfma_f32_16x16x32_bf16 v[184:187], v[76:79], v[112:115], v[184:187]\n"
    "s_add_u32 s70, s70, s63\n"
    "ds_read_b64_tr_b16 v[60:61], v11 offset:24576\n"
    "v_mfma_f32_16x16x32_bf16 v[188:191], v[76:79], v[116:119], v[188:191]\n"
    "s_waitcnt lgkmcnt(7)\n"
    "ds_read_b64_tr_b16 v[62:63], v11 offset:28672\n"
    "v_mfma_f32_16x16x32_bf16 v[192:195], v[32:35], v[112:115], v[192:195]\n"
    "s_add_u32 s57, s70, 0x0\n"
    "ds_read_b64_tr_b16 v[64:65], v16 offset:24576\n"
    "v_mfma_f32_16x16x32_bf16 v[196:199], v[32:35], v[116:119], v[196:199]\n"
    "s_add_u32 m0, s65, 16384\n"
    "ds_read_b64_tr_b16 v[66:67], v16 offset:28672\n"
    "v_mfma_f32_16x16x32_bf16 v[200:203], v[36:39], v[112:115], v[200:203]\n"
    "ds_read_b64_tr_b16 v[68:69], v17 offset:24576\n"
    "buffer_load_dwordx4 v28, s[40:43], s57 offen lds\n"
    "v_mfma_f32_16x16x32_bf16 v[204:207], v[36:39], v[116:119], v[204:207]\n"
    "s_add_u32 s56, s69, 0xc00\n"
    "ds_read_b64_tr_b16 v[70:71], v17 offset:28672\n"
    "v_mfma_f32_16x16x32_bf16 v[208:211], v[40:43], v[112:115], v[208:211]\n"
    "s_waitcnt vmcnt(16)\n"
    "ds_read_b64_tr_b16 v[72:73], v18 offset:24576\n"
    "v_mfma_f32_16x16x32_bf16 v[212:215], v[40:43], v[116:119], v[212:215]\n"
    "ds_read_b64_tr_b16 v[74:75], v18 offset:28672\n"
    "buffer_load_dwordx4 v[104:107], v1, s[44:47], s56 offen sc1\n"
    "v_mfma_f32_16x16x32_bf16 v[216:219], v[44:47], v[112:115], v[216:219]\n"
    "s_add_u32 m0, s65, 17408\n"
    "ds_read_b64_tr_b16 v[76:77], v19 offset:24576\n"
    "v_mfma_f32_16x16x32_bf16 v[220:223], v[44:47], v[116:119], v[220:223]\n"
    "s_waitcnt lgkmcnt(7)\n"
    "ds_read_b64_tr_b16 v[78:79], v19 offset:28672\n"
    "s_waitcnt vmcnt(9)\n"
    "v_cmp_eq_u32 vcc, s97, v24\n"
    "s_nop 0\n"
    "s_cmp_eq_u64 vcc, -1\n"
    "s_cbranch_scc1 L_pok_17_%=\n"
    "s_lshl_b32 s96, s54, 2\n"
    "s_lshr_b32 s95, s53, 3\n"
    "s_add_u32 s96, s96, s95\n"
    "s_lshl_b32 s96, s96, 7\n"
    "s_add_u32 s96, s96, 0x4000\n"
    "s_and_b32 s95, s53, 4\n"
    "s_lshl_b32 s95, s95, 10\n"
    "s_add_u32 s96, s96, s95\n"
    "s_mov_b32 s94, 2000000\n"
    "L_psp_18_%=:\n"
    "s_sleep 2\n"
    "buffer_load_dword v24, v21, s[44:47], s96 offen sc1\n"
    "s_waitcnt vmcnt(0)\n"
    "v_cmp_eq_u32 vcc, s97, v24\n"
    "s_nop 4\n"
    "s_cmp_eq_u64 vcc, -1\n"
    "s_cbranch_scc1 L_pok_17_%=\n"
    "s_sub_u32 s94, s94, 1\n"
    "s_cmp_lg_u32 s94, 0\n"
    "s_cbranch_scc1 L_psp_18_%=\n"
    "L_pok_17_%=:\n"
    "v_mfma_f32_16x16x32_bf16 v[160:163], v[48:51], v[120:123], v[160:163]\n"
    "ds_read_b64_tr_b16 v[32:33], v8 offset:32768\n"
    "buffer_load_dwordx4 v29, s[40:43], s57 offen lds\n"
    "v_mfma_f32_16x16x32_bf16 v[164:167], v[48:51], v[124:127], v[164:167]\n"
    "s_add_u32 s56, s69, 0x20c00\n"
    "ds_read_b64_tr_b16 v[34:35], v8 offset:36864\n"
    "v_mfma_f32_16x16x32_bf16 v[168:171], v[52:55], v[120:123], v[168:171]\n"
    "s_add_u32 s57, s70, 0x8000\n"
    "ds_read_b64_tr_b16 v[36:37], v9 offset:32768\n"
    "v_mfma_f32_16x16x32_bf16 v[172:175], v[52:55], v[124:127], v[172:175]\n"
    "ds_read_b64_tr_b16 v[38:39], v9 offset:36864\n"
    "buffer_load_dwordx4 v[108:111], v1, s[44:47], s56 offen sc1\n"
    "v_mfma_f32_16x16x32_bf16 v[176:179], v[56:59], v[120:123], v[176:179]\n"
    "s_add_u32 s56, s53, 1\n"
    "ds_read_b64_tr_b16 v[40:41], v10 offset:32768\n"
    "v_mfma_f32_16x16x32_bf16 v[180:183], v[56:59], v[124:127], v[180:183]\n"
    "s_add_u32 m0, s65, 24576\n"
    "ds_read_b64_tr_b16 v[42:43], v10 offset:36864\n"
    "v_mfma_f32_16x16x32_bf16 v[184:187], v[60:63], v[120:123], v[184:187]\n"
    "ds_read_b64_tr_b16 v[44:45], v11 offset:32768\n"
    "buffer_load_dwordx4 v28, s[40:43], s57 offen lds\n"
    "v_mfma_f32_16x16x32_bf16 v[188:191], v[60:63], v[124:127], v[188:191]\n"
    "s_waitcnt lgkmcnt(7)\n"
    "ds_read_b64_tr_b16 v[46:47], v11 offset:36864\n"
    "v_mfma_f32_16x16x32_bf16 v[192:195], v[64:67], v[120:123], v[192:195]\n"
    "s_and_b32 s56, s56, 7\n"
    "ds_read_b64_tr_b16 v[48:49], v16 offset:32768\n"
    "v_mfma_f32_16x16x32_bf16 v[196:199], v[64:67], v[124:127], v[196:199]\n"
    "s_lshl_b32 s69, s56, 12\n"
    "ds_read_b64_tr_b16 v[50:51], v16 offset:36864\n"
    "v_mfma_f32_16x16x32_bf16 v[200:203], v[68:71], v[120:123], v[200:203]\n"
    "s_add_u32 s69, s69, s61\n"
    "ds_read_b64_tr_b16 v[52:53], v17 offset:32768\n"
    "v_mfma_f32_16x16x32_bf16 v[204:207], v[68:71], v[124:127], v[204:207]\n"
    "s_add_u32 s56, s69, 0x0\n"
    "ds_read_b64_tr_b16 v[54:55], v17 offset:36864\n"
    "v_mfma_f32_16x16x32_bf16 v[208:211], v[72:75], v[120:123], v[208:211]\n"
    "ds_read_b64_tr_b16 v[56:57], v18 offset:32768\n"
    "buffer_load_dwordx4 v[112:115], v1, s[44:47], s56 offen sc1\n"
    "v_mfma_f32_16x16x32_bf16 v[212:215], v[72:75], v[124:127], v[212:215]\n"
    "s_add_u32 m0, s65, 25600\n"
    "ds_read_b64_tr_b16 v[58:59], v18 offset:36864\n"
    "v_mfma_f32_16x16x32_bf16 v[216:219], v[76:79], v[120:123], v[216:219]\n"
    "s_add_u32 s56, s69, 0x20000\n"
    "ds_read_b64_tr_b16 v[60:61], v19 offset:32768\n"
    "v_mfma_f32_16x16x32_bf16 v[220:223], v[76:79], v[124:127], v[220:223]\n"
    "s_waitcnt lgkmcnt(7)\n"
    "ds_read_b64_tr_b16 v[62:63], v19 offset:36864\n"
    "buffer_load_dwordx4 v[116:119], v1, s[44:47], s56 offen sc1\n"
    "v_mfma_f32_16x16x32_bf16 v[160:163], v[32:35], v[128:131], v[160:163]\n"
    "s_add_u32 s56, s69, 0x400\n"
    "ds_read_b64_tr_b16 v[64:65], v8 offset:40960\n"
    "v_mfma_f32_16x16x32_bf16 v[164:167], v[32:35], v[132:135], v[164:167]\n"
    "ds_read_b64_tr_b16 v[66:67], v8 offset:45056\n"
    "buffer_load_dwordx4 v29, s[40:43], s57 offen lds\n"
    "v_mfma_f32_16x16x32_bf16 v[168:171], v[36:39], v[128:131], v[168:171]\n"
    "s_add_u32 s57, s70, 0x10000\n"
    "s_add_u32 m0, s65, 32768\n"
    "ds_read_b64_tr_b16 v[68:69], v9 offset:40960\n"
    "v_mfma_f32_16x16x32_bf16 v[172:175], v[36:39], v[132:135], v[172:175]\n"
    "ds_read_b64_tr_b16 v[70:71], v9 offset:45056\n"
    "v_mfma_f32_16x16x32_bf16 v[176:179], v[40:43], v[128:131], v[176:179]\n"
    "ds_read_b64_tr_b16 v[72:73], v10 offset:40960\n"
    "buffer_load_dwordx4 v28, s[40:43], s57 offen lds\n"
    "v_mfma_f32_16x16x32_bf16 v[180:183], v[40:43], v[132:135], v[180:183]\n"
    "s_add_u32 m0, s65, 33792\n"
    "ds_read_b64_tr_b16 v[74:75], v10 offset:45056\n"
    "v_mfma_f32_16x16x32_bf16 v[184:187], v[44:47], v[128:131], v[184:187]\n"
    "ds_read_b64_tr_b16 v[76:77], v11 offset:40960\n"
    "v_mfma_f32_16x16x32_bf16 v[188:191], v[44:47], v[132:135], v[188:191]\n"
    "s_waitcnt lgkmcnt(7)\n"
    "ds_read_b64_tr_b16 v[78:79], v11 offset:45056\n"
    "v_mfma_f32_16x16x32_bf16 v[192:195], v[48:51], v[128:131], v[192:195]\n"
    "ds_read_b64_tr_b16 v[32:33], v16 offset:40960\n"
    "buffer_load_dwordx4 v29, s[40:43], s57 offen lds\n"
    "v_mfma_f32_16x16x32_bf16 v[196:199], v[48:51], v[132:135], v[196:199]\n"
    "s_add_u32 s57, s70, 0x18000\n"
    "s_add_u32 m0, s65, 40960\n"
    "ds_read_b64_tr_b16 v[34:35], v16 offset:45056\n"
    "v_mfma_f32_16x16x32_bf16 v[200:203], v[52:55], v[128:131], v[200:203]\n"
    "ds_read_b64_tr_b16 v[36:37], v17 offset:40960\n"
    "v_mfma_f32_16x16x32_bf16 v[204:207], v[52:55], v[132:135], v[204:207]\n"
    "ds_read_b64_tr_b16 v[38:39], v17 offset:45056\n"
    "buffer_load_dwordx4 v28, s[40:43], s57 offen lds\n"
    "v_mfma_f32_16x16x32_bf16 v[208:211], v[56:59], v[128:131], v[208:211]\n"
    "s_add_u32 m0, s65, 41984\n"
    "ds_read_b64_tr_b16 v[40:41], v18 offset:40960\n"
    "buffer_load_dwordx4 v[120:123], v1, s[44:47], s56 offen sc1\n"
    "v_mfma_f32_16x16x32_bf16 v[212:215], v[56:59], v[132:135], v[212:215]\n"
    "s_add_u32 s56, s69, 0x20400\n"
    "ds_read_b64_tr_b16 v[42:43], v18 offset:45056\n"
    "v_mfma_f32_16x16x32_bf16 v[216:219], v[60:63], v[128:131], v[216:219]\n"
    "ds_read_b64_tr_b16 v[44:45], v19 offset:40960\n"
    "v_mfma_f32_16x16x32_bf16 v[220:223], v[60:63], v[132:135], v[220:223]\n"
    "s_waitcnt lgkmcnt(7)\n"
    "ds_read_b64_tr_b16 v[46:47], v19 offset:45056\n"
    "buffer_load_dwordx4 v29, s[40:43], s57 offen lds\n"
    "buffer_load_dwordx4 v[124:127], v1, s[44:47], s56 offen sc1\n"
    "s_barrier\n"
    "v_add_u32 v8, s78, v8\n"
    "v_mfma_f32_16x16x32_bf16 v[160:163], v[64:67], v[136:139], v[160:163]\n"
    "s_add_u32 s56, s69, 0x800\n"
    "ds_read_b64_tr_b16 v[48:49], v8 offset:0\n"
    "v_mfma_f32_16x16x32_bf16 v[164:167], v[64:67], v[140:143], v[164:167]\n"
    "s_add_u32 s57, s53, 3\n"
    "ds_read_b64_tr_b16 v[50:51], v8 offset:4096\n"
    "v_add_u32 v9, s78, v9\n"
    "v_mfma_f32_16x16x32_bf16 v[168:171], v[68:71], v[136:139], v[168:171]\n"
    "ds_read_b64_tr_b16 v[52:53], v9 offset:0\n"
    "buffer_load_dwordx4 v[128:131], v1, s[44:47], s56 offen sc1\n"
    "v_mfma_f32_16x16x32_bf16 v[172:175], v[68:71], v[140:143], v[172:175]\n"
    "s_add_u32 s56, s69, 0x20800\n"
    "ds_read_b64_tr_b16 v[54:55], v9 offset:4096\n"
    "v_add_u32 v10, s78, v10\n"
    "v_mfma_f32_16x16x32_bf16 v[176:179], v[72:75], v[136:139], v[176:179]\n"
    "s_and_b32 s57, s57, 7\n"
    "ds_read_b64_tr_b16 v[56:57], v10 offset:0\n"
    "v_mfma_f32_16x16x32_bf16 v[180:183], v[72:75], v[140:143], v[180:183]\n"
    "s_lshl_b32 s70, s57, 17\n"
    "ds_read_b64_tr_b16 v[58:59], v10 offset:4096\n"
    "v_add_u32 v11, s78, v11\n"
    "v_mfma_f32_16x16x32_bf16 v[184:187], v[76:79], v[136:139], v[184:187]\n"
    "ds_read_b64_tr_b16 v[60:61], v11 offset:0\n"
    "buffer_load_dwordx4 v[132:135], v1, s[44:47], s56 offen sc1\n"
    "v_mfma_f32_16x16x32_bf16 v[188:191], v[76:79], v[140:143], v[188:191]\n"
    "s_waitcnt lgkmcnt(7)\n"
    "ds_read_b64_tr_b16 v[62:63], v11 offset:4096\n"
    "v_add_u32 v16, s78, v16\n"
    "v_mfma_f32_16x16x32_bf16 v[192:195], v[32:35], v[136:139], v[192:195]\n"
    "s_add_u32 s70, s70, s63\n"
    "ds_read_b64_tr_b16 v[64:65], v16 offset:0\n"
    "v_mfma_f32_16x16x32_bf16 v[196:199], v[32:35], v[140:143], v[196:199]\n"
    "s_add_u32 s57, s70, 0x0\n"
    "ds_read_b64_tr_b16 v[66:67], v16 offset:4096\n"
    "v_add_u32 v17, s78, v17\n"
    "v_mfma_f32_16x16x32_bf16 v[200:203], v[36:39], v[136:139], v[200:203]\n"
    "s_add_u32 m0, s66, 0\n"
    "ds_read_b64_tr_b16 v[68:69], v17 offset:0\n"
    "v_mfma_f32_16x16x32_bf16 v[204:207], v[36:39], v[140:143], v[204:207]\n"
    "ds_read_b64_tr_b16 v[70:71], v17 offset:4096\n"
    "v_add_u32 v18, s78, v18\n"
    "v_mfma_f32_16x16x32_bf16 v[208:211], v[40:43], v[136:139], v[208:211]\n"
    "ds_read_b64_tr_b16 v[72:73], v18 offset:0\n"
    "v_mfma_f32_16x16x32_bf16 v[212:215], v[40:43], v[140:143], v[212:215]\n"
    "ds_read_b64_tr_b16 v[74:75], v18 offset:4096\n"
    "v_add_u32 v19, s78, v19\n"
    "v_mfma_f32_16x16x32_bf16 v[216:219], v[44:47], v[136:139], v[216:219]\n"
    "ds_read_b64_tr_b16 v[76:77], v19 offset:0\n"
    "buffer_load_dwordx4 v28, s[40:43], s57 offen lds\n"
    "v_mfma_f32_16x16x32_bf16 v[220:223], v[44:47], v[140:143], v[220:223]\n"
    "s_waitcnt lgkmcnt(7)\n"
    "ds_read_b64_tr_b16 v[78:79], v19 offset:4096\n"
    "v_mfma_f32_16x16x32_bf16 v[160:163], v[48:51], v[144:147], v[160:163]\n"
    "s_add_u32 s56, s69, 0xc00\n"
    "ds_read_b64_tr_b16 v[32:33], v8 offset:8192\n"
    "v_mfma_f32_16x16x32_bf16 v[164:167], v[48:51], v[148:151], v[164:167]\n"
    "ds_read_b64_tr_b16 v[34:35], v8 offset:12288\n"
    "buffer_load_dwordx4 v[136:139], v1, s[44:47], s56 offen sc1\n"
    "v_mfma_f32_16x16x32_bf16 v[168:171], v[52:55], v[144:147], v[168:171]\n"
    "s_add_u32 s56, s69, 0x20c00\n"
    "ds_read_b64_tr_b16 v[36:37], v9 offset:8192\n"
    "v_mfma_f32_16x16x32_bf16 v[172:175], v[52:55], v[148:151], v[172:175]\n"
    "s_add_u32 m0, s66, 1024\n"
    "ds_read_b64_tr_b16 v[38:39], v9 offset:12288\n"
    "v_mfma_f32_16x16x32_bf16 v[176:179], v[56:59], v[144:147], v[176:179]\n"
    "ds_read_b64_tr_b16 v[40:41], v10 offset:8192\n"
    "buffer_load_dwordx4 v[140:143], v1, s[44:47], s56 offen sc1\n"
    "v_mfma_f32_16x16x32_bf16 v[180:183], v[56:59], v[148:151], v[180:183]\n"
    "ds_read_b64_tr_b16 v[42:43], v10 offset:12288\n"
    "v_mfma_f32_16x16x32_bf16 v[184:187], v[60:63], v[144:147], v[184:187]\n"
    "ds_read_b64_tr_b16 v[44:45], v11 offset:8192\n"
    "v_mfma_f32_16x16x32_bf16 v[188:191], v[60:63], v[148:151], v[188:191]\n"
    "s_waitcnt lgkmcnt(7)\n"
    "ds_read_b64_tr_b16 v[46:47], v11 offset:12288\n"
    "v_mfma_f32_16x16x32_bf16 v[192:195], v[64:67], v[144:147], v[192:195]\n"
    "ds_read_b64_tr_b16 v[48:49], v16 offset:8192\n"
    "buffer_load_dwordx4 v29, s[40:43], s57 offen lds\n"
    "v_mfma_f32_16x16x32_bf16 v[196:199], v[64:67], v[148:151], v[196:199]\n"
    "s_add_u32 s57, s70, 0x8000\n"
    "ds_read_b64_tr_b16 v[50:51], v16 offset:12288\n"
    "v_mfma_f32_16x16x32_bf16 v[200:203], v[68:71], v[144:147], v[200:203]\n"
    "s_add_u32 m0, s66, 8192\n"
    "ds_read_b64_tr_b16 v[52:53], v17 offset:8192\n"
    "v_mfma_f32_16x16x32_bf16 v[204:207], v[68:71], v[148:151], v[204:207]\n"
    "ds_read_b64_tr_b16 v[54:55], v17 offset:12288\n"
    "buffer_load_dwordx4 v28, s[40:43], s57 offen lds\n"
    "v_mfma_f32_16x16x32_bf16 v[208:211], v[72:75], v[144:147], v[208:211]\n"
    "s_waitcnt vmcnt(24)\n"
    "ds_read_b64_tr_b16 v[56:57], v18 offset:8192\n"
    "v_mfma_f32_16x16x32_bf16 v[212:215], v[72:75], v[148:151], v[212:215]\n"
    "s_add_u32 m0, s66, 9216\n"
    "ds_read_b64_tr_b16 v[58:59], v18 offset:12288\n"
    "v_mfma_f32_16x16x32_bf16 v[216:219], v[76:79], v[144:147], v[216:219]\n"
    "ds_read_b64_tr_b16 v[60:61], v19 offset:8192\n"
    "buffer_load_dwordx4 v29, s[40:43], s57 offen lds\n"
    "v_mfma_f32_16x16x32_bf16 v[220:223], v[76:79], v[148:151], v[220:223]\n"
    "s_waitcnt lgkmcnt(7)\n"
    "ds_read_b64_tr_b16 v[62:63], v19 offset:12288\n"
    "v_mfma_f32_16x16x32_bf16 v[160:163], v[32:35], v[152:155], v[160:163]\n"
    "s_add_u32 s56, s53, 2\n"
    "ds_read_b64_tr_b16 v[64:65], v8 offset:16384\n"
    "v_mfma_f32_16x16x32_bf16 v[164:167], v[32:35], v[156:159], v[164:167]\n"
    "s_and_b32 s56, s56, 7\n"
    "ds_read_b64_tr_b16 v[66:67], v8 offset:20480\n"
    "v_mfma_f32_16x16x32_bf16 v[168:171], v[36:39], v[152:155], v[168:171]\n"
    "s_lshl_b32 s69, s56, 12\n"
    "ds_read_b64_tr_b16 v[68:69], v9 offset:16384\n"
    "v_mfma_f32_16x16x32_bf16 v[172:175], v[36:39], v[156:159], v[172:175]\n"
    "s_add_u32 s69, s69, s61\n"
    "ds_read_b64_tr_b16 v[70:71], v9 offset:20480\n"
    "v_mfma_f32_16x16x32_bf16 v[176:179], v[40:43], v[152:155], v[176:179]\n"
    "s_add_u32 s56, s69, 0x0\n"
    "ds_read_b64_tr_b16 v[72:73], v10 offset:16384\n"
    "v_mfma_f32_16x16x32_bf16 v[180:183], v[40:43], v[156:159], v[180:183]\n"
    "ds_read_b64_tr_b16 v[74:75], v10 offset:20480\n"
    "buffer_load_dwordx4 v[144:147], v1, s[44:47], s56 offen sc1\n"
    "v_mfma_f32_16x16x32_bf16 v[184:187], v[44:47], v[152:155], v[184:187]\n"
    "s_add_u32 s56, s69, 0x20000\n"
    "ds_read_b64_tr_b16 v[76:77], v11 offset:16384\n"
    "v_mfma_f32_16x16x32_bf16 v[188:191], v[44:47], v[156:159], v[188:191]\n"
    "s_waitcnt lgkmcnt(7)\n"
    "ds_read_b64_tr_b16 v[78:79], v11 offset:20480\n"
    "v_mfma_f32_16x16x32_bf16 v[192:195], v[48:51], v[152:155], v[192:195]\n"
    "ds_read_b64_tr_b16 v[32:33], v16 offset:16384\n"
    "buffer_load_dwordx4 v[148:151], v1, s[44:47], s56 offen sc1\n"
    "v_mfma_f32_16x16x32_bf16 v[196:199], v[48:51], v[156:159], v[196:199]\n"
    "s_add_u32 s57, s70, 0x10000\n"
    "ds_read_b64_tr_b16 v[34:35], v16 offset:20480\n"
    "v_mfma_f32_16x16x32_bf16 v[200:203], v[52:55], v[152:155], v[200:203]\n"
    "s_add_u32 m0, s66, 16384\n"
    "ds_read_b64_tr_b16 v[36:37], v17 offset:16384\n"
    "v_mfma_f32_16x16x32_bf16 v[204:207], v[52:55], v[156:159], v[204:207]\n"
    "ds_read_b64_tr_b16 v[38:39], v17 offset:20480\n"
    "buffer_load_dwordx4 v28, s[40:43], s57 offen lds\n"
    "v_mfma_f32_16x16x32_bf16 v[208:211], v[56:59], v[152:155], v[208:211]\n"
    "s_waitcnt vmcnt(25)\n"
    "ds_read_b64_tr_b16 v[40:41], v18 offset:16384\n"
    "v_mfma_f32_16x16x32_bf16 v[212:215], v[56:59], v[156:159], v[212:215]\n"
    "s_add_u32 m0, s66, 17408\n"
    "ds_read_b64_tr_b16 v[42:43], v18 offset:20480\n"
    "v_mfma_f32_16x16x32_bf16 v[216:219], v[60:63], v[152:155], v[216:219]\n"
    "ds_read_b64_tr_b16 v[44:45], v19 offset:16384\n"
    "buffer_load_dwordx4 v29, s[40:43], s57 offen lds\n"
    "v_mfma_f32_16x16x32_bf16 v[220:223], v[60:63], v[156:159], v[220:223]\n"
    "s_waitcnt lgkmcnt(7)\n"
    "ds_read_b64_tr_b16 v[46:47], v19 offset:20480\n"
    "v_mfma_f32_16x16x32_bf16 v[160:163], v[64:67], v[96:99], v[160:163]\n"
    "s_add_u32 s56, s69, 0x400\n"
    "ds_read_b64_tr_b16 v[48:49], v8 offset:24576\n"
    "v_mfma_f32_16x16x32_bf16 v[164:167], v[64:67], v[100:103], v[164:167]\n"
    "ds_read_b64_tr_b16 v[50:51], v8 offset:28672\n"
    "buffer_load_dwordx4 v[152:155], v1, s[44:47], s56 offen sc1\n"
    "v_mfma_f32_16x16x32_bf16 v[168:171], v[68:71], v[96:99], v[168:171]\n"
    "s_add_u32 s56, s69, 0x20400\n"
    "ds_read_b64_tr_b16 v[52:53], v9 offset:24576\n"
    "v_mfma_f32_16x16x32_bf16 v[172:175], v[68:71], v[100:103], v[172:175]\n"
    "s_add_u32 s57, s70, 0x18000\n"
    "ds_read_b64_tr_b16 v[54:55], v9 offset:28672\n"
    "v_mfma_f32_16x16x32_bf16 v[176:179], v[72:75], v[96:99], v[176:179]\n"
    "ds_read_b64_tr_b16 v[56:57], v10 offset:24576\n"
    "buffer_load_dwordx4 v[156:159], v1, s[44:47], s56 offen sc1\n"
    "v_mfma_f32_16x16x32_bf16 v[180:183], v[72:75], v[100:103], v[180:183]\n"
    "s_add_u32 m0, s66, 24576\n"
    "ds_read_b64_tr_b16 v[58:59], v10 offset:28672\n"
    "v_mfma_f32_16x16x32_bf16 v[184:187], v[76:79], v[96:99], v[184:187]\n"
    "ds_read_b64_tr_b16 v[60:61], v11 offset:24576\n"
    "v_mfma_f32_16x16x32_bf16 v[188:191], v[76:79], v[100:103], v[188:191]\n"
    "s_waitcnt lgkmcnt(7)\n"
    "ds_read_b64_tr_b16 v[62:63], v11 offset:28672\n"
    "v_mfma_f32_16x16x32_bf16 v[192:195], v[32:35], v[96:99], v[192:195]\n"
    "ds_read_b64_tr_b16 v[64:65], v16 offset:24576\n"
    "buffer_load_dwordx4 v28, s[40:43], s57 offen lds\n"
    "v_mfma_f32_16x16x32_bf16 v[196:199], v[32:35], v[100:103], v[196:199]\n"
    "s_add_u32 m0, s66, 25600\n"
    "ds_read_b64_tr_b16 v[66:67], v16 offset:28672\n"
    "v_mfma_f32_16x16x32_bf16 v[200:203], v[36:39], v[96:99], v[200:203]\n"
    "ds_read_b64_tr_b16 v[68:69], v17 offset:24576\n"
    "v_mfma_f32_16x16x32_bf16 v[204:207], v[36:39], v[100:103], v[204:207]\n"
    "ds_read_b64_tr_b16 v[70:71], v17 offset:28672\n"
    "buffer_load_dwordx4 v29, s[40:43], s57 offen lds\n"
    "v_mfma_f32_16x16x32_bf16 v[208:211], v[40:43], v[96:99], v[208:211]\n"
    "s_waitcnt vmcnt(26)\n"
    "ds_read_b64_tr_b16 v[72:73], v18 offset:24576\n"
    "v_mfma_f32_16x16x32_bf16 v[212:215], v[40:43], v[100:103], v[212:215]\n"
    "ds_read_b64_tr_b16 v[74:75], v18 offset:28672\n"
    "v_mfma_f32_16x16x32_bf16 v[216:219], v[44:47], v[96:99], v[216:219]\n"
    "ds_read_b64_tr_b16 v[76:77], v19 offset:24576\n"
    "v_mfma_f32_16x16x32_bf16 v[220:223], v[44:47], v[100:103], v[220:223]\n"
    "s_waitcnt lgkmcnt(7)\n"
    "ds_read_b64_tr_b16 v[78:79], v19 offset:28672\n"
    "v_mfma_f32_16x16x32_bf16 v[160:163], v[48:51], v[104:107], v[160:163]\n"
    "s_add_u32 s56, s69, 0x800\n"
    "ds_read_b64_tr_b16 v[32:33], v8 offset:32768\n"
    "v_mfma_f32_16x16x32_bf16 v[164:167], v[48:51], v[108:111], v[164:167]\n"
    "ds_read_b64_tr_b16 v[34:35], v8 offset:36864\n"
    "buffer_load_dwordx4 v[96:99], v1, s[44:47], s56 offen sc1\n"
    "v_mfma_f32_16x16x32_bf16 v[168:171], v[52:55], v[104:107], v[168:171]\n"
    "s_add_u32 s56, s69, 0x20800\n"
    "ds_read_b64_tr_b16 v[36:37], v9 offset:32768\n"
    "v_mfma_f32_16x16x32_bf16 v[172:175], v[52:55], v[108:111], v[172:175]\n"
    "s_add_u32 s57, s53, 4\n"
    "ds_read_b64_tr_b16 v[38:39], v9 offset:36864\n"
    "v_mfma_f32_16x16x32_bf16 v[176:179], v[56:59], v[104:107], v[176:179]\n"
    "ds_read_b64_tr_b16 v[40:41], v10 offset:32768\n"
    "buffer_load_dwordx4 v[100:103], v1, s[44:47], s56 offen sc1\n"
    "v_mfma_f32_16x16x32_bf16 v[180:183], v[56:59], v[108:111], v[180:183]\n"
    "s_and_b32 s57, s57, 7\n"
    "ds_read_b64_tr_b16 v[42:43], v10 offset:36864\n"
    "v_mfma_f32_16x16x32_bf16 v[184:187], v[60:63], v[104:107], v[184:187]\n"
    "s_lshl_b32 s70, s57, 17\n"
    "ds_read_b64_tr_b16 v[44:45], v11 offset:32768\n"
    "v_mfma_f32_16x16x32_bf16 v[188:191], v[60:63], v[108:111], v[188:191]\n"
    "s_waitcnt lgkmcnt(7)\n"
    "ds_read_b64_tr_b16 v[46:47], v11 offset:36864\n"
    "v_mfma_f32_16x16x32_bf16 v[192:195], v[64:67], v[104:107], v[192:195]\n"
    "s_add_u32 s70, s70, s63\n"
    "ds_read_b64_tr_b16 v[48:49], v16 offset:32768\n"
    "v_mfma_f32_16x16x32_bf16 v[196:199], v[64:67], v[108:111], v[196:199]\n"
    "s_add_u32 s57, s70, 0x0\n"
    "ds_read_b64_tr_b16 v[50:51], v16 offset:36864\n"
    "v_mfma_f32_16x16x32_bf16 v[200:203], v[68:71], v[104:107], v[200:203]\n"
    "s_add_u32 m0, s66, 32768\n"
    "ds_read_b64_tr_b16 v[52:53], v17 offset:32768\n"
    "v_mfma_f32_16x16x32_bf16 v[204:207], v[68:71], v[108:111], v[204:207]\n"
    "ds_read_b64_tr_b16 v[54:55], v17 offset:36864\n"
    "buffer_load_dwordx4 v28, s[40:43], s57 offen lds\n"
    "v_mfma_f32_16x16x32_bf16 v[208:211], v[72:75], v[104:107], v[208:211]\n"
    "s_waitcnt vmcnt(26)\n"
    "ds_read_b64_tr_b16 v[56:57], v18 offset:32768\n"
    "v_mfma_f32_16x16x32_bf16 v[212:215], v[72:75], v[108:111], v[212:215]\n"
    "s_add_u32 m0, s66, 33792\n"
    "ds_read_b64_tr_b16 v[58:59], v18 offset:36864\n"
    "v_mfma_f32_16x16x32_bf16 v[216:219], v[76:79], v[104:107], v[216:219]\n"
    "ds_read_b64_tr_b16 v[60:61], v19 offset:32768\n"
    "buffer_load_dwordx4 v29, s[40:43], s57 offen lds\n"
    "v_mfma_f32_16x16x32_bf16 v[220:223], v[76:79], v[108:111], v[220:223]\n"
    "s_waitcnt lgkmcnt(7)\n"
    "ds_read_b64_tr_b16 v[62:63], v19 offset:36864\n"
    "v_mfma_f32_16x16x32_bf16 v[160:163], v[32:35], v[112:115], v[160:163]\n"
    "s_add_u32 s56, s69, 0xc00\n"
    "ds_read_b64_tr_b16 v[64:65], v8 offset:40960\n"
    "v_mfma_f32_16x16x32_bf16 v[164:167], v[32:35], v[116:119], v[164:167]\n"
    "ds_read_b64_tr_b16 v[66:67], v8 offset:45056\n"
    "buffer_load_dwordx4 v[104:107], v1, s[44:47], s56 offen sc1\n"
    "v_mfma_f32_16x16x32_bf16 v[168:171], v[36:39], v[112:115], v[168:171]\n"
    "s_add_u32 s57, s70, 0x8000\n"
    "ds_read_b64_tr_b16 v[68:69], v9 offset:40960\n"
    "v_mfma_f32_16x16x32_bf16 v[172:175], v[36:39], v[116:119], v[172:175]\n"
    "s_add_u32 m0, s66, 40960\n"
    "ds_read_b64_tr_b16 v[70:71], v9 offset:45056\n"
    "v_mfma_f32_16x16x32_bf16 v[176:179], v[40:43], v[112:115], v[176:179]\n"
    "ds_read_b64_tr_b16 v[72:73], v10 offset:40960\n"
    "buffer_load_dwordx4 v28, s[40:43], s57 offen lds\n"
    "v_mfma_f32_16x16x32_bf16 v[180:183], v[40:43], v[116:119], v[180:183]\n"
    "s_add_u32 s56, s69, 0x20c00\n"
    "ds_read_b64_tr_b16 v[74:75], v10 offset:45056\n"
    "v_mfma_f32_16x16x32_bf16 v[184:187], v[44:47], v[112:115], v[184:187]\n"
    "ds_read_b64_tr_b16 v[76:77], v11 offset:40960\n"
    "v_mfma_f32_16x16x32_bf16 v[188:191], v[44:47], v[116:119], v[188:191]\n"
    "s_waitcnt lgkmcnt(7)\n"
    "ds_read_b64_tr_b16 v[78:79], v11 offset:45056\n"
    "v_mfma_f32_16x16x32_bf16 v[192:195], v[48:51], v[112:115], v[192:195]\n"
    "ds_read_b64_tr_b16 v[32:33], v16 offset:40960\n"
    "buffer_load_dwordx4 v[108:111], v1, s[44:47], s56 offen sc1\n"
    "v_mfma_f32_16x16x32_bf16 v[196:199], v[48:51], v[116:119], v[196:199]\n"
    "s_add_u32 m0, s66, 41984\n"
    "ds_read_b64_tr_b16 v[34:35], v16 offset:45056\n"
    "v_mfma_f32_16x16x32_bf16 v[200:203], v[52:55], v[112:115], v[200:203]\n"
    "ds_read_b64_tr_b16 v[36:37], v17 offset:40960\n"
    "v_mfma_f32_16x16x32_bf16 v[204:207], v[52:55], v[116:119], v[204:207]\n"
    "ds_read_b64_tr_b16 v[38:39], v17 offset:45056\n"
    "buffer_load_dwordx4 v29, s[40:43], s57 offen lds\n"
    "v_mfma_f32_16x16x32_bf16 v[208:211], v[56:59], v[112:115], v[208:211]\n"
    "s_waitcnt vmcnt(24)\n"
    "ds_read_b64_tr_b16 v[40:41], v18 offset:40960\n"
    "v_mfma_f32_16x16x32_bf16 v[212:215], v[56:59], v[116:119], v[212:215]\n"
    "ds_read_b64_tr_b16 v[42:43], v18 offset:45056\n"
    "v_mfma_f32_16x16x32_bf16 v[216:219], v[60:63], v[112:115], v[216:219]\n"
    "ds_read_b64_tr_b16 v[44:45], v19 offset:40960\n"
    "v_mfma_f32_16x16x32_bf16 v[220:223], v[60:63], v[116:119], v[220:223]\n"
    "s_waitcnt lgkmcnt(7)\n"
    "ds_read_b64_tr_b16 v[46:47], v19 offset:45056\n"
    "s_barrier\n"
    "v_add_u32 v8, s76, v8\n"
    "v_mfma_f32_16x16x32_bf16 v[160:163], v[64:67], v[120:123], v[160:163]\n"
    "s_add_u32 s56, s53, 3\n"
    "ds_read_b64_tr_b16 v[48:49], v8 offset:0\n"
    "v_mfma_f32_16x16x32_bf16 v[164:167], v[64:67], v[124:127], v[164:167]\n"
    "s_and_b32 s56, s56, 7\n"
    "ds_read_b64_tr_b16 v[50:51], v8 offset:4096\n"
    "v_add_u32 v9, s76, v9\n"
    "v_mfma_f32_16x16x32_bf16 v[168:171], v[68:71], v[120:123], v[168:171]\n"
    "s_lshl_b32 s69, s56, 12\n"
    "ds_read_b64_tr_b16 v[52:53], v9 offset:0\n"
    "v_mfma_f32_16x16x32_bf16 v[172:175], v[68:71], v[124:127], v[172:175]\n"
    "s_add_u32 s69, s69, s61\n"
    "ds_read_b64_tr_b16 v[54:55], v9 offset:4096\n"
    "v_add_u32 v10, s76, v10\n"
    "v_mfma_f32_16x16x32_bf16 v[176:179], v[72:75], v[120:123], v[176:179]\n"
    "s_add_u32 s56, s69, 0x0\n"
    "ds_read_b64_tr_b16 v[56:57], v10 offset:0\n"
    "v_mfma_f32_16x16x32_bf16 v[180:183], v[72:75], v[124:127], v[180:183]\n"
    "s_add_u32 s57, s70, 0x10000\n"
    "ds_read_b64_tr_b16 v[58:59], v10 offset:4096\n"
    "v_add_u32 v11, s76, v11\n"
    "v_mfma_f32_16x16x32_bf16 v[184:187], v[76:79], v[120:123], v[184:187]\n"
    "ds_read_b64_tr_b16 v[60:61], v11 offset:0\n"
    "buffer_load_dwordx4 v[112:115], v1, s[44:47], s56 offen sc1\n"
    "v_mfma_f32_16x16x32_bf16 v[188:191], v[76:79], v[124:127], v[188:191]\n"
    "s_waitcnt lgkmcnt(7)\n"
    "ds_read_b64_tr_b16 v[62:63], v11 offset:4096\n"
    "v_add_u32 v16, s76, v16\n"
    "v_mfma_f32_16x16x32_bf16 v[192:195], v[32:35], v[120:123], v[192:195]\n"
    "s_add_u32 s56, s69, 0x20000\n"
    "ds_read_b64_tr_b16 v[64:65], v16 offset:0\n"
    "v_mfma_f32_16x16x32_bf16 v[196:199], v[32:35], v[124:127], v[196:199]\n"
    "s_add_u32 m0, s64, 0\n"
    "ds_read_b64_tr_b16 v[66:67], v16 offset:4096\n"
    "v_add_u32 v17, s76, v17\n"
    "v_mfma_f32_16x16x32_bf16 v[200:203], v[36:39], v[120:123], v[200:203]\n"
    "ds_read_b64_tr_b16 v[68:69], v17 offset:0\n"
    "buffer_load_dwordx4 v[116:119], v1, s[44:47], s56 offen sc1\n"
    "v_mfma_f32_16x16x32_bf16 v[204:207], v[36:39], v[124:127], v[204:207]\n"
    "ds_read_b64_tr_b16 v[70:71], v17 offset:4096\n"
    "v_add_u32 v18, s76, v18\n"
    "v_mfma_f32_16x16x32_bf16 v[208:211], v[40:43], v[120:123], v[208:211]\n"
    "s_waitcnt vmcnt(24)\n"
    "ds_read_b64_tr_b16 v[72:73], v18 offset:0\n"
    "v_mfma_f32_16x16x32_bf16 v[212:215], v[40:43], v[124:127], v[212:215]\n"
    "ds_read_b64_tr_b16 v[74:75], v18 offset:4096\n"
    "v_add_u32 v19, s76, v19\n"
    "v_mfma_f32_16x16x32_bf16 v[216:219], v[44:47], v[120:123], v[216:219]\n"
    "ds_read_b64_tr_b16 v[76:77], v19 offset:0\n"
    "buffer_load_dwordx4 v28, s[40:43], s57 offen lds\n"
    "v_mfma_f32_16x16x32_bf16 v[220:223], v[44:47], v[124:127], v[220:223]\n"
    "s_waitcnt lgkmcnt(7)\n"
    "ds_read_b64_tr_b16 v[78:79], v19 offset:4096\n"
    "v_mfma_f32_16x16x32_bf16 v[160:163], v[48:51], v[128:131], v[160:163]\n"
    "s_add_u32 s56, s69, 0x400\n"
    "ds_read_b64_tr_b16 v[32:33], v8 offset:8192\n"
    "v_mfma_f32_16x16x32_bf16 v[164:167], v[48:51], v[132:135], v[164:167]\n"
    "ds_read_b64_tr_b16 v[34:35], v8 offset:12288\n"
    "buffer_load_dwordx4 v[120:123], v1, s[44:47], s56 offen sc1\n"
    "v_mfma_f32_16x16x32_bf16 v[168:171], v[52:55], v[128:131], v[168:171]\n"
    "s_add_u32 s56, s69, 0x20400\n"
    "ds_read_b64_tr_b16 v[36:37], v9 offset:8192\n"
    "v_mfma_f32_16x16x32_bf16 v[172:175], v[52:55], v[132:135], v[172:175]\n"
    "s_add_u32 m0, s64, 1024\n"
    "ds_read_b64_tr_b16 v[38:39], v9 offset:12288\n"
    "v_mfma_f32_16x16x32_bf16 v[176:179], v[56:59], v[128:131], v[176:179]\n"
    "ds_read_b64_tr_b16 v[40:41], v10 offset:8192\n"
    "buffer_load_dwordx4 v[124:127], v1, s[44:47], s56 offen sc1\n"
    "v_mfma_f32_16x16x32_bf16 v[180:183], v[56:59], v[132:135], v[180:183]\n"
    "ds_read_b64_tr_b16 v[42:43], v10 offset:12288\n"
    "v_mfma_f32_16x16x32_bf16 v[184:187], v[60:63], v[128:131], v[184:187]\n"
    "ds_read_b64_tr_b16 v[44:45], v11 offset:8192\n"
    "v_mfma_f32_16x16x32_bf16 v[188:191], v[60:63], v[132:135], v[188:191]\n"
    "s_waitcnt lgkmcnt(7)\n"
    "ds_read_b64_tr_b16 v[46:47], v11 offset:12288\n"
    "v_mfma_f32_16x16x32_bf16 v[192:195], v[64:67], v[128:131], v[192:195]\n"
    "ds_read_b64_tr_b16 v[48:49], v16 offset:8192\n"
    "buffer_load_dwordx4 v29, s[40:43], s57 offen lds\n"
    "v_mfma_f32_16x16x32_bf16 v[196:199], v[64:67], v[132:135], v[196:199]\n"
    "s_add_u32 s57, s70, 0x18000\n"
    "ds_read_b64_tr_b16 v[50:51], v16 offset:12288\n"
    "v_mfma_f32_16x16x32_bf16 v[200:203], v[68:71], v[128:131], v[200:203]\n"
    "s_add_u32 m0, s64, 8192\n"
    "ds_read_b64_tr_b16 v[52:53], v17 offset:8192\n"
    "v_mfma_f32_16x16x32_bf16 v[204:207], v[68:71], v[132:135], v[204:207]\n"
    "ds_read_b64_tr_b16 v[54:55], v17 offset:12288\n"
    "buffer_load_dwordx4 v28, s[40:43], s57 offen lds\n"
    "v_mfma_f32_16x16x32_bf16 v[208:211], v[72:75], v[128:131], v[208:211]\n"
    "s_waitcnt vmcnt(26)\n"
    "ds_read_b64_tr_b16 v[56:57], v18 offset:8192\n"
    "v_mfma_f32_16x16x32_bf16 v[212:215], v[72:75], v[132:135], v[212:215]\n"
    "s_add_u32 m0, s64, 9216\n"
    "ds_read_b64_tr_b16 v[58:59], v18 offset:12288\n"
    "v_mfma_f32_16x16x32_bf16 v[216:219], v[76:79], v[128:131], v[216:219]\n"
    "ds_read_b64_tr_b16 v[60:61], v19 offset:8192\n"
    "buffer_load_dwordx4 v29, s[40:43], s57 offen lds\n"
    "v_mfma_f32_16x16x32_bf16 v[220:223], v[76:79], v[132:135], v[220:223]\n"
    "s_waitcnt lgkmcnt(7)\n"
    "ds_read_b64_tr_b16 v[62:63], v19 offset:12288\n"
    "v_mfma_f32_16x16x32_bf16 v[160:163], v[32:35], v[136:139], v[160:163]\n"
    "s_add_u32 s56, s69, 0x800\n"
    "ds_read_b64_tr_b16 v[64:65], v8 offset:16384\n"
    "v_mfma_f32_16x16x32_bf16 v[164:167], v[32:35], v[140:143], v[164:167]\n"
    "ds_read_b64_tr_b16 v[66:67], v8 offset:20480\n"
    "buffer_load_dwordx4 v[128:131], v1, s[44:47], s56 offen sc1\n"
    "v_mfma_f32_16x16x32_bf16 v[168:171], v[36:39], v[136:139], v[168:171]\n"
    "s_add_u32 s56, s69, 0x20800\n"
    "ds_read_b64_tr_b16 v[68:69], v9 offset:16384\n"
    "v_mfma_f32_16x16x32_bf16 v[172:175], v[36:39], v[140:143], v[172:175]\n"
    "s_add_u32 s57, s53, 5\n"
    "ds_read_b64_tr_b16 v[70:71], v9 offset:20480\n"
    "v_mfma_f32_16x16x32_bf16 v[176:179], v[40:43], v[136:139], v[176:179]\n"
    "ds_read_b64_tr_b16 v[72:73], v10 offset:16384\n"
    "buffer_load_dwordx4 v[132:135], v1, s[44:47], s56 offen sc1\n"
    "v_mfma_f32_16x16x32_bf16 v[180:183], v[40:43], v[140:143], v[180:183]\n"
    "s_and_b32 s57, s57, 7\n"
    "ds_read_b64_tr_b16 v[74:75], v10 offset:20480\n"
    "v_mfma_f32_16x16x32_bf16 v[184:187], v[44:47], v[136:139], v[184:187]\n"
    "s_lshl_b32 s70, s57, 17\n"
    "ds_read_b64_tr_b16 v[76:77], v11 offset:16384\n"
    "v_mfma_f32_16x16x32_bf16 v[188:191], v[44:47], v[140:143], v[188:191]\n"
    "s_waitcnt lgkmcnt(7)\n"
    "ds_read_b64_tr_b16 v[78:79], v11 offset:20480\n"
    "v_mfma_f32_16x16x32_bf16 v[192:195], v[48:51], v[136:139], v[192:195]\n"
    "s_add_u32 s70, s70, s63\n"
    "ds_read_b64_tr_b16 v[32:33], v16 offset:16384\n"
    "v_mfma_f32_16x16x32_bf16 v[196:199], v[48:51], v[140:143], v[196:199]\n"
    "s_add_u32 s57, s70, 0x0\n"
    "ds_read_b64_tr_b16 v[34:35], v16 offset:20480\n"
    "v_mfma_f32_16x16x32_bf16 v[200:203], v[52:55], v[136:139], v[200:203]\n"
    "s_add_u32 m0, s64, 16384\n"
    "ds_read_b64_tr_b16 v[36:37], v17 offset:16384\n"
    "v_mfma_f32_16x16x32_bf16 v[204:207], v[52:55], v[140:143], v[204:207]\n"
    "ds_read_b64_tr_b16 v[38:39], v17 offset:20480\n"
    "buffer_load_dwordx4 v28, s[40:43], s57 offen lds\n"
    "v_mfma_f32_16x16x32_bf16 v[208:211], v[56:59], v[136:139], v[208:211]\n"
    "s_waitcnt vmcnt(25)\n"
    "ds_read_b64_tr_b16 v[40:41], v18 offset:16384\n"
    "v_mfma_f32_16x16x32_bf16 v[212:215], v[56:59], v[140:143], v[212:215]\n"
    "s_add_u32 m0, s64, 17408\n"
    "ds_read_b64_tr_b16 v[42:43], v18 offset:20480\n"
    "v_mfma_f32_16x16x32_bf16 v[216:219], v[60:63], v[136:139], v[216:219]\n"
    "ds_read_b64_tr_b16 v[44:45], v19 offset:16384\n"
    "buffer_load_dwordx4 v29, s[40:43], s57 offen lds\n"
    "v_mfma_f32_16x16x32_bf16 v[220:223], v[60:63], v[140:143], v[220:223]\n"
    "s_waitcnt lgkmcnt(7)\n"
    "ds_read_b64_tr_b16 v[46:47], v19 offset:20480\n"
    "v_mfma_f32_16x16x32_bf16 v[160:163], v[64:67], v[144:147], v[160:163]\n"
    "s_add_u32 s56, s69, 0xc00\n"
    "ds_read_b64_tr_b16 v[48:49], v8 offset:24576\n"
    "v_mfma_f32_16x16x32_bf16 v[164:167], v[64:67], v[148:151], v[164:167]\n"
    "ds_read_b64_tr_b16 v[50:51], v8 offset:28672\n"
    "buffer_load_dwordx4 v[136:139], v1, s[44:47], s56 offen sc1\n"
    "v_mfma_f32_16x16x32_bf16 v[168:171], v[68:71], v[144:147], v[168:171]\n"
    "s_add_u32 s56, s69, 0x20c00\n"
    "ds_read_b64_tr_b16 v[52:53], v9 offset:24576\n"
    "v_mfma_f32_16x16x32_bf16 v[172:175], v[68:71], v[148:151], v[172:175]\n"
    "s_add_u32 s57, s70, 0x8000\n"
    "ds_read_b64_tr_b16 v[54:55], v9 offset:28672\n"
    "v_mfma_f32_16x16x32_bf16 v[176:179], v[72:75], v[144:147], v[176:179]\n"
    "ds_read_b64_tr_b16 v[56:57], v10 offset:24576\n"
    "buffer_load_dwordx4 v[140:143], v1, s[44:47], s56 offen sc1\n"
    "v_mfma_f32_16x16x32_bf16 v[180:183], v[72:75], v[148:151], v[180:183]\n"
    "s_add_u32 m0, s64, 24576\n"
    "ds_read_b64_tr_b16 v[58:59], v10 offset:28672\n"
    "v_mfma_f32_16x16x32_bf16 v[184:187], v[76:79], v[144:147], v[184:187]\n"
    "ds_read_b64_tr_b16 v[60:61], v11 offset:24576\n"
    "v_mfma_f32_16x16x32_bf16 v[188:191], v[76:79], v[148:151], v[188:191]\n"
    "s_waitcnt lgkmcnt(7)\n"
    "ds_read_b64_tr_b16 v[62:63], v11 offset:28672\n"
    "v_mfma_f32_16x16x32_bf16 v[192:195], v[32:35], v[144:147], v[192:195]\n"
    "ds_read_b64_tr_b16 v[64:65], v16 offset:24576\n"
    "buffer_load_dwordx4 v28, s[40:43], s57 offen lds\n"
    "v_mfma_f32_16x16x32_bf16 v[196:199], v[32:35], v[148:151], v[196:199]\n"
    "ds_read_b64_tr_b16 v[66:67], v16 offset:28672\n"
    "v_mfma_f32_16x16x32_bf16 v[200:203], v[36:39], v[144:147], v[200:203]\n"
    "s_add_u32 m0, s64, 25600\n"
    "ds_read_b64_tr_b16 v[68:69], v17 offset:24576\n"
    "v_mfma_f32_16x16x32_bf16 v[204:207], v[36:39], v[148:151], v[204:207]\n"
    "ds_read_b64_tr_b16 v[70:71], v17 offset:28672\n"
    "buffer_load_dwordx4 v29, s[40:43], s57 offen lds\n"
    "v_mfma_f32_16x16x32_bf16 v[208:211], v[40:43], v[144:147], v[208:211]\n"
    "s_waitcnt vmcnt(26)\n"
    "ds_read_b64_tr_b16 v[72:73], v18 offset:24576\n"
    "v_mfma_f32_16x16x32_bf16 v[212:215], v[40:43], v[148:151], v[212:215]\n"
    "ds_read_b64_tr_b16 v[74:75], v18 offset:28672\n"
    "v_mfma_f32_16x16x32_bf16 v[216:219], v[44:47], v[144:147], v[216:219]\n"
    "ds_read_b64_tr_b16 v[76:77], v19 offset:24576\n"
    "v_mfma_f32_16x16x32_bf16 v[220:223], v[44:47], v[148:151], v[220:223]\n"
    "s_waitcnt lgkmcnt(7)\n"
    "ds_read_b64_tr_b16 v[78:79], v19 offset:28672\n"
    "v_mfma_f32_16x16x32_bf16 v[160:163], v[48:51], v[152:155], v[160:163]\n"
    "s_add_u32 s56, s53, 4\n"
    "ds_read_b64_tr_b16 v[32:33], v8 offset:32768\n"
    "v_mfma_f32_16x16x32_bf16 v[164:167], v[48:51], v[156:159], v[164:167]\n"
    "s_and_b32 s56, s56, 7\n"
    "ds_read_b64_tr_b16 v[34:35], v8 offset:36864\n"
    "v_mfma_f32_16x16x32_bf16 v[168:171], v[52:55], v[152:155], v[168:171]\n"
    "s_lshl_b32 s69, s56, 12\n"
    "ds_read_b64_tr_b16 v[36:37], v9 offset:32768\n"
    "v_mfma_f32_16x16x32_bf16 v[172:175], v[52:55], v[156:159], v[172:175]\n"
    "s_add_u32 s69, s69, s61\n"
    "ds_read_b64_tr_b16 v[38:39], v9 offset:36864\n"
    "v_mfma_f32_16x16x32_bf16 v[176:179], v[56:59], v[152:155], v[176:179]\n"
    "s_add_u32 s56, s69, 0x0\n"
    "ds_read_b64_tr_b16 v[40:41], v10 offset:32768\n"
    "v_mfma_f32_16x16x32_bf16 v[180:183], v[56:59], v[156:159], v[180:183]\n"
    "ds_read_b64_tr_b16 v[42:43], v10 offset:36864\n"
    "buffer_load_dwordx4 v[144:147], v1, s[44:47], s56 offen sc1\n"
    "v_mfma_f32_16x16x32_bf16 v[184:187], v[60:63], v[152:155], v[184:187]\n"
    "s_add_u32 s56, s69, 0x20000\n"
    "ds_read_b64_tr_b16 v[44:45], v11 offset:32768\n"
    "v_mfma_f32_16x16x32_bf16 v[188:191], v[60:63], v[156:159], v[188:191]\n"
    "s_waitcnt lgkmcnt(7)\n"
    "ds_read_b64_tr_b16 v[46:47], v11 offset:36864\n"
    "v_mfma_f32_16x16x32_bf16 v[192:195], v[64:67], v[152:155], v[192:195]\n"
    "ds_read_b64_tr_b16 v[48:49], v16 offset:32768\n"
    "buffer_load_dwordx4 v[148:151], v1, s[44:47], s56 offen sc1\n"
    "v_mfma_f32_16x16x32_bf16 v[196:199], v[64:67], v[156:159], v[196:199]\n"
    "s_add_u32 s57, s70, 0x10000\n"
    "ds_read_b64_tr_b16 v[50:51], v16 offset:36864\n"
    "v_mfma_f32_16x16x32_bf16 v[200:203], v[68:71], v[152:155], v[200:203]\n"
    "s_add_u32 m0, s64, 32768\n"
    "ds_read_b64_tr_b16 v[52:53], v17 offset:32768\n"
    "v_mfma_f32_16x16x32_bf16 v[204:207], v[68:71], v[156:159], v[204:207]\n"
    "ds_read_b64_tr_b16 v[54:55], v17 offset:36864\n"
    "buffer_load_dwordx4 v28, s[40:43], s57 offen lds\n"
    "v_mfma_f32_16x16x32_bf16 v[208:211], v[72:75], v[152:155], v[208:211]\n"
    "s_waitcnt vmcnt(25)\n"
    "ds_read_b64_tr_b16 v[56:57], v18 offset:32768\n"
    "v_mfma_f32_16x16x32_bf16 v[212:215], v[72:75], v[156:159], v[212:215]\n"
    "s_add_u32 m0, s64, 33792\n"
    "ds_read_b64_tr_b16 v[58:59], v18 offset:36864\n"
    "v_mfma_f32_16x16x32_bf16 v[216:219], v[76:79], v[152:155], v[216:219]\n"
    "ds_read_b64_tr_b16 v[60:61], v19 offset:32768\n"
    "buffer_load_dwordx4 v29, s[40:43], s57 offen lds\n"
    "v_mfma_f32_16x16x32_bf16 v[220:223], v[76:79], v[156:159], v[220:223]\n"
    "s_waitcnt lgkmcnt(7)\n"
    "ds_read_b64_tr_b16 v[62:63], v19 offset:36864\n"
    "v_mfma_f32_16x16x32_bf16 v[160:163], v[32:35], v[96:99], v[160:163]\n"
    "s_add_u32 s57, s70, 0x18000\n"
    "ds_read_b64_tr_b16 v[64:65], v8 offset:40960\n"
    "v_mfma_f32_16x16x32_bf16 v[164:167], v[32:35], v[100:103], v[164:167]\n"
    "s_add_u32 m0, s64, 40960\n"
    "ds_read_b64_tr_b16 v[66:67], v8 offset:45056\n"
    "v_mfma_f32_16x16x32_bf16 v[168:171], v[36:39], v[96:99], v[168:171]\n"
    "ds_read_b64_tr_b16 v[68:69], v9 offset:40960\n"
    "buffer_load_dwordx4 v28, s[40:43], s57 offen lds\n"
    "v_mfma_f32_16x16x32_bf16 v[172:175], v[36:39], v[100:103], v[172:175]\n"
    "s_add_u32 s56, s69, 0x400\n"
    "ds_read_b64_tr_b16 v[70:71], v9 offset:45056\n"
    "v_mfma_f32_16x16x32_bf16 v[176:179], v[40:43], v[96:99], v[176:179]\n"
    "ds_read_b64_tr_b16 v[72:73], v10 offset:40960\n"
    "v_mfma_f32_16x16x32_bf16 v[180:183], v[40:43], v[100:103], v[180:183]\n"
    "ds_read_b64_tr_b16 v[74:75], v10 offset:45056\n"
    "buffer_load_dwordx4 v[152:155], v1, s[44:47], s56 offen sc1\n"
    "v_mfma_f32_16x16x32_bf16 v[184:187], v[44:47], v[96:99], v[184:187]\n"
    "s_add_u32 s56, s69, 0x20400\n"
    "ds_read_b64_tr_b16 v[76:77], v11 offset:40960\n"
    "v_mfma_f32_16x16x32_bf16 v[188:191], v[44:47], v[100:103], v[188:191]\n"
    "s_waitcnt lgkmcnt(7)\n"
    "ds_read_b64_tr_b16 v[78:79], v11 offset:45056\n"
    "v_mfma_f32_16x16x32_bf16 v[192:195], v[48:51], v[96:99], v[192:195]\n"
    "ds_read_b64_tr_b16 v[32:33], v16 offset:40960\n"
    "buffer_load_dwordx4 v[156:159], v1, s[44:47], s56 offen sc1\n"
    "v_mfma_f32_16x16x32_bf16 v[196:199], v[48:51], v[100:103], v[196:199]\n"
    "s_add_u32 m0, s64, 41984\n"
    "ds_read_b64_tr_b16 v[34:35], v16 offset:45056\n"
    "v_mfma_f32_16x16x32_bf16 v[200:203], v[52:55], v[96:99], v[200:203]\n"
    "ds_read_b64_tr_b16 v[36:37], v17 offset:40960\n"
    "v_mfma_f32_16x16x32_bf16 v[204:207], v[52:55], v[100:103], v[204:207]\n"
    "ds_read_b64_tr_b16 v[38:39], v17 offset:45056\n"
    "buffer_load_dwordx4 v29, s[40:43], s57 offen lds\n"
    "v_mfma_f32_16x16x32_bf16 v[208:211], v[56:59], v[96:99], v[208:211]\n"
    "s_waitcnt vmcnt(25)\n"
    "ds_read_b64_tr_b16 v[40:41], v18 offset:40960\n"
    "v_mfma_f32_16x16x32_bf16 v[212:215], v[56:59], v[100:103], v[212:215]\n"
    "ds_read_b64_tr_b16 v[42:43], v18 offset:45056\n"
    "v_mfma_f32_16x16x32_bf16 v[216:219], v[60:63], v[96:99], v[216:219]\n"
    "ds_read_b64_tr_b16 v[44:45], v19 offset:40960\n"
    "v_mfma_f32_16x16x32_bf16 v[220:223], v[60:63], v[100:103], v[220:223]\n"
    "s_waitcnt lgkmcnt(7)\n"
    "ds_read_b64_tr_b16 v[46:47], v19 offset:45056\n"
    "s_waitcnt vmcnt(24)\n"
    "s_barrier\n"
    "v_add_u32 v8, s77, v8\n"
    "v_mfma_f32_16x16x32_bf16 v[160:163], v[64:67], v[104:107], v[160:163]\n"
    "s_add_u32 s56, s69, 0x800\n"
    "ds_read_b64_tr_b16 v[48:49], v8 offset:0\n"
    "v_mfma_f32_16x16x32_bf16 v[164:167], v[64:67], v[108:111], v[164:167]\n"
    "s_add_u32 s57, s53, 6\n"
    "ds_read_b64_tr_b16 v[50:51], v8 offset:4096\n"
    "v_add_u32 v9, s77, v9\n"
    "v_mfma_f32_16x16x32_bf16 v[168:171], v[68:71], v[104:107], v[168:171]\n"
    "ds_read_b64_tr_b16 v[52:53], v9 offset:0\n"
    "buffer_load_dwordx4 v[96:99], v1, s[44:47], s56 offen sc1\n"
    "v_mfma_f32_16x16x32_bf16 v[172:175], v[68:71], v[108:111], v[172:175]\n"
    "s_add_u32 s56, s69, 0x20800\n"
    "ds_read_b64_tr_b16 v[54:55], v9 offset:4096\n"
    "v_add_u32 v10, s77, v10\n"
    "v_mfma_f32_16x16x32_bf16 v[176:179], v[72:75], v[104:107], v[176:179]\n"
    "s_and_b32 s57, s57, 7\n"
    "ds_read_b64_tr_b16 v[56:57], v10 offset:0\n"
    "v_mfma_f32_16x16x32_bf16 v[180:183], v[72:75], v[108:111], v[180:183]\n"
    "s_lshl_b32 s70, s57, 17\n"
    "ds_read_b64_tr_b16 v[58:59], v10 offset:4096\n"
    "v_add_u32 v11, s77, v11\n"
    "v_mfma_f32_16x16x32_bf16 v[184:187], v[76:79], v[104:107], v[184:187]\n"
    "ds_read_b64_tr_b16 v[60:61], v11 offset:0\n"
    "buffer_load_dwordx4 v[100:103], v1, s[44:47], s56 offen sc1\n"
    "v_mfma_f32_16x16x32_bf16 v[188:191], v[76:79], v[108:111], v[188:191]\n"
    "s_waitcnt lgkmcnt(7)\n"
    "ds_read_b64_tr_b16 v[62:63], v11 offset:4096\n"
    "v_add_u32 v16, s77, v16\n"
    "v_mfma_f32_16x16x32_bf16 v[192:195], v[32:35], v[104:107], v[192:195]\n"
    "s_add_u32 s70, s70, s63\n"
    "ds_read_b64_tr_b16 v[64:65], v16 offset:0\n"
    "v_mfma_f32_16x16x32_bf16 v[196:199], v[32:35], v[108:111], v[196:199]\n"
    "s_add_u32 s57, s70, 0x0\n"
    "ds_read_b64_tr_b16 v[66:67], v16 offset:4096\n"
    "v_add_u32 v17, s77, v17\n"
    "v_mfma_f32_16x16x32_bf16 v[200:203], v[36:39], v[104:107], v[200:203]\n"
    "s_add_u32 m0, s65, 0\n"
    "ds_read_b64_tr_b16 v[68:69], v17 offset:0\n"
    "v_mfma_f32_16x16x32_bf16 v[204:207], v[36:39], v[108:111], v[204:207]\n"
    "ds_read_b64_tr_b16 v[70:71], v17 offset:4096\n"
    "v_add_u32 v18, s77, v18\n"
    "v_mfma_f32_16x16x32_bf16 v[208:211], v[40:43], v[104:107], v[208:211]\n"
    "s_waitcnt vmcnt(24)\n"
    "ds_read_b64_tr_b16 v[72:73], v18 offset:0\n"
    "v_mfma_f32_16x16x32_bf16 v[212:215], v[40:43], v[108:111], v[212:215]\n"
    "ds_read_b64_tr_b16 v[74:75], v18 offset:4096\n"
    "v_add_u32 v19, s77, v19\n"
    "v_mfma_f32_16x16x32_bf16 v[216:219], v[44:47], v[104:107], v[216:219]\n"
    "ds_read_b64_tr_b16 v[76:77], v19 offset:0\n"
    "buffer_load_dwordx4 v28, s[40:43], s57 offen lds\n"
    "v_mfma_f32_16x16x32_bf16 v[220:223], v[44:47], v[108:111], v[220:223]\n"
    "s_waitcnt lgkmcnt(7)\n"
    "ds_read_b64_tr_b16 v[78:79], v19 offset:4096\n"
    "v_mfma_f32_16x16x32_bf16 v[160:163], v[48:51], v[112:115], v[160:163]\n"
    "s_add_u32 s56, s69, 0xc00\n"
    "ds_read_b64_tr_b16 v[32:33], v8 offset:8192\n"
    "v_mfma_f32_16x16x32_bf16 v[164:167], v[48:51], v[116:119], v[164:167]\n"
    "ds_read_b64_tr_b16 v[34:35], v8 offset:12288\n"
    "buffer_load_dwordx4 v[104:107], v1, s[44:47], s56 offen sc1\n"
    "v_mfma_f32_16x16x32_bf16 v[168:171], v[52:55], v[112:115], v[168:171]\n"
    "s_add_u32 s56, s69, 0x20c00\n"
    "ds_read_b64_tr_b16 v[36:37], v9 offset:8192\n"
    "v_mfma_f32_16x16x32_bf16 v[172:175], v[52:55], v[116:119], v[172:175]\n"
    "s_add_u32 m0, s65, 1024\n"
    "ds_read_b64_tr_b16 v[38:39], v9 offset:12288\n"
    "v_mfma_f32_16x16x32_bf16 v[176:179], v[56:59], v[112:115], v[176:179]\n"
    "ds_read_b64_tr_b16 v[40:41], v10 offset:8192\n"
    "buffer_load_dwordx4 v[108:111], v1, s[44:47], s56 offen sc1\n"
    "v_mfma_f32_16x16x32_bf16 v[180:183], v[56:59], v[116:119], v[180:183]\n"
    "ds_read_b64_tr_b16 v[42:43], v10 offset:12288\n"
    "v_mfma_f32_16x16x32_bf16 v[184:187], v[60:63], v[112:115], v[184:187]\n"
    "ds_read_b64_tr_b16 v[44:45], v11 offset:8192\n"
    "v_mfma_f32_16x16x32_bf16 v[188:191], v[60:63], v[116:119], v[188:191]\n"
    "s_waitcnt lgkmcnt(7)\n"
    "ds_read_b64_tr_b16 v[46:47], v11 offset:12288\n"
    "v_mfma_f32_16x16x32_bf16 v[192:195], v[64:67], v[112:115], v[192:195]\n"
    "ds_read_b64_tr_b16 v[48:49], v16 offset:8192\n"
    "buffer_load_dwordx4 v29, s[40:43], s57 offen lds\n"
    "v_mfma_f32_16x16x32_bf16 v[196:199], v[64:67], v[116:119], v[196:199]\n"
    "s_add_u32 s57, s70, 0x8000\n"
    "ds_read_b64_tr_b16 v[50:51], v16 offset:12288\n"
    "v_mfma_f32_16x16x32_bf16 v[200:203], v[68:71], v[112:115], v[200:203]\n"
    "s_add_u32 m0, s65, 8192\n"
    "ds_read_b64_tr_b16 v[52:53], v17 offset:8192\n"
    "v_mfma_f32_16x16x32_bf16 v[204:207], v[68:71], v[116:119], v[204:207]\n"
    "ds_read_b64_tr_b16 v[54:55], v17 offset:12288\n"
    "buffer_load_dwordx4 v28, s[40:43], s57 offen lds\n"
    "v_mfma_f32_16x16x32_bf16 v[208:211], v[72:75], v[112:115], v[208:211]\n"
    "s_waitcnt vmcnt(26)\n"
    "ds_read_b64_tr_b16 v[56:57], v18 offset:8192\n"
    "v_mfma_f32_16x16x32_bf16 v[212:215], v[72:75], v[116:119], v[212:215]\n"
    "s_add_u32 m0, s65, 9216\n"
    "ds_read_b64_tr_b16 v[58:59], v18 offset:12288\n"
    "v_mfma_f32_16x16x32_bf16 v[216:219], v[76:79], v[112:115], v[216:219]\n"
    "ds_read_b64_tr_b16 v[60:61], v19 offset:8192\n"
    "buffer_load_dwordx4 v29, s[40:43], s57 offen lds\n"
    "v_mfma_f32_16x16x32_bf16 v[220:223], v[76:79], v[116:119], v[220:223]\n"
    "s_waitcnt lgkmcnt(7)\n"
    "ds_read_b64_tr_b16 v[62:63], v19 offset:12288\n"
    "v_mfma_f32_16x16x32_bf16 v[160:163], v[32:35], v[120:123], v[160:163]\n"
    "s_add_u32 s56, s53, 5\n"
    "ds_read_b64_tr_b16 v[64:65], v8 offset:16384\n"
    "v_mfma_f32_16x16x32_bf16 v[164:167], v[32:35], v[124:127], v[164:167]\n"
    "s_and_b32 s56, s56, 7\n"
    "ds_read_b64_tr_b16 v[66:67], v8 offset:20480\n"
    "v_mfma_f32_16x16x32_bf16 v[168:171], v[36:39], v[120:123], v[168:171]\n"
    "s_lshl_b32 s69, s56, 12\n"
    "ds_read_b64_tr_b16 v[68:69], v9 offset:16384\n"
    "v_mfma_f32_16x16x32_bf16 v[172:175], v[36:39], v[124:127], v[172:175]\n"
    "s_add_u32 s69, s69, s61\n"
    "ds_read_b64_tr_b16 v[70:71], v9 offset:20480\n"
    "v_mfma_f32_16x16x32_bf16 v[176:179], v[40:43], v[120:123], v[176:179]\n"
    "s_add_u32 s56, s69, 0x0\n"
    "ds_read_b64_tr_b16 v[72:73], v10 offset:16384\n"
    "v_mfma_f32_16x16x32_bf16 v[180:183], v[40:43], v[124:127], v[180:183]\n"
    "ds_read_b64_tr_b16 v[74:75], v10 offset:20480\n"
    "buffer_load_dwordx4 v[112:115], v1, s[44:47], s56 offen sc1\n"
    "v_mfma_f32_16x16x32_bf16 v[184:187], v[44:47], v[120:123], v[184:187]\n"
    "s_add_u32 s56, s69, 0x20000\n"
    "ds_read_b64_tr_b16 v[76:77], v11 offset:16384\n"
    "v_mfma_f32_16x16x32_bf16 v[188:191], v[44:47], v[124:127], v[188:191]\n"
    "s_waitcnt lgkmcnt(7)\n"
    "ds_read_b64_tr_b16 v[78:79], v11 offset:20480\n"
    "v_mfma_f32_16x16x32_bf16 v[192:195], v[48:51], v[120:123], v[192:195]\n"
    "ds_read_b64_tr_b16 v[32:33], v16 offset:16384\n"
    "buffer_load_dwordx4 v[116:119], v1, s[44:47], s56 offen sc1\n"
    "v_mfma_f32_16x16x32_bf16 v[196:199], v[48:51], v[124:127], v[196:199]\n"
    "s_add_u32 s57, s70, 0x10000\n"
    "ds_read_b64_tr_b16 v[34:35], v16 offset:20480\n"
    "v_mfma_f32_16x16x32_bf16 v[200:203], v[52:55], v[120:123], v[200:203]\n"
    "s_add_u32 m0, s65, 16384\n"
    "ds_read_b64_tr_b16 v[36:37], v17 offset:16384\n"
    "v_mfma_f32_16x16x32_bf16 v[204:207], v[52:55], v[124:127], v[204:207]\n"
    "ds_read_b64_tr_b16 v[38:39], v17 offset:20480\n"
    "buffer_load_dwordx4 v28, s[40:43], s57 offen lds\n"
    "v_mfma_f32_16x16x32_bf16 v[208:211], v[56:59], v[120:123], v[208:211]\n"
    "s_waitcnt vmcnt(25)\n"
    "ds_read_b64_tr_b16 v[40:41], v18 offset:16384\n"
    "v_mfma_f32_16x16x32_bf16 v[212:215], v[56:59], v[124:127], v[212:215]\n"
    "s_add_u32 m0, s65, 17408\n"
    "ds_read_b64_tr_b16 v[42:43], v18 offset:20480\n"
    "v_mfma_f32_16x16x32_bf16 v[216:219], v[60:63], v[120:123], v[216:219]\n"
    "ds_read_b64_tr_b16 v[44:45], v19 offset:16384\n"
    "buffer_load_dwordx4 v29, s[40:43], s57 offen lds\n"
    "v_mfma_f32_16x16x32_bf16 v[220:223], v[60:63], v[124:127], v[220:223]\n"
    "s_waitcnt lgkmcnt(7)\n"
    "ds_read_b64_tr_b16 v[46:47], v19 offset:20480\n"
    "v_mfma_f32_16x16x32_bf16 v[160:163], v[64:67], v[128:131], v[160:163]\n"
    "s_add_u32 s56, s69, 0x400\n"
    "ds_read_b64_tr_b16 v[48:49], v8 offset:24576\n"
    "v_mfma_f32_16x16x32_bf16 v[164:167], v[64:67], v[132:135], v[164:167]\n"
    "ds_read_b64_tr_b16 v[50:51], v8 offset:28672\n"
    "buffer_load_dwordx4 v[120:123], v1, s[44:47], s56 offen sc1\n"
    "v_mfma_f32_16x16x32_bf16 v[168:171], v[68:71], v[128:131], v[168:171]\n"
    "s_add_u32 s56, s69, 0x20400\n"
    "ds_read_b64_tr_b16 v[52:53], v9 offset:24576\n"
    "v_mfma_f32_16x16x32_bf16 v[172:175], v[68:71], v[132:135], v[172:175]\n"
    "s_add_u32 s57, s70, 0x18000\n"
    "ds_read_b64_tr_b16 v[54:55], v9 offset:28672\n"
    "v_mfma_f32_16x16x32_bf16 v[176:179], v[72:75], v[128:131], v[176:179]\n"
    "ds_read_b64_tr_b16 v[56:57], v10 offset:24576\n"
    "buffer_load_dwordx4 v[124:127], v1, s[44:47], s56 offen sc1\n"
    "v_mfma_f32_16x16x32_bf16 v[180:183], v[72:75], v[132:135], v[180:183]\n"
    "s_add_u32 m0, s65, 24576\n"
    "ds_read_b64_tr_b16 v[58:59], v10 offset:28672\n"
    "v_mfma_f32_16x16x32_bf16 v[184:187], v[76:79], v[128:131], v[184:187]\n"
    "ds_read_b64_tr_b16 v[60:61], v11 offset:24576\n"
    "v_mfma_f32_16x16x32_bf16 v[188:191], v[76:79], v[132:135], v[188:191]\n"
    "s_waitcnt lgkmcnt(7)\n"
    "ds_read_b64_tr_b16 v[62:63], v11 offset:28672\n"
    "v_mfma_f32_16x16x32_bf16 v[192:195], v[32:35], v[128:131], v[192:195]\n"
    "ds_read_b64_tr_b16 v[64:65], v16 offset:24576\n"
    "buffer_load_dwordx4 v28, s[40:43], s57 offen lds\n"
    "v_mfma_f32_16x16x32_bf16 v[196:199], v[32:35], v[132:135], v[196:199]\n"
    "s_add_u32 m0, s65, 25600\n"
    "ds_read_b64_tr_b16 v[66:67], v16 offset:28672\n"
    "v_mfma_f32_16x16x32_bf16 v[200:203], v[36:39], v[128:131], v[200:203]\n"
    "ds_read_b64_tr_b16 v[68:69], v17 offset:24576\n"
    "v_mfma_f32_16x16x32_bf16 v[204:207], v[36:39], v[132:135], v[204:207]\n"
    "ds_read_b64_tr_b16 v[70:71], v17 offset:28672\n"
    "buffer_load_dwordx4 v29, s[40:43], s57 offen lds\n"
    "v_mfma_f32_16x16x32_bf16 v[208:211], v[40:43], v[128:131], v[208:211]\n"
    "s_waitcnt vmcnt(26)\n"
    "ds_read_b64_tr_b16 v[72:73], v18 offset:24576\n"
    "v_mfma_f32_16x16x32_bf16 v[212:215], v[40:43], v[132:135], v[212:215]\n"
    "ds_read_b64_tr_b16 v[74:75], v18 offset:28672\n"
    "v_mfma_f32_16x16x32_bf16 v[216:219], v[44:47], v[128:131], v[216:219]\n"
    "ds_read_b64_tr_b16 v[76:77], v19 offset:24576\n"
    "v_mfma_f32_16x16x32_bf16 v[220:223], v[44:47], v[132:135], v[220:223]\n"
    "s_waitcnt lgkmcnt(7)\n"
    "ds_read_b64_tr_b16 v[78:79], v19 offset:28672\n"
    "v_mfma_f32_16x16x32_bf16 v[160:163], v[48:51], v[136:139], v[160:163]\n"
    "s_add_u32 s56, s69, 0x800\n"
    "ds_read_b64_tr_b16 v[32:33], v8 offset:32768\n"
    "v_mfma_f32_16x16x32_bf16 v[164:167], v[48:51], v[140:143], v[164:167]\n"
    "ds_read_b64_tr_b16 v[34:35], v8 offset:36864\n"
    "buffer_load_dwordx4 v[128:131], v1, s[44:47], s56 offen sc1\n"
    "v_mfma_f32_16x16x32_bf16 v[168:171], v[52:55], v[136:139], v[168:171]\n"
    "s_add_u32 s56, s69, 0x20800\n"
    "ds_read_b64_tr_b16 v[36:37], v9 offset:32768\n"
    "v_mfma_f32_16x16x32_bf16 v[172:175], v[52:55], v[140:143], v[172:175]\n"
    "s_add_u32 s57, s53, 7\n"
    "ds_read_b64_tr_b16 v[38:39], v9 offset:36864\n"
    "v_mfma_f32_16x16x32_bf16 v[176:179], v[56:59], v[136:139], v[176:179]\n"
    "ds_read_b64_tr_b16 v[40:41], v10 offset:32768\n"
    "buffer_load_dwordx4 v[132:135], v1, s[44:47], s56 offen sc1\n"
    "v_mfma_f32_16x16x32_bf16 v[180:183], v[56:59], v[140:143], v[180:183]\n"
    "s_and_b32 s57, s57, 7\n"
    "ds_read_b64_tr_b16 v[42:43], v10 offset:36864\n"
    "v_mfma_f32_16x16x32_bf16 v[184:187], v[60:63], v[136:139], v[184:187]\n"
    "s_lshl_b32 s70, s57, 17\n"
    "ds_read_b64_tr_b16 v[44:45], v11 offset:32768\n"
    "v_mfma_f32_16x16x32_bf16 v[188:191], v[60:63], v[140:143], v[188:191]\n"
    "s_waitcnt lgkmcnt(7)\n"
    "ds_read_b64_tr_b16 v[46:47], v11 offset:36864\n"
    "v_mfma_f32_16x16x32_bf16 v[192:195], v[64:67], v[136:139], v[192:195]\n"
    "s_add_u32 s70, s70, s63\n"
    "ds_read_b64_tr_b16 v[48:49], v16 offset:32768\n"
    "v_mfma_f32_16x16x32_bf16 v[196:199], v[64:67], v[140:143], v[196:199]\n"
    "s_add_u32 s57, s70, 0x0\n"
    "ds_read_b64_tr_b16 v[50:51], v16 offset:36864\n"
    "v_mfma_f32_16x16x32_bf16 v[200:203], v[68:71], v[136:139], v[200:203]\n"
    "s_add_u32 m0, s65, 32768\n"
    "ds_read_b64_tr_b16 v[52:53], v17 offset:32768\n"
    "v_mfma_f32_16x16x32_bf16 v[204:207], v[68:71], v[140:143], v[204:207]\n"
    "ds_read_b64_tr_b16 v[54:55], v17 offset:36864\n"
    "buffer_load_dwordx4 v28, s[40:43], s57 offen lds\n"
    "v_mfma_f32_16x16x32_bf16 v[208:211], v[72:75], v[136:139], v[208:211]\n"
    "s_waitcnt vmcnt(25)\n"
    "ds_read_b64_tr_b16 v[56:57], v18 offset:32768\n"
    "v_mfma_f32_16x16x32_bf16 v[212:215], v[72:75], v[140:143], v[212:215]\n"
    "s_add_u32 m0, s65, 33792\n"
    "ds_read_b64_tr_b16 v[58:59], v18 offset:36864\n"
    "v_mfma_f32_16x16x32_bf16 v[216:219], v[76:79], v[136:139], v[216:219]\n"
    "ds_read_b64_tr_b16 v[60:61], v19 offset:32768\n"
    "buffer_load_dwordx4 v29, s[40:43], s57 offen lds\n"
    "v_mfma_f32_16x16x32_bf16 v[220:223], v[76:79], v[140:143], v[220:223]\n"
    "s_waitcnt lgkmcnt(7)\n"
    "ds_read_b64_tr_b16 v[62:63], v19 offset:36864\n"
    "v_mfma_f32_16x16x32_bf16 v[160:163], v[32:35], v[144:147], v[160:163]\n"
    "s_add_u32 s56, s69, 0xc00\n"
    "ds_read_b64_tr_b16 v[64:65], v8 offset:40960\n"
    "v_mfma_f32_16x16x32_bf16 v[164:167], v[32:35], v[148:151], v[164:167]\n"
    "ds_read_b64_tr_b16 v[66:67], v8 offset:45056\n"
    "buffer_load_dwordx4 v[136:139], v1, s[44:47], s56 offen sc1\n"
    "v_mfma_f32_16x16x32_bf16 v[168:171], v[36:39], v[144:147], v[168:171]\n"
    "s_add_u32 s57, s70, 0x8000\n"
    "ds_read_b64_tr_b16 v[68:69], v9 offset:40960\n"
    "v_mfma_f32_16x16x32_bf16 v[172:175], v[36:39], v[148:151], v[172:175]\n"
    "s_add_u32 m0, s65, 40960\n"
    "ds_read_b64_tr_b16 v[70:71], v9 offset:45056\n"
    "v_mfma_f32_16x16x32_bf16 v[176:179], v[40:43], v[144:147], v[176:179]\n"
    "ds_read_b64_tr_b16 v[72:73], v10 offset:40960\n"
    "buffer_load_dwordx4 v28, s[40:43], s57 offen lds\n"
    "v_mfma_f32_16x16x32_bf16 v[180:183], v[40:43], v[148:151], v[180:183]\n"
    "s_add_u32 s56, s69, 0x20c00\n"
    "ds_read_b64_tr_b16 v[74:75], v10 offset:45056\n"
    "v_mfma_f32_16x16x32_bf16 v[184:187], v[44:47], v[144:147], v[184:187]\n"
    "ds_read_b64_tr_b16 v[76:77], v11 offset:40960\n"
    "v_mfma_f32_16x16x32_bf16 v[188:191], v[44:47], v[148:151], v[188:191]\n"
    "s_waitcnt lgkmcnt(7)\n"
    "ds_read_b64_tr_b16 v[78:79], v11 offset:45056\n"
    "v_mfma_f32_16x16x32_bf16 v[192:195], v[48:51], v[144:147], v[192:195]\n"
    "ds_read_b64_tr_b16 v[32:33], v16 offset:40960\n"
    "buffer_load_dwordx4 v[140:143], v1, s[44:47], s56 offen sc1\n"
    "v_mfma_f32_16x16x32_bf16 v[196:199], v[48:51], v[148:151], v[196:199]\n"
    "s_add_u32 m0, s65, 41984\n"
    "ds_read_b64_tr_b16 v[34:35], v16 offset:45056\n"
    "v_mfma_f32_16x16x32_bf16 v[200:203], v[52:55], v[144:147], v[200:203]\n"
    "ds_read_b64_tr_b16 v[36:37], v17 offset:40960\n"
    "v_mfma_f32_16x16x32_bf16 v[204:207], v[52:55], v[148:151], v[204:207]\n"
    "ds_read_b64_tr_b16 v[38:39], v17 offset:45056\n"
    "buffer_load_dwordx4 v29, s[40:43], s57 offen lds\n"
    "v_mfma_f32_16x16x32_bf16 v[208:211], v[56:59], v[144:147], v[208:211]\n"
    "s_waitcnt vmcnt(25)\n"
    "ds_read_b64_tr_b16 v[40:41], v18 offset:40960\n"
    "v_mfma_f32_16x16x32_bf16 v[212:215], v[56:59], v[148:151], v[212:215]\n"
    "ds_read_b64_tr_b16 v[42:43], v18 offset:45056\n"
    "v_mfma_f32_16x16x32_bf16 v[216:219], v[60:63], v[144:147], v[216:219]\n"
    "ds_read_b64_tr_b16 v[44:45], v19 offset:40960\n"
    "v_mfma_f32_16x16x32_bf16 v[220:223], v[60:63], v[148:151], v[220:223]\n"
    "s_waitcnt lgkmcnt(7)\n"
    "ds_read_b64_tr_b16 v[46:47], v19 offset:45056\n"
    "s_waitcnt vmcnt(24)\n"
    "s_barrier\n"
    "v_add_u32 v8, s78, v8\n"
    "v_mfma_f32_16x16x32_bf16 v[160:163], v[64:67], v[152:155], v[160:163]\n"
    "s_add_u32 s56, s53, 6\n"
    "ds_read_b64_tr_b16 v[48:49], v8 offset:0\n"
    "v_mfma_f32_16x16x32_bf16 v[164:167], v[64:67], v[156:159], v[164:167]\n"
    "s_and_b32 s56, s56, 7\n"
    "ds_read_b64_tr_b16 v[50:51], v8 offset:4096\n"
    "v_add_u32 v9, s78, v9\n"
    "v_mfma_f32_16x16x32_bf16 v[168:171], v[68:71], v[152:155], v[168:171]\n"
    "s_lshl_b32 s69, s56, 12\n"
    "ds_read_b64_tr_b16 v[52:53], v9 offset:0\n"
    "v_mfma_f32_16x16x32_bf16 v[172:175], v[68:71], v[156:159], v[172:175]\n"
    "s_add_u32 s69, s69, s61\n"
    "ds_read_b64_tr_b16 v[54:55], v9 offset:4096\n"
    "v_add_u32 v10, s78, v10\n"
    "v_mfma_f32_16x16x32_bf16 v[176:179], v[72:75], v[152:155], v[176:179]\n"
    "s_add_u32 s56, s69, 0x0\n"
    "ds_read_b64_tr_b16 v[56:57], v10 offset:0\n"
    "v_mfma_f32_16x16x32_bf16 v[180:183], v[72:75], v[156:159], v[180:183]\n"
    "s_add_u32 s57, s70, 0x10000\n"
    "ds_read_b64_tr_b16 v[58:59], v10 offset:4096\n"
    "v_add_u32 v11, s78, v11\n"
    "v_mfma_f32_16x16x32_bf16 v[184:187], v[76:79], v[152:155], v[184:187]\n"
    "ds_read_b64_tr_b16 v[60:61], v11 offset:0\n"
    "buffer_load_dwordx4 v[144:147], v1, s[44:47], s56 offen sc1\n"
    "v_mfma_f32_16x16x32_bf16 v[188:191], v[76:79], v[156:159], v[188:191]\n"
    "s_waitcnt lgkmcnt(7)\n"
    "ds_read_b64_tr_b16 v[62:63], v11 offset:4096\n"
    "v_add_u32 v16, s78, v16\n"
    "v_mfma_f32_16x16x32_bf16 v[192:195], v[32:35], v[152:155], v[192:195]\n"
    "s_add_u32 s56, s69, 0x20000\n"
    "ds_read_b64_tr_b16 v[64:65], v16 offset:0\n"
    "v_mfma_f32_16x16x32_bf16 v[196:199], v[32:35], v[156:159], v[196:199]\n"
    "s_add_u32 m0, s66, 0\n"
    "ds_read_b64_tr_b16 v[66:67], v16 offset:4096\n"
    "v_add_u32 v17, s78, v17\n"
    "v_mfma_f32_16x16x32_bf16 v[200:203], v[36:39], v[152:155], v[200:203]\n"
    "ds_read_b64_tr_b16 v[68:69], v17 offset:0\n"
    "buffer_load_dwordx4 v[148:151], v1, s[44:47], s56 offen sc1\n"
    "v_mfma_f32_16x16x32_bf16 v[204:207], v[36:39], v[156:159], v[204:207]\n"
    "ds_read_b64_tr_b16 v[70:71], v17 offset:4096\n"
    "v_add_u32 v18, s78, v18\n"
    "v_mfma_f32_16x16x32_bf16 v[208:211], v[40:43], v[152:155], v[208:211]\n"
    "s_waitcnt vmcnt(24)\n"
    "ds_read_b64_tr_b16 v[72:73], v18 offset:0\n"
    "v_mfma_f32_16x16x32_bf16 v[212:215], v[40:43], v[156:159], v[212:215]\n"
    "ds_read_b64_tr_b16 v[74:75], v18 offset:4096\n"
    "v_add_u32 v19, s78, v19\n"
    "v_mfma_f32_16x16x32_bf16 v[216:219], v[44:47], v[152:155], v[216:219]\n"
    "ds_read_b64_tr_b16 v[76:77], v19 offset:0\n"
    "buffer_load_dwordx4 v28, s[40:43], s57 offen lds\n"
    "v_mfma_f32_16x16x32_bf16 v[220:223], v[44:47], v[156:159], v[220:223]\n"
    "s_waitcnt lgkmcnt(7)\n"
    "ds_read_b64_tr_b16 v[78:79], v19 offset:4096\n"
    "v_mfma_f32_16x16x32_bf16 v[160:163], v[48:51], v[96:99], v[160:163]\n"
    "s_add_u32 s56, s69, 0x400\n"
    "ds_read_b64_tr_b16 v[32:33], v8 offset:8192\n"
    "v_mfma_f32_16x16x32_bf16 v[164:167], v[48:51], v[100:103], v[164:167]\n"
    "ds_read_b64_tr_b16 v[34:35], v8 offset:12288\n"
    "buffer_load_dwordx4 v[152:155], v1, s[44:47], s56 offen sc1\n"
    "v_mfma_f32_16x16x32_bf16 v[168:171], v[52:55], v[96:99], v[168:171]\n"
    "s_add_u32 s56, s69, 0x20400\n"
    "ds_read_b64_tr_b16 v[36:37], v9 offset:8192\n"
    "v_mfma_f32_16x16x32_bf16 v[172:175], v[52:55], v[100:103], v[172:175]\n"
    "ds_read_b64_tr_b16 v[38:39], v9 offset:12288\n"
    "v_mfma_f32_16x16x32_bf16 v[176:179], v[56:59], v[96:99], v[176:179]\n"
    "ds_read_b64_tr_b16 v[40:41], v10 offset:8192\n"
    "buffer_load_dwordx4 v[156:159], v1, s[44:47], s56 offen sc1\n"
    "v_mfma_f32_16x16x32_bf16 v[180:183], v[56:59], v[100:103], v[180:183]\n"
    "ds_read_b64_tr_b16 v[42:43], v10 offset:12288\n"
    "v_mfma_f32_16x16x32_bf16 v[184:187], v[60:63], v[96:99], v[184:187]\n"
    "ds_read_b64_tr_b16 v[44:45], v11 offset:8192\n"
    "v_mfma_f32_16x16x32_bf16 v[188:191], v[60:63], v[100:103], v[188:191]\n"
    "s_waitcnt lgkmcnt(7)\n"
    "ds_read_b64_tr_b16 v[46:47], v11 offset:12288\n"
    "v_mfma_f32_16x16x32_bf16 v[192:195], v[64:67], v[96:99], v[192:195]\n"
    "ds_read_b64_tr_b16 v[48:49], v16 offset:8192\n"
    "v_mfma_f32_16x16x32_bf16 v[196:199], v[64:67], v[100:103], v[196:199]\n"
    "s_add_u32 m0, s66, 1024\n"
    "ds_read_b64_tr_b16 v[50:51], v16 offset:12288\n"
    "v_mfma_f32_16x16x32_bf16 v[200:203], v[68:71], v[96:99], v[200:203]\n"
    "ds_read_b64_tr_b16 v[52:53], v17 offset:8192\n"
    "v_mfma_f32_16x16x32_bf16 v[204:207], v[68:71], v[100:103], v[204:207]\n"
    "ds_read_b64_tr_b16 v[54:55], v17 offset:12288\n"
    "v_mfma_f32_16x16x32_bf16 v[208:211], v[72:75], v[96:99], v[208:211]\n"
    "s_waitcnt vmcnt(24)\n"
    "ds_read_b64_tr_b16 v[56:57], v18 offset:8192\n"
    "v_mfma_f32_16x16x32_bf16 v[212:215], v[72:75], v[100:103], v[212:215]\n"
    "ds_read_b64_tr_b16 v[58:59], v18 offset:12288\n"
    "buffer_load_dwordx4 v29, s[40:43], s57 offen lds\n"
    "v_mfma_f32_16x16x32_bf16 v[216:219], v[76:79], v[96:99], v[216:219]\n"
    "ds_read_b64_tr_b16 v[60:61], v19 offset:8192\n"
    "v_mfma_f32_16x16x32_bf16 v[220:223], v[76:79], v[100:103], v[220:223]\n"
    "s_waitcnt lgkmcnt(7)\n"
    "ds_read_b64_tr_b16 v[62:63], v19 offset:12288\n"
    "v_mfma_f32_16x16x32_bf16 v[160:163], v[32:35], v[104:107], v[160:163]\n"
    "s_add_u32 s56, s69, 0x800\n"
    "ds_read_b64_tr_b16 v[64:65], v8 offset:16384\n"
    "v_mfma_f32_16x16x32_bf16 v[164:167], v[32:35], v[108:111], v[164:167]\n"
    "ds_read_b64_tr_b16 v[66:67], v8 offset:20480\n"
    "buffer_load_dwordx4 v[96:99], v1, s[44:47], s56 offen sc1\n"
    "v_mfma_f32_16x16x32_bf16 v[168:171], v[36:39], v[104:107], v[168:171]\n"
    "s_add_u32 s56, s69, 0x20800\n"
    "ds_read_b64_tr_b16 v[68:69], v9 offset:16384\n"
    "v_mfma_f32_16x16x32_bf16 v[172:175], v[36:39], v[108:111], v[172:175]\n"
    "ds_read_b64_tr_b16 v[70:71], v9 offset:20480\n"
    "v_mfma_f32_16x16x32_bf16 v[176:179], v[40:43], v[104:107], v[176:179]\n"
    "ds_read_b64_tr_b16 v[72:73], v10 offset:16384\n"
    "buffer_load_dwordx4 v[100:103], v1, s[44:47], s56 offen sc1\n"
    "v_mfma_f32_16x16x32_bf16 v[180:183], v[40:43], v[108:111], v[180:183]\n"
    "ds_read_b64_tr_b16 v[74:75], v10 offset:20480\n"
    "v_mfma_f32_16x16x32_bf16 v[184:187], v[44:47], v[104:107], v[184:187]\n"
    "ds_read_b64_tr_b16 v[76:77], v11 offset:16384\n"
    "v_mfma_f32_16x16x32_bf16 v[188:191], v[44:47], v[108:111], v[188:191]\n"
    "s_waitcnt lgkmcnt(7)\n"
    "ds_read_b64_tr_b16 v[78:79], v11 offset:20480\n"
    "v_mfma_f32_16x16x32_bf16 v[192:195], v[48:51], v[104:107], v[192:195]\n"
    "ds_read_b64_tr_b16 v[32:33], v16 offset:16384\n"
    "v_mfma_f32_16x16x32_bf16 v[196:199], v[48:51], v[108:111], v[196:199]\n"
    "ds_read_b64_tr_b16 v[34:35], v16 offset:20480\n"
    "v_mfma_f32_16x16x32_bf16 v[200:203], v[52:55], v[104:107], v[200:203]\n"
    "ds_read_b64_tr_b16 v[36:37], v17 offset:16384\n"
    "v_mfma_f32_16x16x32_bf16 v[204:207], v[52:55], v[108:111], v[204:207]\n"
    "ds_read_b64_tr_b16 v[38:39], v17 offset:20480\n"
    "v_mfma_f32_16x16x32_bf16 v[208:211], v[56:59], v[104:107], v[208:211]\n"
    "s_waitcnt vmcnt(22)\n"
    "ds_read_b64_tr_b16 v[40:41], v18 offset:16384\n"
    "v_mfma_f32_16x16x32_bf16 v[212:215], v[56:59], v[108:111], v[212:215]\n"
    "ds_read_b64_tr_b16 v[42:43], v18 offset:20480\n"
    "v_mfma_f32_16x16x32_bf16 v[216:219], v[60:63], v[104:107], v[216:219]\n"
    "ds_read_b64_tr_b16 v[44:45], v19 offset:16384\n"
    "v_mfma_f32_16x16x32_bf16 v[220:223], v[60:63], v[108:111], v[220:223]\n"
    "s_waitcnt lgkmcnt(7)\n"
    "ds_read_b64_tr_b16 v[46:47], v19 offset:20480\n"
    "v_mfma_f32_16x16x32_bf16 v[160:163], v[64:67], v[112:115], v[160:163]\n"
    "ds_read_b64_tr_b16 v[48:49], v8 offset:24576\n"
    "buffer_load_dwordx2 v[80:81], v20, s[48:51], s73 offen offset:0\n"
    "v_mfma_f32_16x16x32_bf16 v[164:167], v[64:67], v[116:119], v[164:167]\n"
    "s_add_u32 s56, s69, 0xc00\n"
    "ds_read_b64_tr_b16 v[50:51], v8 offset:28672\n"
    "v_mfma_f32_16x16x32_bf16 v[168:171], v[68:71], v[112:115], v[168:171]\n"
    "s_add_u32 s57, s70, 0x18000\n"
    "ds_read_b64_tr_b16 v[52:53], v9 offset:24576\n"
    "v_mfma_f32_16x16x32_bf16 v[172:175], v[68:71], v[116:119], v[172:175]\n"
    "ds_read_b64_tr_b16 v[54:55], v9 offset:28672\n"
    "buffer_load_dwordx4 v[104:107], v1, s[44:47], s56 offen sc1\n"
    "v_mfma_f32_16x16x32_bf16 v[176:179], v[72:75], v[112:115], v[176:179]\n"
    "s_add_u32 s56, s69, 0x20c00\n"
    "ds_read_b64_tr_b16 v[56:57], v10 offset:24576\n"
    "v_mfma_f32_16x16x32_bf16 v[180:183], v[72:75], v[116:119], v[180:183]\n"
    "s_add_u32 m0, s66, 8192\n"
    "ds_read_b64_tr_b16 v[58:59], v10 offset:28672\n"
    "v_mfma_f32_16x16x32_bf16 v[184:187], v[76:79], v[112:115], v[184:187]\n"
    "ds_read_b64_tr_b16 v[60:61], v11 offset:24576\n"
    "buffer_load_dwordx4 v[108:111], v1, s[44:47], s56 offen sc1\n"
    "v_mfma_f32_16x16x32_bf16 v[188:191], v[76:79], v[116:119], v[188:191]\n"
    "s_waitcnt lgkmcnt(7)\n"
    "ds_read_b64_tr_b16 v[62:63], v11 offset:28672\n"
    "v_mfma_f32_16x16x32_bf16 v[192:195], v[32:35], v[112:115], v[192:195]\n"
    "ds_read_b64_tr_b16 v[64:65], v16 offset:24576\n"
    "v_mfma_f32_16x16x32_bf16 v[196:199], v[32:35], v[116:119], v[196:199]\n"
    "ds_read_b64_tr_b16 v[66:67], v16 offset:28672\n"
    "buffer_load_dwordx4 v28, s[40:43], s57 offen lds\n"
    "v_mfma_f32_16x16x32_bf16 v[200:203], v[36:39], v[112:115], v[200:203]\n"
    "ds_read_b64_tr_b16 v[68:69], v17 offset:24576\n"
    "v_mfma_f32_16x16x32_bf16 v[204:207], v[36:39], v[116:119], v[204:207]\n"
    "ds_read_b64_tr_b16 v[70:71], v17 offset:28672\n"
    "v_mfma_f32_16x16x32_bf16 v[208:211], v[40:43], v[112:115], v[208:211]\n"
    "s_waitcnt vmcnt(22)\n"
    "ds_read_b64_tr_b16 v[72:73], v18 offset:24576\n"
    "v_mfma_f32_16x16x32_bf16 v[212:215], v[40:43], v[116:119], v[212:215]\n"
    "ds_read_b64_tr_b16 v[74:75], v18 offset:28672\n"
    "v_mfma_f32_16x16x32_bf16 v[216:219], v[44:47], v[112:115], v[216:219]\n"
    "ds_read_b64_tr_b16 v[76:77], v19 offset:24576\n"
    "v_mfma_f32_16x16x32_bf16 v[220:223], v[44:47], v[116:119], v[220:223]\n"
    "s_waitcnt lgkmcnt(7)\n"
    "ds_read_b64_tr_b16 v[78:79], v19 offset:28672\n"
    "v_mfma_f32_16x16x32_bf16 v[160:163], v[48:51], v[120:123], v[160:163]\n"
    "ds_read_b64_tr_b16 v[32:33], v8 offset:32768\n"
    "buffer_load_dwordx2 v[82:83], v20, s[48:51], s72 offen offset:0\n"
    "v_mfma_f32_16x16x32_bf16 v[164:167], v[48:51], v[124:127], v[164:167]\n"
    "s_add_u32 s56, s53, 7\n"
    "ds_read_b64_tr_b16 v[34:35], v8 offset:36864\n"
    "v_mfma_f32_16x16x32_bf16 v[168:171], v[52:55], v[120:123], v[168:171]\n"
    "s_and_b32 s56, s56, 7\n"
    "ds_read_b64_tr_b16 v[36:37], v9 offset:32768\n"
    "v_mfma_f32_16x16x32_bf16 v[172:175], v[52:55], v[124:127], v[172:175]\n"
    "s_lshl_b32 s69, s56, 12\n"
    "ds_read_b64_tr_b16 v[38:39], v9 offset:36864\n"
    "v_mfma_f32_16x16x32_bf16 v[176:179], v[56:59], v[120:123], v[176:179]\n"
    "s_add_u32 s69, s69, s61\n"
    "ds_read_b64_tr_b16 v[40:41], v10 offset:32768\n"
    "v_mfma_f32_16x16x32_bf16 v[180:183], v[56:59], v[124:127], v[180:183]\n"
    "s_add_u32 s56, s69, 0x0\n"
    "ds_read_b64_tr_b16 v[42:43], v10 offset:36864\n"
    "v_mfma_f32_16x16x32_bf16 v[184:187], v[60:63], v[120:123], v[184:187]\n"
    "ds_read_b64_tr_b16 v[44:45], v11 offset:32768\n"
    "buffer_load_dwordx4 v[112:115], v1, s[44:47], s56 offen sc1\n"
    "v_mfma_f32_16x16x32_bf16 v[188:191], v[60:63], v[124:127], v[188:191]\n"
    "s_waitcnt lgkmcnt(7)\n"
    "ds_read_b64_tr_b16 v[46:47], v11 offset:36864\n"
    "v_mfma_f32_16x16x32_bf16 v[192:195], v[64:67], v[120:123], v[192:195]\n"
    "s_add_u32 s56, s69, 0x20000\n"
    "ds_read_b64_tr_b16 v[48:49], v16 offset:32768\n"
    "v_mfma_f32_16x16x32_bf16 v[196:199], v[64:67], v[124:127], v[196:199]\n"
    "ds_read_b64_tr_b16 v[50:51], v16 offset:36864\n"
    "buffer_load_dwordx4 v[116:119], v1, s[44:47], s56 offen sc1\n"
    "v_mfma_f32_16x16x32_bf16 v[200:203], v[68:71], v[120:123], v[200:203]\n"
    "s_add_u32 m0, s66, 9216\n"
    "ds_read_b64_tr_b16 v[52:53], v17 offset:32768\n"
    "v_mfma_f32_16x16x32_bf16 v[204:207], v[68:71], v[124:127], v[204:207]\n"
    "ds_read_b64_tr_b16 v[54:55], v17 offset:36864\n"
    "v_mfma_f32_16x16x32_bf16 v[208:211], v[72:75], v[120:123], v[208:211]\n"
    "s_waitcnt vmcnt(21)\n"
    "ds_read_b64_tr_b16 v[56:57], v18 offset:32768\n"
    "v_mfma_f32_16x16x32_bf16 v[212:215], v[72:75], v[124:127], v[212:215]\n"
    "ds_read_b64_tr_b16 v[58:59], v18 offset:36864\n"
    "buffer_load_dwordx4 v29, s[40:43], s57 offen lds\n"
    "v_mfma_f32_16x16x32_bf16 v[216:219], v[76:79], v[120:123], v[216:219]\n"
    "ds_read_b64_tr_b16 v[60:61], v19 offset:32768\n"
    "v_mfma_f32_16x16x32_bf16 v[220:223], v[76:79], v[124:127], v[220:223]\n"
    "s_waitcnt lgkmcnt(7)\n"
    "ds_read_b64_tr_b16 v[62:63], v19 offset:36864\n"
    "s_and_b32 s96, s53, 7\n"
    "s_lshl_b32 s96, s96, 3\n"
    "s_add_u32 s96, s96, s54\n"
    "s_lshl_b32 s96, s96, 2\n"
    "s_add_u32 s96, s96, s52\n"
    "s_lshl_b32 s96, s96, 4\n"
    "s_add_u32 s96, s96, 0x6000\n"
    "buffer_load_dword v26, v22, s[44:47], s96 offen sc1\n"
    "v_mfma_f32_16x16x32_bf16 v[160:163], v[32:35], v[128:131], v[160:163]\n"
    "ds_read_b64_tr_b16 v[64:65], v8 offset:40960\n"
    "buffer_load_dwordx2 v[84:85], v20, s[48:51], s73 offen offset:32\n"
    "v_mfma_f32_16x16x32_bf16 v[164:167], v[32:35], v[132:135], v[164:167]\n"
    "s_add_u32 s56, s69, 0x400\n"
    "ds_read_b64_tr_b16 v[66:67], v8 offset:45056\n"
    "v_mfma_f32_16x16x32_bf16 v[168:171], v[36:39], v[128:131], v[168:171]\n"
    "ds_read_b64_tr_b16 v[68:69], v9 offset:40960\n"
    "v_mfma_f32_16x16x32_bf16 v[172:175], v[36:39], v[132:135], v[172:175]\n"
    "ds_read_b64_tr_b16 v[70:71], v9 offset:45056\n"
    "buffer_load_dwordx4 v[120:123], v1, s[44:47], s56 offen sc1\n"
    "v_mfma_f32_16x16x32_bf16 v[176:179], v[40:43], v[128:131], v[176:179]\n"
    "s_add_u32 s56, s69, 0x20400\n"
    "ds_read_b64_tr_b16 v[72:73], v10 offset:40960\n"
    "v_mfma_f32_16x16x32_bf16 v[180:183], v[40:43], v[132:135], v[180:183]\n"
    "ds_read_b64_tr_b16 v[74:75], v10 offset:45056\n"
    "v_mfma_f32_16x16x32_bf16 v[184:187], v[44:47], v[128:131], v[184:187]\n"
    "ds_read_b64_tr_b16 v[76:77], v11 offset:40960\n"
    "buffer_load_dwordx4 v[124:127], v1, s[44:47], s56 offen sc1\n"
    "v_mfma_f32_16x16x32_bf16 v[188:191], v[44:47], v[132:135], v[188:191]\n"
    "s_waitcnt lgkmcnt(7)\n"
    "ds_read_b64_tr_b16 v[78:79], v11 offset:45056\n"
    "v_mfma_f32_16x16x32_bf16 v[192:195], v[48:51], v[128:131], v[192:195]\n"
    "ds_read_b64_tr_b16 v[32:33], v16 offset:40960\n"
    "v_mfma_f32_16x16x32_bf16 v[196:199], v[48:51], v[132:135], v[196:199]\n"
    "ds_read_b64_tr_b16 v[34:35], v16 offset:45056\n"
    "v_mfma_f32_16x16x32_bf16 v[200:203], v[52:55], v[128:131], v[200:203]\n"
    "ds_read_b64_tr_b16 v[36:37], v17 offset:40960\n"
    "v_mfma_f32_16x16x32_bf16 v[204:207], v[52:55], v[132:135], v[204:207]\n"
    "ds_read_b64_tr_b16 v[38:39], v17 offset:45056\n"
    "v_mfma_f32_16x16x32_bf16 v[208:211], v[56:59], v[128:131], v[208:211]\n"
    "s_waitcnt vmcnt(21)\n"
    "ds_read_b64_tr_b16 v[40:41], v18 offset:40960\n"
    "v_mfma_f32_16x16x32_bf16 v[212:215], v[56:59], v[132:135], v[212:215]\n"
    "ds_read_b64_tr_b16 v[42:43], v18 offset:45056\n"
    "v_mfma_f32_16x16x32_bf16 v[216:219], v[60:63], v[128:131], v[216:219]\n"
    "ds_read_b64_tr_b16 v[44:45], v19 offset:40960\n"
    "v_mfma_f32_16x16x32_bf16 v[220:223], v[60:63], v[132:135], v[220:223]\n"
    "s_waitcnt lgkmcnt(7)\n"
    "ds_read_b64_tr_b16 v[46:47], v19 offset:45056\n"
    "s_waitcnt vmcnt(20)\n"
    "s_barrier\n"
    "v_add_u32 v10, s76, v10\n"
    "v_mfma_f32_16x16x32_bf16 v[160:163], v[64:67], v[136:139], v[160:163]\n"
    "ds_read_b64_tr_b16 v[48:49], v10 offset:0\n"
    "buffer_load_dwordx2 v[86:87], v20, s[48:51], s72 offen offset:32\n"
    "v_mfma_f32_16x16x32_bf16 v[164:167], v[64:67], v[140:143], v[164:167]\n"
    "s_add_u32 s56, s69, 0x800\n"
    "ds_read_b64_tr_b16 v[50:51], v10 offset:4096\n"
    "v_add_u32 v11, s76, v11\n"
    "v_mfma_f32_16x16x32_bf16 v[168:171], v[68:71], v[136:139], v[168:171]\n"
    "ds_read_b64_tr_b16 v[52:53], v11 offset:0\n"
    "v_mfma_f32_16x16x32_bf16 v[172:175], v[68:71], v[140:143], v[172:175]\n"
    "ds_read_b64_tr_b16 v[54:55], v11 offset:4096\n"
    "v_add_u32 v16, s76, v16\n"
    "v_mfma_f32_16x16x32_bf16 v[176:179], v[72:75], v[136:139], v[176:179]\n"
    "ds_read_b64_tr_b16 v[56:57], v16 offset:0\n"
    "buffer_load_dwordx4 v[128:131], v1, s[44:47], s56 offen sc1\n"
    "v_mfma_f32_16x16x32_bf16 v[180:183], v[72:75], v[140:143], v[180:183]\n"
    "s_add_u32 s56, s69, 0x20800\n"
    "ds_read_b64_tr_b16 v[58:59], v16 offset:4096\n"
    "v_add_u32 v17, s76, v17\n"
    "v_mfma_f32_16x16x32_bf16 v[184:187], v[76:79], v[136:139], v[184:187]\n"
    "ds_read_b64_tr_b16 v[60:61], v17 offset:0\n"
    "v_mfma_f32_16x16x32_bf16 v[188:191], v[76:79], v[140:143], v[188:191]\n"
    "s_waitcnt lgkmcnt(7)\n"
    "ds_read_b64_tr_b16 v[62:63], v17 offset:4096\n"
    "v_add_u32 v18, s76, v18\n"
    "v_mfma_f32_16x16x32_bf16 v[192:195], v[32:35], v[136:139], v[192:195]\n"
    "ds_read_b64_tr_b16 v[64:65], v18 offset:0\n"
    "buffer_load_dwordx4 v[132:135], v1, s[44:47], s56 offen sc1\n"
    "v_mfma_f32_16x16x32_bf16 v[196:199], v[32:35], v[140:143], v[196:199]\n"
    "ds_read_b64_tr_b16 v[66:67], v18 offset:4096\n"
    "v_add_u32 v19, s76, v19\n"
    "v_mfma_f32_16x16x32_bf16 v[200:203], v[36:39], v[136:139], v[200:203]\n"
    "ds_read_b64_tr_b16 v[68:69], v19 offset:0\n"
    "v_mfma_f32_16x16x32_bf16 v[204:207], v[36:39], v[140:143], v[204:207]\n"
    "ds_read_b64_tr_b16 v[70:71], v19 offset:4096\n"
    "v_mfma_f32_16x16x32_bf16 v[208:211], v[40:43], v[136:139], v[208:211]\n"
    "s_waitcnt vmcnt(21)\n"
    "ds_read_b64_tr_b16 v[72:73], v10 offset:8192\n"
    "v_mfma_f32_16x16x32_bf16 v[212:215], v[40:43], v[140:143], v[212:215]\n"
    "ds_read_b64_tr_b16 v[74:75], v10 offset:12288\n"
    "v_mfma_f32_16x16x32_bf16 v[216:219], v[44:47], v[136:139], v[216:219]\n"
    "ds_read_b64_tr_b16 v[76:77], v11 offset:8192\n"
    "v_mfma_f32_16x16x32_bf16 v[220:223], v[44:47], v[140:143], v[220:223]\n"
    "s_waitcnt lgkmcnt(7)\n"
    "ds_read_b64_tr_b16 v[78:79], v11 offset:12288\n"
    "v_mfma_f32_16x16x32_bf16 v[176:179], v[48:51], v[144:147], v[176:179]\n"
    "s_add_u32 s56, s69, 0xc00\n"
    "ds_read_b64_tr_b16 v[32:33], v16 offset:8192\n"
    "v_mfma_f32_16x16x32_bf16 v[180:183], v[48:51], v[148:151], v[180:183]\n"
    "ds_read_b64_tr_b16 v[34:35], v16 offset:12288\n"
    "buffer_load_dwordx4 v[136:139], v1, s[44:47], s56 offen sc1\n"
    "v_mfma_f32_16x16x32_bf16 v[184:187], v[52:55], v[144:147], v[184:187]\n"
    "s_add_u32 s56, s69, 0x20c00\n"
    "ds_read_b64_tr_b16 v[36:37], v17 offset:8192\n"
    "v_mfma_f32_16x16x32_bf16 v[188:191], v[52:55], v[148:151], v[188:191]\n"
    "ds_read_b64_tr_b16 v[38:39], v17 offset:12288\n"
    "v_mfma_f32_16x16x32_bf16 v[192:195], v[56:59], v[144:147], v[192:195]\n"
    "ds_read_b64_tr_b16 v[40:41], v18 offset:8192\n"
    "buffer_load_dwordx4 v[140:143], v1, s[44:47], s56 offen sc1\n"
    "v_mfma_f32_16x16x32_bf16 v[196:199], v[56:59], v[148:151], v[196:199]\n"
    "ds_read_b64_tr_b16 v[42:43], v18 offset:12288\n"
    "v_mfma_f32_16x16x32_bf16 v[200:203], v[60:63], v[144:147], v[200:203]\n"
    "ds_read_b64_tr_b16 v[44:45], v19 offset:8192\n"
    "v_mfma_f32_16x16x32_bf16 v[204:207], v[60:63], v[148:151], v[204:207]\n"
    "s_waitcnt lgkmcnt(7)\n"
    "ds_read_b64_tr_b16 v[46:47], v19 offset:12288\n"
    "v_mfma_f32_16x16x32_bf16 v[208:211], v[64:67], v[144:147], v[208:211]\n"
    "s_waitcnt vmcnt(20)\n"
    "ds_read_b64_tr_b16 v[48:49], v10 offset:16384\n"
    "v_mfma_f32_16x16x32_bf16 v[212:215], v[64:67], v[148:151], v[212:215]\n"
    "ds_read_b64_tr_b16 v[50:51], v10 offset:20480\n"
    "v_mfma_f32_16x16x32_bf16 v[216:219], v[68:71], v[144:147], v[216:219]\n"
    "ds_read_b64_tr_b16 v[52:53], v11 offset:16384\n"
    "v_mfma_f32_16x16x32_bf16 v[220:223], v[68:71], v[148:151], v[220:223]\n"
    "ds_read_b64_tr_b16 v[54:55], v11 offset:20480\n"
    "v_mfma_f32_16x16x32_bf16 v[176:179], v[72:75], v[152:155], v[176:179]\n"
    "ds_read_b64_tr_b16 v[56:57], v16 offset:16384\n"
    "v_mfma_f32_16x16x32_bf16 v[180:183], v[72:75], v[156:159], v[180:183]\n"
    "ds_read_b64_tr_b16 v[58:59], v16 offset:20480\n"
    "v_mfma_f32_16x16x32_bf16 v[184:187], v[76:79], v[152:155], v[184:187]\n"
    "ds_read_b64_tr_b16 v[60:61], v17 offset:16384\n"
    "v_mfma_f32_16x16x32_bf16 v[188:191], v[76:79], v[156:159], v[188:191]\n"
    "s_waitcnt lgkmcnt(7)\n"
    "ds_read_b64_tr_b16 v[62:63], v17 offset:20480\n"
    "v_mfma_f32_16x16x32_bf16 v[192:195], v[32:35], v[152:155], v[192:195]\n"
    "ds_read_b64_tr_b16 v[64:65], v18 offset:16384\n"
    "v_mfma_f32_16x16x32_bf16 v[196:199], v[32:35], v[156:159], v[196:199]\n"
    "ds_read_b64_tr_b16 v[66:67], v18 offset:20480\n"
    "v_mfma_f32_16x16x32_bf16 v[200:203], v[36:39], v[152:155], v[200:203]\n"
    "ds_read_b64_tr_b16 v[68:69], v19 offset:16384\n"
    "v_mfma_f32_16x16x32_bf16 v[204:207], v[36:39], v[156:159], v[204:207]\n"
    "ds_read_b64_tr_b16 v[70:71], v19 offset:20480\n"
    "v_mfma_f32_16x16x32_bf16 v[208:211], v[40:43], v[152:155], v[208:211]\n"
    "s_waitcnt vmcnt(17)\n"
    "ds_read_b64_tr_b16 v[72:73], v10 offset:24576\n"
    "v_mfma_f32_16x16x32_bf16 v[212:215], v[40:43], v[156:159], v[212:215]\n"
    "ds_read_b64_tr_b16 v[74:75], v10 offset:28672\n"
    "v_mfma_f32_16x16x32_bf16 v[216:219], v[44:47], v[152:155], v[216:219]\n"
    "ds_read_b64_tr_b16 v[76:77], v11 offset:24576\n"
    "v_mfma_f32_16x16x32_bf16 v[220:223], v[44:47], v[156:159], v[220:223]\n"
    "s_waitcnt lgkmcnt(7)\n"
    "ds_read_b64_tr_b16 v[78:79], v11 offset:28672\n"
    "v_mfma_f32_16x16x32_bf16 v[176:179], v[48:51], v[96:99], v[176:179]\n"
    "ds_read_b64_tr_b16 v[32:33], v16 offset:24576\n"
    "v_mfma_f32_16x16x32_bf16 v[180:183], v[48:51], v[100:103], v[180:183]\n"
    "ds_read_b64_tr_b16 v[34:35], v16 offset:28672\n"
    "v_mfma_f32_16x16x32_bf16 v[184:187], v[52:55], v[96:99], v[184:187]\n"
    "ds_read_b64_tr_b16 v[36:37], v17 offset:24576\n"
    "v_mfma_f32_16x16x32_bf16 v[188:191], v[52:55], v[100:103], v[188:191]\n"
    "ds_read_b64_tr_b16 v[38:39], v17 offset:28672\n"
    "v_mfma_f32_16x16x32_bf16 v[192:195], v[56:59], v[96:99], v[192:195]\n"
    "ds_read_b64_tr_b16 v[40:41], v18 offset:24576\n"
    "v_mfma_f32_16x16x32_bf16 v[196:199], v[56:59], v[100:103], v[196:199]\n"
    "ds_read_b64_tr_b16 v[42:43], v18 offset:28672\n"
    "v_mfma_f32_16x16x32_bf16 v[200:203], v[60:63], v[96:99], v[200:203]\n"
    "ds_read_b64_tr_b16 v[44:45], v19 offset:24576\n"
    "v_mfma_f32_16x16x32_bf16 v[204:207], v[60:63], v[100:103], v[204:207]\n"
    "s_waitcnt lgkmcnt(7)\n"
    "ds_read_b64_tr_b16 v[46:47], v19 offset:28672\n"
    "v_mfma_f32_16x16x32_bf16 v[208:211], v[64:67], v[96:99], v[208:211]\n"
    "s_waitcnt vmcnt(14)\n"
    "ds_read_b64_tr_b16 v[48:49], v10 offset:32768\n"
    "v_mfma_f32_16x16x32_bf16 v[212:215], v[64:67], v[100:103], v[212:215]\n"
    "ds_read_b64_tr_b16 v[50:51], v10 offset:36864\n"
    "v_mfma_f32_16x16x32_bf16 v[216:219], v[68:71], v[96:99], v[216:219]\n"
    "ds_read_b64_tr_b16 v[52:53], v11 offset:32768\n"
    "v_mfma_f32_16x16x32_bf16 v[220:223], v[68:71], v[100:103], v[220:223]\n"
    "ds_read_b64_tr_b16 v[54:55], v11 offset:36864\n"
    "v_mfma_f32_16x16x32_bf16 v[176:179], v[72:75], v[104:107], v[176:179]\n"
    "ds_read_b64_tr_b16 v[56:57], v16 offset:32768\n"
    "v_mfma_f32_16x16x32_bf16 v[180:183], v[72:75], v[108:111], v[180:183]\n"
    "ds_read_b64_tr_b16 v[58:59], v16 offset:36864\n"
    "v_mfma_f32_16x16x32_bf16 v[184:187], v[76:79], v[104:107], v[184:187]\n"
    "ds_read_b64_tr_b16 v[60:61], v17 offset:32768\n"
    "v_mfma_f32_16x16x32_bf16 v[188:191], v[76:79], v[108:111], v[188:191]\n"
    "s_waitcnt lgkmcnt(7)\n"
    "ds_read_b64_tr_b16 v[62:63], v17 offset:36864\n"
    "v_mfma_f32_16x16x32_bf16 v[192:195], v[32:35], v[104:107], v[192:195]\n"
    "ds_read_b64_tr_b16 v[64:65], v18 offset:32768\n"
    "v_mfma_f32_16x16x32_bf16 v[196:199], v[32:35], v[108:111], v[196:199]\n"
    "ds_read_b64_tr_b16 v[66:67], v18 offset:36864\n"
    "v_mfma_f32_16x16x32_bf16 v[200:203], v[36:39], v[104:107], v[200:203]\n"
    "ds_read_b64_tr_b16 v[68:69], v19 offset:32768\n"
    "v_mfma_f32_16x16x32_bf16 v[204:207], v[36:39], v[108:111], v[204:207]\n"
    "ds_read_b64_tr_b16 v[70:71], v19 offset:36864\n"
    "v_mfma_f32_16x16x32_bf16 v[208:211], v[40:43], v[104:107], v[208:211]\n"
    "s_waitcnt vmcnt(10)\n"
    "ds_read_b64_tr_b16 v[72:73], v10 offset:40960\n"
    "v_mfma_f32_16x16x32_bf16 v[212:215], v[40:43], v[108:111], v[212:215]\n"
    "ds_read_b64_tr_b16 v[74:75], v10 offset:45056\n"
    "v_mfma_f32_16x16x32_bf16 v[216:219], v[44:47], v[104:107], v[216:219]\n"
    "ds_read_b64_tr_b16 v[76:77], v11 offset:40960\n"
    "v_mfma_f32_16x16x32_bf16 v[220:223], v[44:47], v[108:111], v[220:223]\n"
    "s_waitcnt lgkmcnt(7)\n"
    "ds_read_b64_tr_b16 v[78:79], v11 offset:45056\n"
    "v_mfma_f32_16x16x32_bf16 v[176:179], v[48:51], v[112:115], v[176:179]\n"
    "ds_read_b64_tr_b16 v[32:33], v16 offset:40960\n"
    "v_mfma_f32_16x16x32_bf16 v[180:183], v[48:51], v[116:119], v[180:183]\n"
    "ds_read_b64_tr_b16 v[34:35], v16 offset:45056\n"
    "v_mfma_f32_16x16x32_bf16 v[184:187], v[52:55], v[112:115], v[184:187]\n"
    "ds_read_b64_tr_b16 v[36:37], v17 offset:40960\n"
    "v_mfma_f32_16x16x32_bf16 v[188:191], v[52:55], v[116:119], v[188:191]\n"
    "ds_read_b64_tr_b16 v[38:39], v17 offset:45056\n"
    "v_mfma_f32_16x16x32_bf16 v[192:195], v[56:59], v[112:115], v[192:195]\n"
    "ds_read_b64_tr_b16 v[40:41], v18 offset:40960\n"
    "v_mfma_f32_16x16x32_bf16 v[196:199], v[56:59], v[116:119], v[196:199]\n"
    "ds_read_b64_tr_b16 v[42:43], v18 offset:45056\n"
    "v_mfma_f32_16x16x32_bf16 v[200:203], v[60:63], v[112:115], v[200:203]\n"
    "ds_read_b64_tr_b16 v[44:45], v19 offset:40960\n"
    "v_mfma_f32_16x16x32_bf16 v[204:207], v[60:63], v[116:119], v[204:207]\n"
    "s_waitcnt lgkmcnt(7)\n"
    "ds_read_b64_tr_b16 v[46:47], v19 offset:45056\n"
    "s_waitcnt vmcnt(9)\n"
    "s_barrier\n"
    "v_add_u32 v10, s77, v10\n"
    "v_mfma_f32_16x16x32_bf16 v[208:211], v[64:67], v[112:115], v[208:211]\n"
    "s_waitcnt vmcnt(5)\n"
    "ds_read_b64_tr_b16 v[48:49], v10 offset:0\n"
    "v_mfma_f32_16x16x32_bf16 v[212:215], v[64:67], v[116:119], v[212:215]\n"
    "ds_read_b64_tr_b16 v[50:51], v10 offset:4096\n"
    "v_add_u32 v11, s77, v11\n"
    "v_mfma_f32_16x16x32_bf16 v[216:219], v[68:71], v[112:115], v[216:219]\n"
    "ds_read_b64_tr_b16 v[52:53], v11 offset:0\n"
    "v_mfma_f32_16x16x32_bf16 v[220:223], v[68:71], v[116:119], v[220:223]\n"
    "ds_read_b64_tr_b16 v[54:55], v11 offset:4096\n"
    "v_add_u32 v16, s77, v16\n"
    "v_mfma_f32_16x16x32_bf16 v[176:179], v[72:75], v[120:123], v[176:179]\n"
    "ds_read_b64_tr_b16 v[56:57], v16 offset:0\n"
    "v_mfma_f32_16x16x32_bf16 v[180:183], v[72:75], v[124:127], v[180:183]\n"
    "ds_read_b64_tr_b16 v[58:59], v16 offset:4096\n"
    "v_add_u32 v17, s77, v17\n"
    "v_mfma_f32_16x16x32_bf16 v[184:187], v[76:79], v[120:123], v[184:187]\n"
    "ds_read_b64_tr_b16 v[60:61], v17 offset:0\n"
    "v_mfma_f32_16x16x32_bf16 v[188:191], v[76:79], v[124:127], v[188:191]\n"
    "s_waitcnt lgkmcnt(7)\n"
    "ds_read_b64_tr_b16 v[62:63], v17 offset:4096\n"
    "v_add_u32 v18, s77, v18\n"
    "v_mfma_f32_16x16x32_bf16 v[192:195], v[32:35], v[120:123], v[192:195]\n"
    "ds_read_b64_tr_b16 v[64:65], v18 offset:0\n"
    "v_mfma_f32_16x16x32_bf16 v[196:199], v[32:35], v[124:127], v[196:199]\n"
    "ds_read_b64_tr_b16 v[66:67], v18 offset:4096\n"
    "v_add_u32 v19, s77, v19\n"
    "v_mfma_f32_16x16x32_bf16 v[200:203], v[36:39], v[120:123], v[200:203]\n"
    "ds_read_b64_tr_b16 v[68:69], v19 offset:0\n"
    "v_mfma_f32_16x16x32_bf16 v[204:207], v[36:39], v[124:127], v[204:207]\n"
    "ds_read_b64_tr_b16 v[70:71], v19 offset:4096\n"
    "v_mfma_f32_16x16x32_bf16 v[208:211], v[40:43], v[120:123], v[208:211]\n"
    "s_waitcnt vmcnt(2)\n"
    "ds_read_b64_tr_b16 v[72:73], v10 offset:8192\n"
    "v_mfma_f32_16x16x32_bf16 v[212:215], v[40:43], v[124:127], v[212:215]\n"
    "ds_read_b64_tr_b16 v[74:75], v10 offset:12288\n"
    "v_mfma_f32_16x16x32_bf16 v[216:219], v[44:47], v[120:123], v[216:219]\n"
    "ds_read_b64_tr_b16 v[76:77], v11 offset:8192\n"
    "v_mfma_f32_16x16x32_bf16 v[220:223], v[44:47], v[124:127], v[220:223]\n"
    "s_waitcnt lgkmcnt(7)\n"
    "ds_read_b64_tr_b16 v[78:79], v11 offset:12288\n"
    "v_mfma_f32_16x16x32_bf16 v[176:179], v[48:51], v[128:131], v[176:179]\n"
    "ds_read_b64_tr_b16 v[32:33], v16 offset:8192\n"
    "v_mfma_f32_16x16x32_bf16 v[180:183], v[48:51], v[132:135], v[180:183]\n"
    "ds_read_b64_tr_b16 v[34:35], v16 offset:12288\n"
    "v_mfma_f32_16x16x32_bf16 v[184:187], v[52:55], v[128:131], v[184:187]\n"
    "ds_read_b64_tr_b16 v[36:37], v17 offset:8192\n"
    "v_mfma_f32_16x16x32_bf16 v[188:191], v[52:55], v[132:135], v[188:191]\n"
    "ds_read_b64_tr_b16 v[38:39], v17 offset:12288\n"
    "v_mfma_f32_16x16x32_bf16 v[192:195], v[56:59], v[128:131], v[192:195]\n"
    "ds_read_b64_tr_b16 v[40:41], v18 offset:8192\n"
    "v_mfma_f32_16x16x32_bf16 v[196:199], v[56:59], v[132:135], v[196:199]\n"
    "ds_read_b64_tr_b16 v[42:43], v18 offset:12288\n"
    "v_mfma_f32_16x16x32_bf16 v[200:203], v[60:63], v[128:131], v[200:203]\n"
    "ds_read_b64_tr_b16 v[44:45], v19 offset:8192\n"
    "v_mfma_f32_16x16x32_bf16 v[204:207], v[60:63], v[132:135], v[204:207]\n"
    "s_waitcnt lgkmcnt(7)\n"
    "ds_read_b64_tr_b16 v[46:47], v19 offset:12288\n"
    "v_add_u32 v8, s76, v8\n"
    "v_mfma_f32_16x16x32_bf16 v[208:211], v[64:67], v[128:131], v[208:211]\n"
    "s_waitcnt vmcnt(0)\n"
    "ds_read_b64_tr_b16 v[48:49], v8 offset:0\n"
    "v_mfma_f32_16x16x32_bf16 v[212:215], v[64:67], v[132:135], v[212:215]\n"
    "ds_read_b64_tr_b16 v[50:51], v8 offset:4096\n"
    "v_add_u32 v9, s76, v9\n"
    "v_mfma_f32_16x16x32_bf16 v[216:219], v[68:71], v[128:131], v[216:219]\n"
    "ds_read_b64_tr_b16 v[52:53], v9 offset:0\n"
    "v_mfma_f32_16x16x32_bf16 v[220:223], v[68:71], v[132:135], v[220:223]\n"
    "ds_read_b64_tr_b16 v[54:55], v9 offset:4096\n"
    "v_mfma_f32_16x16x32_bf16 v[176:179], v[72:75], v[136:139], v[176:179]\n"
    "ds_read_b64_tr_b16 v[56:57], v8 offset:8192\n"
    "v_mfma_f32_16x16x32_bf16 v[180:183], v[72:75], v[140:143], v[180:183]\n"
    "ds_read_b64_tr_b16 v[58:59], v8 offset:12288\n"
    "v_mfma_f32_16x16x32_bf16 v[184:187], v[76:79], v[136:139], v[184:187]\n"
    "ds_read_b64_tr_b16 v[60:61], v9 offset:8192\n"
    "v_mfma_f32_16x16x32_bf16 v[188:191], v[76:79], v[140:143], v[188:191]\n"
    "s_waitcnt lgkmcnt(7)\n"
    "ds_read_b64_tr_b16 v[62:63], v9 offset:12288\n"
    "v_mfma_f32_16x16x32_bf16 v[192:195], v[32:35], v[136:139], v[192:195]\n"
    "ds_read_b64_tr_b16 v[64:65], v8 offset:16384\n"
    "v_mfma_f32_16x16x32_bf16 v[196:199], v[32:35], v[140:143], v[196:199]\n"
    "ds_read_b64_tr_b16 v[66:67], v8 offset:20480\n"
    "v_mfma_f32_16x16x32_bf16 v[200:203], v[36:39], v[136:139], v[200:203]\n"
    "ds_read_b64_tr_b16 v[68:69], v9 offset:16384\n"
    "v_mfma_f32_16x16x32_bf16 v[204:207], v[36:39], v[140:143], v[204:207]\n"
    "ds_read_b64_tr_b16 v[70:71], v9 offset:20480\n"
    "v_cmp_eq_u32 vcc, s97, v26\n"
    "s_nop 0\n"
    "s_cmp_eq_u64 vcc, -1\n"
    "s_cbranch_scc1 L_pok_19_%=\n"
    "s_and_b32 s96, s53, 7\n"
    "s_lshl_b32 s96, s96, 3\n"
    "s_add_u32 s96, s96, s54\n"
    "s_lshl_b32 s96, s96, 2\n"
    "s_add_u32 s96, s96, s52\n"
    "s_lshl_b32 s96, s96, 4\n"
    "s_add_u32 s96, s96, 0x6000\n"
    "s_mov_b32 s94, 2000000\n"
    "L_psp_20_%=:\n"
    "s_sleep 2\n"
    "buffer_load_dword v26, v22, s[44:47], s96 offen sc1\n"
    "s_waitcnt vmcnt(0)\n"
    "v_cmp_eq_u32 vcc, s97, v26\n"
    "s_nop 4\n"
    "s_cmp_eq_u64 vcc, -1\n"
    "s_cbranch_scc1 L_pok_19_%=\n"
    "s_sub_u32 s94, s94, 1\n"
    "s_cmp_lg_u32 s94, 0\n"
    "s_cbranch_scc1 L_psp_20_%=\n"
    "L_pok_19_%=:\n"
    "v_mfma_f32_16x16x32_bf16 v[208:211], v[40:43], v[136:139], v[208:211]\n"
    "ds_read_b64_tr_b16 v[72:73], v8 offset:24576\n"
    "v_mfma_f32_16x16x32_bf16 v[212:215], v[40:43], v[140:143], v[212:215]\n"
    "ds_read_b64_tr_b16 v[74:75], v8 offset:28672\n"
    "v_mfma_f32_16x16x32_bf16 v[216:219], v[44:47], v[136:139], v[216:219]\n"
    "ds_read_b64_tr_b16 v[76:77], v9 offset:24576\n"
    "v_cvt_pk_bf16_f32 v224, v176, v177\n"
    "v_cvt_pk_bf16_f32 v225, v178, v179\n"
    "v_mfma_f32_16x16x32_bf16 v[220:223], v[44:47], v[140:143], v[220:223]\n"
    "s_waitcnt lgkmcnt(7)\n"
    "ds_read_b64_tr_b16 v[78:79], v9 offset:28672\n"
    "v_cvt_pk_bf16_f32 v226, v180, v181\n"
    "v_cvt_pk_bf16_f32 v227, v182, v183\n"
    "v_mfma_f32_16x16x32_bf16 v[160:163], v[48:51], v[144:147], v[160:163]\n"
    "ds_read_b64_tr_b16 v[32:33], v8 offset:32768\n"
    "v_min_u32 v224, -2, v224\n"
    "v_min_u32 v225, -2, v225\n"
    "v_min_u32 v226, -2, v226\n"
    "v_mfma_f32_16x16x32_bf16 v[164:167], v[48:51], v[148:151], v[164:167]\n"
    "ds_read_b64_tr_b16 v[34:35], v8 offset:36864\n"
    "v_min_u32 v227, -2, v227\n"
    "v_mfma_f32_16x16x32_bf16 v[168:171], v[52:55], v[144:147], v[168:171]\n"
    "ds_read_b64_tr_b16 v[36:37], v9 offset:32768\n"
    "buffer_store_dwordx4 v[224:227], v1, s[44:47], s81 offen offset:0 sc1\n"
    "v_mfma_f32_16x16x32_bf16 v[172:175], v[52:55], v[148:151], v[172:175]\n"
    "ds_read_b64_tr_b16 v[38:39], v9 offset:36864\n"
    "v_cvt_pk_bf16_f32 v228, v184, v185\n"
    "v_mfma_f32_16x16x32_bf16 v[160:163], v[56:59], v[152:155], v[160:163]\n"
    "ds_read_b64_tr_b16 v[40:41], v8 offset:40960\n"
    "v_cvt_pk_bf16_f32 v229, v186, v187\n"
    "v_cvt_pk_bf16_f32 v230, v188, v189\n"
    "v_mfma_f32_16x16x32_bf16 v[164:167], v[56:59], v[156:159], v[164:167]\n"
    "ds_read_b64_tr_b16 v[42:43], v8 offset:45056\n"
    "v_cvt_pk_bf16_f32 v231, v190, v191\n"
    "v_min_u32 v228, -2, v228\n"
    "v_mfma_f32_16x16x32_bf16 v[168:171], v[60:63], v[152:155], v[168:171]\n"
    "ds_read_b64_tr_b16 v[44:45], v9 offset:40960\n"
    "v_min_u32 v229, -2, v229\n"
    "v_min_u32 v230, -2, v230\n"
    "v_min_u32 v231, -2, v231\n"
    "v_mfma_f32_16x16x32_bf16 v[172:175], v[60:63], v[156:159], v[172:175]\n"
    "s_waitcnt lgkmcnt(7)\n"
    "ds_read_b64_tr_b16 v[46:47], v9 offset:45056\n"
    "v_add_u32 v8, s77, v8\n"
    "v_mfma_f32_16x16x32_bf16 v[160:163], v[64:67], v[96:99], v[160:163]\n"
    "ds_read_b64_tr_b16 v[48:49], v8 offset:0\n"
    "buffer_store_dwordx4 v[228:231], v1, s[44:47], s81 offen offset:1024 sc1\n"
    "v_mfma_f32_16x16x32_bf16 v[164:167], v[64:67], v[100:103], v[164:167]\n"
    "ds_read_b64_tr_b16 v[50:51], v8 offset:4096\n"
    "v_add_u32 v9, s77, v9\n"
    "v_cvt_pk_bf16_f32 v232, v192, v193\n"
    "v_mfma_f32_16x16x32_bf16 v[168:171], v[68:71], v[96:99], v[168:171]\n"
    "ds_read_b64_tr_b16 v[52:53], v9 offset:0\n"
    "v_cvt_pk_bf16_f32 v233, v194, v195\n"
    "v_cvt_pk_bf16_f32 v234, v196, v197\n"
    "v_cvt_pk_bf16_f32 v235, v198, v199\n"
    "v_mfma_f32_16x16x32_bf16 v[172:175], v[68:71], v[100:103], v[172:175]\n"
    "ds_read_b64_tr_b16 v[54:55], v9 offset:4096\n"
    "v_min_u32 v232, -2, v232\n"
    "v_min_u32 v233, -2, v233\n"
    "v_mfma_f32_16x16x32_bf16 v[160:163], v[72:75], v[104:107], v[160:163]\n"
    "ds_read_b64_tr_b16 v[56:57], v8 offset:8192\n"
    "v_min_u32 v234, -2, v234\n"
    "v_min_u32 v235, -2, v235\n"
    "v_mfma_f32_16x16x32_bf16 v[164:167], v[72:75], v[108:111], v[164:167]\n"
    "ds_read_b64_tr_b16 v[58:59], v8 offset:12288\n"
    "buffer_store_dwordx4 v[232:235], v1, s[44:47], s82 offen offset:0 sc1\n"
    "v_mfma_f32_16x16x32_bf16 v[168:171], v[76:79], v[104:107], v[168:171]\n"
    "ds_read_b64_tr_b16 v[60:61], v9 offset:8192\n"
    "v_cvt_pk_bf16_f32 v236, v200, v201\n"
    "v_mfma_f32_16x16x32_bf16 v[172:175], v[76:79], v[108:111], v[172:175]\n"
    "s_waitcnt lgkmcnt(7)\n"
    "ds_read_b64_tr_b16 v[62:63], v9 offset:12288\n"
    "v_cvt_pk_bf16_f32 v237, v202, v203\n"
    "v_mfma_f32_16x16x32_bf16 v[160:163], v[32:35], v[112:115], v[160:163]\n"
    "v_cvt_pk_bf16_f32 v238, v204, v205\n"
    "v_cvt_pk_bf16_f32 v239, v206, v207\n"
    "v_mfma_f32_16x16x32_bf16 v[164:167], v[32:35], v[116:119], v[164:167]\n"
    "v_min_u32 v236, -2, v236\n"
    "v_min_u32 v237, -2, v237\n"
    "v_min_u32 v238, -2, v238\n"
    "v_mfma_f32_16x16x32_bf16 v[168:171], v[36:39], v[112:115], v[168:171]\n"
    "v_min_u32 v239, -2, v239\n"
    "v_mfma_f32_16x16x32_bf16 v[172:175], v[36:39], v[116:119], v[172:175]\n"
    "buffer_store_dwordx4 v[236:239], v1, s[44:47], s82 offen offset:1024 sc1\n"
    "v_cvt_pk_bf16_f32 v240, v208, v209\n"
    "v_mfma_f32_16x16x32_bf16 v[160:163], v[40:43], v[120:123], v[160:163]\n"
    "v_cvt_pk_bf16_f32 v241, v210, v211\n"
    "v_cvt_pk_bf16_f32 v242, v212, v213\n"
    "v_mfma_f32_16x16x32_bf16 v[164:167], v[40:43], v[124:127], v[164:167]\n"
    "v_cvt_pk_bf16_f32 v243, v214, v215\n"
    "v_min_u32 v240, -2, v240\n"
    "v_mfma_f32_16x16x32_bf16 v[168:171], v[44:47], v[120:123], v[168:171]\n"
    "v_min_u32 v241, -2, v241\n"
    "v_mfma_f32_16x16x32_bf16 v[172:175], v[44:47], v[124:127], v[172:175]\n"
    "s_waitcnt lgkmcnt(0)\n"
    "v_min_u32 v242, -2, v242\n"
    "v_min_u32 v243, -2, v243\n"
    "v_mfma_f32_16x16x32_bf16 v[160:163], v[48:51], v[128:131], v[160:163]\n"
    "buffer_store_dwordx4 v[240:243], v1, s[44:47], s83 offen offset:0 sc1\n"
    "v_cvt_pk_bf16_f32 v244, v216, v217\n"
    "v_mfma_f32_16x16x32_bf16 v[164:167], v[48:51], v[132:135], v[164:167]\n"
    "v_cvt_pk_bf16_f32 v245, v218, v219\n"
    "v_cvt_pk_bf16_f32 v246, v220, v221\n"
    "v_mfma_f32_16x16x32_bf16 v[168:171], v[52:55], v[128:131], v[168:171]\n"
    "v_cvt_pk_bf16_f32 v247, v222, v223\n"
    "v_min_u32 v244, -2, v244\n"
    "v_mfma_f32_16x16x32_bf16 v[172:175], v[52:55], v[132:135], v[172:175]\n"
    "v_min_u32 v245, -2, v245\n"
    "v_mfma_f32_16x16x32_bf16 v[160:163], v[56:59], v[136:139], v[160:163]\n"
    "v_min_u32 v246, -2, v246\n"
    "v_min_u32 v247, -2, v247\n"
    "v_mfma_f32_16x16x32_bf16 v[164:167], v[56:59], v[140:143], v[164:167]\n"
    "buffer_store_dwordx4 v[244:247], v1, s[44:47], s83 offen offset:1024 sc1\n"
    "v_mfma_f32_16x16x32_bf16 v[168:171], v[60:63], v[136:139], v[168:171]\n"
    "v_mfma_f32_16x16x32_bf16 v[172:175], v[60:63], v[140:143], v[172:175]\n"
    "s_lshl_b32 s57, s75, 13\n"
    "s_add_u32 s74, s74, s57\n"
    "s_add_u32 s74, s74, 2048\n"
    "s_add_u32 s75, s74, 4096\n"
    "buffer_load_dwordx4 v[32:35], v1, s[44:47], s74 offen offset:0 sc1\n"
    "buffer_load_dwordx4 v[36:39], v1, s[44:47], s74 offen offset:1024 sc1\n"
    "buffer_load_dwordx4 v[40:43], v1, s[44:47], s74 offen offset:2048 sc1\n"
    "buffer_load_dwordx4 v[44:47], v1, s[44:47], s74 offen offset:3072 sc1\n"
    "buffer_load_dwordx4 v[48:51], v1, s[44:47], s75 offen offset:0 sc1\n"
    "buffer_load_dwordx4 v[52:55], v1, s[44:47], s75 offen offset:1024 sc1\n"
    "v_lshlrev_b32 v22, 16, v80\n"
    "v_and_b32 v23, 0xffff0000, v80\n"
    "v_add_f32 v160, v160, v22\n"
    "v_add_f32 v161, v161, v23\n"
    "v_lshlrev_b32 v22, 16, v81\n"
    "v_and_b32 v23, 0xffff0000, v81\n"
    "v_add_f32 v162, v162, v22\n"
    "v_add_f32 v163, v163, v23\n"
    "v_lshlrev_b32 v22, 16, v82\n"
    "v_and_b32 v23, 0xffff0000, v82\n"
    "v_add_f32 v164, v164, v22\n"
    "v_add_f32 v165, v165, v23\n"
    "v_lshlrev_b32 v22, 16, v83\n"
    "v_and_b32 v23, 0xffff0000, v83\n"
    "v_add_f32 v166, v166, v22\n"
    "v_add_f32 v167, v167, v23\n"
    "v_lshlrev_b32 v22, 16, v84\n"
    "v_and_b32 v23, 0xffff0000, v84\n"
    "v_add_f32 v168, v168, v22\n"
    "v_add_f32 v169, v169, v23\n"
    "v_lshlrev_b32 v22, 16, v85\n"
    "v_and_b32 v23, 0xffff0000, v85\n"
    "v_add_f32 v170, v170, v22\n"
    "v_add_f32 v171, v171, v23\n"
    "v_lshlrev_b32 v22, 16, v86\n"
    "v_and_b32 v23, 0xffff0000, v86\n"
    "v_add_f32 v172, v172, v22\n"
    "v_add_f32 v173, v173, v23\n"
    "v_lshlrev_b32 v22, 16, v87\n"
    "v_and_b32 v23, 0xffff0000, v87\n"
    "v_add_f32 v174, v174, v22\n"
    "v_add_f32 v175, v175, v23\n"
    "s_load_dwordx2 s[48:49], %11, 0x48\n"
    "s_waitcnt lgkmcnt(0)\n"
    "s_mov_b64 s[48:49], s[48:49]\n"
    "s_and_b32 s49, s49, 0xffff\n"
    "s_mov_b32 s50, 0x200000\n"
    "s_mov_b32 s51, 0x20000\n"
    "s_waitcnt vmcnt(0)\n"
    "v_max3_u32 v22, v32, v33, v34\n"
    "v_max3_u32 v23, v35, v36, v37\n"
    "v_max3_u32 v23, v23, v38, v39\n"
    "v_max3_u32 v22, v22, v40, v41\n"
    "v_max3_u32 v23, v23, v42, v43\n"
    "v_max3_u32 v22, v22, v44, v45\n"
    "v_max3_u32 v23, v23, v46, v47\n"
    "v_max3_u32 v22, v22, v48, v49\n"
    "v_max3_u32 v23, v23, v50, v51\n"
    "v_max3_u32 v22, v22, v52, v53\n"
    "v_max3_u32 v23, v23, v54, v55\n"
    "v_max_u32 v22, v22, v23\n"
    "v_cmp_eq_u32 vcc, -1, v22\n"
    "s_nop 0\n"
    "s_cmp_eq_u64 vcc, 0\n"
    "s_cbranch_scc1 L_xok_21_%=\n"
    "s_mov_b32 s94, 2000000\n"
    "L_xsp_22_%=:\n"
    "s_sleep 1\n"
    "buffer_load_dwordx4 v[32:35], v1, s[44:47], s74 offen offset:0 sc1\n"
    "buffer_load_dwordx4 v[36:39], v1, s[44:47], s74 offen offset:1024 sc1\n"
    "buffer_load_dwordx4 v[40:43], v1, s[44:47], s74 offen offset:2048 sc1\n"
    "buffer_load_dwordx4 v[44:47], v1, s[44:47], s74 offen offset:3072 sc1\n"
    "buffer_load_dwordx4 v[48:51], v1, s[44:47], s75 offen offset:0 sc1\n"
    "buffer_load_dwordx4 v[52:55], v1, s[44:47], s75 offen offset:1024 sc1\n"
    "s_waitcnt vmcnt(0)\n"
    "v_max3_u32 v22, v32, v33, v34\n"
    "v_max3_u32 v23, v35, v36, v37\n"
    "v_max3_u32 v23, v23, v38, v39\n"
    "v_max3_u32 v22, v22, v40, v41\n"
    "v_max3_u32 v23, v23, v42, v43\n"
    "v_max3_u32 v22, v22, v44, v45\n"
    "v_max3_u32 v23, v23, v46, v47\n"
    "v_max3_u32 v22, v22, v48, v49\n"
    "v_max3_u32 v23, v23, v50, v51\n"
    "v_max3_u32 v22, v22, v52, v53\n"
    "v_max3_u32 v23, v23, v54, v55\n"
    "v_max_u32 v22, v22, v23\n"
    "v_cmp_eq_u32 vcc, -1, v22\n"
    "s_nop 4\n"
    "s_cmp_eq_u64 vcc, 0\n"
    "s_cbranch_scc1 L_xok_21_%=\n"
    "s_sub_u32 s94, s94, 1\n"
    "s_cmp_lg_u32 s94, 0\n"
    "s_cbranch_scc1 L_xsp_22_%=\n"
    "L_xok_21_%=:\n"
    "s_or_b32 s56, %9, s52\n"
    "s_cmp_eq_u32 s56, 0\n"
    "s_cbranch_scc0 L_noep_23_%=\n"
    "s_add_u32 s56, s99, 1\n"
    "v_mov_b32 v22, s56\n"
    "s_mov_b32 s56, 0\n"
    "buffer_store_dword v22, v7, s[44:47], s56 offen sc1\n"
    "L_noep_23_%=:\n"
    "v_lshlrev_b32 v56, 16, v32\n"
    "v_and_b32 v57, 0xffff0000, v32\n"
    "v_pk_add_f32 v[160:161], v[160:161], v[56:57]\n"
    "v_lshlrev_b32 v58, 16, v33\n"
    "v_and_b32 v59, 0xffff0000, v33\n"
    "v_pk_add_f32 v[162:163], v[162:163], v[58:59]\n"
    "v_lshlrev_b32 v60, 16, v40\n"
    "v_and_b32 v61, 0xffff0000, v40\n"
    "v_pk_add_f32 v[160:161], v[160:161], v[60:61]\n"
    "v_lshlrev_b32 v62, 16, v41\n"
    "v_and_b32 v63, 0xffff0000, v41\n"
    "v_pk_add_f32 v[162:163], v[162:163], v[62:63]\n"
    "v_lshlrev_b32 v56, 16, v48\n"
    "v_and_b32 v57, 0xffff0000, v48\n"
    "v_pk_add_f32 v[160:161], v[160:161], v[56:57]\n"
    "v_lshlrev_b32 v58, 16, v49\n"
    "v_and_b32 v59, 0xffff0000, v49\n"
    "v_pk_add_f32 v[162:163], v[162:163], v[58:59]\n"
    "v_cvt_pk_bf16_f32 v120, v160, v161\n"
    "v_cvt_pk_bf16_f32 v121, v162, v163\n"
    "buffer_store_dwordx2 v[120:121], v20, s[48:51], s73 offen offset:0\n"
    "v_lshlrev_b32 v56, 16, v34\n"
    "v_and_b32 v57, 0xffff0000, v34\n"
    "v_pk_add_f32 v[164:165], v[164:165], v[56:57]\n"
    "v_lshlrev_b32 v58, 16, v35\n"
    "v_and_b32 v59, 0xffff0000, v35\n"
    "v_pk_add_f32 v[166:167], v[166:167], v[58:59]\n"
    "v_lshlrev_b32 v60, 16, v42\n"
    "v_and_b32 v61, 0xffff0000, v42\n"
    "v_pk_add_f32 v[164:165], v[164:165], v[60:61]\n"
    "v_lshlrev_b32 v62, 16, v43\n"
    "v_and_b32 v63, 0xffff0000, v43\n"
    "v_pk_add_f32 v[166:167], v[166:167], v[62:63]\n"
    "v_lshlrev_b32 v56, 16, v50\n"
    "v_and_b32 v57, 0xffff0000, v50\n"
    "v_pk_add_f32 v[164:165], v[164:165], v[56:57]\n"
    "v_lshlrev_b32 v58, 16, v51\n"
    "v_and_b32 v59, 0xffff0000, v51\n"
    "v_pk_add_f32 v[166:167], v[166:167], v[58:59]\n"
    "v_cvt_pk_bf16_f32 v122, v164, v165\n"
    "v_cvt_pk_bf16_f32 v123, v166, v167\n"
    "buffer_store_dwordx2 v[122:123], v20, s[48:51], s72 offen offset:0\n"
    "v_lshlrev_b32 v56, 16, v36\n"
    "v_and_b32 v57, 0xffff0000, v36\n"
    "v_pk_add_f32 v[168:169], v[168:169], v[56:57]\n"
    "v_lshlrev_b32 v58, 16, v37\n"
    "v_and_b32 v59, 0xffff0000, v37\n"
    "v_pk_add_f32 v[170:171], v[170:171], v[58:59]\n"
    "v_lshlrev_b32 v60, 16, v44\n"
    "v_and_b32 v61, 0xffff0000, v44\n"
    "v_pk_add_f32 v[168:169], v[168:169], v[60:61]\n"
    "v_lshlrev_b32 v62, 16, v45\n"
    "v_and_b32 v63, 0xffff0000, v45\n"
    "v_pk_add_f32 v[170:171], v[170:171], v[62:63]\n"
    "v_lshlrev_b32 v56, 16, v52\n"
    "v_and_b32 v57, 0xffff0000, v52\n"
    "v_pk_add_f32 v[168:169], v[168:169], v[56:57]\n"
    "v_lshlrev_b32 v58, 16, v53\n"
    "v_and_b32 v59, 0xffff0000, v53\n"
    "v_pk_add_f32 v[170:171], v[170:171], v[58:59]\n"
    "v_cvt_pk_bf16_f32 v124, v168, v169\n"
    "v_cvt_pk_bf16_f32 v125, v170, v171\n"
    "buffer_store_dwordx2 v[124:125], v20, s[48:51], s73 offen offset:32\n"
    "v_lshlrev_b32 v56, 16, v38\n"
    "v_and_b32 v57, 0xffff0000, v38\n"
    "v_pk_add_f32 v[172:173], v[172:173], v[56:57]\n"
    "v_lshlrev_b32 v58, 16, v39\n"
    "v_and_b32 v59, 0xffff0000, v39\n"
    "v_pk_add_f32 v[174:175], v[174:175], v[58:59]\n"
    "v_lshlrev_b32 v60, 16, v46\n"
    "v_and_b32 v61, 0xffff0000, v46\n"
    "v_pk_add_f32 v[172:173], v[172:173], v[60:61]\n"
    "v_lshlrev_b32 v62, 16, v47\n"
    "v_and_b32 v63, 0xffff0000, v47\n"
    "v_pk_add_f32 v[174:175], v[174:175], v[62:63]\n"
    "v_lshlrev_b32 v56, 16, v54\n"
    "v_and_b32 v57, 0xffff0000, v54\n"
    "v_pk_add_f32 v[172:173], v[172:173], v[56:57]\n"
    "v_lshlrev_b32 v58, 16, v55\n"
    "v_and_b32 v59, 0xffff0000, v55\n"
    "v_pk_add_f32 v[174:175], v[174:175], v[58:59]\n"
    "v_cvt_pk_bf16_f32 v126, v172, v173\n"
    "v_cvt_pk_bf16_f32 v127, v174, v175\n"
    "buffer_store_dwordx2 v[126:127], v20, s[48:51], s72 offen offset:32\n"
    "L_endA_16_%=:\n"
    "s_waitcnt vmcnt(0)\n"
    ".Lkm_end:\n"
   : "=&s"(bad),"=&s"(epoch) : "s"(ckv),"s"(wuk),"s"(wuv),"s"(wg),"s"(wu),"s"(wq),"s"(ws),"s"(blockIdx.x),"s"(x),"s"(ka),"v"(threadIdx.x) : "v1", "v2", "v3", "v4", "v5", "v6", "v7", "v8", "v9", "v10", "v11", "v12", "v13", "v14", "v15", "v16", "v17", "v18", "v19", "v20", "v21", "v22", "v23", "v24", "v25", "v26", "v27", "v28", "v29", "v30", "v31", "v32", "v33", "v34", "v35", "v36", "v37", "v38", "v39", "v40", "v41", "v42", "v43", "v44", "v45", "v46", "v47", "v48", "v49", "v50", "v51", "v52", "v53", "v54", "v55", "v56", "v57", "v58", "v59", "v60", "v61", "v62", "v63", "v64", "v65", "v66", "v67", "v68", "v69", "v70", "v71", "v72", "v73", "v74", "v75", "v76", "v77", "v78", "v79", "v80", "v81", "v82", "v83", "v84", "v85", "v86", "v87", "v88", "v89", "v90", "v91", "v92", "v93", "v94", "v95", "v96", "v97", "v98", "v99", "v100", "v101", "v102", "v103", "v104", "v105", "v106", "v107", "v108", "v109", "v110", "v111", "v112", "v113", "v114", "v115", "v116", "v117", "v118", "v119", "v120", "v121", "v122", "v123", "v124", "v125", "v126", "v127", "v128", "v129", "v130", "v131", "v132", "v133", "v134", "v135", "v136", "v137", "v138", "v139", "v140", "v141", "v142", "v143", "v144", "v145", "v146", "v147", "v148", "v149", "v150", "v151", "v152", "v153", "v154", "v155", "v156", "v157", "v158", "v159", "v160", "v161", "v162", "v163", "v164", "v165", "v166", "v167", "v168", "v169", "v170", "v171", "v172", "v173", "v174", "v175", "v176", "v177", "v178", "v179", "v180", "v181", "v182", "v183", "v184", "v185", "v186", "v187", "v188", "v189", "v190", "v191", "v192", "v193", "v194", "v195", "v196", "v197", "v198", "v199", "v200", "v201", "v202", "v203", "v204", "v205", "v206", "v207", "v208", "v209", "v210", "v211", "v212", "v213", "v214", "v215", "v216", "v217", "v218", "v219", "v220", "v221", "v222", "v223", "v224", "v225", "v226", "v227", "v228", "v229", "v230", "v231", "v232", "v233", "v234", "v235", "v236", "v237", "v238", "v239", "v240", "v241", "v242", "v243", "v244", "v245", "v246", "v247", "v248", "v249", "v250", "v251", "v252", "v253", "v254", "v255", "a0", "a1", "a2", "a3", "a4", "a5", "a6", "a7", "a8", "a9", "a10", "a11", "a12", "a13", "a14", "a15", "a16", "a17", "a18", "a19", "a20", "a21", "a22", "a23", "a24", "a25", "a26", "a27", "a28", "a29", "a30", "a31", "a32", "a33", "a34", "a35", "a36", "a37", "a38", "a39", "a40", "a41", "a42", "a43", "a44", "a45", "a46", "a47", "a48", "a49", "a50", "a51", "a52", "a53", "a54", "a55", "a56", "a57", "a58", "a59", "a60", "a61", "a62", "a63", "a64", "a65", "a66", "a67", "a68", "a69", "a70", "a71", "a72", "a73", "a74", "a75", "a76", "a77", "a78", "a79", "a80", "a81", "a82", "a83", "a84", "a85", "a86", "a87", "a88", "a89", "a90", "a91", "a92", "a93", "a94", "a95", "a96", "a97", "a98", "a99", "a100", "a101", "a102", "a103", "a104", "a105", "a106", "a107", "a108", "a109", "a110", "a111", "a112", "a113", "a114", "a115", "a116", "a117", "a118", "a119", "a120", "a121", "a122", "a123", "a124", "a125", "a126", "a127", "a128", "a129", "a130", "a131", "a132", "a133", "a134", "a135", "a136", "a137", "a138", "a139", "a140", "a141", "a142", "a143", "a144", "a145", "a146", "a147", "a148", "a149", "a150", "a151", "a152", "a153", "a154", "a155", "a156", "a157", "a158", "a159", "a160", "a161", "a162", "a163", "a164", "a165", "a166", "a167", "a168", "a169", "a170", "a171", "a172", "a173", "a174", "a175", "a176", "a177", "a178", "a179", "a180", "a181", "a182", "a183", "a184", "a185", "a186", "a187", "a188", "a189", "a190", "a191", "a192", "a193", "a194", "a195", "a196", "a197", "a198", "a199", "a200", "a201", "a202", "a203", "a204", "a205", "a206", "a207", "a208", "a209", "a210", "a211", "a212", "a213", "a214", "a215", "a216", "a217", "a218", "a219", "a220", "a221", "a222", "a223", "a224", "a225", "a226", "a227", "a228", "a229", "a230", "a231", "a232", "a233", "a234", "a235", "a236", "a237", "a238", "a239", "a240", "a241", "a242", "a243", "a244", "a245", "a246", "a247", "a248", "a249", "a250", "a251", "a252", "a253", "a254", "a255", "s36", "s37", "s38", "s39", "s40", "s41", "s42", "s43", "s44", "s45", "s46", "s47", "s48", "s49", "s50", "s51", "s52", "s53", "s54", "s55", "s56", "s57", "s58", "s59", "s60", "s61", "s62", "s63", "s64", "s65", "s66", "s67", "s68", "s69", "s70", "s71", "s72", "s73", "s74", "s75", "s76", "s77", "s78", "s79", "s80", "s81", "s82", "s83", "s84", "s85", "s86", "s87", "s88", "s89", "s90", "s91", "s92", "s93", "s94", "s95", "s96", "s97", "s98", "s99", "vcc", "scc", "memory");
  if (bad) {
    asm volatile("" : "+s"(ka));
    x=ka->x; wq=ka->wq; wo=ka->wo; wd=ka->wd; out=ka->out; ws=ka->ws;
    const int b=blockIdx.x; slow_attention(ws,wq,4*(b&7)+(b>>6),(b>>3)&7);
    asm volatile(
    "s_mov_b64 s[44:45], %4\n"
    "s_and_b32 s45, s45, 0xffff\n"
    "s_mov_b32 s46, 0x6000000\n"
    "s_mov_b32 s47, 0x20000\n"
    "v_lshrrev_b32 v12, 6, v0\n"
    "v_and_b32 v13, 63, v0\n"
    "v_readfirstlane_b32 s52, v12\n"
    "s_and_b32 s56, %5, 7\n"
    "s_lshr_b32 s57, %5, 3\n"
    "s_lshr_b32 s58, s57, 3\n"
    "s_lshl_b32 s56, s56, 2\n"
    "s_add_u32 s53, s56, s58\n"
    "s_and_b32 s54, s57, 7\n"
    "v_lshlrev_b32 v1, 4, v13\n"
    "v_cmp_eq_u32 vcc, 0, v13\n"
    "v_mov_b32 v14, 0x7ffffff0\n"
    "v_mov_b32 v15, 0\n"
    "v_cndmask_b32 v7, v14, v15, vcc\n"
    "s_mov_b32 s99, %6\n"
    "s_mul_i32 s97, s99, 0x9e3779b1\n"
    "s_add_u32 s97, s97, 0x7f4a7c15\n"
    "s_mov_b64 s[40:41], %0\n"
    "s_mov_b64 s[36:37], %1\n"
    "s_lshr_b32 s58, s53, 3\n"
    "s_mul_i32 s59, s58, 1048576\n"
    "s_add_u32 s40, s40, s59\n"
    "s_addc_u32 s41, s41, 0\n"
    "s_and_b32 s41, s41, 0xffff\n"
    "s_mov_b32 s42, 0x100000\n"
    "s_mov_b32 s43, 0x20000\n"
    "s_mul_i32 s59, s58, 458752\n"
    "s_add_u32 s36, s36, s59\n"
    "s_addc_u32 s37, s37, 0\n"
    "s_and_b32 s37, s37, 0xffff\n"
    "s_mov_b32 s38, 0x70000\n"
    "s_mov_b32 s39, 0x20000\n"
    "s_sub_u32 s56, 15, s54\n"
    "s_bitcmp1_b32 s53, 2\n"
    "s_cselect_b32 s56, s54, s56\n"
    "s_lshl_b32 s56, s56, 2\n"
    "s_add_u32 s56, s56, s52\n"
    "s_lshr_b32 s57, s53, 3\n"
    "s_mul_i32 s60, s56, 114688\n"
    "s_mul_i32 s58, s57, 14336\n"
    "s_add_u32 s60, s60, s58\n"
    "s_add_u32 s60, s60, 0x2400000\n"
    "s_lshl_b32 s61, s56, 18\n"
    "s_lshl_b32 s58, s57, 15\n"
    "s_add_u32 s61, s61, s58\n"
    "s_add_u32 s61, s61, 0x2c00000\n"
    "s_xor_b32 s58, s53, 2\n"
    "s_and_b32 s58, s58, 3\n"
    "s_lshl_b32 s58, s58, 8\n"
    "s_lshl_b32 s59, s52, 13\n"
    "s_add_u32 s58, s58, s59\n"
    "s_mov_b32 s62, s58\n"
    "s_mov_b32 s63, s58\n"
    "s_lshl_b32 s59, s52, 11\n"
    "s_and_b32 s58, s53, 3\n"
    "s_cmp_eq_u32 s58, 3\n"
    "s_mov_b32 s64, 0x0\n"
    "s_cselect_b32 s64, 0x10000, s64\n"
    "s_add_u32 s64, s64, s59\n"
    "s_cmp_eq_u32 s58, 3\n"
    "s_mov_b32 s65, 0xc000\n"
    "s_cselect_b32 s65, 0x1c000, s65\n"
    "s_add_u32 s65, s65, s59\n"
    "s_cmp_eq_u32 s58, 3\n"
    "s_mov_b32 s66, 0x18000\n"
    "s_cselect_b32 s66, 0x0, s66\n"
    "s_add_u32 s66, s66, s59\n"
    "s_cmp_eq_u32 s58, 3\n"
    "s_cselect_b32 s67, 0, 0x18000\n"
    "s_mov_b32 s68, 0x16000\n"
    "s_cselect_b32 s68, 0x26000, s68\n"
    "v_lshrrev_b32 v16, 4, v1\n"
    "v_lshrrev_b32 v17, 4, v16\n"
    "v_and_b32 v18, 15, v16\n"
    "v_lshlrev_b32 v19, 1, v17\n"
    "v_xor_b32 v18, v18, v19\n"
    "v_lshlrev_b32 v18, 4, v18\n"
    "v_lshlrev_b32 v17, 10, v17\n"
    "v_add_u32 v28, v17, v18\n"
    "v_lshrrev_b32 v17, 4, v16\n"
    "v_add_u32 v17, 4, v17\n"
    "v_and_b32 v18, 15, v16\n"
    "v_lshlrev_b32 v19, 1, v17\n"
    "v_xor_b32 v18, v18, v19\n"
    "v_lshlrev_b32 v18, 4, v18\n"
    "v_lshlrev_b32 v17, 10, v17\n"
    "v_add_u32 v29, v17, v18\n"
    "s_and_b32 s58, s53, 7\n"
    "s_lshl_b32 s69, s58, 12\n"
    "s_add_u32 s69, s69, s61\n"
    "s_lshl_b32 s70, s58, 17\n"
    "s_add_u32 s70, s70, s63\n"
    "s_add_u32 s57, s62, 0x0\n"
    "s_add_u32 m0, s64, 0\n"
    "s_nop 0\n"
    "buffer_load_dwordx4 v28, s[36:39], s57 offen lds\n"
    "s_add_u32 m0, s64, 1024\n"
    "s_nop 0\n"
    "buffer_load_dwordx4 v29, s[36:39], s57 offen lds\n"
    "s_add_u32 s57, s62, 0x8000\n"
    "s_add_u32 m0, s64, 8192\n"
    "s_nop 0\n"
    "buffer_load_dwordx4 v28, s[36:39], s57 offen lds\n"
    "s_add_u32 m0, s64, 9216\n"
    "s_nop 0\n"
    "buffer_load_dwordx4 v29, s[36:39], s57 offen lds\n"
    "s_add_u32 s57, s62, 0x10000\n"
    "s_add_u32 m0, s64, 16384\n"
    "s_nop 0\n"
    "buffer_load_dwordx4 v28, s[36:39], s57 offen lds\n"
    "s_add_u32 m0, s64, 17408\n"
    "s_nop 0\n"
    "buffer_load_dwordx4 v29, s[36:39], s57 offen lds\n"
    "s_add_u32 s57, s62, 0x18000\n"
    "s_add_u32 m0, s64, 24576\n"
    "s_nop 0\n"
    "buffer_load_dwordx4 v28, s[36:39], s57 offen lds\n"
    "s_add_u32 m0, s64, 25600\n"
    "s_nop 0\n"
    "buffer_load_dwordx4 v29, s[36:39], s57 offen lds\n"
    "s_add_u32 s57, s62, 0x20000\n"
    "s_add_u32 m0, s65, 0\n"
    "s_nop 0\n"
    "buffer_load_dwordx4 v28, s[36:39], s57 offen lds\n"
    "s_add_u32 m0, s65, 1024\n"
    "s_nop 0\n"
    "buffer_load_dwordx4 v29, s[36:39], s57 offen lds\n"
    "s_add_u32 s57, s62, 0x28000\n"
    "s_add_u32 m0, s65, 8192\n"
    "s_nop 0\n"
    "buffer_load_dwordx4 v28, s[36:39], s57 offen lds\n"
    "s_add_u32 m0, s65, 9216\n"
    "s_nop 0\n"
    "buffer_load_dwordx4 v29, s[36:39], s57 offen lds\n"
    "s_add_u32 s57, s62, 0x30000\n"
    "s_add_u32 m0, s65, 16384\n"
    "s_nop 0\n"
    "buffer_load_dwordx4 v28, s[36:39], s57 offen lds\n"
    "s_add_u32 m0, s65, 17408\n"
    "s_nop 0\n"
    "buffer_load_dwordx4 v29, s[36:39], s57 offen lds\n"
    "s_add_u32 s57, s62, 0x38000\n"
    "s_add_u32 m0, s65, 24576\n"
    "s_nop 0\n"
    "buffer_load_dwordx4 v28, s[36:39], s57 offen lds\n"
    "s_add_u32 m0, s65, 25600\n"
    "s_nop 0\n"
    "buffer_load_dwordx4 v29, s[36:39], s57 offen lds\n"
    "s_add_u32 s56, s60, 0x0\n"
    "buffer_load_dwordx4 v[96:99], v1, s[44:47], s56 offen sc1\n"
    "s_add_u32 s56, s60, 0xe000\n"
    "buffer_load_dwordx4 v[100:103], v1, s[44:47], s56 offen sc1\n"
    "s_add_u32 s56, s60, 0x400\n"
    "buffer_load_dwordx4 v[104:107], v1, s[44:47], s56 offen sc1\n"
    "s_add_u32 s56, s60, 0xe400\n"
    "buffer_load_dwordx4 v[108:111], v1, s[44:47], s56 offen sc1\n"
    "s_add_u32 s56, s60, 0x800\n"
    "buffer_load_dwordx4 v[112:115], v1, s[44:47], s56 offen sc1\n"
    "s_add_u32 s56, s60, 0xe800\n"
    "buffer_load_dwordx4 v[116:119], v1, s[44:47], s56 offen sc1\n"
    "s_add_u32 s56, s60, 0xc00\n"
    "buffer_load_dwordx4 v[120:123], v1, s[44:47], s56 offen sc1\n"
    "s_add_u32 s56, s60, 0xec00\n"
    "buffer_load_dwordx4 v[124:127], v1, s[44:47], s56 offen sc1\n"
    "s_add_u32 s56, s60, 0x1000\n"
    "buffer_load_dwordx4 v[128:131], v1, s[44:47], s56 offen sc1\n"
    "s_add_u32 s56, s60, 0xf000\n"
    "buffer_load_dwordx4 v[132:135], v1, s[44:47], s56 offen sc1\n"
    "s_add_u32 s56, s60, 0x1400\n"
    "buffer_load_dwordx4 v[136:139], v1, s[44:47], s56 offen sc1\n"
    "s_add_u32 s56, s60, 0xf400\n"
    "buffer_load_dwordx4 v[140:143], v1, s[44:47], s56 offen sc1\n"
    "s_waitcnt vmcnt(0)\n"
    "s_barrier\n"
    "v_and_b32 v13, 63, v0\n"
    "s_lshr_b32 s75, s53, 3\n"
    "s_lshl_b32 s59, s52, 11\n"
    "s_sub_u32 s59, s64, s59\n"
    "v_lshrrev_b32 v12, 4, v13\n"
    "v_bfe_u32 v14, v13, 2, 2\n"
    "v_lshlrev_b32 v15, 2, v12\n"
    "v_add_u32 v15, v15, v14\n"
    "v_and_b32 v14, 7, v15\n"
    "v_lshlrev_b32 v15, 8, v15\n"
    "v_and_b32 v12, 3, v13\n"
    "v_lshlrev_b32 v12, 3, v12\n"
    "v_add_u32 v15, v15, v12\n"
    "v_add_u32 v15, s59, v15\n"
    "s_lshl_b32 s57, s75, 1\n"
    "s_xor_b32 s58, s57, 0\n"
    "v_xor_b32 v12, s58, v14\n"
    "v_lshlrev_b32 v12, 5, v12\n"
    "v_add_u32 v8, v15, v12\n"
    "s_xor_b32 s58, s57, 1\n"
    "v_xor_b32 v12, s58, v14\n"
    "v_lshlrev_b32 v12, 5, v12\n"
    "v_add_u32 v9, v15, v12\n"
    "s_xor_b32 s58, s57, 2\n"
    "v_xor_b32 v12, s58, v14\n"
    "v_lshlrev_b32 v12, 5, v12\n"
    "v_add_u32 v10, v15, v12\n"
    "s_xor_b32 s58, s57, 3\n"
    "v_xor_b32 v12, s58, v14\n"
    "v_lshlrev_b32 v12, 5, v12\n"
    "v_add_u32 v11, v15, v12\n"
    "s_xor_b32 s58, s57, 4\n"
    "v_xor_b32 v12, s58, v14\n"
    "v_lshlrev_b32 v12, 5, v12\n"
    "v_add_u32 v16, v15, v12\n"
    "s_xor_b32 s58, s57, 5\n"
    "v_xor_b32 v12, s58, v14\n"
    "v_lshlrev_b32 v12, 5, v12\n"
    "v_add_u32 v17, v15, v12\n"
    "s_xor_b32 s58, s57, 6\n"
    "v_xor_b32 v12, s58, v14\n"
    "v_lshlrev_b32 v12, 5, v12\n"
    "v_add_u32 v18, v15, v12\n"
    "s_xor_b32 s58, s57, 7\n"
    "v_xor_b32 v12, s58, v14\n"
    "v_lshlrev_b32 v12, 5, v12\n"
    "v_add_u32 v19, v15, v12\n"
    "ds_read_b64_tr_b16 v[32:33], v8 offset:0\n"
    "ds_read_b64_tr_b16 v[34:35], v8 offset:4096\n"
    "ds_read_b64_tr_b16 v[36:37], v9 offset:0\n"
    "ds_read_b64_tr_b16 v[38:39], v9 offset:4096\n"
    "ds_read_b64_tr_b16 v[40:41], v10 offset:0\n"
    "ds_read_b64_tr_b16 v[42:43], v10 offset:4096\n"
    "ds_read_b64_tr_b16 v[44:45], v11 offset:0\n"
    "ds_read_b64_tr_b16 v[46:47], v11 offset:4096\n"
    "ds_read_b64_tr_b16 v[48:49], v16 offset:0\n"
    "ds_read_b64_tr_b16 v[50:51], v16 offset:4096\n"
    "ds_read_b64_tr_b16 v[52:53], v17 offset:0\n"
    "ds_read_b64_tr_b16 v[54:55], v17 offset:4096\n"
    "ds_read_b64_tr_b16 v[56:57], v18 offset:0\n"
    "ds_read_b64_tr_b16 v[58:59], v18 offset:4096\n"
    "ds_read_b64_tr_b16 v[60:61], v19 offset:0\n"
    "s_waitcnt lgkmcnt(8)\n"
    "ds_read_b64_tr_b16 v[62:63], v19 offset:4096\n"
    "s_sub_u32 s76, s65, s64\n"
    "s_sub_u32 s77, s66, s65\n"
    "s_sub_u32 s78, s64, s66\n"
    "s_sub_u32 s56, 15, s54\n"
    "s_bitcmp1_b32 s53, 2\n"
    "s_cselect_b32 s56, s54, s56\n"
    "s_lshl_b32 s56, s56, 2\n"
    "s_add_u32 s56, s56, s52\n"
    "s_lshl_b32 s73, s56, 15\n"
    "s_xor_b32 s56, s53, 2\n"
    "s_and_b32 s56, s56, 3\n"
    "s_lshl_b32 s56, s56, 8\n"
    "s_add_u32 s73, s73, s56\n"
    "s_lshl_b32 s56, s75, 6\n"
    "s_add_u32 s73, s73, s56\n"
    "s_add_u32 s72, s73, 16384\n"
    "v_and_b32 v12, 15, v13\n"
    "v_lshlrev_b32 v12, 10, v12\n"
    "v_lshrrev_b32 v14, 4, v13\n"
    "v_lshlrev_b32 v14, 3, v14\n"
    "v_add_u32 v20, v12, v14\n"
    "s_mov_b64 s[48:49], %2\n"
    "s_and_b32 s49, s49, 0xffff\n"
    "s_mov_b32 s50, 0x200000\n"
    "s_mov_b32 s51, 0x20000\n"
    "s_waitcnt lgkmcnt(8)\n"
    "s_and_b32 s56, s53, 7\n"
    "s_lshl_b32 s56, s56, 3\n"
    "s_add_u32 s56, s56, s54\n"
    "s_lshl_b32 s56, s56, 2\n"
    "s_add_u32 s56, s56, s52\n"
    "s_lshl_b32 s74, s56, 15\n"
    "s_add_u32 s74, s74, 0x4000000\n"
    "s_xor_b32 s57, s75, 1\n"
    "s_lshl_b32 s57, s57, 13\n"
    "s_add_u32 s57, s57, 2048\n"
    "s_add_u32 s81, s57, s74\n"
    "s_xor_b32 s57, s75, 2\n"
    "s_lshl_b32 s57, s57, 13\n"
    "s_add_u32 s57, s57, 4096\n"
    "s_add_u32 s82, s57, s74\n"
    "s_xor_b32 s57, s75, 3\n"
    "s_lshl_b32 s57, s57, 13\n"
    "s_add_u32 s57, s57, 6144\n"
    "s_add_u32 s83, s57, s74\n"
    "s_lshl_b32 s96, s54, 2\n"
    "s_lshr_b32 s95, s53, 3\n"
    "s_add_u32 s96, s96, s95\n"
    "s_lshl_b32 s96, s96, 7\n"
    "s_add_u32 s96, s96, 0x5000\n"
    "s_and_b32 s95, s53, 7\n"
    "s_lshl_b32 s95, s95, 2\n"
    "s_add_u32 s95, s95, s52\n"
    "s_lshl_b32 s95, s95, 2\n"
    "s_add_u32 s96, s96, s95\n"
    "v_mov_b32 v24, s97\n"
    "buffer_store_dword v24, v7, s[44:47], s96 offen sc1\n"
    "s_nop 1\n"
    "v_lshrrev_b32 v21, 2, v1\n"
    "v_and_b32 v21, 0x7c, v21\n"
    "v_and_b32 v22, 12, v21\n"
    "v_mfma_f32_16x16x32_bf16 v[160:163], v[32:35], v[96:99], 0\n"
    "s_add_u32 s56, s60, 0x1800\n"
    "ds_read_b64_tr_b16 v[64:65], v8 offset:8192\n"
    "v_mfma_f32_16x16x32_bf16 v[164:167], v[32:35], v[100:103], 0\n"
    "ds_read_b64_tr_b16 v[66:67], v8 offset:12288\n"
    "buffer_load_dwordx4 v[144:147], v1, s[44:47], s56 offen sc1\n"
    "v_mfma_f32_16x16x32_bf16 v[168:171], v[36:39], v[96:99], 0\n"
    "s_add_u32 s57, s62, 0x40000\n"
    "ds_read_b64_tr_b16 v[68:69], v9 offset:8192\n"
    "v_mfma_f32_16x16x32_bf16 v[172:175], v[36:39], v[100:103], 0\n"
    "s_add_u32 m0, s66, 0\n"
    "ds_read_b64_tr_b16 v[70:71], v9 offset:12288\n"
    "v_mfma_f32_16x16x32_bf16 v[176:179], v[40:43], v[96:99], 0\n"
    "ds_read_b64_tr_b16 v[72:73], v10 offset:8192\n"
    "buffer_load_dwordx4 v28, s[36:39], s57 offen lds\n"
    "v_mfma_f32_16x16x32_bf16 v[180:183], v[40:43], v[100:103], 0\n"
    "s_add_u32 s56, s60, 0xf800\n"
    "ds_read_b64_tr_b16 v[74:75], v10 offset:12288\n"
    "v_mfma_f32_16x16x32_bf16 v[184:187], v[44:47], v[96:99], 0\n"
    "s_add_u32 m0, s66, 1024\n"
    "ds_read_b64_tr_b16 v[76:77], v11 offset:8192\n"
    "v_mfma_f32_16x16x32_bf16 v[188:191], v[44:47], v[100:103], 0\n"
    "s_waitcnt lgkmcnt(7)\n"
    "ds_read_b64_tr_b16 v[78:79], v11 offset:12288\n"
    "v_mfma_f32_16x16x32_bf16 v[192:195], v[48:51], v[96:99], 0\n"
    "ds_read_b64_tr_b16 v[32:33], v16 offset:8192\n"
    "buffer_load_dwordx4 v29, s[36:39], s57 offen lds\n"
    "v_mfma_f32_16x16x32_bf16 v[196:199], v[48:51], v[100:103], 0\n"
    "ds_read_b64_tr_b16 v[34:35], v16 offset:12288\n"
    "v_mfma_f32_16x16x32_bf16 v[200:203], v[52:55], v[96:99], 0\n"
    "ds_read_b64_tr_b16 v[36:37], v17 offset:8192\n"
    "v_mfma_f32_16x16x32_bf16 v[204:207], v[52:55], v[100:103], 0\n"
    "ds_read_b64_tr_b16 v[38:39], v17 offset:12288\n"
    "buffer_load_dwordx4 v[148:151], v1, s[44:47], s56 offen sc1\n"
    "v_mfma_f32_16x16x32_bf16 v[208:211], v[56:59], v[96:99], 0\n"
    "s_add_u32 s57, s62, 0x48000\n"
    "ds_read_b64_tr_b16 v[40:41], v18 offset:8192\n"
    "v_mfma_f32_16x16x32_bf16 v[212:215], v[56:59], v[100:103], 0\n"
    "s_add_u32 m0, s66, 8192\n"
    "ds_read_b64_tr_b16 v[42:43], v18 offset:12288\n"
    "v_mfma_f32_16x16x32_bf16 v[216:219], v[60:63], v[96:99], 0\n"
    "ds_read_b64_tr_b16 v[44:45], v19 offset:8192\n"
    "buffer_load_dwordx4 v28, s[36:39], s57 offen lds\n"
    "v_mfma_f32_16x16x32_bf16 v[220:223], v[60:63], v[100:103], 0\n"
    "s_waitcnt lgkmcnt(7)\n"
    "ds_read_b64_tr_b16 v[46:47], v19 offset:12288\n"
    "v_mfma_f32_16x16x32_bf16 v[160:163], v[64:67], v[104:107], v[160:163]\n"
    "s_add_u32 m0, s66, 9216\n"
    "ds_read_b64_tr_b16 v[48:49], v8 offset:16384\n"
    "v_mfma_f32_16x16x32_bf16 v[164:167], v[64:67], v[108:111], v[164:167]\n"
    "ds_read_b64_tr_b16 v[50:51], v8 offset:20480\n"
    "buffer_load_dwordx4 v29, s[36:39], s57 offen lds\n"
    "v_mfma_f32_16x16x32_bf16 v[168:171], v[68:71], v[104:107], v[168:171]\n"
    "s_add_u32 s56, s60, 0x1c00\n"
    "ds_read_b64_tr_b16 v[52:53], v9 offset:16384\n"
    "v_mfma_f32_16x16x32_bf16 v[172:175], v[68:71], v[108:111], v[172:175]\n"
    "ds_read_b64_tr_b16 v[54:55], v9 offset:20480\n"
    "v_mfma_f32_16x16x32_bf16 v[176:179], v[72:75], v[104:107], v[176:179]\n"
    "ds_read_b64_tr_b16 v[56:57], v10 offset:16384\n"
    "buffer_load_dwordx4 v[152:155], v1, s[44:47], s56 offen sc1\n"
    "v_mfma_f32_16x16x32_bf16 v[180:183], v[72:75], v[108:111], v[180:183]\n"
    "s_add_u32 s57, s62, 0x50000\n"
    "ds_read_b64_tr_b16 v[58:59], v10 offset:20480\n"
    "v_mfma_f32_16x16x32_bf16 v[184:187], v[76:79], v[104:107], v[184:187]\n"
    "s_add_u32 m0, s66, 16384\n"
    "ds_read_b64_tr_b16 v[60:61], v11 offset:16384\n"
    "v_mfma_f32_16x16x32_bf16 v[188:191], v[76:79], v[108:111], v[188:191]\n"
    "s_waitcnt lgkmcnt(7)\n"
    "ds_read_b64_tr_b16 v[62:63], v11 offset:20480\n"
    "v_mfma_f32_16x16x32_bf16 v[192:195], v[32:35], v[104:107], v[192:195]\n"
    "ds_read_b64_tr_b16 v[64:65], v16 offset:16384\n"
    "buffer_load_dwordx4 v28, s[36:39], s57 offen lds\n"
    "v_mfma_f32_16x16x32_bf16 v[196:199], v[32:35], v[108:111], v[196:199]\n"
    "s_add_u32 s56, s60, 0xfc00\n"
    "ds_read_b64_tr_b16 v[66:67], v16 offset:20480\n"
    "v_mfma_f32_16x16x32_bf16 v[200:203], v[36:39], v[104:107], v[200:203]\n"
    "ds_read_b64_tr_b16 v[68:69], v17 offset:16384\n"
    "v_mfma_f32_16x16x32_bf16 v[204:207], v[36:39], v[108:111], v[204:207]\n"
    "ds_read_b64_tr_b16 v[70:71], v17 offset:20480\n"
    "buffer_load_dwordx4 v[156:159], v1, s[44:47], s56 offen sc1\n"
    "v_mfma_f32_16x16x32_bf16 v[208:211], v[40:43], v[104:107], v[208:211]\n"
    "s_add_u32 m0, s66, 17408\n"
    "ds_read_b64_tr_b16 v[72:73], v18 offset:16384\n"
    "v_mfma_f32_16x16x32_bf16 v[212:215], v[40:43], v[108:111], v[212:215]\n"
    "s_add_u32 s56, s60, 0x2000\n"
    "ds_read_b64_tr_b16 v[74:75], v18 offset:20480\n"
    "v_mfma_f32_16x16x32_bf16 v[216:219], v[44:47], v[104:107], v[216:219]\n"
    "ds_read_b64_tr_b16 v[76:77], v19 offset:16384\n"
    "buffer_load_dwordx4 v29, s[36:39], s57 offen lds\n"
    "v_mfma_f32_16x16x32_bf16 v[220:223], v[44:47], v[108:111], v[220:223]\n"
    "s_waitcnt lgkmcnt(7)\n"
    "ds_read_b64_tr_b16 v[78:79], v19 offset:20480\n"
    "v_mfma_f32_16x16x32_bf16 v[160:163], v[48:51], v[112:115], v[160:163]\n"
    "s_add_u32 s57, s62, 0x58000\n"
    "ds_read_b64_tr_b16 v[32:33], v8 offset:24576\n"
    "v_mfma_f32_16x16x32_bf16 v[164:167], v[48:51], v[116:119], v[164:167]\n"
    "ds_read_b64_tr_b16 v[34:35], v8 offset:28672\n"
    "buffer_load_dwordx4 v[96:99], v1, s[44:47], s56 offen sc1\n"
    "v_mfma_f32_16x16x32_bf16 v[168:171], v[52:55], v[112:115], v[168:171]\n"
    "s_add_u32 m0, s66, 24576\n"
    "ds_read_b64_tr_b16 v[36:37], v9 offset:24576\n"
    "v_mfma_f32_16x16x32_bf16 v[172:175], v[52:55], v[116:119], v[172:175]\n"
    "s_add_u32 s56, s60, 0x10000\n"
    "ds_read_b64_tr_b16 v[38:39], v9 offset:28672\n"
    "v_mfma_f32_16x16x32_bf16 v[176:179], v[56:59], v[112:115], v[176:179]\n"
    "ds_read_b64_tr_b16 v[40:41], v10 offset:24576\n"
    "buffer_load_dwordx4 v28, s[36:39], s57 offen lds\n"
    "v_mfma_f32_16x16x32_bf16 v[180:183], v[56:59], v[116:119], v[180:183]\n"
    "s_add_u32 m0, s66, 25600\n"
    "ds_read_b64_tr_b16 v[42:43], v10 offset:28672\n"
    "v_mfma_f32_16x16x32_bf16 v[184:187], v[60:63], v[112:115], v[184:187]\n"
    "ds_read_b64_tr_b16 v[44:45], v11 offset:24576\n"
    "v_mfma_f32_16x16x32_bf16 v[188:191], v[60:63], v[116:119], v[188:191]\n"
    "s_waitcnt lgkmcnt(7)\n"
    "ds_read_b64_tr_b16 v[46:47], v11 offset:28672\n"
    "v_mfma_f32_16x16x32_bf16 v[192:195], v[64:67], v[112:115], v[192:195]\n"
    "ds_read_b64_tr_b16 v[48:49], v16 offset:24576\n"
    "buffer_load_dwordx4 v29, s[36:39], s57 offen lds\n"
    "v_mfma_f32_16x16x32_bf16 v[196:199], v[64:67], v[116:119], v[196:199]\n"
    "ds_read_b64_tr_b16 v[50:51], v16 offset:28672\n"
    "v_mfma_f32_16x16x32_bf16 v[200:203], v[68:71], v[112:115], v[200:203]\n"
    "s_add_u32 s57, s62, 0x60000\n"
    "ds_read_b64_tr_b16 v[52:53], v17 offset:24576\n"
    "v_mfma_f32_16x16x32_bf16 v[204:207], v[68:71], v[116:119], v[204:207]\n"
    "ds_read_b64_tr_b16 v[54:55], v17 offset:28672\n"
    "buffer_load_dwordx4 v[100:103], v1, s[44:47], s56 offen sc1\n"
    "v_mfma_f32_16x16x32_bf16 v[208:211], v[72:75], v[112:115], v[208:211]\n"
    "s_add_u32 m0, s66, 32768\n"
    "ds_read_b64_tr_b16 v[56:57], v18 offset:24576\n"
    "v_mfma_f32_16x16x32_bf16 v[212:215], v[72:75], v[116:119], v[212:215]\n"
    "s_add_u32 s56, s60, 0x2400\n"
    "ds_read_b64_tr_b16 v[58:59], v18 offset:28672\n"
    "v_mfma_f32_16x16x32_bf16 v[216:219], v[76:79], v[112:115], v[216:219]\n"
    "ds_read_b64_tr_b16 v[60:61], v19 offset:24576\n"
    "buffer_load_dwordx4 v28, s[36:39], s57 offen lds\n"
    "v_mfma_f32_16x16x32_bf16 v[220:223], v[76:79], v[116:119], v[220:223]\n"
    "s_waitcnt lgkmcnt(7)\n"
    "ds_read_b64_tr_b16 v[62:63], v19 offset:28672\n"
    "s_lshl_b32 s96, s54, 2\n"
    "s_lshr_b32 s95, s53, 3\n"
    "s_add_u32 s96, s96, s95\n"
    "s_lshl_b32 s96, s96, 7\n"
    "s_add_u32 s96, s96, 0x4000\n"
    "s_and_b32 s95, s53, 7\n"
    "s_lshl_b32 s95, s95, 2\n"
    "s_add_u32 s95, s95, s52\n"
    "s_lshl_b32 s95, s95, 2\n"
    "s_add_u32 s96, s96, s95\n"
    "v_mov_b32 v24, s97\n"
    "buffer_store_dword v24, v7, s[44:47], s96 offen sc1\n"
    "s_barrier\n"
    "v_add_u32 v8, s76, v8\n"
    "v_mfma_f32_16x16x32_bf16 v[160:163], v[32:35], v[120:123], v[160:163]\n"
    "s_add_u32 m0, s66, 33792\n"
    "ds_read_b64_tr_b16 v[64:65], v8 offset:0\n"
    "v_mfma_f32_16x16x32_bf16 v[164:167], v[32:35], v[124:127], v[164:167]\n"
    "ds_read_b64_tr_b16 v[66:67], v8 offset:4096\n"
    "v_add_u32 v9, s76, v9\n"
    "v_mfma_f32_16x16x32_bf16 v[168:171], v[36:39], v[120:123], v[168:171]\n"
    "ds_read_b64_tr_b16 v[68:69], v9 offset:0\n"
    "buffer_load_dwordx4 v29, s[36:39], s57 offen lds\n"
    "v_mfma_f32_16x16x32_bf16 v[172:175], v[36:39], v[124:127], v[172:175]\n"
    "ds_read_b64_tr_b16 v[70:71], v9 offset:4096\n"
    "v_add_u32 v10, s76, v10\n"
    "v_mfma_f32_16x16x32_bf16 v[176:179], v[40:43], v[120:123], v[176:179]\n"
    "s_add_u32 s57, s62, 0x68000\n"
    "ds_read_b64_tr_b16 v[72:73], v10 offset:0\n"
    "v_mfma_f32_16x16x32_bf16 v[180:183], v[40:43], v[124:127], v[180:183]\n"
    "s_add_u32 m0, s66, 40960\n"
    "ds_read_b64_tr_b16 v[74:75], v10 offset:4096\n"
    "v_add_u32 v11, s76, v11\n"
    "v_mfma_f32_16x16x32_bf16 v[184:187], v[44:47], v[120:123], v[184:187]\n"
    "ds_read_b64_tr_b16 v[76:77], v11 offset:0\n"
    "buffer_load_dwordx4 v28, s[36:39], s57 offen lds\n"
    "v_mfma_f32_16x16x32_bf16 v[188:191], v[44:47], v[124:127], v[188:191]\n"
    "s_waitcnt lgkmcnt(7)\n"
    "ds_read_b64_tr_b16 v[78:79], v11 offset:4096\n"
    "v_add_u32 v16, s76, v16\n"
    "v_mfma_f32_16x16x32_bf16 v[192:195], v[48:51], v[120:123], v[192:195]\n"
    "ds_read_b64_tr_b16 v[32:33], v16 offset:0\n"
    "v_mfma_f32_16x16x32_bf16 v[196:199], v[48:51], v[124:127], v[196:199]\n"
    "ds_read_b64_tr_b16 v[34:35], v16 offset:4096\n"
    "v_add_u32 v17, s76, v17\n"
    "v_mfma_f32_16x16x32_bf16 v[200:203], v[52:55], v[120:123], v[200:203]\n"
    "ds_read_b64_tr_b16 v[36:37], v17 offset:0\n"
    "buffer_load_dwordx4 v[104:107], v1, s[44:47], s56 offen sc1\n"
    "v_mfma_f32_16x16x32_bf16 v[204:207], v[52:55], v[124:127], v[204:207]\n"
    "s_add_u32 m0, s66, 41984\n"
    "ds_read_b64_tr_b16 v[38:39], v17 offset:4096\n"
    "v_add_u32 v18, s76, v18\n"
    "v_mfma_f32_16x16x32_bf16 v[208:211], v[56:59], v[120:123], v[208:211]\n"
    "s_add_u32 s56, s60, 0x10400\n"
    "ds_read_b64_tr_b16 v[40:41], v18 offset:0\n"
    "v_mfma_f32_16x16x32_bf16 v[212:215], v[56:59], v[124:127], v[212:215]\n"
    "ds_read_b64_tr_b16 v[42:43], v18 offset:4096\n"
    "v_add_u32 v19, s76, v19\n"
    "v_mfma_f32_16x16x32_bf16 v[216:219], v[60:63], v[120:123], v[216:219]\n"
    "ds_read_b64_tr_b16 v[44:45], v19 offset:0\n"
    "buffer_load_dwordx4 v29, s[36:39], s57 offen lds\n"
    "v_mfma_f32_16x16x32_bf16 v[220:223], v[60:63], v[124:127], v[220:223]\n"
    "s_waitcnt lgkmcnt(7)\n"
    "ds_read_b64_tr_b16 v[46:47], v19 offset:4096\n"
    "v_mfma_f32_16x16x32_bf16 v[160:163], v[64:67], v[128:131], v[160:163]\n"
    "s_add_u32 s57, s70, 0x0\n"
    "ds_read_b64_tr_b16 v[48:49], v8 offset:8192\n"
    "v_mfma_f32_16x16x32_bf16 v[164:167], v[64:67], v[132:135], v[164:167]\n"
    "ds_read_b64_tr_b16 v[50:51], v8 offset:12288\n"
    "buffer_load_dwordx4 v[108:111], v1, s[44:47], s56 offen sc1\n"
    "v_mfma_f32_16x16x32_bf16 v[168:171], v[68:71], v[128:131], v[168:171]\n"
    "s_add_u32 m0, s64, 0\n"
    "ds_read_b64_tr_b16 v[52:53], v9 offset:8192\n"
    "v_mfma_f32_16x16x32_bf16 v[172:175], v[68:71], v[132:135], v[172:175]\n"
    "s_add_u32 s56, s60, 0x2800\n"
    "ds_read_b64_tr_b16 v[54:55], v9 offset:12288\n"
    "v_mfma_f32_16x16x32_bf16 v[176:179], v[72:75], v[128:131], v[176:179]\n"
    "ds_read_b64_tr_b16 v[56:57], v10 offset:8192\n"
    "buffer_load_dwordx4 v28, s[40:43], s57 offen lds\n"
    "v_mfma_f32_16x16x32_bf16 v[180:183], v[72:75], v[132:135], v[180:183]\n"
    "s_add_u32 m0, s64, 1024\n"
    "ds_read_b64_tr_b16 v[58:59], v10 offset:12288\n"
    "v_mfma_f32_16x16x32_bf16 v[184:187], v[76:79], v[128:131], v[184:187]\n"
    "ds_read_b64_tr_b16 v[60:61], v11 offset:8192\n"
    "v_mfma_f32_16x16x32_bf16 v[188:191], v[76:79], v[132:135], v[188:191]\n"
    "s_waitcnt lgkmcnt(7)\n"
    "ds_read_b64_tr_b16 v[62:63], v11 offset:12288\n"
    "v_mfma_f32_16x16x32_bf16 v[192:195], v[32:35], v[128:131], v[192:195]\n"
    "ds_read_b64_tr_b16 v[64:65], v16 offset:8192\n"
    "buffer_load_dwordx4 v29, s[40:43], s57 offen lds\n"
    "v_mfma_f32_16x16x32_bf16 v[196:199], v[32:35], v[132:135], v[196:199]\n"
    "s_add_u32 s57, s70, 0x8000\n"
    "ds_read_b64_tr_b16 v[66:67], v16 offset:12288\n"
    "v_mfma_f32_16x16x32_bf16 v[200:203], v[36:39], v[128:131], v[200:203]\n"
    "s_add_u32 m0, s64, 8192\n"
    "ds_read_b64_tr_b16 v[68:69], v17 offset:8192\n"
    "v_mfma_f32_16x16x32_bf16 v[204:207], v[36:39], v[132:135], v[204:207]\n"
    "ds_read_b64_tr_b16 v[70:71], v17 offset:12288\n"
    "buffer_load_dwordx4 v28, s[40:43], s57 offen lds\n"
    "v_mfma_f32_16x16x32_bf16 v[208:211], v[40:43], v[128:131], v[208:211]\n"
    "s_add_u32 m0, s64, 9216\n"
    "ds_read_b64_tr_b16 v[72:73], v18 offset:8192\n"
    "v_mfma_f32_16x16x32_bf16 v[212:215], v[40:43], v[132:135], v[212:215]\n"
    "ds_read_b64_tr_b16 v[74:75], v18 offset:12288\n"
    "v_mfma_f32_16x16x32_bf16 v[216:219], v[44:47], v[128:131], v[216:219]\n"
    "ds_read_b64_tr_b16 v[76:77], v19 offset:8192\n"
    "buffer_load_dwordx4 v29, s[40:43], s57 offen lds\n"
    "v_mfma_f32_16x16x32_bf16 v[220:223], v[44:47], v[132:135], v[220:223]\n"
    "s_waitcnt lgkmcnt(7)\n"
    "ds_read_b64_tr_b16 v[78:79], v19 offset:12288\n"
    "v_mfma_f32_16x16x32_bf16 v[160:163], v[48:51], v[136:139], v[160:163]\n"
    "s_add_u32 s57, s70, 0x10000\n"
    "ds_read_b64_tr_b16 v[32:33], v8 offset:16384\n"
    "v_mfma_f32_16x16x32_bf16 v[164:167], v[48:51], v[140:143], v[164:167]\n"
    "ds_read_b64_tr_b16 v[34:35], v8 offset:20480\n"
    "buffer_load_dwordx4 v[112:115], v1, s[44:47], s56 offen sc1\n"
    "v_mfma_f32_16x16x32_bf16 v[168:171], v[52:55], v[136:139], v[168:171]\n"
    "s_add_u32 m0, s64, 16384\n"
    "ds_read_b64_tr_b16 v[36:37], v9 offset:16384\n"
    "v_mfma_f32_16x16x32_bf16 v[172:175], v[52:55], v[140:143], v[172:175]\n"
    "s_add_u32 s56, s60, 0x10800\n"
    "ds_read_b64_tr_b16 v[38:39], v9 offset:20480\n"
    "v_mfma_f32_16x16x32_bf16 v[176:179], v[56:59], v[136:139], v[176:179]\n"
    "ds_read_b64_tr_b16 v[40:41], v10 offset:16384\n"
    "buffer_load_dwordx4 v28, s[40:43], s57 offen lds\n"
    "v_mfma_f32_16x16x32_bf16 v[180:183], v[56:59], v[140:143], v[180:183]\n"
    "s_add_u32 m0, s64, 17408\n"
    "ds_read_b64_tr_b16 v[42:43], v10 offset:20480\n"
    "v_mfma_f32_16x16x32_bf16 v[184:187], v[60:63], v[136:139], v[184:187]\n"
    "ds_read_b64_tr_b16 v[44:45], v11 offset:16384\n"
    "v_mfma_f32_16x16x32_bf16 v[188:191], v[60:63], v[140:143], v[188:191]\n"
    "s_waitcnt lgkmcnt(7)\n"
    "ds_read_b64_tr_b16 v[46:47], v11 offset:20480\n"
    "v_mfma_f32_16x16x32_bf16 v[192:195], v[64:67], v[136:139], v[192:195]\n"
    "ds_read_b64_tr_b16 v[48:49], v16 offset:16384\n"
    "buffer_load_dwordx4 v29, s[40:43], s57 offen lds\n"
    "v_mfma_f32_16x16x32_bf16 v[196:199], v[64:67], v[140:143], v[196:199]\n"
    "s_add_u32 s57, s70, 0x18000\n"
    "ds_read_b64_tr_b16 v[50:51], v16 offset:20480\n"
    "v_mfma_f32_16x16x32_bf16 v[200:203], v[68:71], v[136:139], v[200:203]\n"
    "s_add_u32 m0, s64, 24576\n"
    "ds_read_b64_tr_b16 v[52:53], v17 offset:16384\n"
    "v_mfma_f32_16x16x32_bf16 v[204:207], v[68:71], v[140:143], v[204:207]\n"
    "ds_read_b64_tr_b16 v[54:55], v17 offset:20480\n"
    "buffer_load_dwordx4 v28, s[40:43], s57 offen lds\n"
    "v_mfma_f32_16x16x32_bf16 v[208:211], v[72:75], v[136:139], v[208:211]\n"
    "s_waitcnt vmcnt(25)\n"
    "ds_read_b64_tr_b16 v[56:57], v18 offset:16384\n"
    "v_mfma_f32_16x16x32_bf16 v[212:215], v[72:75], v[140:143], v[212:215]\n"
    "s_add_u32 m0, s64, 25600\n"
    "ds_read_b64_tr_b16 v[58:59], v18 offset:20480\n"
    "v_mfma_f32_16x16x32_bf16 v[216:219], v[76:79], v[136:139], v[216:219]\n"
    "ds_read_b64_tr_b16 v[60:61], v19 offset:16384\n"
    "buffer_load_dwordx4 v29, s[40:43], s57 offen lds\n"
    "v_mfma_f32_16x16x32_bf16 v[220:223], v[76:79], v[140:143], v[220:223]\n"
    "s_waitcnt lgkmcnt(7)\n"
    "ds_read_b64_tr_b16 v[62:63], v19 offset:20480\n"
    "v_mfma_f32_16x16x32_bf16 v[160:163], v[32:35], v[144:147], v[160:163]\n"
    "s_add_u32 s57, s53, 1\n"
    "ds_read_b64_tr_b16 v[64:65], v8 offset:24576\n"
    "v_mfma_f32_16x16x32_bf16 v[164:167], v[32:35], v[148:151], v[164:167]\n"
    "s_and_b32 s57, s57, 7\n"
    "s_lshl_b32 s70, s57, 17\n"
    "s_add_u32 s70, s70, s63\n"
    "ds_read_b64_tr_b16 v[66:67], v8 offset:28672\n"
    "v_mfma_f32_16x16x32_bf16 v[168:171], v[36:39], v[144:147], v[168:171]\n"
    "s_add_u32 s57, s70, 0x0\n"
    "s_add_u32 m0, s64, 32768\n"
    "ds_read_b64_tr_b16 v[68:69], v9 offset:24576\n"
    "v_mfma_f32_16x16x32_bf16 v[172:175], v[36:39], v[148:151], v[172:175]\n"
    "ds_read_b64_tr_b16 v[70:71], v9 offset:28672\n"
    "buffer_load_dwordx4 v28, s[40:43], s57 offen lds\n"
    "v_mfma_f32_16x16x32_bf16 v[176:179], v[40:43], v[144:147], v[176:179]\n"
    "s_add_u32 m0, s64, 33792\n"
    "ds_read_b64_tr_b16 v[72:73], v10 offset:24576\n"
    "v_mfma_f32_16x16x32_bf16 v[180:183], v[40:43], v[148:151], v[180:183]\n"
    "ds_read_b64_tr_b16 v[74:75], v10 offset:28672\n"
    "v_mfma_f32_16x16x32_bf16 v[184:187], v[44:47], v[144:147], v[184:187]\n"
    "ds_read_b64_tr_b16 v[76:77], v11 offset:24576\n"
    "buffer_load_dwordx4 v29, s[40:43], s57 offen lds\n"
    "v_mfma_f32_16x16x32_bf16 v[188:191], v[44:47], v[148:151], v[188:191]\n"
    "s_waitcnt lgkmcnt(7)\n"
    "ds_read_b64_tr_b16 v[78:79], v11 offset:28672\n"
    "v_mfma_f32_16x16x32_bf16 v[192:195], v[48:51], v[144:147], v[192:195]\n"
    "ds_read_b64_tr_b16 v[32:33], v16 offset:24576\n"
    "buffer_load_dwordx4 v[116:119], v1, s[44:47], s56 offen sc1\n"
    "v_mfma_f32_16x16x32_bf16 v[196:199], v[48:51], v[148:151], v[196:199]\n"
    "s_add_u32 s57, s70, 0x8000\n"
    "s_add_u32 m0, s64, 40960\n"
    "ds_read_b64_tr_b16 v[34:35], v16 offset:28672\n"
    "v_mfma_f32_16x16x32_bf16 v[200:203], v[52:55], v[144:147], v[200:203]\n"
    "s_add_u32 s56, s60, 0x2c00\n"
    "ds_read_b64_tr_b16 v[36:37], v17 offset:24576\n"
    "v_mfma_f32_16x16x32_bf16 v[204:207], v[52:55], v[148:151], v[204:207]\n"
    "ds_read_b64_tr_b16 v[38:39], v17 offset:28672\n"
    "buffer_load_dwordx4 v28, s[40:43], s57 offen lds\n"
    "v_mfma_f32_16x16x32_bf16 v[208:211], v[56:59], v[144:147], v[208:211]\n"
    "s_waitcnt vmcnt(25)\n"
    "s_add_u32 m0, s64, 41984\n"
    "ds_read_b64_tr_b16 v[40:41], v18 offset:24576\n"
    "v_mfma_f32_16x16x32_bf16 v[212:215], v[56:59], v[148:151], v[212:215]\n"
    "ds_read_b64_tr_b16 v[42:43], v18 offset:28672\n"
    "v_mfma_f32_16x16x32_bf16 v[216:219], v[60:63], v[144:147], v[216:219]\n"
    "ds_read_b64_tr_b16 v[44:45], v19 offset:24576\n"
    "buffer_load_dwordx4 v29, s[40:43], s57 offen lds\n"
    "v_mfma_f32_16x16x32_bf16 v[220:223], v[60:63], v[148:151], v[220:223]\n"
    "s_waitcnt lgkmcnt(7)\n"
    "ds_read_b64_tr_b16 v[46:47], v19 offset:28672\n"
    "s_waitcnt vmcnt(15)\n"
    "s_barrier\n"
    "v_add_u32 v8, s77, v8\n"
    "v_mfma_f32_16x16x32_bf16 v[160:163], v[64:67], v[152:155], v[160:163]\n"
    "ds_read_b64_tr_b16 v[48:49], v8 offset:0\n"
    "v_mfma_f32_16x16x32_bf16 v[164:167], v[64:67], v[156:159], v[164:167]\n"
    "s_add_u32 s57, s70, 0x10000\n"
    "ds_read_b64_tr_b16 v[50:51], v8 offset:4096\n"
    "v_add_u32 v9, s77, v9\n"
    "v_mfma_f32_16x16x32_bf16 v[168:171], v[68:71], v[152:155], v[168:171]\n"
    "ds_read_b64_tr_b16 v[52:53], v9 offset:0\n"
    "buffer_load_dwordx4 v[120:123], v1, s[44:47], s56 offen sc1\n"
    "v_mfma_f32_16x16x32_bf16 v[172:175], v[68:71], v[156:159], v[172:175]\n"
    "s_add_u32 s56, s60, 0x10c00\n"
    "ds_read_b64_tr_b16 v[54:55], v9 offset:4096\n"
    "v_add_u32 v10, s77, v10\n"
    "v_mfma_f32_16x16x32_bf16 v[176:179], v[72:75], v[152:155], v[176:179]\n"
    "s_add_u32 m0, s65, 0\n"
    "ds_read_b64_tr_b16 v[56:57], v10 offset:0\n"
    "v_mfma_f32_16x16x32_bf16 v[180:183], v[72:75], v[156:159], v[180:183]\n"
    "ds_read_b64_tr_b16 v[58:59], v10 offset:4096\n"
    "v_add_u32 v11, s77, v11\n"
    "v_mfma_f32_16x16x32_bf16 v[184:187], v[76:79], v[152:155], v[184:187]\n"
    "ds_read_b64_tr_b16 v[60:61], v11 offset:0\n"
    "buffer_load_dwordx4 v[124:127], v1, s[44:47], s56 offen sc1\n"
    "v_mfma_f32_16x16x32_bf16 v[188:191], v[76:79], v[156:159], v[188:191]\n"
    "s_waitcnt lgkmcnt(7)\n"
    "ds_read_b64_tr_b16 v[62:63], v11 offset:4096\n"
    "v_add_u32 v16, s77, v16\n"
    "v_mfma_f32_16x16x32_bf16 v[192:195], v[32:35], v[152:155], v[192:195]\n"
    "s_add_u32 s56, s60, 0x3000\n"
    "ds_read_b64_tr_b16 v[64:65], v16 offset:0\n"
    "v_mfma_f32_16x16x32_bf16 v[196:199], v[32:35], v[156:159], v[196:199]\n"
    "ds_read_b64_tr_b16 v[66:67], v16 offset:4096\n"
    "v_add_u32 v17, s77, v17\n"
    "v_mfma_f32_16x16x32_bf16 v[200:203], v[36:39], v[152:155], v[200:203]\n"
    "ds_read_b64_tr_b16 v[68:69], v17 offset:0\n"
    "buffer_load_dwordx4 v[128:131], v1, s[44:47], s56 offen sc1\n"
    "v_mfma_f32_16x16x32_bf16 v[204:207], v[36:39], v[156:159], v[204:207]\n"
    "s_add_u32 s56, s60, 0x11000\n"
    "ds_read_b64_tr_b16 v[70:71], v17 offset:4096\n"
    "v_add_u32 v18, s77, v18\n"
    "v_mfma_f32_16x16x32_bf16 v[208:211], v[40:43], v[152:155], v[208:211]\n"
    "ds_read_b64_tr_b16 v[72:73], v18 offset:0\n"
    "v_mfma_f32_16x16x32_bf16 v[212:215], v[40:43], v[156:159], v[212:215]\n"
    "ds_read_b64_tr_b16 v[74:75], v18 offset:4096\n"
    "v_add_u32 v19, s77, v19\n"
    "v_mfma_f32_16x16x32_bf16 v[216:219], v[44:47], v[152:155], v[216:219]\n"
    "ds_read_b64_tr_b16 v[76:77], v19 offset:0\n"
    "buffer_load_dwordx4 v[132:135], v1, s[44:47], s56 offen sc1\n"
    "v_mfma_f32_16x16x32_bf16 v[220:223], v[44:47], v[156:159], v[220:223]\n"
    "s_waitcnt lgkmcnt(7)\n"
    "ds_read_b64_tr_b16 v[78:79], v19 offset:4096\n"
    "v_mfma_f32_16x16x32_bf16 v[160:163], v[48:51], v[96:99], v[160:163]\n"
    "s_add_u32 s56, s60, 0x3400\n"
    "ds_read_b64_tr_b16 v[32:33], v8 offset:8192\n"
    "v_mfma_f32_16x16x32_bf16 v[164:167], v[48:51], v[100:103], v[164:167]\n"
    "ds_read_b64_tr_b16 v[34:35], v8 offset:12288\n"
    "buffer_load_dwordx4 v[136:139], v1, s[44:47], s56 offen sc1\n"
    "v_mfma_f32_16x16x32_bf16 v[168:171], v[52:55], v[96:99], v[168:171]\n"
    "s_add_u32 s56, s60, 0x11400\n"
    "ds_read_b64_tr_b16 v[36:37], v9 offset:8192\n"
    "v_mfma_f32_16x16x32_bf16 v[172:175], v[52:55], v[100:103], v[172:175]\n"
    "ds_read_b64_tr_b16 v[38:39], v9 offset:12288\n"
    "v_mfma_f32_16x16x32_bf16 v[176:179], v[56:59], v[96:99], v[176:179]\n"
    "ds_read_b64_tr_b16 v[40:41], v10 offset:8192\n"
    "buffer_load_dwordx4 v[140:143], v1, s[44:47], s56 offen sc1\n"
    "v_mfma_f32_16x16x32_bf16 v[180:183], v[56:59], v[100:103], v[180:183]\n"
    "s_add_u32 s56, s69, 0x0\n"
    "ds_read_b64_tr_b16 v[42:43], v10 offset:12288\n"
    "v_mfma_f32_16x16x32_bf16 v[184:187], v[60:63], v[96:99], v[184:187]\n"
    "ds_read_b64_tr_b16 v[44:45], v11 offset:8192\n"
    "v_mfma_f32_16x16x32_bf16 v[188:191], v[60:63], v[100:103], v[188:191]\n"
    "s_waitcnt lgkmcnt(7)\n"
    "ds_read_b64_tr_b16 v[46:47], v11 offset:12288\n"
    "v_mfma_f32_16x16x32_bf16 v[192:195], v[64:67], v[96:99], v[192:195]\n"
    "ds_read_b64_tr_b16 v[48:49], v16 offset:8192\n"
    "buffer_load_dwordx4 v[144:147], v1, s[44:47], s56 offen sc1\n"
    "v_mfma_f32_16x16x32_bf16 v[196:199], v[64:67], v[100:103], v[196:199]\n"
    "s_add_u32 s56, s69, 0x20000\n"
    "ds_read_b64_tr_b16 v[50:51], v16 offset:12288\n"
    "v_mfma_f32_16x16x32_bf16 v[200:203], v[68:71], v[96:99], v[200:203]\n"
    "ds_read_b64_tr_b16 v[52:53], v17 offset:8192\n"
    "v_mfma_f32_16x16x32_bf16 v[204:207], v[68:71], v[100:103], v[204:207]\n"
    "ds_read_b64_tr_b16 v[54:55], v17 offset:12288\n"
    "buffer_load_dwordx4 v28, s[40:43], s57 offen lds\n"
    "v_mfma_f32_16x16x32_bf16 v[208:211], v[72:75], v[96:99], v[208:211]\n"
    "s_waitcnt vmcnt(22)\n"
    "ds_read_b64_tr_b16 v[56:57], v18 offset:8192\n"
    "v_mfma_f32_16x16x32_bf16 v[212:215], v[72:75], v[100:103], v[212:215]\n"
    "s_add_u32 m0, s65, 1024\n"
    "ds_read_b64_tr_b16 v[58:59], v18 offset:12288\n"
    "v_mfma_f32_16x16x32_bf16 v[216:219], v[76:79], v[96:99], v[216:219]\n"
    "ds_read_b64_tr_b16 v[60:61], v19 offset:8192\n"
    "buffer_load_dwordx4 v[148:151], v1, s[44:47], s56 offen sc1\n"
    "v_mfma_f32_16x16x32_bf16 v[220:223], v[76:79], v[100:103], v[220:223]\n"
    "s_waitcnt lgkmcnt(7)\n"
    "ds_read_b64_tr_b16 v[62:63], v19 offset:12288\n"
    "s_lshl_b32 s96, s54, 2\n"
    "s_lshr_b32 s95, s53, 3\n"
    "s_add_u32 s96, s96, s95\n"
    "s_lshl_b32 s96, s96, 7\n"
    "s_add_u32 s96, s96, 0x4000\n"
    "s_and_b32 s95, s53, 4\n"
    "s_lshl_b32 s95, s95, 10\n"
    "s_add_u32 s96, s96, s95\n"
    "buffer_load_dword v24, v21, s[44:47], s96 offen sc1\n"
    "v_mfma_f32_16x16x32_bf16 v[160:163], v[32:35], v[104:107], v[160:163]\n"
    "s_add_u32 s56, s69, 0x400\n"
    "ds_read_b64_tr_b16 v[64:65], v8 offset:16384\n"
    "v_mfma_f32_16x16x32_bf16 v[164:167], v[32:35], v[108:111], v[164:167]\n"
    "ds_read_b64_tr_b16 v[66:67], v8 offset:20480\n"
    "buffer_load_dwordx4 v29, s[40:43], s57 offen lds\n"
    "v_mfma_f32_16x16x32_bf16 v[168:171], v[36:39], v[104:107], v[168:171]\n"
    "s_add_u32 s57, s70, 0x18000\n"
    "ds_read_b64_tr_b16 v[68:69], v9 offset:16384\n"
    "v_mfma_f32_16x16x32_bf16 v[172:175], v[36:39], v[108:111], v[172:175]\n"
    "s_add_u32 m0, s65, 8192\n"
    "ds_read_b64_tr_b16 v[70:71], v9 offset:20480\n"
    "v_mfma_f32_16x16x32_bf16 v[176:179], v[40:43], v[104:107], v[176:179]\n"
    "ds_read_b64_tr_b16 v[72:73], v10 offset:16384\n"
    "buffer_load_dwordx4 v[152:155], v1, s[44:47], s56 offen sc1\n"
    "v_mfma_f32_16x16x32_bf16 v[180:183], v[40:43], v[108:111], v[180:183]\n"
    "s_add_u32 s56, s69, 0x20400\n"
    "ds_read_b64_tr_b16 v[74:75], v10 offset:20480\n"
    "v_mfma_f32_16x16x32_bf16 v[184:187], v[44:47], v[104:107], v[184:187]\n"
    "ds_read_b64_tr_b16 v[76:77], v11 offset:16384\n"
    "v_mfma_f32_16x16x32_bf16 v[188:191], v[44:47], v[108:111], v[188:191]\n"
    "s_waitcnt lgkmcnt(7)\n"
    "ds_read_b64_tr_b16 v[78:79], v11 offset:20480\n"
    "v_mfma_f32_16x16x32_bf16 v[192:195], v[48:51], v[104:107], v[192:195]\n"
    "ds_read_b64_tr_b16 v[32:33], v16 offset:16384\n"
    "buffer_load_dwordx4 v28, s[40:43], s57 offen lds\n"
    "v_mfma_f32_16x16x32_bf16 v[196:199], v[48:51], v[108:111], v[196:199]\n"
    "s_add_u32 m0, s65, 9216\n"
    "ds_read_b64_tr_b16 v[34:35], v16 offset:20480\n"
    "v_mfma_f32_16x16x32_bf16 v[200:203], v[52:55], v[104:107], v[200:203]\n"
    "ds_read_b64_tr_b16 v[36:37], v17 offset:16384\n"
    "v_mfma_f32_16x16x32_bf16 v[204:207], v[52:55], v[108:111], v[204:207]\n"
    "ds_read_b64_tr_b16 v[38:39], v17 offset:20480\n"
    "buffer_load_dwordx4 v[156:159], v1, s[44:47], s56 offen sc1\n"
    "v_mfma_f32_16x16x32_bf16 v[208:211], v[56:59], v[104:107], v[208:211]\n"
    "s_waitcnt vmcnt(16)\n"
    "ds_read_b64_tr_b16 v[40:41], v18 offset:16384\n"
    "v_mfma_f32_16x16x32_bf16 v[212:215], v[56:59], v[108:111], v[212:215]\n"
    "s_add_u32 s56, s69, 0x800\n"
    "ds_read_b64_tr_b16 v[42:43], v18 offset:20480\n"
    "v_mfma_f32_16x16x32_bf16 v[216:219], v[60:63], v[104:107], v[216:219]\n"
    "ds_read_b64_tr_b16 v[44:45], v19 offset:16384\n"
    "buffer_load_dwordx4 v[96:99], v1, s[44:47], s56 offen sc1\n"
    "v_mfma_f32_16x16x32_bf16 v[220:223], v[60:63], v[108:111], v[220:223]\n"
    "s_waitcnt lgkmcnt(7)\n"
    "ds_read_b64_tr_b16 v[46:47], v19 offset:20480\n"
    "v_mfma_f32_16x16x32_bf16 v[160:163], v[64:67], v[112:115], v[160:163]\n"
    "s_add_u32 s56, s69, 0x20800\n"
    "ds_read_b64_tr_b16 v[48:49], v8 offset:24576\n"
    "v_mfma_f32_16x16x32_bf16 v[164:167], v[64:67], v[116:119], v[164:167]\n"
    "ds_read_b64_tr_b16 v[50:51], v8 offset:28672\n"
    "buffer_load_dwordx4 v29, s[40:43], s57 offen lds\n"
    "v_mfma_f32_16x16x32_bf16 v[168:171], v[68:71], v[112:115], v[168:171]\n"
    "s_add_u32 s57, s53, 2\n"
    "ds_read_b64_tr_b16 v[52:53], v9 offset:24576\n"
    "v_mfma_f32_16x16x32_bf16 v[172:175], v[68:71], v[116:119], v[172:175]\n"
    "s_and_b32 s57, s57, 7\n"
    "ds_read_b64_tr_b16 v[54:55], v9 offset:28672\n"
    "v_mfma_f32_16x16x32_bf16 v[176:179], v[72:75], v[112:115], v[176:179]\n"
    "ds_read_b64_tr_b16 v[56:57], v10 offset:24576\n"
    "buffer_load_dwordx4 v[100:103], v1, s[44:47], s56 offen sc1\n"
    "v_mfma_f32_16x16x32_bf16 v[180:183], v[72:75], v[116:119], v[180:183]\n"
    "s_lshl_b32 s70, s57, 17\n"
    "ds_read_b64_tr_b16 v[58:59], v10 offset:28672\n"
    "v_mfma_f32_16x16x32_bf16 v[184:187], v[76:79], v[112:115], v[184:187]\n"
    "s_add_u32 s70, s70, s63\n"
    "ds_read_b64_tr_b16 v[60:61], v11 offset:24576\n"
    "v_mfma_f32_16x16x32_bf16 v[188:191], v[76:79], v[116:119], v[188:191]\n"
    "s_waitcnt lgkmcnt(7)\n"
    "ds_read_b64_tr_b16 v[62:63], v11 offset:28672\n"
    "v_mfma_f32_16x16x32_bf16 v[192:195], v[32:35], v[112:115], v[192:195]\n"
    "s_add_u32 s57, s70, 0x0\n"
    "ds_read_b64_tr_b16 v[64:65], v16 offset:24576\n"
    "v_mfma_f32_16x16x32_bf16 v[196:199], v[32:35], v[116:119], v[196:199]\n"
    "s_add_u32 m0, s65, 16384\n"
    "ds_read_b64_tr_b16 v[66:67], v16 offset:28672\n"
    "v_mfma_f32_16x16x32_bf16 v[200:203], v[36:39], v[112:115], v[200:203]\n"
    "ds_read_b64_tr_b16 v[68:69], v17 offset:24576\n"
    "buffer_load_dwordx4 v28, s[40:43], s57 offen lds\n"
    "v_mfma_f32_16x16x32_bf16 v[204:207], v[36:39], v[116:119], v[204:207]\n"
    "s_add_u32 s56, s69, 0xc00\n"
    "ds_read_b64_tr_b16 v[70:71], v17 offset:28672\n"
    "v_mfma_f32_16x16x32_bf16 v[208:211], v[40:43], v[112:115], v[208:211]\n"
    "s_waitcnt vmcnt(16)\n"
    "ds_read_b64_tr_b16 v[72:73], v18 offset:24576\n"
    "v_mfma_f32_16x16x32_bf16 v[212:215], v[40:43], v[116:119], v[212:215]\n"
    "ds_read_b64_tr_b16 v[74:75], v18 offset:28672\n"
    "buffer_load_dwordx4 v[104:107], v1, s[44:47], s56 offen sc1\n"
    "v_mfma_f32_16x16x32_bf16 v[216:219], v[44:47], v[112:115], v[216:219]\n"
    "s_add_u32 m0, s65, 17408\n"
    "ds_read_b64_tr_b16 v[76:77], v19 offset:24576\n"
    "v_mfma_f32_16x16x32_bf16 v[220:223], v[44:47], v[116:119], v[220:223]\n"
    "s_waitcnt lgkmcnt(7)\n"
    "ds_read_b64_tr_b16 v[78:79], v19 offset:28672\n"
    "s_waitcnt vmcnt(9)\n"
    "v_cmp_eq_u32 vcc, s97, v24\n"
    "s_nop 0\n"
    "s_cmp_eq_u64 vcc, -1\n"
    "s_cbranch_scc1 L_pok_1_%=\n"
    "s_lshl_b32 s96, s54, 2\n"
    "s_lshr_b32 s95, s53, 3\n"
    "s_add_u32 s96, s96, s95\n"
    "s_lshl_b32 s96, s96, 7\n"
    "s_add_u32 s96, s96, 0x4000\n"
    "s_and_b32 s95, s53, 4\n"
    "s_lshl_b32 s95, s95, 10\n"
    "s_add_u32 s96, s96, s95\n"
    "s_mov_b32 s94, 2000000\n"
    "L_psp_2_%=:\n"
    "s_sleep 2\n"
    "buffer_load_dword v24, v21, s[44:47], s96 offen sc1\n"
    "s_waitcnt vmcnt(0)\n"
    "v_cmp_eq_u32 vcc, s97, v24\n"
    "s_nop 4\n"
    "s_cmp_eq_u64 vcc, -1\n"
    "s_cbranch_scc1 L_pok_1_%=\n"
    "s_sub_u32 s94, s94, 1\n"
    "s_cmp_lg_u32 s94, 0\n"
    "s_cbranch_scc1 L_psp_2_%=\n"
    "L_pok_1_%=:\n"
    "v_mfma_f32_16x16x32_bf16 v[160:163], v[48:51], v[120:123], v[160:163]\n"
    "ds_read_b64_tr_b16 v[32:33], v8 offset:32768\n"
    "buffer_load_dwordx4 v29, s[40:43], s57 offen lds\n"
    "v_mfma_f32_16x16x32_bf16 v[164:167], v[48:51], v[124:127], v[164:167]\n"
    "s_add_u32 s56, s69, 0x20c00\n"
    "ds_read_b64_tr_b16 v[34:35], v8 offset:36864\n"
    "v_mfma_f32_16x16x32_bf16 v[168:171], v[52:55], v[120:123], v[168:171]\n"
    "s_add_u32 s57, s70, 0x8000\n"
    "ds_read_b64_tr_b16 v[36:37], v9 offset:32768\n"
    "v_mfma_f32_16x16x32_bf16 v[172:175], v[52:55], v[124:127], v[172:175]\n"
    "ds_read_b64_tr_b16 v[38:39], v9 offset:36864\n"
    "buffer_load_dwordx4 v[108:111], v1, s[44:47], s56 offen sc1\n"
    "v_mfma_f32_16x16x32_bf16 v[176:179], v[56:59], v[120:123], v[176:179]\n"
    "s_add_u32 s56, s53, 1\n"
    "ds_read_b64_tr_b16 v[40:41], v10 offset:32768\n"
    "v_mfma_f32_16x16x32_bf16 v[180:183], v[56:59], v[124:127], v[180:183]\n"
    "s_add_u32 m0, s65, 24576\n"
    "ds_read_b64_tr_b16 v[42:43], v10 offset:36864\n"
    "v_mfma_f32_16x16x32_bf16 v[184:187], v[60:63], v[120:123], v[184:187]\n"
    "ds_read_b64_tr_b16 v[44:45], v11 offset:32768\n"
    "buffer_load_dwordx4 v28, s[40:43], s57 offen lds\n"
    "v_mfma_f32_16x16x32_bf16 v[188:191], v[60:63], v[124:127], v[188:191]\n"
    "s_waitcnt lgkmcnt(7)\n"
    "ds_read_b64_tr_b16 v[46:47], v11 offset:36864\n"
    "v_mfma_f32_16x16x32_bf16 v[192:195], v[64:67], v[120:123], v[192:195]\n"
    "s_and_b32 s56, s56, 7\n"
    "ds_read_b64_tr_b16 v[48:49], v16 offset:32768\n"
    "v_mfma_f32_16x16x32_bf16 v[196:199], v[64:67], v[124:127], v[196:199]\n"
    "s_lshl_b32 s69, s56, 12\n"
    "ds_read_b64_tr_b16 v[50:51], v16 offset:36864\n"
    "v_mfma_f32_16x16x32_bf16 v[200:203], v[68:71], v[120:123], v[200:203]\n"
    "s_add_u32 s69, s69, s61\n"
    "ds_read_b64_tr_b16 v[52:53], v17 offset:32768\n"
    "v_mfma_f32_16x16x32_bf16 v[204:207], v[68:71], v[124:127], v[204:207]\n"
    "s_add_u32 s56, s69, 0x0\n"
    "ds_read_b64_tr_b16 v[54:55], v17 offset:36864\n"
    "v_mfma_f32_16x16x32_bf16 v[208:211], v[72:75], v[120:123], v[208:211]\n"
    "ds_read_b64_tr_b16 v[56:57], v18 offset:32768\n"
    "buffer_load_dwordx4 v[112:115], v1, s[44:47], s56 offen sc1\n"
    "v_mfma_f32_16x16x32_bf16 v[212:215], v[72:75], v[124:127], v[212:215]\n"
    "s_add_u32 m0, s65, 25600\n"
    "ds_read_b64_tr_b16 v[58:59], v18 offset:36864\n"
    "v_mfma_f32_16x16x32_bf16 v[216:219], v[76:79], v[120:123], v[216:219]\n"
    "s_add_u32 s56, s69, 0x20000\n"
    "ds_read_b64_tr_b16 v[60:61], v19 offset:32768\n"
    "v_mfma_f32_16x16x32_bf16 v[220:223], v[76:79], v[124:127], v[220:223]\n"
    "s_waitcnt lgkmcnt(7)\n"
    "ds_read_b64_tr_b16 v[62:63], v19 offset:36864\n"
    "buffer_load_dwordx4 v[116:119], v1, s[44:47], s56 offen sc1\n"
    "v_mfma_f32_16x16x32_bf16 v[160:163], v[32:35], v[128:131], v[160:163]\n"
    "s_add_u32 s56, s69, 0x400\n"
    "ds_read_b64_tr_b16 v[64:65], v8 offset:40960\n"
    "v_mfma_f32_16x16x32_bf16 v[164:167], v[32:35], v[132:135], v[164:167]\n"
    "ds_read_b64_tr_b16 v[66:67], v8 offset:45056\n"
    "buffer_load_dwordx4 v29, s[40:43], s57 offen lds\n"
    "v_mfma_f32_16x16x32_bf16 v[168:171], v[36:39], v[128:131], v[168:171]\n"
    "s_add_u32 s57, s70, 0x10000\n"
    "s_add_u32 m0, s65, 32768\n"
    "ds_read_b64_tr_b16 v[68:69], v9 offset:40960\n"
    "v_mfma_f32_16x16x32_bf16 v[172:175], v[36:39], v[132:135], v[172:175]\n"
    "ds_read_b64_tr_b16 v[70:71], v9 offset:45056\n"
    "v_mfma_f32_16x16x32_bf16 v[176:179], v[40:43], v[128:131], v[176:179]\n"
    "ds_read_b64_tr_b16 v[72:73], v10 offset:40960\n"
    "buffer_load_dwordx4 v28, s[40:43], s57 offen lds\n"
    "v_mfma_f32_16x16x32_bf16 v[180:183], v[40:43], v[132:135], v[180:183]\n"
    "s_add_u32 m0, s65, 33792\n"
    "ds_read_b64_tr_b16 v[74:75], v10 offset:45056\n"
    "v_mfma_f32_16x16x32_bf16 v[184:187], v[44:47], v[128:131], v[184:187]\n"
    "ds_read_b64_tr_b16 v[76:77], v11 offset:40960\n"
    "v_mfma_f32_16x16x32_bf16 v[188:191], v[44:47], v[132:135], v[188:191]\n"
    "s_waitcnt lgkmcnt(7)\n"
    "ds_read_b64_tr_b16 v[78:79], v11 offset:45056\n"
    "v_mfma_f32_16x16x32_bf16 v[192:195], v[48:51], v[128:131], v[192:195]\n"
    "ds_read_b64_tr_b16 v[32:33], v16 offset:40960\n"
    "buffer_load_dwordx4 v29, s[40:43], s57 offen lds\n"
    "v_mfma_f32_16x16x32_bf16 v[196:199], v[48:51], v[132:135], v[196:199]\n"
    "s_add_u32 s57, s70, 0x18000\n"
    "s_add_u32 m0, s65, 40960\n"
    "ds_read_b64_tr_b16 v[34:35], v16 offset:45056\n"
    "v_mfma_f32_16x16x32_bf16 v[200:203], v[52:55], v[128:131], v[200:203]\n"
    "ds_read_b64_tr_b16 v[36:37], v17 offset:40960\n"
    "v_mfma_f32_16x16x32_bf16 v[204:207], v[52:55], v[132:135], v[204:207]\n"
    "ds_read_b64_tr_b16 v[38:39], v17 offset:45056\n"
    "buffer_load_dwordx4 v28, s[40:43], s57 offen lds\n"
    "v_mfma_f32_16x16x32_bf16 v[208:211], v[56:59], v[128:131], v[208:211]\n"
    "s_add_u32 m0, s65, 41984\n"
    "ds_read_b64_tr_b16 v[40:41], v18 offset:40960\n"
    "buffer_load_dwordx4 v[120:123], v1, s[44:47], s56 offen sc1\n"
    "v_mfma_f32_16x16x32_bf16 v[212:215], v[56:59], v[132:135], v[212:215]\n"
    "s_add_u32 s56, s69, 0x20400\n"
    "ds_read_b64_tr_b16 v[42:43], v18 offset:45056\n"
    "v_mfma_f32_16x16x32_bf16 v[216:219], v[60:63], v[128:131], v[216:219]\n"
    "ds_read_b64_tr_b16 v[44:45], v19 offset:40960\n"
    "v_mfma_f32_16x16x32_bf16 v[220:223], v[60:63], v[132:135], v[220:223]\n"
    "s_waitcnt lgkmcnt(7)\n"
    "ds_read_b64_tr_b16 v[46:47], v19 offset:45056\n"
    "buffer_load_dwordx4 v29, s[40:43], s57 offen lds\n"
    "buffer_load_dwordx4 v[124:127], v1, s[44:47], s56 offen sc1\n"
    "s_barrier\n"
    "v_add_u32 v8, s78, v8\n"
    "v_mfma_f32_16x16x32_bf16 v[160:163], v[64:67], v[136:139], v[160:163]\n"
    "s_add_u32 s56, s69, 0x800\n"
    "ds_read_b64_tr_b16 v[48:49], v8 offset:0\n"
    "v_mfma_f32_16x16x32_bf16 v[164:167], v[64:67], v[140:143], v[164:167]\n"
    "s_add_u32 s57, s53, 3\n"
    "ds_read_b64_tr_b16 v[50:51], v8 offset:4096\n"
    "v_add_u32 v9, s78, v9\n"
    "v_mfma_f32_16x16x32_bf16 v[168:171], v[68:71], v[136:139], v[168:171]\n"
    "ds_read_b64_tr_b16 v[52:53], v9 offset:0\n"
    "buffer_load_dwordx4 v[128:131], v1, s[44:47], s56 offen sc1\n"
    "v_mfma_f32_16x16x32_bf16 v[172:175], v[68:71], v[140:143], v[172:175]\n"
    "s_add_u32 s56, s69, 0x20800\n"
    "ds_read_b64_tr_b16 v[54:55], v9 offset:4096\n"
    "v_add_u32 v10, s78, v10\n"
    "v_mfma_f32_16x16x32_bf16 v[176:179], v[72:75], v[136:139], v[176:179]\n"
    "s_and_b32 s57, s57, 7\n"
    "ds_read_b64_tr_b16 v[56:57], v10 offset:0\n"
    "v_mfma_f32_16x16x32_bf16 v[180:183], v[72:75], v[140:143], v[180:183]\n"
    "s_lshl_b32 s70, s57, 17\n"
    "ds_read_b64_tr_b16 v[58:59], v10 offset:4096\n"
    "v_add_u32 v11, s78, v11\n"
    "v_mfma_f32_16x16x32_bf16 v[184:187], v[76:79], v[136:139], v[184:187]\n"
    "ds_read_b64_tr_b16 v[60:61], v11 offset:0\n"
    "buffer_load_dwordx4 v[132:135], v1, s[44:47], s56 offen sc1\n"
    "v_mfma_f32_16x16x32_bf16 v[188:191], v[76:79], v[140:143], v[188:191]\n"
    "s_waitcnt lgkmcnt(7)\n"
    "ds_read_b64_tr_b16 v[62:63], v11 offset:4096\n"
    "v_add_u32 v16, s78, v16\n"
    "v_mfma_f32_16x16x32_bf16 v[192:195], v[32:35], v[136:139], v[192:195]\n"
    "s_add_u32 s70, s70, s63\n"
    "ds_read_b64_tr_b16 v[64:65], v16 offset:0\n"
    "v_mfma_f32_16x16x32_bf16 v[196:199], v[32:35], v[140:143], v[196:199]\n"
    "s_add_u32 s57, s70, 0x0\n"
    "ds_read_b64_tr_b16 v[66:67], v16 offset:4096\n"
    "v_add_u32 v17, s78, v17\n"
    "v_mfma_f32_16x16x32_bf16 v[200:203], v[36:39], v[136:139], v[200:203]\n"
    "s_add_u32 m0, s66, 0\n"
    "ds_read_b64_tr_b16 v[68:69], v17 offset:0\n"
    "v_mfma_f32_16x16x32_bf16 v[204:207], v[36:39], v[140:143], v[204:207]\n"
    "ds_read_b64_tr_b16 v[70:71], v17 offset:4096\n"
    "v_add_u32 v18, s78, v18\n"
    "v_mfma_f32_16x16x32_bf16 v[208:211], v[40:43], v[136:139], v[208:211]\n"
    "ds_read_b64_tr_b16 v[72:73], v18 offset:0\n"
    "v_mfma_f32_16x16x32_bf16 v[212:215], v[40:43], v[140:143], v[212:215]\n"
    "ds_read_b64_tr_b16 v[74:75], v18 offset:4096\n"
    "v_add_u32 v19, s78, v19\n"
    "v_mfma_f32_16x16x32_bf16 v[216:219], v[44:47], v[136:139], v[216:219]\n"
    "ds_read_b64_tr_b16 v[76:77], v19 offset:0\n"
    "buffer_load_dwordx4 v28, s[40:43], s57 offen lds\n"
    "v_mfma_f32_16x16x32_bf16 v[220:223], v[44:47], v[140:143], v[220:223]\n"
    "s_waitcnt lgkmcnt(7)\n"
    "ds_read_b64_tr_b16 v[78:79], v19 offset:4096\n"
    "v_mfma_f32_16x16x32_bf16 v[160:163], v[48:51], v[144:147], v[160:163]\n"
    "s_add_u32 s56, s69, 0xc00\n"
    "ds_read_b64_tr_b16 v[32:33], v8 offset:8192\n"
    "v_mfma_f32_16x16x32_bf16 v[164:167], v[48:51], v[148:151], v[164:167]\n"
    "ds_read_b64_tr_b16 v[34:35], v8 offset:12288\n"
    "buffer_load_dwordx4 v[136:139], v1, s[44:47], s56 offen sc1\n"
    "v_mfma_f32_16x16x32_bf16 v[168:171], v[52:55], v[144:147], v[168:171]\n"
    "s_add_u32 s56, s69, 0x20c00\n"
    "ds_read_b64_tr_b16 v[36:37], v9 offset:8192\n"
    "v_mfma_f32_16x16x32_bf16 v[172:175], v[52:55], v[148:151], v[172:175]\n"
    "s_add_u32 m0, s66, 1024\n"
    "ds_read_b64_tr_b16 v[38:39], v9 offset:12288\n"
    "v_mfma_f32_16x16x32_bf16 v[176:179], v[56:59], v[144:147], v[176:179]\n"
    "ds_read_b64_tr_b16 v[40:41], v10 offset:8192\n"
    "buffer_load_dwordx4 v[140:143], v1, s[44:47], s56 offen sc1\n"
    "v_mfma_f32_16x16x32_bf16 v[180:183], v[56:59], v[148:151], v[180:183]\n"
    "ds_read_b64_tr_b16 v[42:43], v10 offset:12288\n"
    "v_mfma_f32_16x16x32_bf16 v[184:187], v[60:63], v[144:147], v[184:187]\n"
    "ds_read_b64_tr_b16 v[44:45], v11 offset:8192\n"
    "v_mfma_f32_16x16x32_bf16 v[188:191], v[60:63], v[148:151], v[188:191]\n"
    "s_waitcnt lgkmcnt(7)\n"
    "ds_read_b64_tr_b16 v[46:47], v11 offset:12288\n"
    "v_mfma_f32_16x16x32_bf16 v[192:195], v[64:67], v[144:147], v[192:195]\n"
    "ds_read_b64_tr_b16 v[48:49], v16 offset:8192\n"
    "buffer_load_dwordx4 v29, s[40:43], s57 offen lds\n"
    "v_mfma_f32_16x16x32_bf16 v[196:199], v[64:67], v[148:151], v[196:199]\n"
    "s_add_u32 s57, s70, 0x8000\n"
    "ds_read_b64_tr_b16 v[50:51], v16 offset:12288\n"
    "v_mfma_f32_16x16x32_bf16 v[200:203], v[68:71], v[144:147], v[200:203]\n"
    "s_add_u32 m0, s66, 8192\n"
    "ds_read_b64_tr_b16 v[52:53], v17 offset:8192\n"
    "v_mfma_f32_16x16x32_bf16 v[204:207], v[68:71], v[148:151], v[204:207]\n"
    "ds_read_b64_tr_b16 v[54:55], v17 offset:12288\n"
    "buffer_load_dwordx4 v28, s[40:43], s57 offen lds\n"
    "v_mfma_f32_16x16x32_bf16 v[208:211], v[72:75], v[144:147], v[208:211]\n"
    "s_waitcnt vmcnt(24)\n"
    "ds_read_b64_tr_b16 v[56:57], v18 offset:8192\n"
    "v_mfma_f32_16x16x32_bf16 v[212:215], v[72:75], v[148:151], v[212:215]\n"
    "s_add_u32 m0, s66, 9216\n"
    "ds_read_b64_tr_b16 v[58:59], v18 offset:12288\n"
    "v_mfma_f32_16x16x32_bf16 v[216:219], v[76:79], v[144:147], v[216:219]\n"
    "ds_read_b64_tr_b16 v[60:61], v19 offset:8192\n"
    "buffer_load_dwordx4 v29, s[40:43], s57 offen lds\n"
    "v_mfma_f32_16x16x32_bf16 v[220:223], v[76:79], v[148:151], v[220:223]\n"
    "s_waitcnt lgkmcnt(7)\n"
    "ds_read_b64_tr_b16 v[62:63], v19 offset:12288\n"
    "v_mfma_f32_16x16x32_bf16 v[160:163], v[32:35], v[152:155], v[160:163]\n"
    "s_add_u32 s56, s53, 2\n"
    "ds_read_b64_tr_b16 v[64:65], v8 offset:16384\n"
    "v_mfma_f32_16x16x32_bf16 v[164:167], v[32:35], v[156:159], v[164:167]\n"
    "s_and_b32 s56, s56, 7\n"
    "ds_read_b64_tr_b16 v[66:67], v8 offset:20480\n"
    "v_mfma_f32_16x16x32_bf16 v[168:171], v[36:39], v[152:155], v[168:171]\n"
    "s_lshl_b32 s69, s56, 12\n"
    "ds_read_b64_tr_b16 v[68:69], v9 offset:16384\n"
    "v_mfma_f32_16x16x32_bf16 v[172:175], v[36:39], v[156:159], v[172:175]\n"
    "s_add_u32 s69, s69, s61\n"
    "ds_read_b64_tr_b16 v[70:71], v9 offset:20480\n"
    "v_mfma_f32_16x16x32_bf16 v[176:179], v[40:43], v[152:155], v[176:179]\n"
    "s_add_u32 s56, s69, 0x0\n"
    "ds_read_b64_tr_b16 v[72:73], v10 offset:16384\n"
    "v_mfma_f32_16x16x32_bf16 v[180:183], v[40:43], v[156:159], v[180:183]\n"
    "ds_read_b64_tr_b16 v[74:75], v10 offset:20480\n"
    "buffer_load_dwordx4 v[144:147], v1, s[44:47], s56 offen sc1\n"
    "v_mfma_f32_16x16x32_bf16 v[184:187], v[44:47], v[152:155], v[184:187]\n"
    "s_add_u32 s56, s69, 0x20000\n"
    "ds_read_b64_tr_b16 v[76:77], v11 offset:16384\n"
    "v_mfma_f32_16x16x32_bf16 v[188:191], v[44:47], v[156:159], v[188:191]\n"
    "s_waitcnt lgkmcnt(7)\n"
    "ds_read_b64_tr_b16 v[78:79], v11 offset:20480\n"
    "v_mfma_f32_16x16x32_bf16 v[192:195], v[48:51], v[152:155], v[192:195]\n"
    "ds_read_b64_tr_b16 v[32:33], v16 offset:16384\n"
    "buffer_load_dwordx4 v[148:151], v1, s[44:47], s56 offen sc1\n"
    "v_mfma_f32_16x16x32_bf16 v[196:199], v[48:51], v[156:159], v[196:199]\n"
    "s_add_u32 s57, s70, 0x10000\n"
    "ds_read_b64_tr_b16 v[34:35], v16 offset:20480\n"
    "v_mfma_f32_16x16x32_bf16 v[200:203], v[52:55], v[152:155], v[200:203]\n"
    "s_add_u32 m0, s66, 16384\n"
    "ds_read_b64_tr_b16 v[36:37], v17 offset:16384\n"
    "v_mfma_f32_16x16x32_bf16 v[204:207], v[52:55], v[156:159], v[204:207]\n"
    "ds_read_b64_tr_b16 v[38:39], v17 offset:20480\n"
    "buffer_load_dwordx4 v28, s[40:43], s57 offen lds\n"
    "v_mfma_f32_16x16x32_bf16 v[208:211], v[56:59], v[152:155], v[208:211]\n"
    "s_waitcnt vmcnt(25)\n"
    "ds_read_b64_tr_b16 v[40:41], v18 offset:16384\n"
    "v_mfma_f32_16x16x32_bf16 v[212:215], v[56:59], v[156:159], v[212:215]\n"
    "s_add_u32 m0, s66, 17408\n"
    "ds_read_b64_tr_b16 v[42:43], v18 offset:20480\n"
    "v_mfma_f32_16x16x32_bf16 v[216:219], v[60:63], v[152:155], v[216:219]\n"
    "ds_read_b64_tr_b16 v[44:45], v19 offset:16384\n"
    "buffer_load_dwordx4 v29, s[40:43], s57 offen lds\n"
    "v_mfma_f32_16x16x32_bf16 v[220:223], v[60:63], v[156:159], v[220:223]\n"
    "s_waitcnt lgkmcnt(7)\n"
    "ds_read_b64_tr_b16 v[46:47], v19 offset:20480\n"
    "v_mfma_f32_16x16x32_bf16 v[160:163], v[64:67], v[96:99], v[160:163]\n"
    "s_add_u32 s56, s69, 0x400\n"
    "ds_read_b64_tr_b16 v[48:49], v8 offset:24576\n"
    "v_mfma_f32_16x16x32_bf16 v[164:167], v[64:67], v[100:103], v[164:167]\n"
    "ds_read_b64_tr_b16 v[50:51], v8 offset:28672\n"
    "buffer_load_dwordx4 v[152:155], v1, s[44:47], s56 offen sc1\n"
    "v_mfma_f32_16x16x32_bf16 v[168:171], v[68:71], v[96:99], v[168:171]\n"
    "s_add_u32 s56, s69, 0x20400\n"
    "ds_read_b64_tr_b16 v[52:53], v9 offset:24576\n"
    "v_mfma_f32_16x16x32_bf16 v[172:175], v[68:71], v[100:103], v[172:175]\n"
    "s_add_u32 s57, s70, 0x18000\n"
    "ds_read_b64_tr_b16 v[54:55], v9 offset:28672\n"
    "v_mfma_f32_16x16x32_bf16 v[176:179], v[72:75], v[96:99], v[176:179]\n"
    "ds_read_b64_tr_b16 v[56:57], v10 offset:24576\n"
    "buffer_load_dwordx4 v[156:159], v1, s[44:47], s56 offen sc1\n"
    "v_mfma_f32_16x16x32_bf16 v[180:183], v[72:75], v[100:103], v[180:183]\n"
    "s_add_u32 m0, s66, 24576\n"
    "ds_read_b64_tr_b16 v[58:59], v10 offset:28672\n"
    "v_mfma_f32_16x16x32_bf16 v[184:187], v[76:79], v[96:99], v[184:187]\n"
    "ds_read_b64_tr_b16 v[60:61], v11 offset:24576\n"
    "v_mfma_f32_16x16x32_bf16 v[188:191], v[76:79], v[100:103], v[188:191]\n"
    "s_waitcnt lgkmcnt(7)\n"
    "ds_read_b64_tr_b16 v[62:63], v11 offset:28672\n"
    "v_mfma_f32_16x16x32_bf16 v[192:195], v[32:35], v[96:99], v[192:195]\n"
    "ds_read_b64_tr_b16 v[64:65], v16 offset:24576\n"
    "buffer_load_dwordx4 v28, s[40:43], s57 offen lds\n"
    "v_mfma_f32_16x16x32_bf16 v[196:199], v[32:35], v[100:103], v[196:199]\n"
    "s_add_u32 m0, s66, 25600\n"
    "ds_read_b64_tr_b16 v[66:67], v16 offset:28672\n"
    "v_mfma_f32_16x16x32_bf16 v[200:203], v[36:39], v[96:99], v[200:203]\n"
    "ds_read_b64_tr_b16 v[68:69], v17 offset:24576\n"
    "v_mfma_f32_16x16x32_bf16 v[204:207], v[36:39], v[100:103], v[204:207]\n"
    "ds_read_b64_tr_b16 v[70:71], v17 offset:28672\n"
    "buffer_load_dwordx4 v29, s[40:43], s57 offen lds\n"
    "v_mfma_f32_16x16x32_bf16 v[208:211], v[40:43], v[96:99], v[208:211]\n"
    "s_waitcnt vmcnt(26)\n"
    "ds_read_b64_tr_b16 v[72:73], v18 offset:24576\n"
    "v_mfma_f32_16x16x32_bf16 v[212:215], v[40:43], v[100:103], v[212:215]\n"
    "ds_read_b64_tr_b16 v[74:75], v18 offset:28672\n"
    "v_mfma_f32_16x16x32_bf16 v[216:219], v[44:47], v[96:99], v[216:219]\n"
    "ds_read_b64_tr_b16 v[76:77], v19 offset:24576\n"
    "v_mfma_f32_16x16x32_bf16 v[220:223], v[44:47], v[100:103], v[220:223]\n"
    "s_waitcnt lgkmcnt(7)\n"
    "ds_read_b64_tr_b16 v[78:79], v19 offset:28672\n"
    "v_mfma_f32_16x16x32_bf16 v[160:163], v[48:51], v[104:107], v[160:163]\n"
    "s_add_u32 s56, s69, 0x800\n"
    "ds_read_b64_tr_b16 v[32:33], v8 offset:32768\n"
    "v_mfma_f32_16x16x32_bf16 v[164:167], v[48:51], v[108:111], v[164:167]\n"
    "ds_read_b64_tr_b16 v[34:35], v8 offset:36864\n"
    "buffer_load_dwordx4 v[96:99], v1, s[44:47], s56 offen sc1\n"
    "v_mfma_f32_16x16x32_bf16 v[168:171], v[52:55], v[104:107], v[168:171]\n"
    "s_add_u32 s56, s69, 0x20800\n"
    "ds_read_b64_tr_b16 v[36:37], v9 offset:32768\n"
    "v_mfma_f32_16x16x32_bf16 v[172:175], v[52:55], v[108:111], v[172:175]\n"
    "s_add_u32 s57, s53, 4\n"
    "ds_read_b64_tr_b16 v[38:39], v9 offset:36864\n"
    "v_mfma_f32_16x16x32_bf16 v[176:179], v[56:59], v[104:107], v[176:179]\n"
    "ds_read_b64_tr_b16 v[40:41], v10 offset:32768\n"
    "buffer_load_dwordx4 v[100:103], v1, s[44:47], s56 offen sc1\n"
    "v_mfma_f32_16x16x32_bf16 v[180:183], v[56:59], v[108:111], v[180:183]\n"
    "s_and_b32 s57, s57, 7\n"
    "ds_read_b64_tr_b16 v[42:43], v10 offset:36864\n"
    "v_mfma_f32_16x16x32_bf16 v[184:187], v[60:63], v[104:107], v[184:187]\n"
    "s_lshl_b32 s70, s57, 17\n"
    "ds_read_b64_tr_b16 v[44:45], v11 offset:32768\n"
    "v_mfma_f32_16x16x32_bf16 v[188:191], v[60:63], v[108:111], v[188:191]\n"
    "s_waitcnt lgkmcnt(7)\n"
    "ds_read_b64_tr_b16 v[46:47], v11 offset:36864\n"
    "v_mfma_f32_16x16x32_bf16 v[192:195], v[64:67], v[104:107], v[192:195]\n"
    "s_add_u32 s70, s70, s63\n"
    "ds_read_b64_tr_b16 v[48:49], v16 offset:32768\n"
    "v_mfma_f32_16x16x32_bf16 v[196:199], v[64:67], v[108:111], v[196:199]\n"
    "s_add_u32 s57, s70, 0x0\n"
    "ds_read_b64_tr_b16 v[50:51], v16 offset:36864\n"
    "v_mfma_f32_16x16x32_bf16 v[200:203], v[68:71], v[104:107], v[200:203]\n"
    "s_add_u32 m0, s66, 32768\n"
    "ds_read_b64_tr_b16 v[52:53], v17 offset:32768\n"
    "v_mfma_f32_16x16x32_bf16 v[204:207], v[68:71], v[108:111], v[204:207]\n"
    "ds_read_b64_tr_b16 v[54:55], v17 offset:36864\n"
    "buffer_load_dwordx4 v28, s[40:43], s57 offen lds\n"
    "v_mfma_f32_16x16x32_bf16 v[208:211], v[72:75], v[104:107], v[208:211]\n"
    "s_waitcnt vmcnt(26)\n"
    "ds_read_b64_tr_b16 v[56:57], v18 offset:32768\n"
    "v_mfma_f32_16x16x32_bf16 v[212:215], v[72:75], v[108:111], v[212:215]\n"
    "s_add_u32 m0, s66, 33792\n"
    "ds_read_b64_tr_b16 v[58:59], v18 offset:36864\n"
    "v_mfma_f32_16x16x32_bf16 v[216:219], v[76:79], v[104:107], v[216:219]\n"
    "ds_read_b64_tr_b16 v[60:61], v19 offset:32768\n"
    "buffer_load_dwordx4 v29, s[40:43], s57 offen lds\n"
    "v_mfma_f32_16x16x32_bf16 v[220:223], v[76:79], v[108:111], v[220:223]\n"
    "s_waitcnt lgkmcnt(7)\n"
    "ds_read_b64_tr_b16 v[62:63], v19 offset:36864\n"
    "v_mfma_f32_16x16x32_bf16 v[160:163], v[32:35], v[112:115], v[160:163]\n"
    "s_add_u32 s56, s69, 0xc00\n"
    "ds_read_b64_tr_b16 v[64:65], v8 offset:40960\n"
    "v_mfma_f32_16x16x32_bf16 v[164:167], v[32:35], v[116:119], v[164:167]\n"
    "ds_read_b64_tr_b16 v[66:67], v8 offset:45056\n"
    "buffer_load_dwordx4 v[104:107], v1, s[44:47], s56 offen sc1\n"
    "v_mfma_f32_16x16x32_bf16 v[168:171], v[36:39], v[112:115], v[168:171]\n"
    "s_add_u32 s57, s70, 0x8000\n"
    "ds_read_b64_tr_b16 v[68:69], v9 offset:40960\n"
    "v_mfma_f32_16x16x32_bf16 v[172:175], v[36:39], v[116:119], v[172:175]\n"
    "s_add_u32 m0, s66, 40960\n"
    "ds_read_b64_tr_b16 v[70:71], v9 offset:45056\n"
    "v_mfma_f32_16x16x32_bf16 v[176:179], v[40:43], v[112:115], v[176:179]\n"
    "ds_read_b64_tr_b16 v[72:73], v10 offset:40960\n"
    "buffer_load_dwordx4 v28, s[40:43], s57 offen lds\n"
    "v_mfma_f32_16x16x32_bf16 v[180:183], v[40:43], v[116:119], v[180:183]\n"
    "s_add_u32 s56, s69, 0x20c00\n"
    "ds_read_b64_tr_b16 v[74:75], v10 offset:45056\n"
    "v_mfma_f32_16x16x32_bf16 v[184:187], v[44:47], v[112:115], v[184:187]\n"
    "ds_read_b64_tr_b16 v[76:77], v11 offset:40960\n"
    "v_mfma_f32_16x16x32_bf16 v[188:191], v[44:47], v[116:119], v[188:191]\n"
    "s_waitcnt lgkmcnt(7)\n"
    "ds_read_b64_tr_b16 v[78:79], v11 offset:45056\n"
    "v_mfma_f32_16x16x32_bf16 v[192:195], v[48:51], v[112:115], v[192:195]\n"
    "ds_read_b64_tr_b16 v[32:33], v16 offset:40960\n"
    "buffer_load_dwordx4 v[108:111], v1, s[44:47], s56 offen sc1\n"
    "v_mfma_f32_16x16x32_bf16 v[196:199], v[48:51], v[116:119], v[196:199]\n"
    "s_add_u32 m0, s66, 41984\n"
    "ds_read_b64_tr_b16 v[34:35], v16 offset:45056\n"
    "v_mfma_f32_16x16x32_bf16 v[200:203], v[52:55], v[112:115], v[200:203]\n"
    "ds_read_b64_tr_b16 v[36:37], v17 offset:40960\n"
    "v_mfma_f32_16x16x32_bf16 v[204:207], v[52:55], v[116:119], v[204:207]\n"
    "ds_read_b64_tr_b16 v[38:39], v17 offset:45056\n"
    "buffer_load_dwordx4 v29, s[40:43], s57 offen lds\n"
    "v_mfma_f32_16x16x32_bf16 v[208:211], v[56:59], v[112:115], v[208:211]\n"
    "s_waitcnt vmcnt(24)\n"
    "ds_read_b64_tr_b16 v[40:41], v18 offset:40960\n"
    "v_mfma_f32_16x16x32_bf16 v[212:215], v[56:59], v[116:119], v[212:215]\n"
    "ds_read_b64_tr_b16 v[42:43], v18 offset:45056\n"
    "v_mfma_f32_16x16x32_bf16 v[216:219], v[60:63], v[112:115], v[216:219]\n"
    "ds_read_b64_tr_b16 v[44:45], v19 offset:40960\n"
    "v_mfma_f32_16x16x32_bf16 v[220:223], v[60:63], v[116:119], v[220:223]\n"
    "s_waitcnt lgkmcnt(7)\n"
    "ds_read_b64_tr_b16 v[46:47], v19 offset:45056\n"
    "s_barrier\n"
    "v_add_u32 v8, s76, v8\n"
    "v_mfma_f32_16x16x32_bf16 v[160:163], v[64:67], v[120:123], v[160:163]\n"
    "s_add_u32 s56, s53, 3\n"
    "ds_read_b64_tr_b16 v[48:49], v8 offset:0\n"
    "v_mfma_f32_16x16x32_bf16 v[164:167], v[64:67], v[124:127], v[164:167]\n"
    "s_and_b32 s56, s56, 7\n"
    "ds_read_b64_tr_b16 v[50:51], v8 offset:4096\n"
    "v_add_u32 v9, s76, v9\n"
    "v_mfma_f32_16x16x32_bf16 v[168:171], v[68:71], v[120:123], v[168:171]\n"
    "s_lshl_b32 s69, s56, 12\n"
    "ds_read_b64_tr_b16 v[52:53], v9 offset:0\n"
    "v_mfma_f32_16x16x32_bf16 v[172:175], v[68:71], v[124:127], v[172:175]\n"
    "s_add_u32 s69, s69, s61\n"
    "ds_read_b64_tr_b16 v[54:55], v9 offset:4096\n"
    "v_add_u32 v10, s76, v10\n"
    "v_mfma_f32_16x16x32_bf16 v[176:179], v[72:75], v[120:123], v[176:179]\n"
    "s_add_u32 s56, s69, 0x0\n"
    "ds_read_b64_tr_b16 v[56:57], v10 offset:0\n"
    "v_mfma_f32_16x16x32_bf16 v[180:183], v[72:75], v[124:127], v[180:183]\n"
    "s_add_u32 s57, s70, 0x10000\n"
    "ds_read_b64_tr_b16 v[58:59], v10 offset:4096\n"
    "v_add_u32 v11, s76, v11\n"
    "v_mfma_f32_16x16x32_bf16 v[184:187], v[76:79], v[120:123], v[184:187]\n"
    "ds_read_b64_tr_b16 v[60:61], v11 offset:0\n"
    "buffer_load_dwordx4 v[112:115], v1, s[44:47], s56 offen sc1\n"
    "v_mfma_f32_16x16x32_bf16 v[188:191], v[76:79], v[124:127], v[188:191]\n"
    "s_waitcnt lgkmcnt(7)\n"
    "ds_read_b64_tr_b16 v[62:63], v11 offset:4096\n"
    "v_add_u32 v16, s76, v16\n"
    "v_mfma_f32_16x16x32_bf16 v[192:195], v[32:35], v[120:123], v[192:195]\n"
    "s_add_u32 s56, s69, 0x20000\n"
    "ds_read_b64_tr_b16 v[64:65], v16 offset:0\n"
    "v_mfma_f32_16x16x32_bf16 v[196:199], v[32:35], v[124:127], v[196:199]\n"
    "s_add_u32 m0, s64, 0\n"
    "ds_read_b64_tr_b16 v[66:67], v16 offset:4096\n"
    "v_add_u32 v17, s76, v17\n"
    "v_mfma_f32_16x16x32_bf16 v[200:203], v[36:39], v[120:123], v[200:203]\n"
    "ds_read_b64_tr_b16 v[68:69], v17 offset:0\n"
    "buffer_load_dwordx4 v[116:119], v1, s[44:47], s56 offen sc1\n"
    "v_mfma_f32_16x16x32_bf16 v[204:207], v[36:39], v[124:127], v[204:207]\n"
    "ds_read_b64_tr_b16 v[70:71], v17 offset:4096\n"
    "v_add_u32 v18, s76, v18\n"
    "v_mfma_f32_16x16x32_bf16 v[208:211], v[40:43], v[120:123], v[208:211]\n"
    "s_waitcnt vmcnt(24)\n"
    "ds_read_b64_tr_b16 v[72:73], v18 offset:0\n"
    "v_mfma_f32_16x16x32_bf16 v[212:215], v[40:43], v[124:127], v[212:215]\n"
    "ds_read_b64_tr_b16 v[74:75], v18 offset:4096\n"
    "v_add_u32 v19, s76, v19\n"
    "v_mfma_f32_16x16x32_bf16 v[216:219], v[44:47], v[120:123], v[216:219]\n"
    "ds_read_b64_tr_b16 v[76:77], v19 offset:0\n"
    "buffer_load_dwordx4 v28, s[40:43], s57 offen lds\n"
    "v_mfma_f32_16x16x32_bf16 v[220:223], v[44:47], v[124:127], v[220:223]\n"
    "s_waitcnt lgkmcnt(7)\n"
    "ds_read_b64_tr_b16 v[78:79], v19 offset:4096\n"
    "v_mfma_f32_16x16x32_bf16 v[160:163], v[48:51], v[128:131], v[160:163]\n"
    "s_add_u32 s56, s69, 0x400\n"
    "ds_read_b64_tr_b16 v[32:33], v8 offset:8192\n"
    "v_mfma_f32_16x16x32_bf16 v[164:167], v[48:51], v[132:135], v[164:167]\n"
    "ds_read_b64_tr_b16 v[34:35], v8 offset:12288\n"
    "buffer_load_dwordx4 v[120:123], v1, s[44:47], s56 offen sc1\n"
    "v_mfma_f32_16x16x32_bf16 v[168:171], v[52:55], v[128:131], v[168:171]\n"
    "s_add_u32 s56, s69, 0x20400\n"
    "ds_read_b64_tr_b16 v[36:37], v9 offset:8192\n"
    "v_mfma_f32_16x16x32_bf16 v[172:175], v[52:55], v[132:135], v[172:175]\n"
    "s_add_u32 m0, s64, 1024\n"
    "ds_read_b64_tr_b16 v[38:39], v9 offset:12288\n"
    "v_mfma_f32_16x16x32_bf16 v[176:179], v[56:59], v[128:131], v[176:179]\n"
    "ds_read_b64_tr_b16 v[40:41], v10 offset:8192\n"
    "buffer_load_dwordx4 v[124:127], v1, s[44:47], s56 offen sc1\n"
    "v_mfma_f32_16x16x32_bf16 v[180:183], v[56:59], v[132:135], v[180:183]\n"
    "ds_read_b64_tr_b16 v[42:43], v10 offset:12288\n"
    "v_mfma_f32_16x16x32_bf16 v[184:187], v[60:63], v[128:131], v[184:187]\n"
    "ds_read_b64_tr_b16 v[44:45], v11 offset:8192\n"
    "v_mfma_f32_16x16x32_bf16 v[188:191], v[60:63], v[132:135], v[188:191]\n"
    "s_waitcnt lgkmcnt(7)\n"
    "ds_read_b64_tr_b16 v[46:47], v11 offset:12288\n"
    "v_mfma_f32_16x16x32_bf16 v[192:195], v[64:67], v[128:131], v[192:195]\n"
    "ds_read_b64_tr_b16 v[48:49], v16 offset:8192\n"
    "buffer_load_dwordx4 v29, s[40:43], s57 offen lds\n"
    "v_mfma_f32_16x16x32_bf16 v[196:199], v[64:67], v[132:135], v[196:199]\n"
    "s_add_u32 s57, s70, 0x18000\n"
    "ds_read_b64_tr_b16 v[50:51], v16 offset:12288\n"
    "v_mfma_f32_16x16x32_bf16 v[200:203], v[68:71], v[128:131], v[200:203]\n"
    "s_add_u32 m0, s64, 8192\n"
    "ds_read_b64_tr_b16 v[52:53], v17 offset:8192\n"
    "v_mfma_f32_16x16x32_bf16 v[204:207], v[68:71], v[132:135], v[204:207]\n"
    "ds_read_b64_tr_b16 v[54:55], v17 offset:12288\n"
    "buffer_load_dwordx4 v28, s[40:43], s57 offen lds\n"
    "v_mfma_f32_16x16x32_bf16 v[208:211], v[72:75], v[128:131], v[208:211]\n"
    "s_waitcnt vmcnt(26)\n"
    "ds_read_b64_tr_b16 v[56:57], v18 offset:8192\n"
    "v_mfma_f32_16x16x32_bf16 v[212:215], v[72:75], v[132:135], v[212:215]\n"
    "s_add_u32 m0, s64, 9216\n"
    "ds_read_b64_tr_b16 v[58:59], v18 offset:12288\n"
    "v_mfma_f32_16x16x32_bf16 v[216:219], v[76:79], v[128:131], v[216:219]\n"
    "ds_read_b64_tr_b16 v[60:61], v19 offset:8192\n"
    "buffer_load_dwordx4 v29, s[40:43], s57 offen lds\n"
    "v_mfma_f32_16x16x32_bf16 v[220:223], v[76:79], v[132:135], v[220:223]\n"
    "s_waitcnt lgkmcnt(7)\n"
    "ds_read_b64_tr_b16 v[62:63], v19 offset:12288\n"
    "v_mfma_f32_16x16x32_bf16 v[160:163], v[32:35], v[136:139], v[160:163]\n"
    "s_add_u32 s56, s69, 0x800\n"
    "ds_read_b64_tr_b16 v[64:65], v8 offset:16384\n"
    "v_mfma_f32_16x16x32_bf16 v[164:167], v[32:35], v[140:143], v[164:167]\n"
    "ds_read_b64_tr_b16 v[66:67], v8 offset:20480\n"
    "buffer_load_dwordx4 v[128:131], v1, s[44:47], s56 offen sc1\n"
    "v_mfma_f32_16x16x32_bf16 v[168:171], v[36:39], v[136:139], v[168:171]\n"
    "s_add_u32 s56, s69, 0x20800\n"
    "ds_read_b64_tr_b16 v[68:69], v9 offset:16384\n"
    "v_mfma_f32_16x16x32_bf16 v[172:175], v[36:39], v[140:143], v[172:175]\n"
    "s_add_u32 s57, s53, 5\n"
    "ds_read_b64_tr_b16 v[70:71], v9 offset:20480\n"
    "v_mfma_f32_16x16x32_bf16 v[176:179], v[40:43], v[136:139], v[176:179]\n"
    "ds_read_b64_tr_b16 v[72:73], v10 offset:16384\n"
    "buffer_load_dwordx4 v[132:135], v1, s[44:47], s56 offen sc1\n"
    "v_mfma_f32_16x16x32_bf16 v[180:183], v[40:43], v[140:143], v[180:183]\n"
    "s_and_b32 s57, s57, 7\n"
    "ds_read_b64_tr_b16 v[74:75], v10 offset:20480\n"
    "v_mfma_f32_16x16x32_bf16 v[184:187], v[44:47], v[136:139], v[184:187]\n"
    "s_lshl_b32 s70, s57, 17\n"
    "ds_read_b64_tr_b16 v[76:77], v11 offset:16384\n"
    "v_mfma_f32_16x16x32_bf16 v[188:191], v[44:47], v[140:143], v[188:191]\n"
    "s_waitcnt lgkmcnt(7)\n"
    "ds_read_b64_tr_b16 v[78:79], v11 offset:20480\n"
    "v_mfma_f32_16x16x32_bf16 v[192:195], v[48:51], v[136:139], v[192:195]\n"
    "s_add_u32 s70, s70, s63\n"
    "ds_read_b64_tr_b16 v[32:33], v16 offset:16384\n"
    "v_mfma_f32_16x16x32_bf16 v[196:199], v[48:51], v[140:143], v[196:199]\n"
    "s_add_u32 s57, s70, 0x0\n"
    "ds_read_b64_tr_b16 v[34:35], v16 offset:20480\n"
    "v_mfma_f32_16x16x32_bf16 v[200:203], v[52:55], v[136:139], v[200:203]\n"
    "s_add_u32 m0, s64, 16384\n"
    "ds_read_b64_tr_b16 v[36:37], v17 offset:16384\n"
    "v_mfma_f32_16x16x32_bf16 v[204:207], v[52:55], v[140:143], v[204:207]\n"
    "ds_read_b64_tr_b16 v[38:39], v17 offset:20480\n"
    "buffer_load_dwordx4 v28, s[40:43], s57 offen lds\n"
    "v_mfma_f32_16x16x32_bf16 v[208:211], v[56:59], v[136:139], v[208:211]\n"
    "s_waitcnt vmcnt(25)\n"
    "ds_read_b64_tr_b16 v[40:41], v18 offset:16384\n"
    "v_mfma_f32_16x16x32_bf16 v[212:215], v[56:59], v[140:143], v[212:215]\n"
    "s_add_u32 m0, s64, 17408\n"
    "ds_read_b64_tr_b16 v[42:43], v18 offset:20480\n"
    "v_mfma_f32_16x16x32_bf16 v[216:219], v[60:63], v[136:139], v[216:219]\n"
    "ds_read_b64_tr_b16 v[44:45], v19 offset:16384\n"
    "buffer_load_dwordx4 v29, s[40:43], s57 offen lds\n"
    "v_mfma_f32_16x16x32_bf16 v[220:223], v[60:63], v[140:143], v[220:223]\n"
    "s_waitcnt lgkmcnt(7)\n"
    "ds_read_b64_tr_b16 v[46:47], v19 offset:20480\n"
    "v_mfma_f32_16x16x32_bf16 v[160:163], v[64:67], v[144:147], v[160:163]\n"
    "s_add_u32 s56, s69, 0xc00\n"
    "ds_read_b64_tr_b16 v[48:49], v8 offset:24576\n"
    "v_mfma_f32_16x16x32_bf16 v[164:167], v[64:67], v[148:151], v[164:167]\n"
    "ds_read_b64_tr_b16 v[50:51], v8 offset:28672\n"
    "buffer_load_dwordx4 v[136:139], v1, s[44:47], s56 offen sc1\n"
    "v_mfma_f32_16x16x32_bf16 v[168:171], v[68:71], v[144:147], v[168:171]\n"
    "s_add_u32 s56, s69, 0x20c00\n"
    "ds_read_b64_tr_b16 v[52:53], v9 offset:24576\n"
    "v_mfma_f32_16x16x32_bf16 v[172:175], v[68:71], v[148:151], v[172:175]\n"
    "s_add_u32 s57, s70, 0x8000\n"
    "ds_read_b64_tr_b16 v[54:55], v9 offset:28672\n"
    "v_mfma_f32_16x16x32_bf16 v[176:179], v[72:75], v[144:147], v[176:179]\n"
    "ds_read_b64_tr_b16 v[56:57], v10 offset:24576\n"
    "buffer_load_dwordx4 v[140:143], v1, s[44:47], s56 offen sc1\n"
    "v_mfma_f32_16x16x32_bf16 v[180:183], v[72:75], v[148:151], v[180:183]\n"
    "s_add_u32 m0, s64, 24576\n"
    "ds_read_b64_tr_b16 v[58:59], v10 offset:28672\n"
    "v_mfma_f32_16x16x32_bf16 v[184:187], v[76:79], v[144:147], v[184:187]\n"
    "ds_read_b64_tr_b16 v[60:61], v11 offset:24576\n"
    "v_mfma_f32_16x16x32_bf16 v[188:191], v[76:79], v[148:151], v[188:191]\n"
    "s_waitcnt lgkmcnt(7)\n"
    "ds_read_b64_tr_b16 v[62:63], v11 offset:28672\n"
    "v_mfma_f32_16x16x32_bf16 v[192:195], v[32:35], v[144:147], v[192:195]\n"
    "ds_read_b64_tr_b16 v[64:65], v16 offset:24576\n"
    "buffer_load_dwordx4 v28, s[40:43], s57 offen lds\n"
    "v_mfma_f32_16x16x32_bf16 v[196:199], v[32:35], v[148:151], v[196:199]\n"
    "ds_read_b64_tr_b16 v[66:67], v16 offset:28672\n"
    "v_mfma_f32_16x16x32_bf16 v[200:203], v[36:39], v[144:147], v[200:203]\n"
    "s_add_u32 m0, s64, 25600\n"
    "ds_read_b64_tr_b16 v[68:69], v17 offset:24576\n"
    "v_mfma_f32_16x16x32_bf16 v[204:207], v[36:39], v[148:151], v[204:207]\n"
    "ds_read_b64_tr_b16 v[70:71], v17 offset:28672\n"
    "buffer_load_dwordx4 v29, s[40:43], s57 offen lds\n"
    "v_mfma_f32_16x16x32_bf16 v[208:211], v[40:43], v[144:147], v[208:211]\n"
    "s_waitcnt vmcnt(26)\n"
    "ds_read_b64_tr_b16 v[72:73], v18 offset:24576\n"
    "v_mfma_f32_16x16x32_bf16 v[212:215], v[40:43], v[148:151], v[212:215]\n"
    "ds_read_b64_tr_b16 v[74:75], v18 offset:28672\n"
    "v_mfma_f32_16x16x32_bf16 v[216:219], v[44:47], v[144:147], v[216:219]\n"
    "ds_read_b64_tr_b16 v[76:77], v19 offset:24576\n"
    "v_mfma_f32_16x16x32_bf16 v[220:223], v[44:47], v[148:151], v[220:223]\n"
    "s_waitcnt lgkmcnt(7)\n"
    "ds_read_b64_tr_b16 v[78:79], v19 offset:28672\n"
    "v_mfma_f32_16x16x32_bf16 v[160:163], v[48:51], v[152:155], v[160:163]\n"
    "s_add_u32 s56, s53, 4\n"
    "ds_read_b64_tr_b16 v[32:33], v8 offset:32768\n"
    "v_mfma_f32_16x16x32_bf16 v[164:167], v[48:51], v[156:159], v[164:167]\n"
    "s_and_b32 s56, s56, 7\n"
    "ds_read_b64_tr_b16 v[34:35], v8 offset:36864\n"
    "v_mfma_f32_16x16x32_bf16 v[168:171], v[52:55], v[152:155], v[168:171]\n"
    "s_lshl_b32 s69, s56, 12\n"
    "ds_read_b64_tr_b16 v[36:37], v9 offset:32768\n"
    "v_mfma_f32_16x16x32_bf16 v[172:175], v[52:55], v[156:159], v[172:175]\n"
    "s_add_u32 s69, s69, s61\n"
    "ds_read_b64_tr_b16 v[38:39], v9 offset:36864\n"
    "v_mfma_f32_16x16x32_bf16 v[176:179], v[56:59], v[152:155], v[176:179]\n"
    "s_add_u32 s56, s69, 0x0\n"
    "ds_read_b64_tr_b16 v[40:41], v10 offset:32768\n"
    "v_mfma_f32_16x16x32_bf16 v[180:183], v[56:59], v[156:159], v[180:183]\n"
    "ds_read_b64_tr_b16 v[42:43], v10 offset:36864\n"
    "buffer_load_dwordx4 v[144:147], v1, s[44:47], s56 offen sc1\n"
    "v_mfma_f32_16x16x32_bf16 v[184:187], v[60:63], v[152:155], v[184:187]\n"
    "s_add_u32 s56, s69, 0x20000\n"
    "ds_read_b64_tr_b16 v[44:45], v11 offset:32768\n"
    "v_mfma_f32_16x16x32_bf16 v[188:191], v[60:63], v[156:159], v[188:191]\n"
    "s_waitcnt lgkmcnt(7)\n"
    "ds_read_b64_tr_b16 v[46:47], v11 offset:36864\n"
    "v_mfma_f32_16x16x32_bf16 v[192:195], v[64:67], v[152:155], v[192:195]\n"
    "ds_read_b64_tr_b16 v[48:49], v16 offset:32768\n"
    "buffer_load_dwordx4 v[148:151], v1, s[44:47], s56 offen sc1\n"
    "v_mfma_f32_16x16x32_bf16 v[196:199], v[64:67], v[156:159], v[196:199]\n"
    "s_add_u32 s57, s70, 0x10000\n"
    "ds_read_b64_tr_b16 v[50:51], v16 offset:36864\n"
    "v_mfma_f32_16x16x32_bf16 v[200:203], v[68:71], v[152:155], v[200:203]\n"
    "s_add_u32 m0, s64, 32768\n"
    "ds_read_b64_tr_b16 v[52:53], v17 offset:32768\n"
    "v_mfma_f32_16x16x32_bf16 v[204:207], v[68:71], v[156:159], v[204:207]\n"
    "ds_read_b64_tr_b16 v[54:55], v17 offset:36864\n"
    "buffer_load_dwordx4 v28, s[40:43], s57 offen lds\n"
    "v_mfma_f32_16x16x32_bf16 v[208:211], v[72:75], v[152:155], v[208:211]\n"
    "s_waitcnt vmcnt(25)\n"
    "ds_read_b64_tr_b16 v[56:57], v18 offset:32768\n"
    "v_mfma_f32_16x16x32_bf16 v[212:215], v[72:75], v[156:159], v[212:215]\n"
    "s_add_u32 m0, s64, 33792\n"
    "ds_read_b64_tr_b16 v[58:59], v18 offset:36864\n"
    "v_mfma_f32_16x16x32_bf16 v[216:219], v[76:79], v[152:155], v[216:219]\n"
    "ds_read_b64_tr_b16 v[60:61], v19 offset:32768\n"
    "buffer_load_dwordx4 v29, s[40:43], s57 offen lds\n"
    "v_mfma_f32_16x16x32_bf16 v[220:223], v[76:79], v[156:159], v[220:223]\n"
    "s_waitcnt lgkmcnt(7)\n"
    "ds_read_b64_tr_b16 v[62:63], v19 offset:36864\n"
    "v_mfma_f32_16x16x32_bf16 v[160:163], v[32:35], v[96:99], v[160:163]\n"
    "s_add_u32 s57, s70, 0x18000\n"
    "ds_read_b64_tr_b16 v[64:65], v8 offset:40960\n"
    "v_mfma_f32_16x16x32_bf16 v[164:167], v[32:35], v[100:103], v[164:167]\n"
    "s_add_u32 m0, s64, 40960\n"
    "ds_read_b64_tr_b16 v[66:67], v8 offset:45056\n"
    "v_mfma_f32_16x16x32_bf16 v[168:171], v[36:39], v[96:99], v[168:171]\n"
    "ds_read_b64_tr_b16 v[68:69], v9 offset:40960\n"
    "buffer_load_dwordx4 v28, s[40:43], s57 offen lds\n"
    "v_mfma_f32_16x16x32_bf16 v[172:175], v[36:39], v[100:103], v[172:175]\n"
    "s_add_u32 s56, s69, 0x400\n"
    "ds_read_b64_tr_b16 v[70:71], v9 offset:45056\n"
    "v_mfma_f32_16x16x32_bf16 v[176:179], v[40:43], v[96:99], v[176:179]\n"
    "ds_read_b64_tr_b16 v[72:73], v10 offset:40960\n"
    "v_mfma_f32_16x16x32_bf16 v[180:183], v[40:43], v[100:103], v[180:183]\n"
    "ds_read_b64_tr_b16 v[74:75], v10 offset:45056\n"
    "buffer_load_dwordx4 v[152:155], v1, s[44:47], s56 offen sc1\n"
    "v_mfma_f32_16x16x32_bf16 v[184:187], v[44:47], v[96:99], v[184:187]\n"
    "s_add_u32 s56, s69, 0x20400\n"
    "ds_read_b64_tr_b16 v[76:77], v11 offset:40960\n"
    "v_mfma_f32_16x16x32_bf16 v[188:191], v[44:47], v[100:103], v[188:191]\n"
    "s_waitcnt lgkmcnt(7)\n"
    "ds_read_b64_tr_b16 v[78:79], v11 offset:45056\n"
    "v_mfma_f32_16x16x32_bf16 v[192:195], v[48:51], v[96:99], v[192:195]\n"
    "ds_read_b64_tr_b16 v[32:33], v16 offset:40960\n"
    "buffer_load_dwordx4 v[156:159], v1, s[44:47], s56 offen sc1\n"
    "v_mfma_f32_16x16x32_bf16 v[196:199], v[48:51], v[100:103], v[196:199]\n"
    "s_add_u32 m0, s64, 41984\n"
    "ds_read_b64_tr_b16 v[34:35], v16 offset:45056\n"
    "v_mfma_f32_16x16x32_bf16 v[200:203], v[52:55], v[96:99], v[200:203]\n"
    "ds_read_b64_tr_b16 v[36:37], v17 offset:40960\n"
    "v_mfma_f32_16x16x32_bf16 v[204:207], v[52:55], v[100:103], v[204:207]\n"
    "ds_read_b64_tr_b16 v[38:39], v17 offset:45056\n"
    "buffer_load_dwordx4 v29, s[40:43], s57 offen lds\n"
    "v_mfma_f32_16x16x32_bf16 v[208:211], v[56:59], v[96:99], v[208:211]\n"
    "s_waitcnt vmcnt(25)\n"
    "ds_read_b64_tr_b16 v[40:41], v18 offset:40960\n"
    "v_mfma_f32_16x16x32_bf16 v[212:215], v[56:59], v[100:103], v[212:215]\n"
    "ds_read_b64_tr_b16 v[42:43], v18 offset:45056\n"
    "v_mfma_f32_16x16x32_bf16 v[216:219], v[60:63], v[96:99], v[216:219]\n"
    "ds_read_b64_tr_b16 v[44:45], v19 offset:40960\n"
    "v_mfma_f32_16x16x32_bf16 v[220:223], v[60:63], v[100:103], v[220:223]\n"
    "s_waitcnt lgkmcnt(7)\n"
    "ds_read_b64_tr_b16 v[46:47], v19 offset:45056\n"
    "s_waitcnt vmcnt(24)\n"
    "s_barrier\n"
    "v_add_u32 v8, s77, v8\n"
    "v_mfma_f32_16x16x32_bf16 v[160:163], v[64:67], v[104:107], v[160:163]\n"
    "s_add_u32 s56, s69, 0x800\n"
    "ds_read_b64_tr_b16 v[48:49], v8 offset:0\n"
    "v_mfma_f32_16x16x32_bf16 v[164:167], v[64:67], v[108:111], v[164:167]\n"
    "s_add_u32 s57, s53, 6\n"
    "ds_read_b64_tr_b16 v[50:51], v8 offset:4096\n"
    "v_add_u32 v9, s77, v9\n"
    "v_mfma_f32_16x16x32_bf16 v[168:171], v[68:71], v[104:107], v[168:171]\n"
    "ds_read_b64_tr_b16 v[52:53], v9 offset:0\n"
    "buffer_load_dwordx4 v[96:99], v1, s[44:47], s56 offen sc1\n"
    "v_mfma_f32_16x16x32_bf16 v[172:175], v[68:71], v[108:111], v[172:175]\n"
    "s_add_u32 s56, s69, 0x20800\n"
    "ds_read_b64_tr_b16 v[54:55], v9 offset:4096\n"
    "v_add_u32 v10, s77, v10\n"
    "v_mfma_f32_16x16x32_bf16 v[176:179], v[72:75], v[104:107], v[176:179]\n"
    "s_and_b32 s57, s57, 7\n"
    "ds_read_b64_tr_b16 v[56:57], v10 offset:0\n"
    "v_mfma_f32_16x16x32_bf16 v[180:183], v[72:75], v[108:111], v[180:183]\n"
    "s_lshl_b32 s70, s57, 17\n"
    "ds_read_b64_tr_b16 v[58:59], v10 offset:4096\n"
    "v_add_u32 v11, s77, v11\n"
    "v_mfma_f32_16x16x32_bf16 v[184:187], v[76:79], v[104:107], v[184:187]\n"
    "ds_read_b64_tr_b16 v[60:61], v11 offset:0\n"
    "buffer_load_dwordx4 v[100:103], v1, s[44:47], s56 offen sc1\n"
    "v_mfma_f32_16x16x32_bf16 v[188:191], v[76:79], v[108:111], v[188:191]\n"
    "s_waitcnt lgkmcnt(7)\n"
    "ds_read_b64_tr_b16 v[62:63], v11 offset:4096\n"
    "v_add_u32 v16, s77, v16\n"
    "v_mfma_f32_16x16x32_bf16 v[192:195], v[32:35], v[104:107], v[192:195]\n"
    "s_add_u32 s70, s70, s63\n"
    "ds_read_b64_tr_b16 v[64:65], v16 offset:0\n"
    "v_mfma_f32_16x16x32_bf16 v[196:199], v[32:35], v[108:111], v[196:199]\n"
    "s_add_u32 s57, s70, 0x0\n"
    "ds_read_b64_tr_b16 v[66:67], v16 offset:4096\n"
    "v_add_u32 v17, s77, v17\n"
    "v_mfma_f32_16x16x32_bf16 v[200:203], v[36:39], v[104:107], v[200:203]\n"
    "s_add_u32 m0, s65, 0\n"
    "ds_read_b64_tr_b16 v[68:69], v17 offset:0\n"
    "v_mfma_f32_16x16x32_bf16 v[204:207], v[36:39], v[108:111], v[204:207]\n"
    "ds_read_b64_tr_b16 v[70:71], v17 offset:4096\n"
    "v_add_u32 v18, s77, v18\n"
    "v_mfma_f32_16x16x32_bf16 v[208:211], v[40:43], v[104:107], v[208:211]\n"
    "s_waitcnt vmcnt(24)\n"
    "ds_read_b64_tr_b16 v[72:73], v18 offset:0\n"
    "v_mfma_f32_16x16x32_bf16 v[212:215], v[40:43], v[108:111], v[212:215]\n"
    "ds_read_b64_tr_b16 v[74:75], v18 offset:4096\n"
    "v_add_u32 v19, s77, v19\n"
    "v_mfma_f32_16x16x32_bf16 v[216:219], v[44:47], v[104:107], v[216:219]\n"
    "ds_read_b64_tr_b16 v[76:77], v19 offset:0\n"
    "buffer_load_dwordx4 v28, s[40:43], s57 offen lds\n"
    "v_mfma_f32_16x16x32_bf16 v[220:223], v[44:47], v[108:111], v[220:223]\n"
    "s_waitcnt lgkmcnt(7)\n"
    "ds_read_b64_tr_b16 v[78:79], v19 offset:4096\n"
    "v_mfma_f32_16x16x32_bf16 v[160:163], v[48:51], v[112:115], v[160:163]\n"
    "s_add_u32 s56, s69, 0xc00\n"
    "ds_read_b64_tr_b16 v[32:33], v8 offset:8192\n"
    "v_mfma_f32_16x16x32_bf16 v[164:167], v[48:51], v[116:119], v[164:167]\n"
    "ds_read_b64_tr_b16 v[34:35], v8 offset:12288\n"
    "buffer_load_dwordx4 v[104:107], v1, s[44:47], s56 offen sc1\n"
    "v_mfma_f32_16x16x32_bf16 v[168:171], v[52:55], v[112:115], v[168:171]\n"
    "s_add_u32 s56, s69, 0x20c00\n"
    "ds_read_b64_tr_b16 v[36:37], v9 offset:8192\n"
    "v_mfma_f32_16x16x32_bf16 v[172:175], v[52:55], v[116:119], v[172:175]\n"
    "s_add_u32 m0, s65, 1024\n"
    "ds_read_b64_tr_b16 v[38:39], v9 offset:12288\n"
    "v_mfma_f32_16x16x32_bf16 v[176:179], v[56:59], v[112:115], v[176:179]\n"
    "ds_read_b64_tr_b16 v[40:41], v10 offset:8192\n"
    "buffer_load_dwordx4 v[108:111], v1, s[44:47], s56 offen sc1\n"
    "v_mfma_f32_16x16x32_bf16 v[180:183], v[56:59], v[116:119], v[180:183]\n"
    "ds_read_b64_tr_b16 v[42:43], v10 offset:12288\n"
    "v_mfma_f32_16x16x32_bf16 v[184:187], v[60:63], v[112:115], v[184:187]\n"
    "ds_read_b64_tr_b16 v[44:45], v11 offset:8192\n"
    "v_mfma_f32_16x16x32_bf16 v[188:191], v[60:63], v[116:119], v[188:191]\n"
    "s_waitcnt lgkmcnt(7)\n"
    "ds_read_b64_tr_b16 v[46:47], v11 offset:12288\n"
    "v_mfma_f32_16x16x32_bf16 v[192:195], v[64:67], v[112:115], v[192:195]\n"
    "ds_read_b64_tr_b16 v[48:49], v16 offset:8192\n"
    "buffer_load_dwordx4 v29, s[40:43], s57 offen lds\n"
    "v_mfma_f32_16x16x32_bf16 v[196:199], v[64:67], v[116:119], v[196:199]\n"
    "s_add_u32 s57, s70, 0x8000\n"
    "ds_read_b64_tr_b16 v[50:51], v16 offset:12288\n"
    "v_mfma_f32_16x16x32_bf16 v[200:203], v[68:71], v[112:115], v[200:203]\n"
    "s_add_u32 m0, s65, 8192\n"
    "ds_read_b64_tr_b16 v[52:53], v17 offset:8192\n"
    "v_mfma_f32_16x16x32_bf16 v[204:207], v[68:71], v[116:119], v[204:207]\n"
    "ds_read_b64_tr_b16 v[54:55], v17 offset:12288\n"
    "buffer_load_dwordx4 v28, s[40:43], s57 offen lds\n"
    "v_mfma_f32_16x16x32_bf16 v[208:211], v[72:75], v[112:115], v[208:211]\n"
    "s_waitcnt vmcnt(26)\n"
    "ds_read_b64_tr_b16 v[56:57], v18 offset:8192\n"
    "v_mfma_f32_16x16x32_bf16 v[212:215], v[72:75], v[116:119], v[212:215]\n"
    "s_add_u32 m0, s65, 9216\n"
    "ds_read_b64_tr_b16 v[58:59], v18 offset:12288\n"
    "v_mfma_f32_16x16x32_bf16 v[216:219], v[76:79], v[112:115], v[216:219]\n"
    "ds_read_b64_tr_b16 v[60:61], v19 offset:8192\n"
    "buffer_load_dwordx4 v29, s[40:43], s57 offen lds\n"
    "v_mfma_f32_16x16x32_bf16 v[220:223], v[76:79], v[116:119], v[220:223]\n"
    "s_waitcnt lgkmcnt(7)\n"
    "ds_read_b64_tr_b16 v[62:63], v19 offset:12288\n"
    "v_mfma_f32_16x16x32_bf16 v[160:163], v[32:35], v[120:123], v[160:163]\n"
    "s_add_u32 s56, s53, 5\n"
    "ds_read_b64_tr_b16 v[64:65], v8 offset:16384\n"
    "v_mfma_f32_16x16x32_bf16 v[164:167], v[32:35], v[124:127], v[164:167]\n"
    "s_and_b32 s56, s56, 7\n"
    "ds_read_b64_tr_b16 v[66:67], v8 offset:20480\n"
    "v_mfma_f32_16x16x32_bf16 v[168:171], v[36:39], v[120:123], v[168:171]\n"
    "s_lshl_b32 s69, s56, 12\n"
    "ds_read_b64_tr_b16 v[68:69], v9 offset:16384\n"
    "v_mfma_f32_16x16x32_bf16 v[172:175], v[36:39], v[124:127], v[172:175]\n"
    "s_add_u32 s69, s69, s61\n"
    "ds_read_b64_tr_b16 v[70:71], v9 offset:20480\n"
    "v_mfma_f32_16x16x32_bf16 v[176:179], v[40:43], v[120:123], v[176:179]\n"
    "s_add_u32 s56, s69, 0x0\n"
    "ds_read_b64_tr_b16 v[72:73], v10 offset:16384\n"
    "v_mfma_f32_16x16x32_bf16 v[180:183], v[40:43], v[124:127], v[180:183]\n"
    "ds_read_b64_tr_b16 v[74:75], v10 offset:20480\n"
    "buffer_load_dwordx4 v[112:115], v1, s[44:47], s56 offen sc1\n"
    "v_mfma_f32_16x16x32_bf16 v[184:187], v[44:47], v[120:123], v[184:187]\n"
    "s_add_u32 s56, s69, 0x20000\n"
    "ds_read_b64_tr_b16 v[76:77], v11 offset:16384\n"
    "v_mfma_f32_16x16x32_bf16 v[188:191], v[44:47], v[124:127], v[188:191]\n"
    "s_waitcnt lgkmcnt(7)\n"
    "ds_read_b64_tr_b16 v[78:79], v11 offset:20480\n"
    "v_mfma_f32_16x16x32_bf16 v[192:195], v[48:51], v[120:123], v[192:195]\n"
    "ds_read_b64_tr_b16 v[32:33], v16 offset:16384\n"
    "buffer_load_dwordx4 v[116:119], v1, s[44:47], s56 offen sc1\n"
    "v_mfma_f32_16x16x32_bf16 v[196:199], v[48:51], v[124:127], v[196:199]\n"
    "s_add_u32 s57, s70, 0x10000\n"
    "ds_read_b64_tr_b16 v[34:35], v16 offset:20480\n"
    "v_mfma_f32_16x16x32_bf16 v[200:203], v[52:55], v[120:123], v[200:203]\n"
    "s_add_u32 m0, s65, 16384\n"
    "ds_read_b64_tr_b16 v[36:37], v17 offset:16384\n"
    "v_mfma_f32_16x16x32_bf16 v[204:207], v[52:55], v[124:127], v[204:207]\n"
    "ds_read_b64_tr_b16 v[38:39], v17 offset:20480\n"
    "buffer_load_dwordx4 v28, s[40:43], s57 offen lds\n"
    "v_mfma_f32_16x16x32_bf16 v[208:211], v[56:59], v[120:123], v[208:211]\n"
    "s_waitcnt vmcnt(25)\n"
    "ds_read_b64_tr_b16 v[40:41], v18 offset:16384\n"
    "v_mfma_f32_16x16x32_bf16 v[212:215], v[56:59], v[124:127], v[212:215]\n"
    "s_add_u32 m0, s65, 17408\n"
    "ds_read_b64_tr_b16 v[42:43], v18 offset:20480\n"
    "v_mfma_f32_16x16x32_bf16 v[216:219], v[60:63], v[120:123], v[216:219]\n"
    "ds_read_b64_tr_b16 v[44:45], v19 offset:16384\n"
    "buffer_load_dwordx4 v29, s[40:43], s57 offen lds\n"
    "v_mfma_f32_16x16x32_bf16 v[220:223], v[60:63], v[124:127], v[220:223]\n"
    "s_waitcnt lgkmcnt(7)\n"
    "ds_read_b64_tr_b16 v[46:47], v19 offset:20480\n"
    "v_mfma_f32_16x16x32_bf16 v[160:163], v[64:67], v[128:131], v[160:163]\n"
    "s_add_u32 s56, s69, 0x400\n"
    "ds_read_b64_tr_b16 v[48:49], v8 offset:24576\n"
    "v_mfma_f32_16x16x32_bf16 v[164:167], v[64:67], v[132:135], v[164:167]\n"
    "ds_read_b64_tr_b16 v[50:51], v8 offset:28672\n"
    "buffer_load_dwordx4 v[120:123], v1, s[44:47], s56 offen sc1\n"
    "v_mfma_f32_16x16x32_bf16 v[168:171], v[68:71], v[128:131], v[168:171]\n"
    "s_add_u32 s56, s69, 0x20400\n"
    "ds_read_b64_tr_b16 v[52:53], v9 offset:24576\n"
    "v_mfma_f32_16x16x32_bf16 v[172:175], v[68:71], v[132:135], v[172:175]\n"
    "s_add_u32 s57, s70, 0x18000\n"
    "ds_read_b64_tr_b16 v[54:55], v9 offset:28672\n"
    "v_mfma_f32_16x16x32_bf16 v[176:179], v[72:75], v[128:131], v[176:179]\n"
    "ds_read_b64_tr_b16 v[56:57], v10 offset:24576\n"
    "buffer_load_dwordx4 v[124:127], v1, s[44:47], s56 offen sc1\n"
    "v_mfma_f32_16x16x32_bf16 v[180:183], v[72:75], v[132:135], v[180:183]\n"
    "s_add_u32 m0, s65, 24576\n"
    "ds_read_b64_tr_b16 v[58:59], v10 offset:28672\n"
    "v_mfma_f32_16x16x32_bf16 v[184:187], v[76:79], v[128:131], v[184:187]\n"
    "ds_read_b64_tr_b16 v[60:61], v11 offset:24576\n"
    "v_mfma_f32_16x16x32_bf16 v[188:191], v[76:79], v[132:135], v[188:191]\n"
    "s_waitcnt lgkmcnt(7)\n"
    "ds_read_b64_tr_b16 v[62:63], v11 offset:28672\n"
    "v_mfma_f32_16x16x32_bf16 v[192:195], v[32:35], v[128:131], v[192:195]\n"
    "ds_read_b64_tr_b16 v[64:65], v16 offset:24576\n"
    "buffer_load_dwordx4 v28, s[40:43], s57 offen lds\n"
    "v_mfma_f32_16x16x32_bf16 v[196:199], v[32:35], v[132:135], v[196:199]\n"
    "s_add_u32 m0, s65, 25600\n"
    "ds_read_b64_tr_b16 v[66:67], v16 offset:28672\n"
    "v_mfma_f32_16x16x32_bf16 v[200:203], v[36:39], v[128:131], v[200:203]\n"
    "ds_read_b64_tr_b16 v[68:69], v17 offset:24576\n"
    "v_mfma_f32_16x16x32_bf16 v[204:207], v[36:39], v[132:135], v[204:207]\n"
    "ds_read_b64_tr_b16 v[70:71], v17 offset:28672\n"
    "buffer_load_dwordx4 v29, s[40:43], s57 offen lds\n"
    "v_mfma_f32_16x16x32_bf16 v[208:211], v[40:43], v[128:131], v[208:211]\n"
    "s_waitcnt vmcnt(26)\n"
    "ds_read_b64_tr_b16 v[72:73], v18 offset:24576\n"
    "v_mfma_f32_16x16x32_bf16 v[212:215], v[40:43], v[132:135], v[212:215]\n"
    "ds_read_b64_tr_b16 v[74:75], v18 offset:28672\n"
    "v_mfma_f32_16x16x32_bf16 v[216:219], v[44:47], v[128:131], v[216:219]\n"
    "ds_read_b64_tr_b16 v[76:77], v19 offset:24576\n"
    "v_mfma_f32_16x16x32_bf16 v[220:223], v[44:47], v[132:135], v[220:223]\n"
    "s_waitcnt lgkmcnt(7)\n"
    "ds_read_b64_tr_b16 v[78:79], v19 offset:28672\n"
    "v_mfma_f32_16x16x32_bf16 v[160:163], v[48:51], v[136:139], v[160:163]\n"
    "s_add_u32 s56, s69, 0x800\n"
    "ds_read_b64_tr_b16 v[32:33], v8 offset:32768\n"
    "v_mfma_f32_16x16x32_bf16 v[164:167], v[48:51], v[140:143], v[164:167]\n"
    "ds_read_b64_tr_b16 v[34:35], v8 offset:36864\n"
    "buffer_load_dwordx4 v[128:131], v1, s[44:47], s56 offen sc1\n"
    "v_mfma_f32_16x16x32_bf16 v[168:171], v[52:55], v[136:139], v[168:171]\n"
    "s_add_u32 s56, s69, 0x20800\n"
    "ds_read_b64_tr_b16 v[36:37], v9 offset:32768\n"
    "v_mfma_f32_16x16x32_bf16 v[172:175], v[52:55], v[140:143], v[172:175]\n"
    "s_add_u32 s57, s53, 7\n"
    "ds_read_b64_tr_b16 v[38:39], v9 offset:36864\n"
    "v_mfma_f32_16x16x32_bf16 v[176:179], v[56:59], v[136:139], v[176:179]\n"
    "ds_read_b64_tr_b16 v[40:41], v10 offset:32768\n"
    "buffer_load_dwordx4 v[132:135], v1, s[44:47], s56 offen sc1\n"
    "v_mfma_f32_16x16x32_bf16 v[180:183], v[56:59], v[140:143], v[180:183]\n"
    "s_and_b32 s57, s57, 7\n"
    "ds_read_b64_tr_b16 v[42:43], v10 offset:36864\n"
    "v_mfma_f32_16x16x32_bf16 v[184:187], v[60:63], v[136:139], v[184:187]\n"
    "s_lshl_b32 s70, s57, 17\n"
    "ds_read_b64_tr_b16 v[44:45], v11 offset:32768\n"
    "v_mfma_f32_16x16x32_bf16 v[188:191], v[60:63], v[140:143], v[188:191]\n"
    "s_waitcnt lgkmcnt(7)\n"
    "ds_read_b64_tr_b16 v[46:47], v11 offset:36864\n"
    "v_mfma_f32_16x16x32_bf16 v[192:195], v[64:67], v[136:139], v[192:195]\n"
    "s_add_u32 s70, s70, s63\n"
    "ds_read_b64_tr_b16 v[48:49], v16 offset:32768\n"
    "v_mfma_f32_16x16x32_bf16 v[196:199], v[64:67], v[140:143], v[196:199]\n"
    "s_add_u32 s57, s70, 0x0\n"
    "ds_read_b64_tr_b16 v[50:51], v16 offset:36864\n"
    "v_mfma_f32_16x16x32_bf16 v[200:203], v[68:71], v[136:139], v[200:203]\n"
    "s_add_u32 m0, s65, 32768\n"
    "ds_read_b64_tr_b16 v[52:53], v17 offset:32768\n"
    "v_mfma_f32_16x16x32_bf16 v[204:207], v[68:71], v[140:143], v[204:207]\n"
    "ds_read_b64_tr_b16 v[54:55], v17 offset:36864\n"
    "buffer_load_dwordx4 v28, s[40:43], s57 offen lds\n"
    "v_mfma_f32_16x16x32_bf16 v[208:211], v[72:75], v[136:139], v[208:211]\n"
    "s_waitcnt vmcnt(25)\n"
    "ds_read_b64_tr_b16 v[56:57], v18 offset:32768\n"
    "v_mfma_f32_16x16x32_bf16 v[212:215], v[72:75], v[140:143], v[212:215]\n"
    "s_add_u32 m0, s65, 33792\n"
    "ds_read_b64_tr_b16 v[58:59], v18 offset:36864\n"
    "v_mfma_f32_16x16x32_bf16 v[216:219], v[76:79], v[136:139], v[216:219]\n"
    "ds_read_b64_tr_b16 v[60:61], v19 offset:32768\n"
    "buffer_load_dwordx4 v29, s[40:43], s57 offen lds\n"
    "v_mfma_f32_16x16x32_bf16 v[220:223], v[76:79], v[140:143], v[220:223]\n"
    "s_waitcnt lgkmcnt(7)\n"
    "ds_read_b64_tr_b16 v[62:63], v19 offset:36864\n"
    "v_mfma_f32_16x16x32_bf16 v[160:163], v[32:35], v[144:147], v[160:163]\n"
    "s_add_u32 s56, s69, 0xc00\n"
    "ds_read_b64_tr_b16 v[64:65], v8 offset:40960\n"
    "v_mfma_f32_16x16x32_bf16 v[164:167], v[32:35], v[148:151], v[164:167]\n"
    "ds_read_b64_tr_b16 v[66:67], v8 offset:45056\n"
    "buffer_load_dwordx4 v[136:139], v1, s[44:47], s56 offen sc1\n"
    "v_mfma_f32_16x16x32_bf16 v[168:171], v[36:39], v[144:147], v[168:171]\n"
    "s_add_u32 s57, s70, 0x8000\n"
    "ds_read_b64_tr_b16 v[68:69], v9 offset:40960\n"
    "v_mfma_f32_16x16x32_bf16 v[172:175], v[36:39], v[148:151], v[172:175]\n"
    "s_add_u32 m0, s65, 40960\n"
    "ds_read_b64_tr_b16 v[70:71], v9 offset:45056\n"
    "v_mfma_f32_16x16x32_bf16 v[176:179], v[40:43], v[144:147], v[176:179]\n"
    "ds_read_b64_tr_b16 v[72:73], v10 offset:40960\n"
    "buffer_load_dwordx4 v28, s[40:43], s57 offen lds\n"
    "v_mfma_f32_16x16x32_bf16 v[180:183], v[40:43], v[148:151], v[180:183]\n"
    "s_add_u32 s56, s69, 0x20c00\n"
    "ds_read_b64_tr_b16 v[74:75], v10 offset:45056\n"
    "v_mfma_f32_16x16x32_bf16 v[184:187], v[44:47], v[144:147], v[184:187]\n"
    "ds_read_b64_tr_b16 v[76:77], v11 offset:40960\n"
    "v_mfma_f32_16x16x32_bf16 v[188:191], v[44:47], v[148:151], v[188:191]\n"
    "s_waitcnt lgkmcnt(7)\n"
    "ds_read_b64_tr_b16 v[78:79], v11 offset:45056\n"
    "v_mfma_f32_16x16x32_bf16 v[192:195], v[48:51], v[144:147], v[192:195]\n"
    "ds_read_b64_tr_b16 v[32:33], v16 offset:40960\n"
    "buffer_load_dwordx4 v[140:143], v1, s[44:47], s56 offen sc1\n"
    "v_mfma_f32_16x16x32_bf16 v[196:199], v[48:51], v[148:151], v[196:199]\n"
    "s_add_u32 m0, s65, 41984\n"
    "ds_read_b64_tr_b16 v[34:35], v16 offset:45056\n"
    "v_mfma_f32_16x16x32_bf16 v[200:203], v[52:55], v[144:147], v[200:203]\n"
    "ds_read_b64_tr_b16 v[36:37], v17 offset:40960\n"
    "v_mfma_f32_16x16x32_bf16 v[204:207], v[52:55], v[148:151], v[204:207]\n"
    "ds_read_b64_tr_b16 v[38:39], v17 offset:45056\n"
    "buffer_load_dwordx4 v29, s[40:43], s57 offen lds\n"
    "v_mfma_f32_16x16x32_bf16 v[208:211], v[56:59], v[144:147], v[208:211]\n"
    "s_waitcnt vmcnt(25)\n"
    "ds_read_b64_tr_b16 v[40:41], v18 offset:40960\n"
    "v_mfma_f32_16x16x32_bf16 v[212:215], v[56:59], v[148:151], v[212:215]\n"
    "ds_read_b64_tr_b16 v[42:43], v18 offset:45056\n"
    "v_mfma_f32_16x16x32_bf16 v[216:219], v[60:63], v[144:147], v[216:219]\n"
    "ds_read_b64_tr_b16 v[44:45], v19 offset:40960\n"
    "v_mfma_f32_16x16x32_bf16 v[220:223], v[60:63], v[148:151], v[220:223]\n"
    "s_waitcnt lgkmcnt(7)\n"
    "ds_read_b64_tr_b16 v[46:47], v19 offset:45056\n"
    "s_waitcnt vmcnt(24)\n"
    "s_barrier\n"
    "v_add_u32 v8, s78, v8\n"
    "v_mfma_f32_16x16x32_bf16 v[160:163], v[64:67], v[152:155], v[160:163]\n"
    "s_add_u32 s56, s53, 6\n"
    "ds_read_b64_tr_b16 v[48:49], v8 offset:0\n"
    "v_mfma_f32_16x16x32_bf16 v[164:167], v[64:67], v[156:159], v[164:167]\n"
    "s_and_b32 s56, s56, 7\n"
    "ds_read_b64_tr_b16 v[50:51], v8 offset:4096\n"
    "v_add_u32 v9, s78, v9\n"
    "v_mfma_f32_16x16x32_bf16 v[168:171], v[68:71], v[152:155], v[168:171]\n"
    "s_lshl_b32 s69, s56, 12\n"
    "ds_read_b64_tr_b16 v[52:53], v9 offset:0\n"
    "v_mfma_f32_16x16x32_bf16 v[172:175], v[68:71], v[156:159], v[172:175]\n"
    "s_add_u32 s69, s69, s61\n"
    "ds_read_b64_tr_b16 v[54:55], v9 offset:4096\n"
    "v_add_u32 v10, s78, v10\n"
    "v_mfma_f32_16x16x32_bf16 v[176:179], v[72:75], v[152:155], v[176:179]\n"
    "s_add_u32 s56, s69, 0x0\n"
    "ds_read_b64_tr_b16 v[56:57], v10 offset:0\n"
    "v_mfma_f32_16x16x32_bf16 v[180:183], v[72:75], v[156:159], v[180:183]\n"
    "s_add_u32 s57, s70, 0x10000\n"
    "ds_read_b64_tr_b16 v[58:59], v10 offset:4096\n"
    "v_add_u32 v11, s78, v11\n"
    "v_mfma_f32_16x16x32_bf16 v[184:187], v[76:79], v[152:155], v[184:187]\n"
    "ds_read_b64_tr_b16 v[60:61], v11 offset:0\n"
    "buffer_load_dwordx4 v[144:147], v1, s[44:47], s56 offen sc1\n"
    "v_mfma_f32_16x16x32_bf16 v[188:191], v[76:79], v[156:159], v[188:191]\n"
    "s_waitcnt lgkmcnt(7)\n"
    "ds_read_b64_tr_b16 v[62:63], v11 offset:4096\n"
    "v_add_u32 v16, s78, v16\n"
    "v_mfma_f32_16x16x32_bf16 v[192:195], v[32:35], v[152:155], v[192:195]\n"
    "s_add_u32 s56, s69, 0x20000\n"
    "ds_read_b64_tr_b16 v[64:65], v16 offset:0\n"
    "v_mfma_f32_16x16x32_bf16 v[196:199], v[32:35], v[156:159], v[196:199]\n"
    "s_add_u32 m0, s66, 0\n"
    "ds_read_b64_tr_b16 v[66:67], v16 offset:4096\n"
    "v_add_u32 v17, s78, v17\n"
    "v_mfma_f32_16x16x32_bf16 v[200:203], v[36:39], v[152:155], v[200:203]\n"
    "ds_read_b64_tr_b16 v[68:69], v17 offset:0\n"
    "buffer_load_dwordx4 v[148:151], v1, s[44:47], s56 offen sc1\n"
    "v_mfma_f32_16x16x32_bf16 v[204:207], v[36:39], v[156:159], v[204:207]\n"
    "ds_read_b64_tr_b16 v[70:71], v17 offset:4096\n"
    "v_add_u32 v18, s78, v18\n"
    "v_mfma_f32_16x16x32_bf16 v[208:211], v[40:43], v[152:155], v[208:211]\n"
    "s_waitcnt vmcnt(24)\n"
    "ds_read_b64_tr_b16 v[72:73], v18 offset:0\n"
    "v_mfma_f32_16x16x32_bf16 v[212:215], v[40:43], v[156:159], v[212:215]\n"
    "ds_read_b64_tr_b16 v[74:75], v18 offset:4096\n"
    "v_add_u32 v19, s78, v19\n"
    "v_mfma_f32_16x16x32_bf16 v[216:219], v[44:47], v[152:155], v[216:219]\n"
    "ds_read_b64_tr_b16 v[76:77], v19 offset:0\n"
    "buffer_load_dwordx4 v28, s[40:43], s57 offen lds\n"
    "v_mfma_f32_16x16x32_bf16 v[220:223], v[44:47], v[156:159], v[220:223]\n"
    "s_waitcnt lgkmcnt(7)\n"
    "ds_read_b64_tr_b16 v[78:79], v19 offset:4096\n"
    "v_mfma_f32_16x16x32_bf16 v[160:163], v[48:51], v[96:99], v[160:163]\n"
    "s_add_u32 s56, s69, 0x400\n"
    "ds_read_b64_tr_b16 v[32:33], v8 offset:8192\n"
    "v_mfma_f32_16x16x32_bf16 v[164:167], v[48:51], v[100:103], v[164:167]\n"
    "ds_read_b64_tr_b16 v[34:35], v8 offset:12288\n"
    "buffer_load_dwordx4 v[152:155], v1, s[44:47], s56 offen sc1\n"
    "v_mfma_f32_16x16x32_bf16 v[168:171], v[52:55], v[96:99], v[168:171]\n"
    "s_add_u32 s56, s69, 0x20400\n"
    "ds_read_b64_tr_b16 v[36:37], v9 offset:8192\n"
    "v_mfma_f32_16x16x32_bf16 v[172:175], v[52:55], v[100:103], v[172:175]\n"
    "ds_read_b64_tr_b16 v[38:39], v9 offset:12288\n"
    "v_mfma_f32_16x16x32_bf16 v[176:179], v[56:59], v[96:99], v[176:179]\n"
    "ds_read_b64_tr_b16 v[40:41], v10 offset:8192\n"
    "buffer_load_dwordx4 v[156:159], v1, s[44:47], s56 offen sc1\n"
    "v_mfma_f32_16x16x32_bf16 v[180:183], v[56:59], v[100:103], v[180:183]\n"
    "ds_read_b64_tr_b16 v[42:43], v10 offset:12288\n"
    "v_mfma_f32_16x16x32_bf16 v[184:187], v[60:63], v[96:99], v[184:187]\n"
    "ds_read_b64_tr_b16 v[44:45], v11 offset:8192\n"
    "v_mfma_f32_16x16x32_bf16 v[188:191], v[60:63], v[100:103], v[188:191]\n"
    "s_waitcnt lgkmcnt(7)\n"
    "ds_read_b64_tr_b16 v[46:47], v11 offset:12288\n"
    "v_mfma_f32_16x16x32_bf16 v[192:195], v[64:67], v[96:99], v[192:195]\n"
    "ds_read_b64_tr_b16 v[48:49], v16 offset:8192\n"
    "v_mfma_f32_16x16x32_bf16 v[196:199], v[64:67], v[100:103], v[196:199]\n"
    "s_add_u32 m0, s66, 1024\n"
    "ds_read_b64_tr_b16 v[50:51], v16 offset:12288\n"
    "v_mfma_f32_16x16x32_bf16 v[200:203], v[68:71], v[96:99], v[200:203]\n"
    "ds_read_b64_tr_b16 v[52:53], v17 offset:8192\n"
    "v_mfma_f32_16x16x32_bf16 v[204:207], v[68:71], v[100:103], v[204:207]\n"
    "ds_read_b64_tr_b16 v[54:55], v17 offset:12288\n"
    "v_mfma_f32_16x16x32_bf16 v[208:211], v[72:75], v[96:99], v[208:211]\n"
    "s_waitcnt vmcnt(24)\n"
    "ds_read_b64_tr_b16 v[56:57], v18 offset:8192\n"
    "v_mfma_f32_16x16x32_bf16 v[212:215], v[72:75], v[100:103], v[212:215]\n"
    "ds_read_b64_tr_b16 v[58:59], v18 offset:12288\n"
    "buffer_load_dwordx4 v29, s[40:43], s57 offen lds\n"
    "v_mfma_f32_16x16x32_bf16 v[216:219], v[76:79], v[96:99], v[216:219]\n"
    "ds_read_b64_tr_b16 v[60:61], v19 offset:8192\n"
    "v_mfma_f32_16x16x32_bf16 v[220:223], v[76:79], v[100:103], v[220:223]\n"
    "s_waitcnt lgkmcnt(7)\n"
    "ds_read_b64_tr_b16 v[62:63], v19 offset:12288\n"
    "v_mfma_f32_16x16x32_bf16 v[160:163], v[32:35], v[104:107], v[160:163]\n"
    "s_add_u32 s56, s69, 0x800\n"
    "ds_read_b64_tr_b16 v[64:65], v8 offset:16384\n"
    "v_mfma_f32_16x16x32_bf16 v[164:167], v[32:35], v[108:111], v[164:167]\n"
    "ds_read_b64_tr_b16 v[66:67], v8 offset:20480\n"
    "buffer_load_dwordx4 v[96:99], v1, s[44:47], s56 offen sc1\n"
    "v_mfma_f32_16x16x32_bf16 v[168:171], v[36:39], v[104:107], v[168:171]\n"
    "s_add_u32 s56, s69, 0x20800\n"
    "ds_read_b64_tr_b16 v[68:69], v9 offset:16384\n"
    "v_mfma_f32_16x16x32_bf16 v[172:175], v[36:39], v[108:111], v[172:175]\n"
    "ds_read_b64_tr_b16 v[70:71], v9 offset:20480\n"
    "v_mfma_f32_16x16x32_bf16 v[176:179], v[40:43], v[104:107], v[176:179]\n"
    "ds_read_b64_tr_b16 v[72:73], v10 offset:16384\n"
    "buffer_load_dwordx4 v[100:103], v1, s[44:47], s56 offen sc1\n"
    "v_mfma_f32_16x16x32_bf16 v[180:183], v[40:43], v[108:111], v[180:183]\n"
    "ds_read_b64_tr_b16 v[74:75], v10 offset:20480\n"
    "v_mfma_f32_16x16x32_bf16 v[184:187], v[44:47], v[104:107], v[184:187]\n"
    "ds_read_b64_tr_b16 v[76:77], v11 offset:16384\n"
    "v_mfma_f32_16x16x32_bf16 v[188:191], v[44:47], v[108:111], v[188:191]\n"
    "s_waitcnt lgkmcnt(7)\n"
    "ds_read_b64_tr_b16 v[78:79], v11 offset:20480\n"
    "v_mfma_f32_16x16x32_bf16 v[192:195], v[48:51], v[104:107], v[192:195]\n"
    "ds_read_b64_tr_b16 v[32:33], v16 offset:16384\n"
    "v_mfma_f32_16x16x32_bf16 v[196:199], v[48:51], v[108:111], v[196:199]\n"
    "ds_read_b64_tr_b16 v[34:35], v16 offset:20480\n"
    "v_mfma_f32_16x16x32_bf16 v[200:203], v[52:55], v[104:107], v[200:203]\n"
    "ds_read_b64_tr_b16 v[36:37], v17 offset:16384\n"
    "v_mfma_f32_16x16x32_bf16 v[204:207], v[52:55], v[108:111], v[204:207]\n"
    "ds_read_b64_tr_b16 v[38:39], v17 offset:20480\n"
    "v_mfma_f32_16x16x32_bf16 v[208:211], v[56:59], v[104:107], v[208:211]\n"
    "s_waitcnt vmcnt(22)\n"
    "ds_read_b64_tr_b16 v[40:41], v18 offset:16384\n"
    "v_mfma_f32_16x16x32_bf16 v[212:215], v[56:59], v[108:111], v[212:215]\n"
    "ds_read_b64_tr_b16 v[42:43], v18 offset:20480\n"
    "v_mfma_f32_16x16x32_bf16 v[216:219], v[60:63], v[104:107], v[216:219]\n"
    "ds_read_b64_tr_b16 v[44:45], v19 offset:16384\n"
    "v_mfma_f32_16x16x32_bf16 v[220:223], v[60:63], v[108:111], v[220:223]\n"
    "s_waitcnt lgkmcnt(7)\n"
    "ds_read_b64_tr_b16 v[46:47], v19 offset:20480\n"
    "v_mfma_f32_16x16x32_bf16 v[160:163], v[64:67], v[112:115], v[160:163]\n"
    "ds_read_b64_tr_b16 v[48:49], v8 offset:24576\n"
    "buffer_load_dwordx2 v[80:81], v20, s[48:51], s73 offen offset:0\n"
    "v_mfma_f32_16x16x32_bf16 v[164:167], v[64:67], v[116:119], v[164:167]\n"
    "s_add_u32 s56, s69, 0xc00\n"
    "ds_read_b64_tr_b16 v[50:51], v8 offset:28672\n"
    "v_mfma_f32_16x16x32_bf16 v[168:171], v[68:71], v[112:115], v[168:171]\n"
    "s_add_u32 s57, s70, 0x18000\n"
    "ds_read_b64_tr_b16 v[52:53], v9 offset:24576\n"
    "v_mfma_f32_16x16x32_bf16 v[172:175], v[68:71], v[116:119], v[172:175]\n"
    "ds_read_b64_tr_b16 v[54:55], v9 offset:28672\n"
    "buffer_load_dwordx4 v[104:107], v1, s[44:47], s56 offen sc1\n"
    "v_mfma_f32_16x16x32_bf16 v[176:179], v[72:75], v[112:115], v[176:179]\n"
    "s_add_u32 s56, s69, 0x20c00\n"
    "ds_read_b64_tr_b16 v[56:57], v10 offset:24576\n"
    "v_mfma_f32_16x16x32_bf16 v[180:183], v[72:75], v[116:119], v[180:183]\n"
    "s_add_u32 m0, s66, 8192\n"
    "ds_read_b64_tr_b16 v[58:59], v10 offset:28672\n"
    "v_mfma_f32_16x16x32_bf16 v[184:187], v[76:79], v[112:115], v[184:187]\n"
    "ds_read_b64_tr_b16 v[60:61], v11 offset:24576\n"
    "buffer_load_dwordx4 v[108:111], v1, s[44:47], s56 offen sc1\n"
    "v_mfma_f32_16x16x32_bf16 v[188:191], v[76:79], v[116:119], v[188:191]\n"
    "s_waitcnt lgkmcnt(7)\n"
    "ds_read_b64_tr_b16 v[62:63], v11 offset:28672\n"
    "v_mfma_f32_16x16x32_bf16 v[192:195], v[32:35], v[112:115], v[192:195]\n"
    "ds_read_b64_tr_b16 v[64:65], v16 offset:24576\n"
    "v_mfma_f32_16x16x32_bf16 v[196:199], v[32:35], v[116:119], v[196:199]\n"
    "ds_read_b64_tr_b16 v[66:67], v16 offset:28672\n"
    "buffer_load_dwordx4 v28, s[40:43], s57 offen lds\n"
    "v_mfma_f32_16x16x32_bf16 v[200:203], v[36:39], v[112:115], v[200:203]\n"
    "ds_read_b64_tr_b16 v[68:69], v17 offset:24576\n"
    "v_mfma_f32_16x16x32_bf16 v[204:207], v[36:39], v[116:119], v[204:207]\n"
    "ds_read_b64_tr_b16 v[70:71], v17 offset:28672\n"
    "v_mfma_f32_16x16x32_bf16 v[208:211], v[40:43], v[112:115], v[208:211]\n"
    "s_waitcnt vmcnt(22)\n"
    "ds_read_b64_tr_b16 v[72:73], v18 offset:24576\n"
    "v_mfma_f32_16x16x32_bf16 v[212:215], v[40:43], v[116:119], v[212:215]\n"
    "ds_read_b64_tr_b16 v[74:75], v18 offset:28672\n"
    "v_mfma_f32_16x16x32_bf16 v[216:219], v[44:47], v[112:115], v[216:219]\n"
    "ds_read_b64_tr_b16 v[76:77], v19 offset:24576\n"
    "v_mfma_f32_16x16x32_bf16 v[220:223], v[44:47], v[116:119], v[220:223]\n"
    "s_waitcnt lgkmcnt(7)\n"
    "ds_read_b64_tr_b16 v[78:79], v19 offset:28672\n"
    "v_mfma_f32_16x16x32_bf16 v[160:163], v[48:51], v[120:123], v[160:163]\n"
    "ds_read_b64_tr_b16 v[32:33], v8 offset:32768\n"
    "buffer_load_dwordx2 v[82:83], v20, s[48:51], s72 offen offset:0\n"
    "v_mfma_f32_16x16x32_bf16 v[164:167], v[48:51], v[124:127], v[164:167]\n"
    "s_add_u32 s56, s53, 7\n"
    "ds_read_b64_tr_b16 v[34:35], v8 offset:36864\n"
    "v_mfma_f32_16x16x32_bf16 v[168:171], v[52:55], v[120:123], v[168:171]\n"
    "s_and_b32 s56, s56, 7\n"
    "ds_read_b64_tr_b16 v[36:37], v9 offset:32768\n"
    "v_mfma_f32_16x16x32_bf16 v[172:175], v[52:55], v[124:127], v[172:175]\n"
    "s_lshl_b32 s69, s56, 12\n"
    "ds_read_b64_tr_b16 v[38:39], v9 offset:36864\n"
    "v_mfma_f32_16x16x32_bf16 v[176:179], v[56:59], v[120:123], v[176:179]\n"
    "s_add_u32 s69, s69, s61\n"
    "ds_read_b64_tr_b16 v[40:41], v10 offset:32768\n"
    "v_mfma_f32_16x16x32_bf16 v[180:183], v[56:59], v[124:127], v[180:183]\n"
    "s_add_u32 s56, s69, 0x0\n"
    "ds_read_b64_tr_b16 v[42:43], v10 offset:36864\n"
    "v_mfma_f32_16x16x32_bf16 v[184:187], v[60:63], v[120:123], v[184:187]\n"
    "ds_read_b64_tr_b16 v[44:45], v11 offset:32768\n"
    "buffer_load_dwordx4 v[112:115], v1, s[44:47], s56 offen sc1\n"
    "v_mfma_f32_16x16x32_bf16 v[188:191], v[60:63], v[124:127], v[188:191]\n"
    "s_waitcnt lgkmcnt(7)\n"
    "ds_read_b64_tr_b16 v[46:47], v11 offset:36864\n"
    "v_mfma_f32_16x16x32_bf16 v[192:195], v[64:67], v[120:123], v[192:195]\n"
    "s_add_u32 s56, s69, 0x20000\n"
    "ds_read_b64_tr_b16 v[48:49], v16 offset:32768\n"
    "v_mfma_f32_16x16x32_bf16 v[196:199], v[64:67], v[124:127], v[196:199]\n"
    "ds_read_b64_tr_b16 v[50:51], v16 offset:36864\n"
    "buffer_load_dwordx4 v[116:119], v1, s[44:47], s56 offen sc1\n"
    "v_mfma_f32_16x16x32_bf16 v[200:203], v[68:71], v[120:123], v[200:203]\n"
    "s_add_u32 m0, s66, 9216\n"
    "ds_read_b64_tr_b16 v[52:53], v17 offset:32768\n"
    "v_mfma_f32_16x16x32_bf16 v[204:207], v[68:71], v[124:127], v[204:207]\n"
    "ds_read_b64_tr_b16 v[54:55], v17 offset:36864\n"
    "v_mfma_f32_16x16x32_bf16 v[208:211], v[72:75], v[120:123], v[208:211]\n"
    "s_waitcnt vmcnt(21)\n"
    "ds_read_b64_tr_b16 v[56:57], v18 offset:32768\n"
    "v_mfma_f32_16x16x32_bf16 v[212:215], v[72:75], v[124:127], v[212:215]\n"
    "ds_read_b64_tr_b16 v[58:59], v18 offset:36864\n"
    "buffer_load_dwordx4 v29, s[40:43], s57 offen lds\n"
    "v_mfma_f32_16x16x32_bf16 v[216:219], v[76:79], v[120:123], v[216:219]\n"
    "ds_read_b64_tr_b16 v[60:61], v19 offset:32768\n"
    "v_mfma_f32_16x16x32_bf16 v[220:223], v[76:79], v[124:127], v[220:223]\n"
    "s_waitcnt lgkmcnt(7)\n"
    "ds_read_b64_tr_b16 v[62:63], v19 offset:36864\n"
    "s_and_b32 s96, s53, 7\n"
    "s_lshl_b32 s96, s96, 3\n"
    "s_add_u32 s96, s96, s54\n"
    "s_lshl_b32 s96, s96, 2\n"
    "s_add_u32 s96, s96, s52\n"
    "s_lshl_b32 s96, s96, 4\n"
    "s_add_u32 s96, s96, 0x6000\n"
    "buffer_load_dword v26, v22, s[44:47], s96 offen sc1\n"
    "v_mfma_f32_16x16x32_bf16 v[160:163], v[32:35], v[128:131], v[160:163]\n"
    "ds_read_b64_tr_b16 v[64:65], v8 offset:40960\n"
    "buffer_load_dwordx2 v[84:85], v20, s[48:51], s73 offen offset:32\n"
    "v_mfma_f32_16x16x32_bf16 v[164:167], v[32:35], v[132:135], v[164:167]\n"
    "s_add_u32 s56, s69, 0x400\n"
    "ds_read_b64_tr_b16 v[66:67], v8 offset:45056\n"
    "v_mfma_f32_16x16x32_bf16 v[168:171], v[36:39], v[128:131], v[168:171]\n"
    "ds_read_b64_tr_b16 v[68:69], v9 offset:40960\n"
    "v_mfma_f32_16x16x32_bf16 v[172:175], v[36:39], v[132:135], v[172:175]\n"
    "ds_read_b64_tr_b16 v[70:71], v9 offset:45056\n"
    "buffer_load_dwordx4 v[120:123], v1, s[44:47], s56 offen sc1\n"
    "v_mfma_f32_16x16x32_bf16 v[176:179], v[40:43], v[128:131], v[176:179]\n"
    "s_add_u32 s56, s69, 0x20400\n"
    "ds_read_b64_tr_b16 v[72:73], v10 offset:40960\n"
    "v_mfma_f32_16x16x32_bf16 v[180:183], v[40:43], v[132:135], v[180:183]\n"
    "ds_read_b64_tr_b16 v[74:75], v10 offset:45056\n"
    "v_mfma_f32_16x16x32_bf16 v[184:187], v[44:47], v[128:131], v[184:187]\n"
    "ds_read_b64_tr_b16 v[76:77], v11 offset:40960\n"
    "buffer_load_dwordx4 v[124:127], v1, s[44:47], s56 offen sc1\n"
    "v_mfma_f32_16x16x32_bf16 v[188:191], v[44:47], v[132:135], v[188:191]\n"
    "s_waitcnt lgkmcnt(7)\n"
    "ds_read_b64_tr_b16 v[78:79], v11 offset:45056\n"
    "v_mfma_f32_16x16x32_bf16 v[192:195], v[48:51], v[128:131], v[192:195]\n"
    "ds_read_b64_tr_b16 v[32:33], v16 offset:40960\n"
    "v_mfma_f32_16x16x32_bf16 v[196:199], v[48:51], v[132:135], v[196:199]\n"
    "ds_read_b64_tr_b16 v[34:35], v16 offset:45056\n"
    "v_mfma_f32_16x16x32_bf16 v[200:203], v[52:55], v[128:131], v[200:203]\n"
    "ds_read_b64_tr_b16 v[36:37], v17 offset:40960\n"
    "v_mfma_f32_16x16x32_bf16 v[204:207], v[52:55], v[132:135], v[204:207]\n"
    "ds_read_b64_tr_b16 v[38:39], v17 offset:45056\n"
    "v_mfma_f32_16x16x32_bf16 v[208:211], v[56:59], v[128:131], v[208:211]\n"
    "s_waitcnt vmcnt(21)\n"
    "ds_read_b64_tr_b16 v[40:41], v18 offset:40960\n"
    "v_mfma_f32_16x16x32_bf16 v[212:215], v[56:59], v[132:135], v[212:215]\n"
    "ds_read_b64_tr_b16 v[42:43], v18 offset:45056\n"
    "v_mfma_f32_16x16x32_bf16 v[216:219], v[60:63], v[128:131], v[216:219]\n"
    "ds_read_b64_tr_b16 v[44:45], v19 offset:40960\n"
    "v_mfma_f32_16x16x32_bf16 v[220:223], v[60:63], v[132:135], v[220:223]\n"
    "s_waitcnt lgkmcnt(7)\n"
    "ds_read_b64_tr_b16 v[46:47], v19 offset:45056\n"
    "s_waitcnt vmcnt(20)\n"
    "s_barrier\n"
    "v_add_u32 v10, s76, v10\n"
    "v_mfma_f32_16x16x32_bf16 v[160:163], v[64:67], v[136:139], v[160:163]\n"
    "ds_read_b64_tr_b16 v[48:49], v10 offset:0\n"
    "buffer_load_dwordx2 v[86:87], v20, s[48:51], s72 offen offset:32\n"
    "v_mfma_f32_16x16x32_bf16 v[164:167], v[64:67], v[140:143], v[164:167]\n"
    "s_add_u32 s56, s69, 0x800\n"
    "ds_read_b64_tr_b16 v[50:51], v10 offset:4096\n"
    "v_add_u32 v11, s76, v11\n"
    "v_mfma_f32_16x16x32_bf16 v[168:171], v[68:71], v[136:139], v[168:171]\n"
    "ds_read_b64_tr_b16 v[52:53], v11 offset:0\n"
    "v_mfma_f32_16x16x32_bf16 v[172:175], v[68:71], v[140:143], v[172:175]\n"
    "ds_read_b64_tr_b16 v[54:55], v11 offset:4096\n"
    "v_add_u32 v16, s76, v16\n"
    "v_mfma_f32_16x16x32_bf16 v[176:179], v[72:75], v[136:139], v[176:179]\n"
    "ds_read_b64_tr_b16 v[56:57], v16 offset:0\n"
    "buffer_load_dwordx4 v[128:131], v1, s[44:47], s56 offen sc1\n"
    "v_mfma_f32_16x16x32_bf16 v[180:183], v[72:75], v[140:143], v[180:183]\n"
    "s_add_u32 s56, s69, 0x20800\n"
    "ds_read_b64_tr_b16 v[58:59], v16 offset:4096\n"
    "v_add_u32 v17, s76, v17\n"
    "v_mfma_f32_16x16x32_bf16 v[184:187], v[76:79], v[136:139], v[184:187]\n"
    "ds_read_b64_tr_b16 v[60:61], v17 offset:0\n"
    "v_mfma_f32_16x16x32_bf16 v[188:191], v[76:79], v[140:143], v[188:191]\n"
    "s_waitcnt lgkmcnt(7)\n"
    "ds_read_b64_tr_b16 v[62:63], v17 offset:4096\n"
    "v_add_u32 v18, s76, v18\n"
    "v_mfma_f32_16x16x32_bf16 v[192:195], v[32:35], v[136:139], v[192:195]\n"
    "ds_read_b64_tr_b16 v[64:65], v18 offset:0\n"
    "buffer_load_dwordx4 v[132:135], v1, s[44:47], s56 offen sc1\n"
    "v_mfma_f32_16x16x32_bf16 v[196:199], v[32:35], v[140:143], v[196:199]\n"
    "ds_read_b64_tr_b16 v[66:67], v18 offset:4096\n"
    "v_add_u32 v19, s76, v19\n"
    "v_mfma_f32_16x16x32_bf16 v[200:203], v[36:39], v[136:139], v[200:203]\n"
    "ds_read_b64_tr_b16 v[68:69], v19 offset:0\n"
    "v_mfma_f32_16x16x32_bf16 v[204:207], v[36:39], v[140:143], v[204:207]\n"
    "ds_read_b64_tr_b16 v[70:71], v19 offset:4096\n"
    "v_mfma_f32_16x16x32_bf16 v[208:211], v[40:43], v[136:139], v[208:211]\n"
    "s_waitcnt vmcnt(21)\n"
    "ds_read_b64_tr_b16 v[72:73], v10 offset:8192\n"
    "v_mfma_f32_16x16x32_bf16 v[212:215], v[40:43], v[140:143], v[212:215]\n"
    "ds_read_b64_tr_b16 v[74:75], v10 offset:12288\n"
    "v_mfma_f32_16x16x32_bf16 v[216:219], v[44:47], v[136:139], v[216:219]\n"
    "ds_read_b64_tr_b16 v[76:77], v11 offset:8192\n"
    "v_mfma_f32_16x16x32_bf16 v[220:223], v[44:47], v[140:143], v[220:223]\n"
    "s_waitcnt lgkmcnt(7)\n"
    "ds_read_b64_tr_b16 v[78:79], v11 offset:12288\n"
    "v_mfma_f32_16x16x32_bf16 v[176:179], v[48:51], v[144:147], v[176:179]\n"
    "s_add_u32 s56, s69, 0xc00\n"
    "ds_read_b64_tr_b16 v[32:33], v16 offset:8192\n"
    "v_mfma_f32_16x16x32_bf16 v[180:183], v[48:51], v[148:151], v[180:183]\n"
    "ds_read_b64_tr_b16 v[34:35], v16 offset:12288\n"
    "buffer_load_dwordx4 v[136:139], v1, s[44:47], s56 offen sc1\n"
    "v_mfma_f32_16x16x32_bf16 v[184:187], v[52:55], v[144:147], v[184:187]\n"
    "s_add_u32 s56, s69, 0x20c00\n"
    "ds_read_b64_tr_b16 v[36:37], v17 offset:8192\n"
    "v_mfma_f32_16x16x32_bf16 v[188:191], v[52:55], v[148:151], v[188:191]\n"
    "ds_read_b64_tr_b16 v[38:39], v17 offset:12288\n"
    "v_mfma_f32_16x16x32_bf16 v[192:195], v[56:59], v[144:147], v[192:195]\n"
    "ds_read_b64_tr_b16 v[40:41], v18 offset:8192\n"
    "buffer_load_dwordx4 v[140:143], v1, s[44:47], s56 offen sc1\n"
    "v_mfma_f32_16x16x32_bf16 v[196:199], v[56:59], v[148:151], v[196:199]\n"
    "ds_read_b64_tr_b16 v[42:43], v18 offset:12288\n"
    "v_mfma_f32_16x16x32_bf16 v[200:203], v[60:63], v[144:147], v[200:203]\n"
    "ds_read_b64_tr_b16 v[44:45], v19 offset:8192\n"
    "v_mfma_f32_16x16x32_bf16 v[204:207], v[60:63], v[148:151], v[204:207]\n"
    "s_waitcnt lgkmcnt(7)\n"
    "ds_read_b64_tr_b16 v[46:47], v19 offset:12288\n"
    "v_mfma_f32_16x16x32_bf16 v[208:211], v[64:67], v[144:147], v[208:211]\n"
    "s_waitcnt vmcnt(20)\n"
    "ds_read_b64_tr_b16 v[48:49], v10 offset:16384\n"
    "v_mfma_f32_16x16x32_bf16 v[212:215], v[64:67], v[148:151], v[212:215]\n"
    "ds_read_b64_tr_b16 v[50:51], v10 offset:20480\n"
    "v_mfma_f32_16x16x32_bf16 v[216:219], v[68:71], v[144:147], v[216:219]\n"
    "ds_read_b64_tr_b16 v[52:53], v11 offset:16384\n"
    "v_mfma_f32_16x16x32_bf16 v[220:223], v[68:71], v[148:151], v[220:223]\n"
    "ds_read_b64_tr_b16 v[54:55], v11 offset:20480\n"
    "v_mfma_f32_16x16x32_bf16 v[176:179], v[72:75], v[152:155], v[176:179]\n"
    "ds_read_b64_tr_b16 v[56:57], v16 offset:16384\n"
    "v_mfma_f32_16x16x32_bf16 v[180:183], v[72:75], v[156:159], v[180:183]\n"
    "ds_read_b64_tr_b16 v[58:59], v16 offset:20480\n"
    "v_mfma_f32_16x16x32_bf16 v[184:187], v[76:79], v[152:155], v[184:187]\n"
    "ds_read_b64_tr_b16 v[60:61], v17 offset:16384\n"
    "v_mfma_f32_16x16x32_bf16 v[188:191], v[76:79], v[156:159], v[188:191]\n"
    "s_waitcnt lgkmcnt(7)\n"
    "ds_read_b64_tr_b16 v[62:63], v17 offset:20480\n"
    "v_mfma_f32_16x16x32_bf16 v[192:195], v[32:35], v[152:155], v[192:195]\n"
    "ds_read_b64_tr_b16 v[64:65], v18 offset:16384\n"
    "v_mfma_f32_16x16x32_bf16 v[196:199], v[32:35], v[156:159], v[196:199]\n"
    "ds_read_b64_tr_b16 v[66:67], v18 offset:20480\n"
    "v_mfma_f32_16x16x32_bf16 v[200:203], v[36:39], v[152:155], v[200:203]\n"
    "ds_read_b64_tr_b16 v[68:69], v19 offset:16384\n"
    "v_mfma_f32_16x16x32_bf16 v[204:207], v[36:39], v[156:159], v[204:207]\n"
    "ds_read_b64_tr_b16 v[70:71], v19 offset:20480\n"
    "v_mfma_f32_16x16x32_bf16 v[208:211], v[40:43], v[152:155], v[208:211]\n"
    "s_waitcnt vmcnt(17)\n"
    "ds_read_b64_tr_b16 v[72:73], v10 offset:24576\n"
    "v_mfma_f32_16x16x32_bf16 v[212:215], v[40:43], v[156:159], v[212:215]\n"
    "ds_read_b64_tr_b16 v[74:75], v10 offset:28672\n"
    "v_mfma_f32_16x16x32_bf16 v[216:219], v[44:47], v[152:155], v[216:219]\n"
    "ds_read_b64_tr_b16 v[76:77], v11 offset:24576\n"
    "v_mfma_f32_16x16x32_bf16 v[220:223], v[44:47], v[156:159], v[220:223]\n"
    "s_waitcnt lgkmcnt(7)\n"
    "ds_read_b64_tr_b16 v[78:79], v11 offset:28672\n"
    "v_mfma_f32_16x16x32_bf16 v[176:179], v[48:51], v[96:99], v[176:179]\n"
    "ds_read_b64_tr_b16 v[32:33], v16 offset:24576\n"
    "v_mfma_f32_16x16x32_bf16 v[180:183], v[48:51], v[100:103], v[180:183]\n"
    "ds_read_b64_tr_b16 v[34:35], v16 offset:28672\n"
    "v_mfma_f32_16x16x32_bf16 v[184:187], v[52:55], v[96:99], v[184:187]\n"
    "ds_read_b64_tr_b16 v[36:37], v17 offset:24576\n"
    "v_mfma_f32_16x16x32_bf16 v[188:191], v[52:55], v[100:103], v[188:191]\n"
    "ds_read_b64_tr_b16 v[38:39], v17 offset:28672\n"
    "v_mfma_f32_16x16x32_bf16 v[192:195], v[56:59], v[96:99], v[192:195]\n"
    "ds_read_b64_tr_b16 v[40:41], v18 offset:24576\n"
    "v_mfma_f32_16x16x32_bf16 v[196:199], v[56:59], v[100:103], v[196:199]\n"
    "ds_read_b64_tr_b16 v[42:43], v18 offset:28672\n"
    "v_mfma_f32_16x16x32_bf16 v[200:203], v[60:63], v[96:99], v[200:203]\n"
    "ds_read_b64_tr_b16 v[44:45], v19 offset:24576\n"
    "v_mfma_f32_16x16x32_bf16 v[204:207], v[60:63], v[100:103], v[204:207]\n"
    "s_waitcnt lgkmcnt(7)\n"
    "ds_read_b64_tr_b16 v[46:47], v19 offset:28672\n"
    "v_mfma_f32_16x16x32_bf16 v[208:211], v[64:67], v[96:99], v[208:211]\n"
    "s_waitcnt vmcnt(14)\n"
    "ds_read_b64_tr_b16 v[48:49], v10 offset:32768\n"
    "v_mfma_f32_16x16x32_bf16 v[212:215], v[64:67], v[100:103], v[212:215]\n"
    "ds_read_b64_tr_b16 v[50:51], v10 offset:36864\n"
    "v_mfma_f32_16x16x32_bf16 v[216:219], v[68:71], v[96:99], v[216:219]\n"
    "ds_read_b64_tr_b16 v[52:53], v11 offset:32768\n"
    "v_mfma_f32_16x16x32_bf16 v[220:223], v[68:71], v[100:103], v[220:223]\n"
    "ds_read_b64_tr_b16 v[54:55], v11 offset:36864\n"
    "v_mfma_f32_16x16x32_bf16 v[176:179], v[72:75], v[104:107], v[176:179]\n"
    "ds_read_b64_tr_b16 v[56:57], v16 offset:32768\n"
    "v_mfma_f32_16x16x32_bf16 v[180:183], v[72:75], v[108:111], v[180:183]\n"
    "ds_read_b64_tr_b16 v[58:59], v16 offset:36864\n"
    "v_mfma_f32_16x16x32_bf16 v[184:187], v[76:79], v[104:107], v[184:187]\n"
    "ds_read_b64_tr_b16 v[60:61], v17 offset:32768\n"
    "v_mfma_f32_16x16x32_bf16 v[188:191], v[76:79], v[108:111], v[188:191]\n"
    "s_waitcnt lgkmcnt(7)\n"
    "ds_read_b64_tr_b16 v[62:63], v17 offset:36864\n"
    "v_mfma_f32_16x16x32_bf16 v[192:195], v[32:35], v[104:107], v[192:195]\n"
    "ds_read_b64_tr_b16 v[64:65], v18 offset:32768\n"
    "v_mfma_f32_16x16x32_bf16 v[196:199], v[32:35], v[108:111], v[196:199]\n"
    "ds_read_b64_tr_b16 v[66:67], v18 offset:36864\n"
    "v_mfma_f32_16x16x32_bf16 v[200:203], v[36:39], v[104:107], v[200:203]\n"
    "ds_read_b64_tr_b16 v[68:69], v19 offset:32768\n"
    "v_mfma_f32_16x16x32_bf16 v[204:207], v[36:39], v[108:111], v[204:207]\n"
    "ds_read_b64_tr_b16 v[70:71], v19 offset:36864\n"
    "v_mfma_f32_16x16x32_bf16 v[208:211], v[40:43], v[104:107], v[208:211]\n"
    "s_waitcnt vmcnt(10)\n"
    "ds_read_b64_tr_b16 v[72:73], v10 offset:40960\n"
    "v_mfma_f32_16x16x32_bf16 v[212:215], v[40:43], v[108:111], v[212:215]\n"
    "ds_read_b64_tr_b16 v[74:75], v10 offset:45056\n"
    "v_mfma_f32_16x16x32_bf16 v[216:219], v[44:47], v[104:107], v[216:219]\n"
    "ds_read_b64_tr_b16 v[76:77], v11 offset:40960\n"
    "v_mfma_f32_16x16x32_bf16 v[220:223], v[44:47], v[108:111], v[220:223]\n"
    "s_waitcnt lgkmcnt(7)\n"
    "ds_read_b64_tr_b16 v[78:79], v11 offset:45056\n"
    "v_mfma_f32_16x16x32_bf16 v[176:179], v[48:51], v[112:115], v[176:179]\n"
    "ds_read_b64_tr_b16 v[32:33], v16 offset:40960\n"
    "v_mfma_f32_16x16x32_bf16 v[180:183], v[48:51], v[116:119], v[180:183]\n"
    "ds_read_b64_tr_b16 v[34:35], v16 offset:45056\n"
    "v_mfma_f32_16x16x32_bf16 v[184:187], v[52:55], v[112:115], v[184:187]\n"
    "ds_read_b64_tr_b16 v[36:37], v17 offset:40960\n"
    "v_mfma_f32_16x16x32_bf16 v[188:191], v[52:55], v[116:119], v[188:191]\n"
    "ds_read_b64_tr_b16 v[38:39], v17 offset:45056\n"
    "v_mfma_f32_16x16x32_bf16 v[192:195], v[56:59], v[112:115], v[192:195]\n"
    "ds_read_b64_tr_b16 v[40:41], v18 offset:40960\n"
    "v_mfma_f32_16x16x32_bf16 v[196:199], v[56:59], v[116:119], v[196:199]\n"
    "ds_read_b64_tr_b16 v[42:43], v18 offset:45056\n"
    "v_mfma_f32_16x16x32_bf16 v[200:203], v[60:63], v[112:115], v[200:203]\n"
    "ds_read_b64_tr_b16 v[44:45], v19 offset:40960\n"
    "v_mfma_f32_16x16x32_bf16 v[204:207], v[60:63], v[116:119], v[204:207]\n"
    "s_waitcnt lgkmcnt(7)\n"
    "ds_read_b64_tr_b16 v[46:47], v19 offset:45056\n"
    "s_waitcnt vmcnt(9)\n"
    "s_barrier\n"
    "v_add_u32 v10, s77, v10\n"
    "v_mfma_f32_16x16x32_bf16 v[208:211], v[64:67], v[112:115], v[208:211]\n"
    "s_waitcnt vmcnt(5)\n"
    "ds_read_b64_tr_b16 v[48:49], v10 offset:0\n"
    "v_mfma_f32_16x16x32_bf16 v[212:215], v[64:67], v[116:119], v[212:215]\n"
    "ds_read_b64_tr_b16 v[50:51], v10 offset:4096\n"
    "v_add_u32 v11, s77, v11\n"
    "v_mfma_f32_16x16x32_bf16 v[216:219], v[68:71], v[112:115], v[216:219]\n"
    "ds_read_b64_tr_b16 v[52:53], v11 offset:0\n"
    "v_mfma_f32_16x16x32_bf16 v[220:223], v[68:71], v[116:119], v[220:223]\n"
    "ds_read_b64_tr_b16 v[54:55], v11 offset:4096\n"
    "v_add_u32 v16, s77, v16\n"
    "v_mfma_f32_16x16x32_bf16 v[176:179], v[72:75], v[120:123], v[176:179]\n"
    "ds_read_b64_tr_b16 v[56:57], v16 offset:0\n"
    "v_mfma_f32_16x16x32_bf16 v[180:183], v[72:75], v[124:127], v[180:183]\n"
    "ds_read_b64_tr_b16 v[58:59], v16 offset:4096\n"
    "v_add_u32 v17, s77, v17\n"
    "v_mfma_f32_16x16x32_bf16 v[184:187], v[76:79], v[120:123], v[184:187]\n"
    "ds_read_b64_tr_b16 v[60:61], v17 offset:0\n"
    "v_mfma_f32_16x16x32_bf16 v[188:191], v[76:79], v[124:127], v[188:191]\n"
    "s_waitcnt lgkmcnt(7)\n"
    "ds_read_b64_tr_b16 v[62:63], v17 offset:4096\n"
    "v_add_u32 v18, s77, v18\n"
    "v_mfma_f32_16x16x32_bf16 v[192:195], v[32:35], v[120:123], v[192:195]\n"
    "ds_read_b64_tr_b16 v[64:65], v18 offset:0\n"
    "v_mfma_f32_16x16x32_bf16 v[196:199], v[32:35], v[124:127], v[196:199]\n"
    "ds_read_b64_tr_b16 v[66:67], v18 offset:4096\n"
    "v_add_u32 v19, s77, v19\n"
    "v_mfma_f32_16x16x32_bf16 v[200:203], v[36:39], v[120:123], v[200:203]\n"
    "ds_read_b64_tr_b16 v[68:69], v19 offset:0\n"
    "v_mfma_f32_16x16x32_bf16 v[204:207], v[36:39], v[124:127], v[204:207]\n"
    "ds_read_b64_tr_b16 v[70:71], v19 offset:4096\n"
    "v_mfma_f32_16x16x32_bf16 v[208:211], v[40:43], v[120:123], v[208:211]\n"
    "s_waitcnt vmcnt(2)\n"
    "ds_read_b64_tr_b16 v[72:73], v10 offset:8192\n"
    "v_mfma_f32_16x16x32_bf16 v[212:215], v[40:43], v[124:127], v[212:215]\n"
    "ds_read_b64_tr_b16 v[74:75], v10 offset:12288\n"
    "v_mfma_f32_16x16x32_bf16 v[216:219], v[44:47], v[120:123], v[216:219]\n"
    "ds_read_b64_tr_b16 v[76:77], v11 offset:8192\n"
    "v_mfma_f32_16x16x32_bf16 v[220:223], v[44:47], v[124:127], v[220:223]\n"
    "s_waitcnt lgkmcnt(7)\n"
    "ds_read_b64_tr_b16 v[78:79], v11 offset:12288\n"
    "v_mfma_f32_16x16x32_bf16 v[176:179], v[48:51], v[128:131], v[176:179]\n"
    "ds_read_b64_tr_b16 v[32:33], v16 offset:8192\n"
    "v_mfma_f32_16x16x32_bf16 v[180:183], v[48:51], v[132:135], v[180:183]\n"
    "ds_read_b64_tr_b16 v[34:35], v16 offset:12288\n"
    "v_mfma_f32_16x16x32_bf16 v[184:187], v[52:55], v[128:131], v[184:187]\n"
    "ds_read_b64_tr_b16 v[36:37], v17 offset:8192\n"
    "v_mfma_f32_16x16x32_bf16 v[188:191], v[52:55], v[132:135], v[188:191]\n"
    "ds_read_b64_tr_b16 v[38:39], v17 offset:12288\n"
    "v_mfma_f32_16x16x32_bf16 v[192:195], v[56:59], v[128:131], v[192:195]\n"
    "ds_read_b64_tr_b16 v[40:41], v18 offset:8192\n"
    "v_mfma_f32_16x16x32_bf16 v[196:199], v[56:59], v[132:135], v[196:199]\n"
    "ds_read_b64_tr_b16 v[42:43], v18 offset:12288\n"
    "v_mfma_f32_16x16x32_bf16 v[200:203], v[60:63], v[128:131], v[200:203]\n"
    "ds_read_b64_tr_b16 v[44:45], v19 offset:8192\n"
    "v_mfma_f32_16x16x32_bf16 v[204:207], v[60:63], v[132:135], v[204:207]\n"
    "s_waitcnt lgkmcnt(7)\n"
    "ds_read_b64_tr_b16 v[46:47], v19 offset:12288\n"
    "v_add_u32 v8, s76, v8\n"
    "v_mfma_f32_16x16x32_bf16 v[208:211], v[64:67], v[128:131], v[208:211]\n"
    "s_waitcnt vmcnt(0)\n"
    "ds_read_b64_tr_b16 v[48:49], v8 offset:0\n"
    "v_mfma_f32_16x16x32_bf16 v[212:215], v[64:67], v[132:135], v[212:215]\n"
    "ds_read_b64_tr_b16 v[50:51], v8 offset:4096\n"
    "v_add_u32 v9, s76, v9\n"
    "v_mfma_f32_16x16x32_bf16 v[216:219], v[68:71], v[128:131], v[216:219]\n"
    "ds_read_b64_tr_b16 v[52:53], v9 offset:0\n"
    "v_mfma_f32_16x16x32_bf16 v[220:223], v[68:71], v[132:135], v[220:223]\n"
    "ds_read_b64_tr_b16 v[54:55], v9 offset:4096\n"
    "v_mfma_f32_16x16x32_bf16 v[176:179], v[72:75], v[136:139], v[176:179]\n"
    "ds_read_b64_tr_b16 v[56:57], v8 offset:8192\n"
    "v_mfma_f32_16x16x32_bf16 v[180:183], v[72:75], v[140:143], v[180:183]\n"
    "ds_read_b64_tr_b16 v[58:59], v8 offset:12288\n"
    "v_mfma_f32_16x16x32_bf16 v[184:187], v[76:79], v[136:139], v[184:187]\n"
    "ds_read_b64_tr_b16 v[60:61], v9 offset:8192\n"
    "v_mfma_f32_16x16x32_bf16 v[188:191], v[76:79], v[140:143], v[188:191]\n"
    "s_waitcnt lgkmcnt(7)\n"
    "ds_read_b64_tr_b16 v[62:63], v9 offset:12288\n"
    "v_mfma_f32_16x16x32_bf16 v[192:195], v[32:35], v[136:139], v[192:195]\n"
    "ds_read_b64_tr_b16 v[64:65], v8 offset:16384\n"
    "v_mfma_f32_16x16x32_bf16 v[196:199], v[32:35], v[140:143], v[196:199]\n"
    "ds_read_b64_tr_b16 v[66:67], v8 offset:20480\n"
    "v_mfma_f32_16x16x32_bf16 v[200:203], v[36:39], v[136:139], v[200:203]\n"
    "ds_read_b64_tr_b16 v[68:69], v9 offset:16384\n"
    "v_mfma_f32_16x16x32_bf16 v[204:207], v[36:39], v[140:143], v[204:207]\n"
    "ds_read_b64_tr_b16 v[70:71], v9 offset:20480\n"
    "v_cmp_eq_u32 vcc, s97, v26\n"
    "s_nop 0\n"
    "s_cmp_eq_u64 vcc, -1\n"
    "s_cbranch_scc1 L_pok_3_%=\n"
    "s_and_b32 s96, s53, 7\n"
    "s_lshl_b32 s96, s96, 3\n"
    "s_add_u32 s96, s96, s54\n"
    "s_lshl_b32 s96, s96, 2\n"
    "s_add_u32 s96, s96, s52\n"
    "s_lshl_b32 s96, s96, 4\n"
    "s_add_u32 s96, s96, 0x6000\n"
    "s_mov_b32 s94, 2000000\n"
    "L_psp_4_%=:\n"
    "s_sleep 2\n"
    "buffer_load_dword v26, v22, s[44:47], s96 offen sc1\n"
    "s_waitcnt vmcnt(0)\n"
    "v_cmp_eq_u32 vcc, s97, v26\n"
    "s_nop 4\n"
    "s_cmp_eq_u64 vcc, -1\n"
    "s_cbranch_scc1 L_pok_3_%=\n"
    "s_sub_u32 s94, s94, 1\n"
    "s_cmp_lg_u32 s94, 0\n"
    "s_cbranch_scc1 L_psp_4_%=\n"
    "L_pok_3_%=:\n"
    "v_mfma_f32_16x16x32_bf16 v[208:211], v[40:43], v[136:139], v[208:211]\n"
    "ds_read_b64_tr_b16 v[72:73], v8 offset:24576\n"
    "v_mfma_f32_16x16x32_bf16 v[212:215], v[40:43], v[140:143], v[212:215]\n"
    "ds_read_b64_tr_b16 v[74:75], v8 offset:28672\n"
    "v_mfma_f32_16x16x32_bf16 v[216:219], v[44:47], v[136:139], v[216:219]\n"
    "ds_read_b64_tr_b16 v[76:77], v9 offset:24576\n"
    "v_cvt_pk_bf16_f32 v224, v176, v177\n"
    "v_cvt_pk_bf16_f32 v225, v178, v179\n"
    "v_mfma_f32_16x16x32_bf16 v[220:223], v[44:47], v[140:143], v[220:223]\n"
    "s_waitcnt lgkmcnt(7)\n"
    "ds_read_b64_tr_b16 v[78:79], v9 offset:28672\n"
    "v_cvt_pk_bf16_f32 v226, v180, v181\n"
    "v_cvt_pk_bf16_f32 v227, v182, v183\n"
    "v_mfma_f32_16x16x32_bf16 v[160:163], v[48:51], v[144:147], v[160:163]\n"
    "ds_read_b64_tr_b16 v[32:33], v8 offset:32768\n"
    "v_min_u32 v224, -2, v224\n"
    "v_min_u32 v225, -2, v225\n"
    "v_min_u32 v226, -2, v226\n"
    "v_mfma_f32_16x16x32_bf16 v[164:167], v[48:51], v[148:151], v[164:167]\n"
    "ds_read_b64_tr_b16 v[34:35], v8 offset:36864\n"
    "v_min_u32 v227, -2, v227\n"
    "v_mfma_f32_16x16x32_bf16 v[168:171], v[52:55], v[144:147], v[168:171]\n"
    "ds_read_b64_tr_b16 v[36:37], v9 offset:32768\n"
    "buffer_store_dwordx4 v[224:227], v1, s[44:47], s81 offen offset:0 sc1\n"
    "v_mfma_f32_16x16x32_bf16 v[172:175], v[52:55], v[148:151], v[172:175]\n"
    "ds_read_b64_tr_b16 v[38:39], v9 offset:36864\n"
    "v_cvt_pk_bf16_f32 v228, v184, v185\n"
    "v_mfma_f32_16x16x32_bf16 v[160:163], v[56:59], v[152:155], v[160:163]\n"
    "ds_read_b64_tr_b16 v[40:41], v8 offset:40960\n"
    "v_cvt_pk_bf16_f32 v229, v186, v187\n"
    "v_cvt_pk_bf16_f32 v230, v188, v189\n"
    "v_mfma_f32_16x16x32_bf16 v[164:167], v[56:59], v[156:159], v[164:167]\n"
    "ds_read_b64_tr_b16 v[42:43], v8 offset:45056\n"
    "v_cvt_pk_bf16_f32 v231, v190, v191\n"
    "v_min_u32 v228, -2, v228\n"
    "v_mfma_f32_16x16x32_bf16 v[168:171], v[60:63], v[152:155], v[168:171]\n"
    "ds_read_b64_tr_b16 v[44:45], v9 offset:40960\n"
    "v_min_u32 v229, -2, v229\n"
    "v_min_u32 v230, -2, v230\n"
    "v_min_u32 v231, -2, v231\n"
    "v_mfma_f32_16x16x32_bf16 v[172:175], v[60:63], v[156:159], v[172:175]\n"
    "s_waitcnt lgkmcnt(7)\n"
    "ds_read_b64_tr_b16 v[46:47], v9 offset:45056\n"
    "v_add_u32 v8, s77, v8\n"
    "v_mfma_f32_16x16x32_bf16 v[160:163], v[64:67], v[96:99], v[160:163]\n"
    "ds_read_b64_tr_b16 v[48:49], v8 offset:0\n"
    "buffer_store_dwordx4 v[228:231], v1, s[44:47], s81 offen offset:1024 sc1\n"
    "v_mfma_f32_16x16x32_bf16 v[164:167], v[64:67], v[100:103], v[164:167]\n"
    "ds_read_b64_tr_b16 v[50:51], v8 offset:4096\n"
    "v_add_u32 v9, s77, v9\n"
    "v_cvt_pk_bf16_f32 v232, v192, v193\n"
    "v_mfma_f32_16x16x32_bf16 v[168:171], v[68:71], v[96:99], v[168:171]\n"
    "ds_read_b64_tr_b16 v[52:53], v9 offset:0\n"
    "v_cvt_pk_bf16_f32 v233, v194, v195\n"
    "v_cvt_pk_bf16_f32 v234, v196, v197\n"
    "v_cvt_pk_bf16_f32 v235, v198, v199\n"
    "v_mfma_f32_16x16x32_bf16 v[172:175], v[68:71], v[100:103], v[172:175]\n"
    "ds_read_b64_tr_b16 v[54:55], v9 offset:4096\n"
    "v_min_u32 v232, -2, v232\n"
    "v_min_u32 v233, -2, v233\n"
    "v_mfma_f32_16x16x32_bf16 v[160:163], v[72:75], v[104:107], v[160:163]\n"
    "ds_read_b64_tr_b16 v[56:57], v8 offset:8192\n"
    "v_min_u32 v234, -2, v234\n"
    "v_min_u32 v235, -2, v235\n"
    "v_mfma_f32_16x16x32_bf16 v[164:167], v[72:75], v[108:111], v[164:167]\n"
    "ds_read_b64_tr_b16 v[58:59], v8 offset:12288\n"
    "buffer_store_dwordx4 v[232:235], v1, s[44:47], s82 offen offset:0 sc1\n"
    "v_mfma_f32_16x16x32_bf16 v[168:171], v[76:79], v[104:107], v[168:171]\n"
    "ds_read_b64_tr_b16 v[60:61], v9 offset:8192\n"
    "v_cvt_pk_bf16_f32 v236, v200, v201\n"
    "v_mfma_f32_16x16x32_bf16 v[172:175], v[76:79], v[108:111], v[172:175]\n"
    "s_waitcnt lgkmcnt(7)\n"
    "ds_read_b64_tr_b16 v[62:63], v9 offset:12288\n"
    "v_cvt_pk_bf16_f32 v237, v202, v203\n"
    "v_mfma_f32_16x16x32_bf16 v[160:163], v[32:35], v[112:115], v[160:163]\n"
    "v_cvt_pk_bf16_f32 v238, v204, v205\n"
    "v_cvt_pk_bf16_f32 v239, v206, v207\n"
    "v_mfma_f32_16x16x32_bf16 v[164:167], v[32:35], v[116:119], v[164:167]\n"
    "v_min_u32 v236, -2, v236\n"
    "v_min_u32 v237, -2, v237\n"
    "v_min_u32 v238, -2, v238\n"
    "v_mfma_f32_16x16x32_bf16 v[168:171], v[36:39], v[112:115], v[168:171]\n"
    "v_min_u32 v239, -2, v239\n"
    "v_mfma_f32_16x16x32_bf16 v[172:175], v[36:39], v[116:119], v[172:175]\n"
    "buffer_store_dwordx4 v[236:239], v1, s[44:47], s82 offen offset:1024 sc1\n"
    "v_cvt_pk_bf16_f32 v240, v208, v209\n"
    "v_mfma_f32_16x16x32_bf16 v[160:163], v[40:43], v[120:123], v[160:163]\n"
    "v_cvt_pk_bf16_f32 v241, v210, v211\n"
    "v_cvt_pk_bf16_f32 v242, v212, v213\n"
    "v_mfma_f32_16x16x32_bf16 v[164:167], v[40:43], v[124:127], v[164:167]\n"
    "v_cvt_pk_bf16_f32 v243, v214, v215\n"
    "v_min_u32 v240, -2, v240\n"
    "v_mfma_f32_16x16x32_bf16 v[168:171], v[44:47], v[120:123], v[168:171]\n"
    "v_min_u32 v241, -2, v241\n"
    "v_mfma_f32_16x16x32_bf16 v[172:175], v[44:47], v[124:127], v[172:175]\n"
    "s_waitcnt lgkmcnt(0)\n"
    "v_min_u32 v242, -2, v242\n"
    "v_min_u32 v243, -2, v243\n"
    "v_mfma_f32_16x16x32_bf16 v[160:163], v[48:51], v[128:131], v[160:163]\n"
    "buffer_store_dwordx4 v[240:243], v1, s[44:47], s83 offen offset:0 sc1\n"
    "v_cvt_pk_bf16_f32 v244, v216, v217\n"
    "v_mfma_f32_16x16x32_bf16 v[164:167], v[48:51], v[132:135], v[164:167]\n"
    "v_cvt_pk_bf16_f32 v245, v218, v219\n"
    "v_cvt_pk_bf16_f32 v246, v220, v221\n"
    "v_mfma_f32_16x16x32_bf16 v[168:171], v[52:55], v[128:131], v[168:171]\n"
    "v_cvt_pk_bf16_f32 v247, v222, v223\n"
    "v_min_u32 v244, -2, v244\n"
    "v_mfma_f32_16x16x32_bf16 v[172:175], v[52:55], v[132:135], v[172:175]\n"
    "v_min_u32 v245, -2, v245\n"
    "v_mfma_f32_16x16x32_bf16 v[160:163], v[56:59], v[136:139], v[160:163]\n"
    "v_min_u32 v246, -2, v246\n"
    "v_min_u32 v247, -2, v247\n"
    "v_mfma_f32_16x16x32_bf16 v[164:167], v[56:59], v[140:143], v[164:167]\n"
    "buffer_store_dwordx4 v[244:247], v1, s[44:47], s83 offen offset:1024 sc1\n"
    "v_mfma_f32_16x16x32_bf16 v[168:171], v[60:63], v[136:139], v[168:171]\n"
    "v_mfma_f32_16x16x32_bf16 v[172:175], v[60:63], v[140:143], v[172:175]\n"
    "s_lshl_b32 s57, s75, 13\n"
    "s_add_u32 s74, s74, s57\n"
    "s_add_u32 s74, s74, 2048\n"
    "s_add_u32 s75, s74, 4096\n"
    "buffer_load_dwordx4 v[32:35], v1, s[44:47], s74 offen offset:0 sc1\n"
    "buffer_load_dwordx4 v[36:39], v1, s[44:47], s74 offen offset:1024 sc1\n"
    "buffer_load_dwordx4 v[40:43], v1, s[44:47], s74 offen offset:2048 sc1\n"
    "buffer_load_dwordx4 v[44:47], v1, s[44:47], s74 offen offset:3072 sc1\n"
    "buffer_load_dwordx4 v[48:51], v1, s[44:47], s75 offen offset:0 sc1\n"
    "buffer_load_dwordx4 v[52:55], v1, s[44:47], s75 offen offset:1024 sc1\n"
    "v_lshlrev_b32 v22, 16, v80\n"
    "v_and_b32 v23, 0xffff0000, v80\n"
    "v_add_f32 v160, v160, v22\n"
    "v_add_f32 v161, v161, v23\n"
    "v_lshlrev_b32 v22, 16, v81\n"
    "v_and_b32 v23, 0xffff0000, v81\n"
    "v_add_f32 v162, v162, v22\n"
    "v_add_f32 v163, v163, v23\n"
    "v_lshlrev_b32 v22, 16, v82\n"
    "v_and_b32 v23, 0xffff0000, v82\n"
    "v_add_f32 v164, v164, v22\n"
    "v_add_f32 v165, v165, v23\n"
    "v_lshlrev_b32 v22, 16, v83\n"
    "v_and_b32 v23, 0xffff0000, v83\n"
    "v_add_f32 v166, v166, v22\n"
    "v_add_f32 v167, v167, v23\n"
    "v_lshlrev_b32 v22, 16, v84\n"
    "v_and_b32 v23, 0xffff0000, v84\n"
    "v_add_f32 v168, v168, v22\n"
    "v_add_f32 v169, v169, v23\n"
    "v_lshlrev_b32 v22, 16, v85\n"
    "v_and_b32 v23, 0xffff0000, v85\n"
    "v_add_f32 v170, v170, v22\n"
    "v_add_f32 v171, v171, v23\n"
    "v_lshlrev_b32 v22, 16, v86\n"
    "v_and_b32 v23, 0xffff0000, v86\n"
    "v_add_f32 v172, v172, v22\n"
    "v_add_f32 v173, v173, v23\n"
    "v_lshlrev_b32 v22, 16, v87\n"
    "v_and_b32 v23, 0xffff0000, v87\n"
    "v_add_f32 v174, v174, v22\n"
    "v_add_f32 v175, v175, v23\n"
    "s_mov_b64 s[48:49], %3\n"
    "s_and_b32 s49, s49, 0xffff\n"
    "s_mov_b32 s50, 0x200000\n"
    "s_mov_b32 s51, 0x20000\n"
    "s_waitcnt vmcnt(0)\n"
    "v_max3_u32 v22, v32, v33, v34\n"
    "v_max3_u32 v23, v35, v36, v37\n"
    "v_max3_u32 v23, v23, v38, v39\n"
    "v_max3_u32 v22, v22, v40, v41\n"
    "v_max3_u32 v23, v23, v42, v43\n"
    "v_max3_u32 v22, v22, v44, v45\n"
    "v_max3_u32 v23, v23, v46, v47\n"
    "v_max3_u32 v22, v22, v48, v49\n"
    "v_max3_u32 v23, v23, v50, v51\n"
    "v_max3_u32 v22, v22, v52, v53\n"
    "v_max3_u32 v23, v23, v54, v55\n"
    "v_max_u32 v22, v22, v23\n"
    "v_cmp_eq_u32 vcc, -1, v22\n"
    "s_nop 0\n"
    "s_cmp_eq_u64 vcc, 0\n"
    "s_cbranch_scc1 L_xok_5_%=\n"
    "s_mov_b32 s94, 2000000\n"
    "L_xsp_6_%=:\n"
    "s_sleep 1\n"
    "buffer_load_dwordx4 v[32:35], v1, s[44:47], s74 offen offset:0 sc1\n"
    "buffer_load_dwordx4 v[36:39], v1, s[44:47], s74 offen offset:1024 sc1\n"
    "buffer_load_dwordx4 v[40:43], v1, s[44:47], s74 offen offset:2048 sc1\n"
    "buffer_load_dwordx4 v[44:47], v1, s[44:47], s74 offen offset:3072 sc1\n"
    "buffer_load_dwordx4 v[48:51], v1, s[44:47], s75 offen offset:0 sc1\n"
    "buffer_load_dwordx4 v[52:55], v1, s[44:47], s75 offen offset:1024 sc1\n"
    "s_waitcnt vmcnt(0)\n"
    "v_max3_u32 v22, v32, v33, v34\n"
    "v_max3_u32 v23, v35, v36, v37\n"
    "v_max3_u32 v23, v23, v38, v39\n"
    "v_max3_u32 v22, v22, v40, v41\n"
    "v_max3_u32 v23, v23, v42, v43\n"
    "v_max3_u32 v22, v22, v44, v45\n"
    "v_max3_u32 v23, v23, v46, v47\n"
    "v_max3_u32 v22, v22, v48, v49\n"
    "v_max3_u32 v23, v23, v50, v51\n"
    "v_max3_u32 v22, v22, v52, v53\n"
    "v_max3_u32 v23, v23, v54, v55\n"
    "v_max_u32 v22, v22, v23\n"
    "v_cmp_eq_u32 vcc, -1, v22\n"
    "s_nop 4\n"
    "s_cmp_eq_u64 vcc, 0\n"
    "s_cbranch_scc1 L_xok_5_%=\n"
    "s_sub_u32 s94, s94, 1\n"
    "s_cmp_lg_u32 s94, 0\n"
    "s_cbranch_scc1 L_xsp_6_%=\n"
    "L_xok_5_%=:\n"
    "s_or_b32 s56, %5, s52\n"
    "s_cmp_eq_u32 s56, 0\n"
    "s_cbranch_scc0 L_noep_7_%=\n"
    "s_add_u32 s56, s99, 1\n"
    "v_mov_b32 v22, s56\n"
    "s_mov_b32 s56, 0\n"
    "buffer_store_dword v22, v7, s[44:47], s56 offen sc1\n"
    "L_noep_7_%=:\n"
    "v_lshlrev_b32 v56, 16, v32\n"
    "v_and_b32 v57, 0xffff0000, v32\n"
    "v_pk_add_f32 v[160:161], v[160:161], v[56:57]\n"
    "v_lshlrev_b32 v58, 16, v33\n"
    "v_and_b32 v59, 0xffff0000, v33\n"
    "v_pk_add_f32 v[162:163], v[162:163], v[58:59]\n"
    "v_lshlrev_b32 v60, 16, v40\n"
    "v_and_b32 v61, 0xffff0000, v40\n"
    "v_pk_add_f32 v[160:161], v[160:161], v[60:61]\n"
    "v_lshlrev_b32 v62, 16, v41\n"
    "v_and_b32 v63, 0xffff0000, v41\n"
    "v_pk_add_f32 v[162:163], v[162:163], v[62:63]\n"
    "v_lshlrev_b32 v56, 16, v48\n"
    "v_and_b32 v57, 0xffff0000, v48\n"
    "v_pk_add_f32 v[160:161], v[160:161], v[56:57]\n"
    "v_lshlrev_b32 v58, 16, v49\n"
    "v_and_b32 v59, 0xffff0000, v49\n"
    "v_pk_add_f32 v[162:163], v[162:163], v[58:59]\n"
    "v_cvt_pk_bf16_f32 v120, v160, v161\n"
    "v_cvt_pk_bf16_f32 v121, v162, v163\n"
    "buffer_store_dwordx2 v[120:121], v20, s[48:51], s73 offen offset:0\n"
    "v_lshlrev_b32 v56, 16, v34\n"
    "v_and_b32 v57, 0xffff0000, v34\n"
    "v_pk_add_f32 v[164:165], v[164:165], v[56:57]\n"
    "v_lshlrev_b32 v58, 16, v35\n"
    "v_and_b32 v59, 0xffff0000, v35\n"
    "v_pk_add_f32 v[166:167], v[166:167], v[58:59]\n"
    "v_lshlrev_b32 v60, 16, v42\n"
    "v_and_b32 v61, 0xffff0000, v42\n"
    "v_pk_add_f32 v[164:165], v[164:165], v[60:61]\n"
    "v_lshlrev_b32 v62, 16, v43\n"
    "v_and_b32 v63, 0xffff0000, v43\n"
    "v_pk_add_f32 v[166:167], v[166:167], v[62:63]\n"
    "v_lshlrev_b32 v56, 16, v50\n"
    "v_and_b32 v57, 0xffff0000, v50\n"
    "v_pk_add_f32 v[164:165], v[164:165], v[56:57]\n"
    "v_lshlrev_b32 v58, 16, v51\n"
    "v_and_b32 v59, 0xffff0000, v51\n"
    "v_pk_add_f32 v[166:167], v[166:167], v[58:59]\n"
    "v_cvt_pk_bf16_f32 v122, v164, v165\n"
    "v_cvt_pk_bf16_f32 v123, v166, v167\n"
    "buffer_store_dwordx2 v[122:123], v20, s[48:51], s72 offen offset:0\n"
    "v_lshlrev_b32 v56, 16, v36\n"
    "v_and_b32 v57, 0xffff0000, v36\n"
    "v_pk_add_f32 v[168:169], v[168:169], v[56:57]\n"
    "v_lshlrev_b32 v58, 16, v37\n"
    "v_and_b32 v59, 0xffff0000, v37\n"
    "v_pk_add_f32 v[170:171], v[170:171], v[58:59]\n"
    "v_lshlrev_b32 v60, 16, v44\n"
    "v_and_b32 v61, 0xffff0000, v44\n"
    "v_pk_add_f32 v[168:169], v[168:169], v[60:61]\n"
    "v_lshlrev_b32 v62, 16, v45\n"
    "v_and_b32 v63, 0xffff0000, v45\n"
    "v_pk_add_f32 v[170:171], v[170:171], v[62:63]\n"
    "v_lshlrev_b32 v56, 16, v52\n"
    "v_and_b32 v57, 0xffff0000, v52\n"
    "v_pk_add_f32 v[168:169], v[168:169], v[56:57]\n"
    "v_lshlrev_b32 v58, 16, v53\n"
    "v_and_b32 v59, 0xffff0000, v53\n"
    "v_pk_add_f32 v[170:171], v[170:171], v[58:59]\n"
    "v_cvt_pk_bf16_f32 v124, v168, v169\n"
    "v_cvt_pk_bf16_f32 v125, v170, v171\n"
    "buffer_store_dwordx2 v[124:125], v20, s[48:51], s73 offen offset:32\n"
    "v_lshlrev_b32 v56, 16, v38\n"
    "v_and_b32 v57, 0xffff0000, v38\n"
    "v_pk_add_f32 v[172:173], v[172:173], v[56:57]\n"
    "v_lshlrev_b32 v58, 16, v39\n"
    "v_and_b32 v59, 0xffff0000, v39\n"
    "v_pk_add_f32 v[174:175], v[174:175], v[58:59]\n"
    "v_lshlrev_b32 v60, 16, v46\n"
    "v_and_b32 v61, 0xffff0000, v46\n"
    "v_pk_add_f32 v[172:173], v[172:173], v[60:61]\n"
    "v_lshlrev_b32 v62, 16, v47\n"
    "v_and_b32 v63, 0xffff0000, v47\n"
    "v_pk_add_f32 v[174:175], v[174:175], v[62:63]\n"
    "v_lshlrev_b32 v56, 16, v54\n"
    "v_and_b32 v57, 0xffff0000, v54\n"
    "v_pk_add_f32 v[172:173], v[172:173], v[56:57]\n"
    "v_lshlrev_b32 v58, 16, v55\n"
    "v_and_b32 v59, 0xffff0000, v55\n"
    "v_pk_add_f32 v[174:175], v[174:175], v[58:59]\n"
    "v_cvt_pk_bf16_f32 v126, v172, v173\n"
    "v_cvt_pk_bf16_f32 v127, v174, v175\n"
    "buffer_store_dwordx2 v[126:127], v20, s[48:51], s72 offen offset:32\n"
    "s_waitcnt vmcnt(0)\n"
     :: "s"(wo),"s"(wd),"s"(x),"s"(out),"s"(ws),"s"(blockIdx.x),"s"(epoch),"v"(threadIdx.x) : "v1", "v2", "v3", "v4", "v5", "v6", "v7", "v8", "v9", "v10", "v11", "v12", "v13", "v14", "v15", "v16", "v17", "v18", "v19", "v20", "v21", "v22", "v23", "v24", "v25", "v26", "v27", "v28", "v29", "v30", "v31", "v32", "v33", "v34", "v35", "v36", "v37", "v38", "v39", "v40", "v41", "v42", "v43", "v44", "v45", "v46", "v47", "v48", "v49", "v50", "v51", "v52", "v53", "v54", "v55", "v56", "v57", "v58", "v59", "v60", "v61", "v62", "v63", "v64", "v65", "v66", "v67", "v68", "v69", "v70", "v71", "v72", "v73", "v74", "v75", "v76", "v77", "v78", "v79", "v80", "v81", "v82", "v83", "v84", "v85", "v86", "v87", "v88", "v89", "v90", "v91", "v92", "v93", "v94", "v95", "v96", "v97", "v98", "v99", "v100", "v101", "v102", "v103", "v104", "v105", "v106", "v107", "v108", "v109", "v110", "v111", "v112", "v113", "v114", "v115", "v116", "v117", "v118", "v119", "v120", "v121", "v122", "v123", "v124", "v125", "v126", "v127", "v128", "v129", "v130", "v131", "v132", "v133", "v134", "v135", "v136", "v137", "v138", "v139", "v140", "v141", "v142", "v143", "v144", "v145", "v146", "v147", "v148", "v149", "v150", "v151", "v152", "v153", "v154", "v155", "v156", "v157", "v158", "v159", "v160", "v161", "v162", "v163", "v164", "v165", "v166", "v167", "v168", "v169", "v170", "v171", "v172", "v173", "v174", "v175", "v176", "v177", "v178", "v179", "v180", "v181", "v182", "v183", "v184", "v185", "v186", "v187", "v188", "v189", "v190", "v191", "v192", "v193", "v194", "v195", "v196", "v197", "v198", "v199", "v200", "v201", "v202", "v203", "v204", "v205", "v206", "v207", "v208", "v209", "v210", "v211", "v212", "v213", "v214", "v215", "v216", "v217", "v218", "v219", "v220", "v221", "v222", "v223", "v224", "v225", "v226", "v227", "v228", "v229", "v230", "v231", "v232", "v233", "v234", "v235", "v236", "v237", "v238", "v239", "v240", "v241", "v242", "v243", "v244", "v245", "v246", "v247", "v248", "v249", "v250", "v251", "v252", "v253", "v254", "v255", "a0", "a1", "a2", "a3", "a4", "a5", "a6", "a7", "a8", "a9", "a10", "a11", "a12", "a13", "a14", "a15", "a16", "a17", "a18", "a19", "a20", "a21", "a22", "a23", "a24", "a25", "a26", "a27", "a28", "a29", "a30", "a31", "a32", "a33", "a34", "a35", "a36", "a37", "a38", "a39", "a40", "a41", "a42", "a43", "a44", "a45", "a46", "a47", "a48", "a49", "a50", "a51", "a52", "a53", "a54", "a55", "a56", "a57", "a58", "a59", "a60", "a61", "a62", "a63", "a64", "a65", "a66", "a67", "a68", "a69", "a70", "a71", "a72", "a73", "a74", "a75", "a76", "a77", "a78", "a79", "a80", "a81", "a82", "a83", "a84", "a85", "a86", "a87", "a88", "a89", "a90", "a91", "a92", "a93", "a94", "a95", "a96", "a97", "a98", "a99", "a100", "a101", "a102", "a103", "a104", "a105", "a106", "a107", "a108", "a109", "a110", "a111", "a112", "a113", "a114", "a115", "a116", "a117", "a118", "a119", "a120", "a121", "a122", "a123", "a124", "a125", "a126", "a127", "a128", "a129", "a130", "a131", "a132", "a133", "a134", "a135", "a136", "a137", "a138", "a139", "a140", "a141", "a142", "a143", "a144", "a145", "a146", "a147", "a148", "a149", "a150", "a151", "a152", "a153", "a154", "a155", "a156", "a157", "a158", "a159", "a160", "a161", "a162", "a163", "a164", "a165", "a166", "a167", "a168", "a169", "a170", "a171", "a172", "a173", "a174", "a175", "a176", "a177", "a178", "a179", "a180", "a181", "a182", "a183", "a184", "a185", "a186", "a187", "a188", "a189", "a190", "a191", "a192", "a193", "a194", "a195", "a196", "a197", "a198", "a199", "a200", "a201", "a202", "a203", "a204", "a205", "a206", "a207", "a208", "a209", "a210", "a211", "a212", "a213", "a214", "a215", "a216", "a217", "a218", "a219", "a220", "a221", "a222", "a223", "a224", "a225", "a226", "a227", "a228", "a229", "a230", "a231", "a232", "a233", "a234", "a235", "a236", "a237", "a238", "a239", "a240", "a241", "a242", "a243", "a244", "a245", "a246", "a247", "a248", "a249", "a250", "a251", "a252", "a253", "a254", "a255", "s36", "s37", "s38", "s39", "s40", "s41", "s42", "s43", "s44", "s45", "s46", "s47", "s48", "s49", "s50", "s51", "s52", "s53", "s54", "s55", "s56", "s57", "s58", "s59", "s60", "s61", "s62", "s63", "s64", "s65", "s66", "s67", "s68", "s69", "s70", "s71", "s72", "s73", "s74", "s75", "s76", "s77", "s78", "s79", "s80", "s81", "s82", "s83", "s84", "s85", "s86", "s87", "s88", "s89", "s90", "s91", "s92", "s93", "s94", "s95", "s96", "s97", "s98", "s99", "vcc", "scc", "memory");
  }
}

extern "C" void kernel_launch(void* const* d_in, const int* in_sizes, int n_in, void* d_out, int out_size,
                              void* d_ws, size_t ws_size, hipStream_t stream) {
  const u16* x=(const u16*)d_in[0]; const u16* ckv=(const u16*)d_in[1]; const u16* wq=(const u16*)d_in[2];
  const u16* wuk=(const u16*)d_in[3]; const u16* wuv=(const u16*)d_in[4]; const u16* wo=(const u16*)d_in[5];
  const u16* wg=(const u16*)d_in[7]; const u16* wu=(const u16*)d_in[8]; const u16* wd=(const u16*)d_in[9];
  (void)in_sizes; (void)n_in; (void)out_size; (void)ws_size;
  (void)hipFuncSetAttribute((const void*)k_main,hipFuncAttributeMaxDynamicSharedMemorySize,163840);
  k_main<<<256,256,163840,stream>>>(x,ckv,wq,wuk,wuv,wo,wg,wu,wd,(u16*)d_out,(char*)d_ws);
}
```
